```python
import torch

def _softmax(x, axis=-1):
    return torch.softmax(x, dim=axis)
def _gelu(x):
    return torch.nn.functional.gelu(x, approximate="tanh")
def _silu(x):
    return torch.nn.functional.silu(x)
def _sigmoid(x):
    return torch.sigmoid(x)
def _layernorm(x, axis=-1, eps=1e-5):
    m = x.mean(dim=axis, keepdim=True)
    v = x.var(dim=axis, keepdim=True, unbiased=False)
    return (x - m) / torch.sqrt(v + eps)
def _scatter_add(tgt, idx, src, axis=0):
    return tgt.clone().index_add_(axis, idx, src)
def _segment_sum(src, seg, n):
    out = torch.zeros((n,) + src.shape[1:], dtype=src.dtype)
    return out.index_add_(0, seg, src)
def _pad(x, pw):
    flat = tuple(v for pair in reversed(pw) for v in pair)
    return torch.nn.functional.pad(x, flat)

kernel_name = 'sequential_preLN__spectral_fnet__moe_arch_mod__prompt'

def setup_inputs():

    _w = (lambda a: (lambda t: t.to(torch.bfloat16) if t.is_floating_point() else t)(torch.as_tensor(a)))
    def _A_setup():
        B, S, D = 1, 2048, 1024
        ns = torch.arange(S)[:, None] * torch.arange(S)[None, :]
        nd = torch.arange(D)[:, None] * torch.arange(D)[None, :]
        Ws_re = torch.cos(-2.0 * torch.pi * ns / S).to(float)
        Ws_im = torch.sin(-2.0 * torch.pi * ns / S).to(float)
        Wd_re = torch.cos(-2.0 * torch.pi * nd / D).to(float)
        Wd_im = torch.sin(-2.0 * torch.pi * nd / D).to(float)
        return {
            "x": _w((lambda *s: torch.randn(*s, dtype=torch.bfloat16) * 0.05)(B, S, D)),
            "dft_seq_re": _w(Ws_re),
            "dft_seq_im": _w(Ws_im),
            "dft_hid_re": _w(Wd_re),
            "dft_hid_im": _w(Wd_im),
        }
    _A_in = _A_setup()

    def _B_setup():
        B, S, D, F, C = 1, 2048, 1024, 3584, 1
        return {
            "x": _w((lambda *s: torch.randn(*s, dtype=torch.bfloat16) * 0.05)(B, S, D)),
            "w_r": _w((lambda *s: torch.randn(*s, dtype=torch.bfloat16) * 1.6)(D)),
            "w_gate": _w((lambda *s: torch.randn(*s, dtype=torch.bfloat16) * 0.4)(D, F)),
            "w_up": _w((lambda *s: torch.randn(*s, dtype=torch.bfloat16) * 0.4)(D, F)),
            "w_down": _w((lambda *s: torch.randn(*s, dtype=torch.bfloat16) * 0.4)(F, D)),
        }
    _B_in = _B_setup()

    return {
        "A_x": _A_in["x"],
        "A_dft_seq_re": _A_in["dft_seq_re"],
        "A_dft_seq_im": _A_in["dft_seq_im"],
        "A_dft_hid_re": _A_in["dft_hid_re"],
        "A_dft_hid_im": _A_in["dft_hid_im"],
        "B_x": _B_in["x"],
        "B_w_r": _B_in["w_r"],
        "B_w_gate": _B_in["w_gate"],
        "B_w_up": _B_in["w_up"],
        "B_w_down": _B_in["w_down"],
    }

def reference(A_x, A_dft_seq_re, A_dft_seq_im, A_dft_hid_re, A_dft_hid_im, B_x, B_w_r, B_w_gate, B_w_up, B_w_down):
    def _A_ref(x, dft_seq_re, dft_seq_im, dft_hid_re, dft_hid_im):
        a = torch.einsum("bsd,ed->bse", x, dft_hid_re)
        b = torch.einsum("bsd,ed->bse", x, dft_hid_im)
        y = torch.einsum("ts,bse->bte", dft_seq_re, a) - torch.einsum("ts,bse->bte", dft_seq_im, b)
        return y

    def _B_ref(x, w_r, w_gate, w_up, w_down):
        B, S, D = x.shape
        C = 1
        r = x @ w_r
        idx = torch.argsort(-r, axis=-1)[:, :C]
        idx_e = (lambda x, n, axis: torch.repeat_interleave(x, n, dim=axis))(idx.reshape(B, C, 1), D, axis=2)
        x_sel = (lambda x, i, axis: torch.take_along_dim(x, i, dim=axis))(x, idx_e, axis=1)
        g = _silu(x_sel @ w_gate)
        u = x_sel @ w_up
        y = (g * u) @ w_down
        r_sel = _sigmoid((lambda x, i, axis: torch.take_along_dim(x, i, dim=axis))(r, idx, axis=1))
        y = y * r_sel.reshape(B, C, 1)
        b_off = (torch.arange(B) * S).reshape(B, 1)
        flat_idx = (idx + b_off).reshape(B * C)
        zero = (x * 0.0).reshape(B * S, D)
        upd = _scatter_add(zero, flat_idx, y.reshape(B * C, D), axis=0)
        return x + upd.reshape(B, S, D)

    x = A_x
    h = x + _A_ref(_layernorm(x, axis=-1), A_dft_seq_re, A_dft_seq_im, A_dft_hid_re, A_dft_hid_im)
    h2 = h + _B_ref(_layernorm(h, axis=-1), B_w_r, B_w_gate, B_w_up, B_w_down)
    return h2
```

```cpp
#include <hip/hip_runtime.h>
#include <hip/hip_bf16.h>
#include <stdint.h>

typedef unsigned short bf16_t;
typedef short bf16x8 __attribute__((ext_vector_type(8)));
typedef short bf16x4 __attribute__((ext_vector_type(4)));
typedef float f32x4 __attribute__((ext_vector_type(4)));
typedef unsigned u32x4 __attribute__((ext_vector_type(4)));
typedef unsigned u32x2 __attribute__((ext_vector_type(2)));
typedef unsigned u32x3 __attribute__((ext_vector_type(3)));
typedef unsigned long long u64;
#define LAS __attribute__((address_space(3)))
#define GAS __attribute__((address_space(1)))

constexpr int S_LEN = 2048, D_LEN = 1024, F_LEN = 3584;
constexpr unsigned SENT = 0xFFFFFFFFu;
constexpr int SPIN_CAP = 1 << 17;

constexpr size_t WS_KEYS = 0;
constexpr size_t WS_FLAGS = 16384;
constexpr size_t WS_XRD = 65536;
constexpr size_t WS_FLAG1 = 196608;
constexpr size_t WS_TAG = 131072;
constexpr size_t WS_VOTE = WS_TAG + 4096;
constexpr size_t WS_PROBE = (size_t)20 << 20;
constexpr size_t WS_AT = (size_t)4 << 20;
constexpr size_t WS_Y = (size_t)12 << 20;
constexpr size_t WS_STATS = (size_t)16 << 20;
constexpr size_t WS_PART = WS_STATS + (512u << 10);
constexpr size_t WS_CAND = WS_STATS + (1024u << 10);
constexpr size_t WS_PRE = WS_STATS + (1152u << 10);
constexpr size_t FILL_BYTES = (size_t)1408 << 10;

__device__ __forceinline__ float bflo(unsigned v) { return __uint_as_float(v << 16); }
__device__ __forceinline__ float bfhi(unsigned v) { return __uint_as_float(v & 0xffff0000u); }
__device__ __forceinline__ unsigned pk_bf16(float lo, float hi) {
    typedef __bf16 bf2 __attribute__((ext_vector_type(2)));
    typedef float f2 __attribute__((ext_vector_type(2)));
    f2 v = {lo, hi};
    bf2 r = __builtin_convertvector(v, bf2);
    return __builtin_bit_cast(unsigned, r);
}
__device__ __forceinline__ float rbf(float x) { return __uint_as_float(pk_bf16(0.f, x)); }

template <int CTRL> __device__ __forceinline__ unsigned dppu(unsigned v) {
    return (unsigned)__builtin_amdgcn_update_dpp(0, (int)v, CTRL, 0xf, 0xf, true);
}
template <int CTRL> __device__ __forceinline__ float dppf(float v) { return __uint_as_float(dppu<CTRL>(__float_as_uint(v))); }
__device__ __forceinline__ void swap16(unsigned v, unsigned& a, unsigned& b) {
    auto r = __builtin_amdgcn_permlane16_swap(v, v, false, false);
    a = r[0]; b = r[1];
}
__device__ __forceinline__ void swap32(unsigned v, unsigned& a, unsigned& b) {
    auto r = __builtin_amdgcn_permlane32_swap(v, v, false, false);
    a = r[0]; b = r[1];
}
__device__ __forceinline__ float sum_rows(float v) {
    unsigned a, b;
    swap16(__float_as_uint(v), a, b); v = __uint_as_float(a) + __uint_as_float(b);
    swap32(__float_as_uint(v), a, b); v = __uint_as_float(a) + __uint_as_float(b);
    return v;
}
__device__ __forceinline__ float wave_sum(float v) {
    v += dppf<0xB1>(v);
    v += dppf<0x4E>(v);
    v += dppf<0x141>(v);
    v += dppf<0x140>(v);
    return sum_rows(v);
}
__device__ __forceinline__ unsigned wave_max_u32(unsigned v) {
    v = max(v, dppu<0xB1>(v));
    v = max(v, dppu<0x4E>(v));
    v = max(v, dppu<0x141>(v));
    v = max(v, dppu<0x140>(v));
    unsigned a, b;
    swap16(v, a, b); v = max(a, b);
    swap32(v, a, b); v = max(a, b);
    return v;
}
__device__ __forceinline__ unsigned wave_max_s(unsigned v) {
    v = max(v, dppu<0xB1>(v));
    v = max(v, dppu<0x4E>(v));
    v = max(v, dppu<0x141>(v));
    v = max(v, dppu<0x140>(v));
    v = max(v, dppu<0x142>(v));
    v = max(v, dppu<0x143>(v));
    return (unsigned)__builtin_amdgcn_readlane((int)v, 63);
}
__device__ __forceinline__ float sel4(int g, float a, float b, float c, float d) {
    const float t0 = (g & 1) ? b : a, t1 = (g & 1) ? d : c;
    return (g & 2) ? t1 : t0;
}
__device__ __forceinline__ float pick(u64 m, float a, float b) {
    float r;
    asm("v_cndmask_b32 %0, %1, %2, %3" : "=v"(r) : "v"(a), "v"(b), "s"(m));
    return r;
}
__device__ __forceinline__ unsigned umax4(u32x4 v) { return max(max(v[0], v[1]), max(v[2], v[3])); }

__device__ __forceinline__ void touch_line(const void* p, unsigned lds_dst) {
    unsigned keep;
    asm volatile("s_mov_b32 %0, m0\n\ts_mov_b32 m0, %2\n\ts_nop 0\n\tglobal_load_lds_dword %1, off\n\ts_mov_b32 m0, %0"
                 : "=&s"(keep) : "v"(p), "s"(lds_dst) : "memory");
}
__device__ __forceinline__ __amdgpu_buffer_rsrc_t mk_rsrc(const void* p, unsigned bytes) {
    return __builtin_amdgcn_make_buffer_rsrc((void*)p, 0, (int)bytes, 0x00020000);
}
#define LDS_LOAD(w, v, o, pol) "buffer_load_" w " " v ", %0, %2 offen" o pol " lds"
#define LDS_M0 "s_mov_b32 m0, %1\n\ts_nop 0\n\t"
__device__ __forceinline__ void dma_nt(__amdgpu_buffer_rsrc_t r, unsigned dst, int voff, int soff) {
    asm volatile(LDS_M0 LDS_LOAD("dwordx4", "%3", "", " nt") :: "s"(r), "s"(dst), "s"(soff), "v"(voff) : "memory", "m0");
}
template <bool SC1> __device__ __forceinline__ void dma_word(__amdgpu_buffer_rsrc_t r, unsigned dst, int voff) {
    if constexpr (SC1) asm volatile(LDS_M0 LDS_LOAD("dword", "%3", "", " sc1") :: "s"(r), "s"(dst), "n"(0), "v"(voff) : "memory", "m0");
    else asm volatile(LDS_M0 LDS_LOAD("dword", "%3", "", "") :: "s"(r), "s"(dst), "n"(0), "v"(voff) : "memory", "m0");
}
template <bool SC1> __device__ __forceinline__ void dma_2k(__amdgpu_buffer_rsrc_t r, unsigned dst, int v0, int v1, int soff) {
    if constexpr (SC1) asm volatile(LDS_M0 LDS_LOAD("dwordx4", "%3", "", " sc1") "\n\t" LDS_LOAD("dwordx4", "%4", " offset:1024", " sc1")
                 :: "s"(r), "s"(dst), "s"(soff), "v"(v0), "v"(v1) : "memory", "m0");
    else asm volatile(LDS_M0 LDS_LOAD("dwordx4", "%3", "", "") "\n\t" LDS_LOAD("dwordx4", "%4", " offset:1024", "")
                 :: "s"(r), "s"(dst), "s"(soff), "v"(v0), "v"(v1) : "memory", "m0");
}
template <bool SC1> __device__ __forceinline__ void dma_4k(__amdgpu_buffer_rsrc_t r, unsigned dst, const int (&v)[4], int soff) {
#define FOUR(pol) asm volatile(LDS_M0 LDS_LOAD("dwordx4", "%3", "", pol) "\n\t" LDS_LOAD("dwordx4", "%4", " offset:1024", pol) "\n\t" \
        LDS_LOAD("dwordx4", "%5", " offset:2048", pol) "\n\t" LDS_LOAD("dwordx4", "%6", " offset:3072", pol) \
        :: "s"(r), "s"(dst), "s"(soff), "v"(v[0]), "v"(v[1]), "v"(v[2]), "v"(v[3]) : "memory", "m0")
    if constexpr (SC1) FOUR(" sc1"); else FOUR("");
#undef FOUR
}
#define CODE_BYTES 26368
__device__ __forceinline__ __amdgpu_buffer_rsrc_t code_rsrc() {
    unsigned long long p; unsigned d;
    asm volatile("s_getpc_b64 %0\n1:\n\ts_mov_b32 %1, 1b-_Z12fused_kernel4Args" : "=s"(p), "=s"(d));
    return __builtin_amdgcn_make_buffer_rsrc((void*)(p - d), 0, CODE_BYTES, 0x00020000);
}
#define LD16_SC1(rs, off) __builtin_amdgcn_raw_buffer_load_b128(rs, off, 0, 16)
#define ST16_SC1(v, rs, off) __builtin_amdgcn_raw_buffer_store_b128(v, rs, off, 0, 16)

constexpr int BT = 128, BK = 64, NSTAGE = 4;
constexpr int TILE_BYTES = BT * BK * 2;
constexpr int STAGE_BYTES = 2 * TILE_BYTES;
constexpr int RING_BYTES = NSTAGE * STAGE_BYTES;
constexpr int L_DUMMY = RING_BYTES;
constexpr int L_CTL = RING_BYTES + 256;
constexpr int L_RED = RING_BYTES + 512;

#define WAIT_VM(n) asm volatile("s_waitcnt vmcnt(" #n ")" ::: "memory")
#define WAIT_VM0() do { asm volatile("" ::: "memory"); __builtin_amdgcn_s_waitcnt(0x0F70); asm volatile("s_waitcnt vmcnt(0)" ::: "memory"); } while (0)
#define WAIT_LGKM0() asm volatile("s_waitcnt lgkmcnt(0)" ::: "memory")
#define BARRIER() do { asm volatile("" ::: "memory"); __builtin_amdgcn_s_barrier(); asm volatile("" ::: "memory"); } while (0)

__device__ __forceinline__ int prow(int rho) {
    const int w = rho >> 6, m = (rho >> 4) & 3, q = (rho >> 2) & 3, r = rho & 3;
    return w * 64 + (m >> 1) * 32 + q * 8 + (m & 1) * 4 + r;
}
__device__ __forceinline__ unsigned r2key(float r, unsigned code) {
    const unsigned u = pk_bf16(0.f, r);
    return ((u ^ ((unsigned)((int)u >> 31) | 0x80000000u)) & 0xffff0000u) | code;
}
__device__ __forceinline__ float key2r(unsigned k) {
    const unsigned o = k >> 16;
    return bflo((o & 0x8000u) ? (o & 0x7fffu) : (~o & 0xffffu));
}
#define CK_T -1, -1, -1, -1, 2, 2, 2, 2
#define STAT_M 5
#define W_PRE 2
#define W_PRE_SL 20
#define BQ 2
#define POLL_T (STAT_M + 5)
#define AUX_SLEEP 12
#define WD_EARLY 2
#define C2_START 0
#define C2_STAG 7
#define TAIL_PRIO 3
#define STAT_DELAY 12
#define CODE_WG 1
#define AUX_STAY 1
#define T_DELAY 0
#define WS_END 1
#define CATCH1 4
#define CPF_MASK1 0x1080u
#define CPF_MASK2 0x1080u
#define CPF_STAG 5
#define CPF_FIX 0x1000u
#define OUT_AUX 0
#define KEY_S 128
#define FIN_SLEEP 1
#define G_N 8
#define POLL_SLEEP 4
__device__ __forceinline__ f32x4 smf(bf16x8 a, bf16x8 b0, bf16x8 b1, f32x4 d, unsigned idx) {
    typedef __bf16 bf8 __attribute__((ext_vector_type(8)));
    typedef __bf16 bf16v __attribute__((ext_vector_type(16)));
    const auto b = __builtin_shufflevector(b0, b1, 0, 1, 2, 3, 4, 5, 6, 7, 8, 9, 10, 11, 12, 13, 14, 15);
    return __builtin_amdgcn_smfmac_f32_16x16x64_bf16(__builtin_bit_cast(bf8, a), __builtin_bit_cast(bf16v, b), d, (int)idx, 0, 0);
}
__device__ __forceinline__ float dot2bf(unsigned a, unsigned b, float c) {
    typedef __bf16 bf2 __attribute__((ext_vector_type(2)));
    return __builtin_amdgcn_fdot2_f32_bf16(__builtin_bit_cast(bf2, a), __builtin_bit_cast(bf2, b), c, false);
}

#define STAMP(i) do { } while (0)
#define CSTAMP(i) do { } while (0)
#define FLUSH() do { } while (0)
#define TSV do { } while (0)

#define KSHIFT 4

constexpr int L_HT = ((32 + KSHIFT) % NSTAGE) * STAGE_BYTES;
constexpr int L_WD0 = ((33 + KSHIFT) % NSTAGE) * STAGE_BYTES;
constexpr int L_WD1 = ((34 + KSHIFT) % NSTAGE) * STAGE_BYTES;
constexpr int L_YB = RING_BYTES;
constexpr int L_ATAB = L_YB;
constexpr int LDS_BYTES = RING_BYTES + 32768;

#define SB() __builtin_amdgcn_sched_barrier(0)
template <int N> __device__ __forceinline__ void wait_vm() { asm volatile("s_waitcnt vmcnt(%0)" :: "n"(N) : "memory"); }
__device__ __forceinline__ int at_tile(int c, int par) { return ((c << 8) & 0x78000) | (((c << 2) & 0x2000) ^ par); }
__device__ __forceinline__ int at_panel(int ep) { return ep << 19; }
__device__ __forceinline__ int pre_off(int slot) { const int s_ = slot >= 56, l = slot - 56 * s_; return (l >> 2) * 16384 + s_ * 8192 + (l & 3) * 2048; }
static_assert(KSHIFT == 4, "flag samples must have replaced older contents of their slot; even tile counts");
struct Ring {
    __amdgpu_buffer_rsrc_t rP, rQ, rF;
    int voP[4], voQ[4], voF;
    LAS unsigned char* fl;
    unsigned lb;
    int sp, sq;
    int poff, par;
    bool img;
    template <int LD, bool PIMG> __device__ __forceinline__ void setup(int lw, int lane) {
        const int r = lane >> 3, v0 = (PIMG ? r : (r & 3) + (r & 4) * 2) * (LD * 2) + (((lane & 7) ^ (lane >> 4)) << 4);
#pragma unroll
        for (int j = 0; j < 4; ++j) {
            voQ[j] = (v0 ^ ((j & 1) * 64)) + (PIMG ? j * 8 : (j & 1) * 16 + (j >> 1) * 4) * (LD * 2) - j * 1024;
            voP[j] = PIMG ? (lw >> 1) * 16384 + (lw & 1) * 4096 + lane * 16 : voQ[j];
        }
        img = PIMG;
    }
    __device__ __forceinline__ void seek(int col, int pof) {
        sq = col; poff = pof;
        sp = (img ? at_tile(sq, par) : sq) + poff;
    }
    __device__ __forceinline__ void step() {
        sq = (sq + BK * 2) & 4095;
        sp = (img ? at_tile(sq, par) : sq) + poff;
        asm volatile("" : "+s"(sq), "+s"(sp));
    }
    __device__ __forceinline__ void sample() { dma_word<true>(rF, (unsigned)(size_t)fl, voF); }
    __device__ __forceinline__ unsigned peek(unsigned addr) {
        unsigned v;
        asm volatile("ds_read_b32 %0, %1\n\ts_waitcnt lgkmcnt(0)" : "=v"(v) : "v"(addr) : "memory");
        return v;
    }
    __device__ __forceinline__ bool seen() { return __builtin_amdgcn_readfirstlane(peek((unsigned)(size_t)fl)) == 1u; }
    __device__ __forceinline__ void gate(unsigned want, unsigned sa, unsigned need, unsigned& have) {
#pragma unroll 1
        for (int spin = 0; spin <= SPIN_CAP && (need & ~have) != 0u; ++spin) {
            if (spin) { sample(); WAIT_VM(0); __builtin_amdgcn_s_sleep(1); }
            const unsigned long long m = __ballot(peek(sa) == want);
            have = (unsigned)m & (unsigned)(m >> 32);
        }
    }
    template <int AUX> __device__ __forceinline__ void issueP(LAS unsigned char* base) { dma_4k<AUX != 0>(rP, (unsigned)(size_t)base, voP, sp); }
    __device__ __forceinline__ void issueQ(LAS unsigned char* base) { dma_4k<false>(rQ, (unsigned)(size_t)base + TILE_BYTES, voQ, sq); }
    template <bool P2> __device__ __forceinline__ void issue(LAS unsigned char* lds) {
        issueP<P2 ? 16 : 0>(lds + lb);
        issueQ(lds + lb);
        SB();
        if (P2) sample();
        lb = (lb + STAGE_BYTES) & (RING_BYTES - 1);
        asm volatile("" : "+s"(lb));
        step();
        SB();
    }
    template <int CAT> __device__ __forceinline__ void ramp(LAS unsigned char* lds) {
#pragma unroll 1
        for (int i = 1; i <= CAT + 1; ++i) {
            issue<false>(lds);
            if (i == CAT + 1) continue;
            WAIT_VM(8);
            BARRIER();
        }
    }
    template <bool P2, bool SAMP, int W = 16> __device__ __forceinline__ void steady(int cnt) {
        int t, u;
#define LD(v, r, s, o) "buffer_load_dwordx4 " v ", " r ", " s " offen" o " lds\n\t"
        if constexpr (P2) {
#define BODY(SMP) asm volatile("s_mov_b32 m0, %0\n\ts_branch 2f\n1:\n\ts_waitcnt vmcnt(%17)\n\ts_barrier\n2:\n\t" \
            LD("%8", "%6", "%2", " sc1") LD("%8", "%6", "%2", " offset:1024 sc1") LD("%8", "%6", "%2", " offset:2048 sc1") LD("%8", "%6", "%2", " offset:3072 sc1") \
            SMP "s_mov_b32 %4, %1\n\ts_add_i32 %1, %1, 0x80\n\ts_add_i32 m0, %0, 0x4000\n\ts_and_b32 %1, %1, 0xfff\n\t" \
            LD("%9", "%7", "%4", "") "s_lshl_b32 %2, %1, 8\n\ts_lshl_b32 %5, %1, 2\n\ts_and_b32 %2, %2, 0x78000\n\t" \
            LD("%10", "%7", "%4", " offset:1024") "s_and_b32 %5, %5, 0x2000\n\ts_xor_b32 %5, %5, %13\n\ts_or_b32 %2, %2, %5\n\t" \
            LD("%11", "%7", "%4", " offset:2048") "s_add_i32 %0, %0, 0x8000\n\ts_and_b32 %0, %0, 0x1ffff\n\ts_add_i32 %3, %3, -1\n\ts_cmp_lg_u32 %3, 0\n\t" \
            LD("%12", "%7", "%4", " offset:3072") "s_mov_b32 m0, %0\n\ts_cbranch_scc1 1b\n\ts_waitcnt vmcnt(%17)\n\ts_barrier" \
            : "+s"(lb), "+s"(sq), "+s"(sp), "+s"(cnt), "=&s"(t), "=&s"(u) \
            : "s"(rP), "s"(rQ), "v"(voP[0]), "v"(voQ[0]), "v"(voQ[1]), "v"(voQ[2]), "v"(voQ[3]), "s"(par | poff), "s"(rF), "v"(voF), "s"((unsigned)(size_t)fl), "n"(W) \
            : "memory", "scc", "m0")
            if constexpr (SAMP) BODY("s_mov_b32 m0, %16\n\ts_nop 0\n\tbuffer_load_dword %15, %14, 0 offen sc1 lds\n\t");
            else BODY("");
#undef BODY
        } else {
            asm volatile("s_mov_b32 m0, %0\n\ts_branch 2f\n1:\n\ts_waitcnt vmcnt(16)\n\ts_barrier\n2:\n\t"
                LD("%6", "%4", "%1", "") LD("%7", "%4", "%1", " offset:1024") LD("%8", "%4", "%1", " offset:2048") LD("%9", "%4", "%1", " offset:3072")
                "s_add_i32 m0, %0, 0x4000\n\ts_mov_b32 %3, %1\n\t"
                LD("%6", "%5", "%3", "") "s_add_i32 %1, %1, 0x80\n\ts_add_i32 %0, %0, 0x8000\n\t"
                LD("%7", "%5", "%3", " offset:1024") "s_and_b32 %0, %0, 0x1ffff\n\ts_add_i32 %2, %2, -1\n\t"
                LD("%8", "%5", "%3", " offset:2048") "s_cmp_lg_u32 %2, 0\n\t"
                LD("%9", "%5", "%3", " offset:3072") "s_mov_b32 m0, %0\n\ts_cbranch_scc1 1b\n\ts_waitcnt vmcnt(16)\n\ts_barrier"
                : "+s"(lb), "+s"(sq), "+s"(cnt), "=&s"(t)
                : "s"(rP), "s"(rQ), "v"(voQ[0]), "v"(voQ[1]), "v"(voQ[2]), "v"(voQ[3])
                : "memory", "scc", "m0");
            (void)u;
        }
#undef LD
    }
};

template <bool PLIN, bool FIRST, class Pre>
__device__ __forceinline__ void compute_loop(const LAS unsigned char* lds, f32x4 (&acc)[4][4], int NT, int wp, int wq, int lane, Pre pre) {
    const int fr = lane & 15, fq = lane >> 4, key = (fr >> 1) & 7;
    const unsigned l0 = (unsigned)(size_t)lds;
    const unsigned oP0 = l0 + (PLIN ? wp * 8192 + lane * 16 : (wp * 64 + fr) * 128 + ((fq ^ key) * 16));
    const unsigned oP1 = l0 + (PLIN ? wp * 8192 + 1024 + lane * 16 : (wp * 64 + fr) * 128 + (((4 + fq) ^ key) * 16));
    const unsigned oQ0 = l0 + TILE_BYTES + (wq * 64 + fr) * 128 + ((fq ^ key) * 16);
    const unsigned oQ1 = l0 + TILE_BYTES + (wq * 64 + fr) * 128 + (((4 + fq) ^ key) * 16);
    unsigned aP0 = oP0, aQ0 = oQ0, aP1 = oP1, aQ1 = oQ1;
    bf16x8 p0[4], p1[4], r0[4], r1[4], q0[4], q1[4];
#pragma unroll
    for (int i = 0; i < 16; ++i) asm volatile("" : "+v"(acc[i >> 2][i & 3]));
    asm volatile("" : "+v"(aP0), "+v"(aQ0), "+v"(aP1), "+v"(aQ1));
#define MM(pp, qq, m, n) do { acc[m][n] = __builtin_amdgcn_mfma_f32_16x16x32_bf16(pp[m], qq[n], acc[m][n], 0, 0, 0); SB(); } while (0)
#define RD(dst, i, a, st) do { dst[i] = *(const LAS bf16x8*)((const LAS unsigned char*)(size_t)(a) + (st) * STAGE_BYTES + (i) * 2048); SB(); } while (0)
#define ADR(a) do { a ^= 2 * STAGE_BYTES; asm volatile("" : "+v"(a)); SB(); } while (0)
    __builtin_amdgcn_s_waitcnt(0xC07F);
    SB();
    if (FIRST) BARRIER();
    SB();
    RD(p0, 0, aP0, 0); RD(q0, 0, aQ0, 0); RD(p1, 0, aP1, 0); RD(q1, 0, aQ1, 0); RD(p1, 1, aP1, 0); RD(p0, 1, aP0, 0);
    RD(p0, 2, aP0, 0); RD(p1, 2, aP1, 0); RD(p1, 3, aP1, 0); RD(p0, 3, aP0, 0);
    pre();
    SB();
    int t = NT;
#pragma unroll 1
    do {
        MM(p0, q0, 0, 0); MM(p1, q1, 0, 0); RD(q0, 1, aQ0, 0);
        MM(p1, q1, 1, 0); MM(p0, q0, 1, 0); RD(q1, 1, aQ1, 0);
        MM(p0, q0, 2, 0); MM(p1, q1, 2, 0); RD(q0, 2, aQ0, 0);
        MM(p1, q1, 3, 0); MM(p0, q0, 3, 0); RD(q1, 2, aQ1, 0);
        MM(p0, q0, 3, 1); MM(p1, q1, 3, 1); RD(q0, 3, aQ0, 0);
        MM(p1, q1, 2, 1); MM(p0, q0, 2, 1); RD(q1, 3, aQ1, 0);
        MM(p0, q0, 1, 1); MM(p1, q1, 1, 1);
        MM(p1, q1, 0, 1); MM(p0, q0, 0, 1);
        MM(p0, q0, 0, 2); MM(p1, q1, 0, 2); __builtin_amdgcn_s_waitcnt(0xC07F); SB(); BARRIER(); SB();
        MM(p1, q1, 1, 2); RD(r0, 0, aP0, 1); MM(p0, q0, 1, 2); RD(q0, 0, aQ0, 1);
        MM(p0, q0, 2, 2); RD(r1, 0, aP1, 1); MM(p1, q1, 2, 2); RD(q1, 0, aQ1, 1);
        MM(p1, q1, 3, 2); MM(p0, q0, 3, 2); RD(r1, 1, aP1, 1);
        MM(p0, q0, 3, 3); RD(r0, 1, aP0, 1); MM(p1, q1, 3, 3);
        MM(p1, q1, 2, 3); RD(r0, 2, aP0, 1); MM(p0, q0, 2, 3); RD(r1, 2, aP1, 1);
        MM(p0, q0, 1, 3); MM(p1, q1, 1, 3); RD(r1, 3, aP1, 1);
        MM(p1, q1, 0, 3); RD(r0, 3, aP0, 1); MM(p0, q0, 0, 3);

        MM(r0, q0, 0, 0); MM(r1, q1, 0, 0); RD(q0, 1, aQ0, 1);
        MM(r1, q1, 1, 0); MM(r0, q0, 1, 0); RD(q1, 1, aQ1, 1);
        MM(r0, q0, 2, 0); MM(r1, q1, 2, 0); RD(q0, 2, aQ0, 1);
        MM(r1, q1, 3, 0); MM(r0, q0, 3, 0); RD(q1, 2, aQ1, 1);
        MM(r0, q0, 3, 1); MM(r1, q1, 3, 1); RD(q0, 3, aQ0, 1);
        MM(r1, q1, 2, 1); MM(r0, q0, 2, 1); RD(q1, 3, aQ1, 1);
        MM(r0, q0, 1, 1); t -= 2; asm volatile("" : "+s"(t)); SB(); MM(r1, q1, 1, 1); ADR(aP0);
        MM(r1, q1, 0, 1); ADR(aP1); MM(r0, q0, 0, 1); ADR(aQ0);
        MM(r0, q0, 0, 2); ADR(aQ1); MM(r1, q1, 0, 2); __builtin_amdgcn_s_waitcnt(0xC07F); SB(); BARRIER(); SB();
        MM(r1, q1, 1, 2); RD(p0, 0, aP0, 0); MM(r0, q0, 1, 2); RD(q0, 0, aQ0, 0);
        MM(r0, q0, 2, 2); RD(p1, 0, aP1, 0); MM(r1, q1, 2, 2); RD(q1, 0, aQ1, 0);
        MM(r1, q1, 3, 2); MM(r0, q0, 3, 2); RD(p1, 1, aP1, 0);
        MM(r0, q0, 3, 3); RD(p0, 1, aP0, 0); MM(r1, q1, 3, 3);
        MM(r1, q1, 2, 3); RD(p0, 2, aP0, 0); MM(r0, q0, 2, 3); RD(p1, 2, aP1, 0);
        MM(r0, q0, 1, 3); MM(r1, q1, 1, 3); RD(p1, 3, aP1, 0);
        MM(r1, q1, 0, 3); RD(p0, 3, aP0, 0); MM(r0, q0, 0, 3);
    } while (t != 0);
    SB();
#undef MM
#undef RD
#undef ADR
}

struct Args {
    const bf16_t* x; const bf16_t* wre; const bf16_t* wim; char* ws; const bf16_t* cr; const bf16_t* ci;
    const bf16_t* w_r; const bf16_t* wg; const bf16_t* wu; const bf16_t* wd; bf16_t* out;
};

__device__ __forceinline__ unsigned poll_winner(const char* ws, int rep, int lane, unsigned& mine) {
    GAS unsigned* kp = (GAS unsigned*)(ws + WS_KEYS + (size_t)(rep >> 2) * 8192 + (rep & 3) * (KEY_S * 16) + ((lane >> 2) & 7) * KEY_S) + (lane & 3);
    unsigned kw;
    for (int spin = 0;; ++spin) {
            asm volatile("" ::: "memory");
        kw = __hip_atomic_load(kp, __ATOMIC_RELAXED, __HIP_MEMORY_SCOPE_AGENT);
        if (__all(kw != 0u) || spin > SPIN_CAP) break;
        __builtin_amdgcn_s_sleep(POLL_SLEEP);
    }
    mine = kw;
    return __builtin_amdgcn_readfirstlane(wave_max_u32(kw));
}

__device__ __forceinline__ void stage2(char* w2, int slot, int lw, LAS unsigned char* img0, LAS unsigned char* img1, LAS unsigned char* abuf, int zoff, int lane) {
    const int g = lane >> 4, n = lane & 15;
    const __amdgpu_buffer_rsrc_t rPi = mk_rsrc(w2 + WS_PRE + pre_off(slot), 2048);
    unsigned pp[8];
    auto request = [&]() {
        asm volatile("" ::: "memory");
#pragma unroll
        for (int k = 0; k < 8; ++k) pp[k] = __builtin_amdgcn_raw_buffer_load_b32(rPi, k * 256 + lane * 4, 0, 16);
        SB();
    };
    request();
    bf16x8 wf[16];
    {
        const int q = (lane >> 2) & 3, ky = q | ((g & 1) << 2);
#pragma unroll
        for (int j = 0; j < 16; ++j) {
            LAS unsigned char* t = (g < 2 ? img0 : img1) + (8 * (g & 1) + q) * 512 + (((j & 7) ^ ky) + (j & 8)) * 32 + (lane & 3) * 8;
            const bf16x4 w0 = __builtin_amdgcn_ds_read_tr16_b64_v4i16((LAS bf16x4*)t);
            const bf16x4 w1 = __builtin_amdgcn_ds_read_tr16_b64_v4i16((LAS bf16x4*)(t + 2048));
            wf[j] = __builtin_shufflevector(w0, w1, 0, 1, 2, 3, 4, 5, 6, 7);
        }
        SB();
#pragma unroll
        for (int j = 0; j < 16; ++j) asm volatile("" : "+v"(wf[j]));
    }
    unsigned aa[5], pto = lane * 16;
#pragma unroll
    for (int m = 0; m < 4; ++m) aa[m] = (unsigned)(size_t)abuf + ((n >> 2) == m && ((n >> 1) & 1) == (g >> 1) ? (n & 1) * 32 + (g & 1) * 16 : zoff + g * 16);
    aa[4] = (unsigned)(size_t)abuf + ((lane >> 1) & 1) * 32 + ((lane >> 4) & 1) * 16 + ((lane >> 2) & 3) * 4 + (lane & 1) * 2;
    unsigned ix = (n & 1) ? 0xEEEEEEEEu : 0x44444444u;
    __amdgpu_buffer_rsrc_t rPt = mk_rsrc(w2 + WS_PART + __builtin_amdgcn_readfirstlane((slot >> 2) * 16384 + (lw >> 1) * 8192 + (slot & 3) * 2048 + (lw & 1) * 1024), 1024);
    asm volatile("" : "+v"(aa[0]), "+v"(aa[1]), "+v"(aa[2]), "+v"(aa[3]), "+v"(aa[4]), "+v"(pto), "+v"(ix));
    asm volatile("" : "+s"(rPt));
    f32x4 d[2] = {{0.f, 0.f, 0.f, 0.f}, {0.f, 0.f, 0.f, 0.f}};
    asm volatile("" : "+v"(d[0]), "+v"(d[1]));
    SB();
    float sv;
#pragma unroll 1
    for (int spin = 0;; ++spin) {
        sv = ((__uint_as_float(pp[0]) + __uint_as_float(pp[1])) + (__uint_as_float(pp[2]) + __uint_as_float(pp[3]))) +
             ((__uint_as_float(pp[4]) + __uint_as_float(pp[5])) + (__uint_as_float(pp[6]) + __uint_as_float(pp[7])));
        if (__builtin_expect(__builtin_amdgcn_ballot_w64(sv != sv) == 0, 1)) break;
        if (__all(max(max(max(pp[0], pp[1]), max(pp[2], pp[3])), max(max(pp[4], pp[5]), max(pp[6], pp[7]))) != SENT) || spin > SPIN_CAP) break;
        request();
    }
    unsigned ug, uu;
    swap32(__float_as_uint(sv), ug, uu);
    const float gg = rbf(__uint_as_float(ug)), up = rbf(__uint_as_float(uu));
    const unsigned act = pk_bf16(rbf(gg * __builtin_amdgcn_rcpf(1.0f + __expf(-gg))) * up, 0.f);
    *(volatile LAS unsigned short*)(size_t)aa[4] = (unsigned short)act;
    bf16x8 af[4];
#pragma unroll
    for (int m = 0; m < 4; ++m) af[m] = *(volatile LAS bf16x8*)(size_t)aa[m];
    SB();
#pragma unroll
    for (int i = 0; i < 8; ++i) {
        const int j = 8 * (i & 1) + 2 * (i >> 1);
        d[i & 1] = smf(af[i >> 1], wf[j], wf[j + 1], d[i & 1], ix);
        SB();
    }
    u32x4 pv;
#pragma unroll
    for (int q = 0; q < 4; ++q) {
        pv[q] = min(__float_as_uint(d[q >> 1][(q & 1) * 2] + d[q >> 1][(q & 1) * 2 + 1]), SENT - 1u);
        asm volatile("" : "+v"(pv[q]));
    }
    ST16_SC1(pv, rPt, pto);
}
__device__ __forceinline__ int wd_off(int lw, int i, int lane) {
    const int r = 2 * (i & 5) + (lane >> 5), ky = (r & 3) | ((r >> 1) & 4), c = lane & 31;
    return (lane >> 5) * (D_LEN * 2) + lw * 512 + (((c >> 1) ^ ky) * 2 + (c & 1)) * 16;
}

__global__ __launch_bounds__(768, 3) void fused_kernel(const Args a) {
    extern __shared__ __attribute__((aligned(16))) unsigned char smem[];
    constexpr int K = S_LEN, NT1 = D_LEN / BK;
    const int tid = threadIdx.x, wid = __builtin_amdgcn_readfirstlane(tid >> 6), lane = tid & 63;
    const int xcd = blockIdx.x & 7, idx = blockIdx.x >> 3;
    TSV;
    LAS unsigned char* lds = (LAS unsigned char*)smem;
    if (idx < CODE_WG && wid < 4) dma_word<false>(code_rsrc(), (unsigned)(size_t)lds + L_DUMMY, tid * 128);
    SB();
    const int h = xcd & 1, xq = xcd >> 1, rt = xq * 4 + (idx & 3), ct = idx >> 2;
    volatile LAS unsigned* ctl1 = (volatile LAS unsigned*)(lds + L_CTL);
#define NBR(k) ((rt + (k)) & 3)
    unsigned kx;
    Ring ring;
    if (__builtin_expect((wid & 12) == 4, 1)) {
        __builtin_amdgcn_s_setprio(3);
        ring.template setup<D_LEN, false>(wid - 4, lane);
        ring.rP = mk_rsrc(a.x + (size_t)(rt * 4 + wid - 4) * 32 * D_LEN, 32 * D_LEN * 2);
        ring.rQ = mk_rsrc((h == 0 ? a.wre : a.wim) + (size_t)(ct * 4 + wid - 4) * 32 * D_LEN, 32 * D_LEN * 2);
        ring.lb = (wid - 4) * 4096;
        ring.sp = ring.sq = ring.poff = 0;
        ring.template issue<false>(lds);
        __builtin_amdgcn_s_setprio(0);
        ring.par = (ct & 4) << 11;
        ring.template ramp<CATCH1>(lds);
        ring.template steady<false, false>(NT1 - 2 - CATCH1);
        ring.template setup<K, true>(wid - 4, lane);
        ring.rQ = mk_rsrc((h == 0 ? a.cr : a.ci) + (size_t)(rt * 4 + wid - 4) * 32 * K, 32 * K * 2);
        ring.seek(rt * 256, 0);
        LAS unsigned char* l0 = lds + (wid - 4) * 4096;
        ring.issueQ(l0); ring.step();
        WAIT_VM(12); BARRIER();
        ring.issueQ(l0 + STAGE_BYTES);
        ring.seek((xq * 4 + NBR(1)) * 256, 0);
        WAIT_VM(8); BARRIER();
        ring.issueQ(l0 + 2 * STAGE_BYTES);
        ring.seek((xq * 4 + NBR(2)) * 256, 0);
        const int xr = (wid & 1) * 8 + (lane >> 4);
        int vx0 = ((((wid >> 1) & 1) * 64 + xr) * D_LEN + (((lane & 15) ^ xr) * 8)) * 2;
        int vx1 = ((((wid >> 1) & 1) * 64 + xr + 4) * D_LEN + (((lane & 15) ^ (xr + 4)) * 8)) * 2 - 1024;
        __amdgpu_buffer_rsrc_t rX = mk_rsrc(a.x + (size_t)rt * BT * D_LEN + ct * BT, BT * D_LEN * 2 - ct * BT * 2);
        unsigned xd = (unsigned)(size_t)lds + ((wid & 2) ? L_YB + 16384 : 3 * STAGE_BYTES) + (wid & 1) * 2048;
        asm volatile("" : "+v"(vx0), "+v"(vx1));
        asm volatile("" : "+s"(rX), "+s"(xd));
        BARRIER();
        if (h != 0) {
#pragma unroll
            for (int k = 0; k < 4; ++k) dma_2k<false>(rX, xd + k * 4096, vx0, vx1, k * (16 * D_LEN * 2));
        }
        ring.issueQ(l0 + 3 * STAGE_BYTES);
        kx = (unsigned)__builtin_amdgcn_readfirstlane((int)ctl1[4]);
        SB();
    }
    char* ws = a.ws + ((0ull - (unsigned long long)a.ws) & 0x1fffffull);
    const int g = lane >> 4, n = lane & 15;
    LAS float* tab = (LAS float*)(lds + L_RED);
    if (wid >= 8) {
        const int aw = wid - 8;
        GAS u64* tagp = (GAS u64*)(ws + WS_TAG);
        const int r0 = xq * 512 + h * 256 + idx * 8 + aw * 2;
        const int w0 = xq * 256 + idx * 8 + aw * 2;
        const u32x4* src = (const u32x4*)((n < 8 ? a.x + (size_t)(r0 + (n >> 2)) * D_LEN
                                                 : (h == 0 ? a.wre : a.wim) + (size_t)(w0 + (n >> 2) - 2) * D_LEN) + g * 32 + (n & 3) * 8);
        if (AUX_SLEEP) __builtin_amdgcn_s_sleep(AUX_SLEEP);
        u64 tag0 = __hip_atomic_load(tagp, __ATOMIC_RELAXED, __HIP_MEMORY_SCOPE_AGENT);
        u32x4 v[8];
        constexpr int CKT[8] = {CK_T};
#pragma unroll
        for (int j = 0; j < 8; ++j) if (CKT[j] < 0) v[j] = src[j * 16];
        __builtin_amdgcn_s_setprio(3);
        SB();
        const char* cb = (const char*)(h ? a.ci : a.cr) + (size_t)rt * (512u << 10) + ct * 65536 + (lane >> 2) * 4096 + (lane & 3) * 128;
        const GAS u32x4* wrp = (const GAS u32x4*)a.w_r + lane;
        asm volatile("" : "+v"(cb), "+v"(wrp));
        unsigned mask = aw == 1 ? CPF_MASK1 : aw == 2 ? CPF_MASK2 : 0u;
        mask = (mask & CPF_FIX) | ((mask & ~CPF_FIX) << (idx % CPF_STAG));
        int ti = 2 * xq + aw - 1;
        auto warm = [&](int t) { if (t >= 4 && ((mask >> t) & 1)) { touch_line(cb + (ti & 7) * 512, L_DUMMY); ti += 2; } };
#pragma unroll
        for (int t = 0; t <= STAT_M; ++t) {
            SB(); BARRIER(); SB();
            if (aw == 1) STAMP(40 + t);
#pragma unroll
            for (int j = 0; j < 8; ++j) if (CKT[j] == t) v[j] = src[j * 16];
            warm(t);
        }
        SB();
        f32x4 d1 = {0.f, 0.f, 0.f, 0.f}, d2 = {0.f, 0.f, 0.f, 0.f};
        {
            const u32x4 ones = {0x3F803F80u, 0x3F803F80u, 0x3F803F80u, 0x3F803F80u};
#pragma unroll
            for (int j = 0; j < 8; ++j) {
                const bf16x8 f = __builtin_bit_cast(bf16x8, v[j]);
                d1 = __builtin_amdgcn_mfma_f32_16x16x32_bf16(f, __builtin_bit_cast(bf16x8, ones), d1, 0, 0, 0);
                d2 = __builtin_amdgcn_mfma_f32_16x16x32_bf16(f, f, d2, 0, 0, 0);
            }
        }
        asm volatile("" : "+v"(tag0), "+v"(d1), "+v"(d2));
        SB(); BARRIER(); SB();
        warm(STAT_M + 1);
        const u64 want = tag0 * 6364136223846793005ull + 1442695040888963407ull;
        const unsigned wl = (unsigned)want, wh = (unsigned)(want >> 32);
        const __amdgpu_buffer_rsrc_t rS = mk_rsrc(ws + WS_XRD, 65536);
        {
            const float sm = (d1[0] + d1[1]) + (d1[2] + d1[3]);
            float sq = sel4(n, d2[0], d2[1], d2[2], d2[3]);
            sq += dppf<0xB1>(sq);
            sq += dppf<0x4E>(sq);
            const float mean = sm * (1.0f / D_LEN);
            const float var = fmaxf(sq * (1.0f / D_LEN) - mean * mean, 0.f);
            const float rden = __builtin_amdgcn_rcpf(rbf(__builtin_amdgcn_sqrtf(rbf(rbf(var) + 1e-5f))));
            const u32x4 rec = {__float_as_uint(g < 2 ? rden : sm), wl, __float_as_uint(rbf(mean) * rden), wh};
            ST16_SC1(rec, rS, n == g * 4 ? (g < 2 ? r0 + g : 2048 - 2 + h * 1024 + w0 + g) * 16 : 65536);
        }
        const __amdgpu_buffer_rsrc_t rV = mk_rsrc(ws + WS_VOTE, 512);
        if (aw == 3 && idx < 8) __builtin_amdgcn_raw_buffer_store_b64((u32x2){ctl1[5], wl}, rV, blockIdx.x * 8 + lane * 512, 0, 16);
        SB(); BARRIER(); SB();
        warm(STAT_M + 2);
        if (aw == 1 || aw == 2) {
#pragma unroll 1
            for (int t = STAT_M + 3; t <= NT1 + AUX_STAY; ++t) {
                BARRIER();
                if (aw == 1 && t <= 16) STAMP(40 + t);
                warm(t);
            }
            FLUSH();
            return;
        }
        if (aw == 3) {
            if (h == 0) {
                const u32x4 w0v = wrp[0], w1v = wrp[64];
                BARRIER(); BARRIER(); BARRIER(); BARRIER();
                SB();
                float t = 0.f;
#pragma unroll
                for (int i = 0; i < 4; ++i) { t = dot2bf(w0v[i], 0x3F803F80u, t); t = dot2bf(w1v[i], 0x3F803F80u, t); }
                SB(); BARRIER(); SB();
                t = wave_sum(t);
                if (lane == 0) ctl1[1] = __float_as_uint(t);
                WAIT_LGKM0();
            }
            return;
        }
#pragma unroll
        for (int t = STAT_M + 3; t <= POLL_T; ++t) BARRIER();
        SB();
        const int o0 = (rt * BT + lane) * 16, o1 = (2048 + (h * 8 + ct) * BT + lane) * 16, os = 1024;
        u32x4 q0, q1, q2, q3;
        u32x2 qv;
        auto request = [&]() {
            asm volatile("" ::: "memory");
            q0 = LD16_SC1(rS, o0); q1 = LD16_SC1(rS, o0 + os); q2 = LD16_SC1(rS, o1); q3 = LD16_SC1(rS, o1 + os);
            qv = __builtin_amdgcn_raw_buffer_load_b64(rV, lane * 8, 0, 16);
            SB();
        };
        request();
        int b = POLL_T + 1;
        for (int spin = 0; spin <= SPIN_CAP; ++spin) {
            if (b < NT1) { BARRIER(); ++b; }
            if (b < NT1) { BARRIER(); ++b; }
            SB();
            unsigned d = ((q0[1] ^ wl) | (q1[1] ^ wl)) | ((q2[1] ^ wl) | (q3[1] ^ wl)) | ((q0[3] ^ wh) | (q1[3] ^ wh)) | ((q2[3] ^ wh) | (q3[3] ^ wh)) | (qv[1] ^ wl);
            asm volatile("" : "+v"(d));
            if (__all(d == 0u)) break;
            request();
        }
        if (b < NT1) BARRIER();
        SB();
        LAS unsigned* tb = (LAS unsigned*)tab;
        const unsigned sg = h == 0 ? 0u : 0x80000000u;
        tb[lane] = q0[0] ^ sg; tb[64 + lane] = q1[0] ^ sg; tb[128 + lane] = q0[2] ^ sg; tb[192 + lane] = q1[2] ^ sg;
        tb[256 + lane] = q2[0]; tb[320 + lane] = q3[0];
        const unsigned vt = qv[0];
        const unsigned kx = __popcll(__ballot(vt == 2u)) > __popcll(__ballot(vt == 1u)) ? 8192u : 0u;
        if (lane == 0) { ctl1[0] = wl | 2u; ctl1[2] = wl; ctl1[3] = wh; ctl1[4] = kx; }
        *(LAS u32x2*)(lds + L_YB + 12288 + 256 + lane * 8) = (u32x2){0u, 0u};
        WAIT_LGKM0();
        return;
    }
#define CTL(i) ((unsigned)__builtin_amdgcn_readfirstlane((int)ctl[i]))
    const int wp = wid & 1, wq = (wid >> 1) & 1;
#define Y_OFF ((int)WS_Y + ((rt & 7) * 8 + ct) * 65536 + (wid & 3) * 16384 + (((rt >> 3) ^ (rt >> 2)) & 1) * 8192)
#define P2_DEFS \
    char* w2 = ws + kx; \
    const int sidx = idx < 28 ? xq * 28 + idx : 112 + xq * 4 + idx - 28;     \
    volatile LAS unsigned* ctl = (volatile LAS unsigned*)(lds + (h == 0 ? L_ATAB : L_CTL)); \
    const int NT = h == 0 ? 32 + KSHIFT : 32 - KSHIFT; \
    LAS float* rstat = (LAS float*)(lds + L_ATAB + 256);     \
    const int hp_ = rt * 2 + wq;                             \
    (void)sidx; (void)ctl; (void)NT; (void)rstat; (void)hp_; (void)w2
    if (__builtin_expect(wid < 4, 1)) {
        {
            const u32x4 sent4 = {SENT, SENT, SENT, SENT}, zero4 = {0u, 0u, 0u, 0u};
            const __amdgpu_buffer_rsrc_t rI = mk_rsrc(ws + WS_STATS + (size_t)blockIdx.x * (FILL_BYTES / 256), FILL_BYTES / 256);
            const __amdgpu_buffer_rsrc_t rZ = mk_rsrc(ws + (size_t)blockIdx.x * 160, 160);
            ST16_SC1(sent4, rI, tid * 16);
            if (wid < 2) ST16_SC1(sent4, rI, 4096 + tid * 16);
            if (wid == 2) ST16_SC1(zero4, rZ, lane * 16);
        }
        if (wid == 3 && idx < 8) {
            const unsigned r = (((unsigned)__builtin_amdgcn_s_memrealtime() * 2654435761u + blockIdx.x * 40503u) >> 9) & 0x1fdf80u;
            const unsigned first = (idx & 1) << 13;
            const char* pb = ws + WS_PROBE;
            unsigned va, vb, left;
            asm volatile("s_waitcnt lgkmcnt(0)\n\ts_load_dword %0, %3, %4\n\ts_load_dword %1, %3, %5\n\ts_waitcnt lgkmcnt(1)\n\t"
                         "s_nop 15\n\ts_getreg_b32 %2, hwreg(HW_REG_IB_STS, 8, 4)\n\ts_waitcnt lgkmcnt(0)"
                         : "=&s"(va), "=&s"(vb), "=&s"(left) : "s"(pb), "s"(r | first), "s"(r | (first ^ 8192u)) : "memory");
            const unsigned nearbit = (left != 0u ? first : first ^ 8192u) >> 13;
            if (lane == 0) ctl1[5] = nearbit == (unsigned)(xcd >> 2) ? 1u : 2u;
        }
        f32x4 acc[4][4];
#pragma unroll
        for (int i = 0; i < 16; ++i) acc[i >> 2][i & 3] = (f32x4){0.f, 0.f, 0.f, 0.f};
        unsigned tc = (unsigned)(size_t)(tab + wp * 64 + g * 8), tw = (unsigned)(size_t)(tab + 256 + prow(wq * 64 + n));
        unsigned ta = (unsigned)(size_t)lds + wp * STAGE_BYTES + wq * 8192 + lane * 16;
        constexpr int NIL = 1 << 30;
        __amdgpu_buffer_rsrc_t rW = mk_rsrc(ws, 18u << 20);
        const int hpan = rt * 2 + wq;
        int sk = (int)WS_AT + at_panel(h * 8 + ct) + at_tile((rt * 2 + wp) * 128, (ct & 4) << 11) + wq * 16384 + lane * 16;
        int st = lane != 0 ? NIL : (int)WS_FLAG1 + (1 - wp) * 32768 + h * 16384 + (rt >> 3) * 8192 + ct * 1024 + (rt & 7) * 128 + wq * 4;
        int so = h != 0 ? Y_OFF + lane * 16 : g != (n >> 2) ? NIL
               : (int)WS_STATS + (hpan & 15) * 32768 + (ct >> 2) * 16384 + (hpan >> 4) * 8192 + ((ct & 3) * 2 + wp) * 1024 + n * 16;
        int sf = lane != 0 ? NIL : (int)WS_FLAGS + (rt * 8 + ct) * 128 + wid * 4;
        u32x4 sel[2], aw[2];
        unsigned one[2];
#pragma unroll
        for (int i = 0; i < 4; ++i) {
            one[i & 1] = g == (n >> 2) && (i & 1) == ((n >> 1) & 1) ? ((n & 1) ? 0x3F800000u : 0x00003F80u) : 0u;
            aw[0][i] = aw[1][i] = (n & 3) == 0 ? 0x3F803F80u : 0u;
        }
        if ((n & 3) == 1) {
            const bf16_t* wr = a.w_r + ct * BT + wp * 64 + g * 8;
            aw[0] = *(const u32x4*)wr; aw[1] = *(const u32x4*)(wr + 32);
        }
        unsigned ea[2];
#pragma unroll
        for (int mp = 0; mp < 2; ++mp)
            ea[mp] = (unsigned)(size_t)lds + (h == 0 ? L_YB + wq * 16384 + wp * 2048 + mp * 1024 + lane * 16
                                                     : (wq ? L_YB + 16384 : 3 * STAGE_BYTES) + n * 256 + (((wp * 8 + mp * 4 + g) ^ n) * 16));
        asm volatile("" : "+v"(tc), "+v"(tw), "+v"(ta), "+v"(sk), "+v"(st), "+v"(so), "+v"(sf), "+v"(one[0]), "+v"(one[1]), "+v"(ea[0]), "+v"(ea[1]));
        asm volatile("" : "+s"(rW));
        unsigned tg;
        auto extra = [&]() __attribute__((always_inline)) {
            bf16x8 ef[4][2];
#pragma unroll
            for (int i = 0; i < 8; ++i) ef[i >> 1][i & 1] = *(const LAS bf16x8*)((const LAS unsigned char*)(size_t)ea[i & 1] + (i >> 1) * 4096);
            SB();
#pragma unroll
            for (int i = 0; i < 16; ++i)
                acc[i & 3][i >> 2] = __builtin_amdgcn_mfma_f32_16x16x32_bf16(__builtin_bit_cast(bf16x8, sel[i & 1]), ef[i >> 2][(i >> 1) & 1], acc[i & 3][i >> 2], 0, 0, 0);
        };
        if (wid == 0) STAMP(0);
        compute_loop<false, true>(lds, acc, NT1, wp, wq, lane, [&]() { if (wid == 0) STAMP(24); WAIT_VM0(); if (wid == 0) STAMP(31); });
        if (wid == 0) STAMP(25);
        {
            f32x4 rd[4], mr[4];
            float wsm[4];
#pragma unroll
            for (int m = 0; m < 4; ++m) {
                rd[m] = *(const LAS f32x4*)((const LAS float*)(size_t)tc + (m >> 1) * 32 + (m & 1) * 4);
                mr[m] = *(const LAS f32x4*)((const LAS float*)(size_t)tc + 128 + (m >> 1) * 32 + (m & 1) * 4);
                wsm[m] = *((const LAS float*)(size_t)tw + (m >> 1) * 32 + (m & 1) * 4);
            }
            const unsigned kv = ctl1[4];
            tg = ctl1[0];
            SB();
            sk += (int)kv; st += (int)kv; so += (int)kv; sf += (int)kv;
            kx = (unsigned)__builtin_amdgcn_readfirstlane((int)kv);
#pragma unroll
            for (int nn = 0; nn < 4; ++nn)
#pragma unroll
                for (int mp = 0; mp < 2; ++mp) {
                    const f32x4 u = acc[2 * mp][nn] * rd[2 * mp] - mr[2 * mp] * wsm[nn];
                    const f32x4 v = acc[2 * mp + 1][nn] * rd[2 * mp + 1] - mr[2 * mp + 1] * wsm[nn];
                    u32x4 w;
                    w[0] = pk_bf16(u[0], u[1]); w[1] = pk_bf16(u[2], u[3]);
                    w[2] = pk_bf16(v[0], v[1]); w[3] = pk_bf16(v[2], v[3]);
                    ST16_SC1(w, rW, sk + (nn * 2 + mp) * 1024);
                    *(LAS u32x4*)((LAS unsigned char*)(size_t)ta + (nn * 2 + mp) * 1024) = w;
                }
        }
        if (wid == 0) STAMP(26);
        WAIT_LGKM0();
        BARRIER();
        SB();
        {
#pragma unroll
            for (int i = 0; i < 16; ++i) { acc[i >> 2][i & 3] = (f32x4){0.f, 0.f, 0.f, 0.f}; asm volatile("" : "+v"(acc[i >> 2][i & 3])); }
            sel[0] = (u32x4){one[0], one[1], 0u, 0u}; sel[1] = (u32x4){0u, 0u, one[0], one[1]};
            asm volatile("" : "+v"(st), "+v"(so), "+v"(sf), "+v"(tg), "+v"(sel[0]), "+v"(sel[1]));
            SB();
            if (h != 0) extra();
            SB();
            WAIT_VM(0);
            __builtin_amdgcn_raw_buffer_store_b32(tg, rW, st, 0, 16);
            SB();
            if (wid == 0) STAMP(27);
        }
        compute_loop<true, false>(lds, acc, h == 0 ? 32 + KSHIFT : 32 - KSHIFT, wp, wq, lane, [&]() {
            if (wid == 0) { STAMP(h == 0 ? 1 : 19); CSTAMP(22); }
        });
        P2_DEFS;
        if (wid == 0) { STAMP(h == 0 ? 2 : 12); CSTAMP(23); }
        u32x4 hq[4][2];
        auto cvt = [&](int nn, int j) __attribute__((always_inline)) {
            const f32x4 c = acc[j][nn];
            hq[nn][j >> 1][(j & 1) * 2] = pk_bf16(c[0], c[1]);
            hq[nn][j >> 1][(j & 1) * 2 + 1] = pk_bf16(c[2], c[3]);
        };
        int hs = h;
        asm volatile("" : "+s"(hs));
        if (__builtin_expect(hs == 0, 1)) {
            bf16x8 ef[4][2];
#pragma unroll
            for (int i = 0; i < 8; ++i) { ef[i >> 1][i & 1] = *(const LAS bf16x8*)((const LAS unsigned char*)(size_t)ea[i & 1] + (i >> 1) * 4096); SB(); }
            f32x4 d1[4], d2[4];
            u32x3 sv;
            auto add = [&](int nn, int j) __attribute__((always_inline)) {
                acc[j][nn] = __builtin_amdgcn_mfma_f32_16x16x32_bf16(__builtin_bit_cast(bf16x8, sel[j & 1]), ef[nn][j >> 1], acc[j][nn], 0, 0, 0);
            };
            auto sum = [&](int nn, int j) __attribute__((always_inline)) {
                const bf16x8 hf = __builtin_bit_cast(bf16x8, hq[nn][j >> 1]);
                const f32x4 z = {0.f, 0.f, 0.f, 0.f};
                if (j & 1) d1[nn] = __builtin_amdgcn_mfma_f32_16x16x32_bf16(__builtin_bit_cast(bf16x8, aw[j >> 1]), hf, j < 2 ? z : d1[nn], 0, 0, 0);
                else d2[nn] = __builtin_amdgcn_mfma_f32_16x16x32_bf16(hf, hf, j < 2 ? z : d2[nn], 0, 0, 0);
            };
            auto out = [&](int nn, int j) __attribute__((always_inline)) {
                if (j == 1) asm volatile("" : "+v"(d2[nn]));
                if (j == 2) asm volatile("" : "+v"(d1[nn]));
                if (j == 1) sv[1] = min(__float_as_uint(sel4(n, d2[nn][0], d2[nn][1], d2[nn][2], d2[nn][3])), SENT - 1u);
                if (j == 2) { sv[0] = min(__float_as_uint(d1[nn][0]), SENT - 1u); sv[2] = min(__float_as_uint(d1[nn][1]), SENT - 1u); }
                if (j == 3) __builtin_amdgcn_raw_buffer_store_b96(sv, rW, so + nn * 256, 0, 16);
            };
#define FOUR(m, v) _Pragma("unroll") for (int j = 0; j < 4; ++j) { m; SB(); v; SB(); }
            FOUR(add(0, j), (void)0) FOUR(add(1, j), cvt(0, j)) FOUR(sum(0, j), cvt(1, j)) FOUR(add(2, j), out(0, j))
            FOUR(sum(1, j), cvt(2, j)) FOUR(add(3, j), out(1, j)) FOUR(sum(2, j), cvt(3, j)) FOUR(sum(3, j), out(2, j))
            FOUR((void)0, out(3, j))
#undef FOUR
            if (wid == 0) STAMP(3);
#pragma unroll
            for (int nn = 0; nn < 4; ++nn)
#pragma unroll
                for (int mp = 0; mp < 2; ++mp)
                    *(LAS u32x4*)(lds + L_HT + (wq * 64 + nn * 16 + n) * 256 + (((wp * 8 + mp * 4 + g) ^ n) * 16)) = hq[nn][mp];
            WAIT_LGKM0();
            BARRIER();
            BARRIER();
            if (wid == 0) STAMP(6);
            const int crow = (int)CTL(2 + wq) + wq * 64;
            const __amdgpu_buffer_rsrc_t rOut = mk_rsrc(a.out + (size_t)rt * BT * D_LEN, BT * D_LEN * 2);
#pragma unroll 2
            for (int j = 0; j < 8; ++j) {
                const int r = wq * 64 + wp * 32 + j * 4 + g;
                const float m_ = rstat[r * 2], d_ = rstat[r * 2 + 1];
                const u32x4 hh = *(const LAS u32x4*)(lds + L_HT + r * 256 + ((n ^ (r & 15)) * 16));
                u32x4 ov;
#pragma unroll
                for (int i = 0; i < 4; ++i) {
                    const unsigned u = hh[i];
                    const float h0 = bflo(u), h1 = bfhi(u);
                    const unsigned dd = pk_bf16(h0 - m_, h1 - m_);
                    const unsigned q = pk_bf16(bflo(dd) * d_, bfhi(dd) * d_);
                    ov[i] = pk_bf16(h0 + bflo(q), h1 + bfhi(q));
                }
                __builtin_amdgcn_raw_buffer_store_b128(ov, rOut, r == crow ? BT * D_LEN * 2 : (r * D_LEN + ct * BT + n * 8) * 2, 0, OUT_AUX);
            }
            FLUSH();
            return;
        }
#pragma unroll
        for (int i = 0; i < 8; ++i) {
            cvt(i >> 1, (i & 1) * 2); cvt(i >> 1, (i & 1) * 2 + 1);
            __builtin_amdgcn_raw_buffer_store_b128(hq[i >> 1][i & 1], rW, so, i * 1024, 16);
            SB();
        }
        WAIT_VM0();
        __builtin_amdgcn_raw_buffer_store_b32(1u, rW, sf, 0, 16);
        if (wid == 0) STAMP(13);
    } else {
        LAS unsigned char* l0 = lds + (wid - 4) * 4096;
        P2_DEFS;
        const __amdgpu_buffer_rsrc_t rD = mk_rsrc(h == 0 ? a.wd + (size_t)sidx * 32 * D_LEN : a.wg,
                                                  sidx >= 112 ? 0 : h != 0 ? D_LEN * F_LEN * 2 : wid < 8 - BQ ? 32 * D_LEN * 2 : 0);
        int vGe = h == 0 ? wd_off(wid & 3, 0, lane)
                : (((sidx & 7) * 128 + (wid & 3) * 16 + (lane >> 5)) * F_LEN + (sidx >> 3) * 256 + ((((lane & 31) >> 1) ^ (lane >> 5)) * 16) + (lane & 1) * 8) * 2;
        auto load_wd = [&](int i) __attribute__((always_inline)) {
            dma_nt(rD, (unsigned)(size_t)lds + (i < 8 ? L_WD0 : L_WD1) + (wid & 3) * 8192 + (i & 7) * 1024, vGe ^ ((2 * (i & 1) | (i & 4)) * 32), i * (4 * D_LEN));
        };
        const int lw = wid - 4;
        constexpr int NIL = 1 << 30;
        ring.rP = ring.rF = mk_rsrc(w2, (unsigned)WS_STATS);
        ring.voP[0] += (int)WS_AT + at_panel(h * 8 + ct);
        ring.voP[1] = ring.voP[2] = ring.voP[3] = ring.voP[0];
#define FLINE(k) (((k) >> 3) * 8192 + ((k) & 7) * 128)
        const int ft = (g & 1) * 32768 + (g >> 1) * 4 + (int)WS_FLAG1 + (lw == 0 ? ct * 1024 : NIL);
        int vall = FLINE(n) + ft + h * 16384;
        ring.voF = (n >> 2) == xq ? vall : NIL;
        ring.fl = lds + L_YB + (h == 0 ? wq * 16384 + wp * 2048 : lw * 1024) + 12288 + 256;
        unsigned sa = (unsigned)(size_t)lds + L_YB + 12288 + 256 + lane * 4;
        const unsigned tg = (unsigned)__builtin_amdgcn_readfirstlane((int)ctl1[0]);
        float wsum = __uint_as_float(ctl1[1]);
        ring.lb = lw * 4096;
        int cq[3], cp[3];
        unsigned mk[3];
#pragma unroll
        for (int k = 0; k < 3; ++k) {
            const int nb = xq * 4 + NBR(k + 1);
            cq[k] = nb * 256; cp[k] = at_tile(cq[k], ring.par); mk[k] = 0x10000u << nb;
            asm volatile("" : "+s"(cq[k]), "+s"(cp[k]), "+s"(mk[k]));
        }
        unsigned m2 = (mk[0] | mk[1] | mk[2]) >> 16;
        asm volatile("" : "+s"(m2), "+s"(ring.rP));
        asm volatile("" : "+v"(vall), "+v"(ring.voF), "+v"(ring.voP[0]), "+v"(sa), "+v"(wsum));
        ring.rF = ring.rP;
        ring.voP[1] = ring.voP[2] = ring.voP[3] = ring.voP[0];
        WAIT_VM(4);
        BARRIER();
        SB();
        int ve = Y_OFF + lane * 16;
        auto fetch = [&]() {
            if (wid == 4 && h == 0) STAMP(21);
            LAS unsigned char* base = lds + L_YB + wq * 16384 + wp * 2048;
#pragma unroll
            for (int k = 0; k < 4; ++k) dma_2k<true>(ring.rP, (unsigned)(size_t)base + k * 4096, ve, ve, k * 2048);
        };
        __amdgpu_buffer_rsrc_t rQ2 = mk_rsrc(a.ci + (size_t)(rt * 4 + lw) * 32 * K, 32 * K * 2);
        int vC2 = (ct * 4 + g) * (K * 2) + ((xq * 1024 + 2048 + n * 128) & 4095);
        int vT2 = h == 0 && n < (KSHIFT + 1) / 2 ? FLINE((xq * 4 - 1 - n) & 15) + ft + 16384 : NIL;
        int vY = lane == 0 ? (int)WS_FLAGS + (rt * 8 + ct) * 128 + lw * 4 : NIL;
        int n0 = C2_START + idx % C2_STAG, n1 = (h == 0 ? 32 : NT) - 11 - n0;
        int c8 = (xq * 1024 + 1024) & 4095, p8 = at_tile(c8, ring.par);
        int cK = (xq * 1024 + (32 - KSHIFT) * 128) & 4095, pK = at_tile(cK, ring.par) + at_panel(8);
        auto early = [&]() __attribute__((always_inline)) {
#pragma unroll
            for (int i = 0; i < G_N; ++i) dma_nt(rD, (unsigned)(size_t)lds + lw * 8192 + i * 1024, vGe ^ ((2 * (i & 1) | (i & 4)) * 32), i * (4 * F_LEN));
        };
        asm volatile("" : "+v"(vGe));
        asm volatile("" : "+s"(rQ2), "+s"(n0), "+s"(n1), "+s"(c8), "+s"(p8), "+s"(cK), "+s"(pK));
        asm volatile("" : "+v"(ve), "+v"(vC2), "+v"(vT2), "+v"(vY));
        unsigned have = 0u;
        SB();
        if (T_DELAY) __builtin_amdgcn_s_sleep(T_DELAY);
#define GATE(m) do { if (__builtin_expect(((m) & ~have) != 0u, 0)) ring.gate(tg, sa, m, have); SB(); } while (0)
#define TILE(k, o, st) do { ring.sp = cp[k] + (o) * 256; ring.sq = cq[k] + (o); ring.template issueP<16>(l0 + (st) * STAGE_BYTES); \
                            ring.issueQ(l0 + (st) * STAGE_BYTES); ring.sample(); SB(); } while (0)
        ring.gate(tg, sa, mk[0], have);
        if (wid == 4) STAMP(28);
        ring.sp = cp[0]; ring.template issueP<16>(l0 + 2 * STAGE_BYTES); ring.sample();
        BARRIER();
        GATE(mk[1]);
        ring.sp = cp[1]; ring.template issueP<16>(l0 + 3 * STAGE_BYTES); ring.sample();
        WAIT_VM(6); BARRIER();
        if (wid == 4) STAMP(30);
        GATE(mk[2]);
        TILE(2, 0, 0);
        GATE(m2);
        ring.voF = vall;
        TILE(0, 128, 1);
        WAIT_VM(19); BARRIER();
        TILE(1, 128, 2);
        WAIT_VM(19); BARRIER();
        TILE(2, 128, 3);
        WAIT_VM(19); BARRIER();
#undef GATE
#undef TILE
        if (wid == 4) STAMP(29);
        if (have != 0xffffffffu) ring.gate(tg, sa, 0xffffffffu, have);
        ring.fl -= 256;
        ring.sq = c8; ring.sp = p8; ring.poff = 0;
        if (n0 > 0) ring.template steady<true, false>(n0);
        ring.voF = vC2;
        ring.rF = ring.rQ;
        ring.template issue<true>(lds);
        if (__builtin_expect(blockIdx.x == 255, 0) && tid == 256) {
            const u64 want = ((u64)ctl1[3] << 32) | ctl1[2];
            __hip_atomic_store((GAS u64*)(ws + WS_TAG), want, __ATOMIC_RELAXED, __HIP_MEMORY_SCOPE_AGENT);
        }
        ring.voF = vT2;
        ring.rF = ring.rP;
        ring.fl += 512;
        ring.sample();
        WAIT_VM(18); BARRIER();
        if (wid == 4) { CSTAMP(34); STAMP(36); }
        ring.template steady<true, false, 18>(2);
        ring.template steady<true, false>(n1);
        if (wid == 4) { CSTAMP(35); STAMP(37); }
        if (h == 0) {
            have = 0u;
            ring.gate(tg, sa + 256, ((1u << ((KSHIFT + 1) / 2)) - 1u) * 0x10001u, have);
            ring.voF = vY;
            ring.fl -= 512;
            ring.rQ = rQ2;
            ring.sq = cK; ring.sp = pK; ring.poff = at_panel(8);
            ring.template steady<true, true>(KSHIFT);
        }
        if (h != 0) {
            early();
            wait_vm<8 + G_N>(); BARRIER();
            wait_vm<G_N>(); BARRIER();
        } else if (ring.seen()) {
            fetch();
            WAIT_VM(16); BARRIER();
            WAIT_VM(8); BARRIER();
        } else {
            ring.sample();
            WAIT_VM(10); BARRIER();
            const bool have = ring.seen();
            if (have) { fetch(); WAIT_VM(9); } else { ring.sample(); WAIT_VM(2); }
            BARRIER();
            if (!have) {
#pragma unroll 1
                for (int spin = 0; spin <= SPIN_CAP && !ring.seen(); ++spin) { ring.sample(); WAIT_VM(0); }
                fetch();
            }
        }
        if (wid == 4 && h == 0) STAMP(20);
        if (h == 0) WAIT_VM0();
        BARRIER();
        if (h == 0) {
            const int slot = sidx;
            LAS unsigned char* abuf = lds + L_ATAB + 2048 + lw * 128;
#pragma unroll
            for (int i = 0; i < WD_EARLY; ++i) load_wd(i);
            SB();
            __amdgpu_buffer_rsrc_t rS = mk_rsrc(w2 + WS_STATS + (hp_ & 15) * 32768 + (hp_ >> 4) * 8192, 24576);
            __amdgpu_buffer_rsrc_t rC = mk_rsrc(w2 + WS_CAND + (size_t)hp_ * 4096, 4096);
            __amdgpu_buffer_rsrc_t rKy = mk_rsrc(w2 + WS_KEYS + (hp_ >> 2) * KEY_S + (hp_ & 3) * 4, ct == 0 ? 8192 + 3 * KEY_S * 16 + 4 : 0);
            int vs = lane * 16, vk = !(lane & 1) ? 1 << 20 : (lane >> 2) * 8192 + (lane & 3) * (KEY_S * 16);
            int vc = (lane < 32 ? 2048 : 0) + ct * 256 + (lane & 31) * 8;
            unsigned code = 0xffffu - (unsigned)(hp_ * 64 + lane), hb = (lane & 31) * 8;
            unsigned ra = (unsigned)(size_t)(rstat + (wq * 64 + lane) * 2), ht = (unsigned)(size_t)lds + L_HT + wq * 16384;
            u64 lo = 0xFFFFFFFFull;
            asm volatile("" : "+s"(rS), "+s"(rC), "+s"(rKy), "+s"(ht), "+s"(lo));
            asm volatile("" : "+v"(vs), "+v"(vk), "+v"(vc), "+v"(code), "+v"(hb), "+v"(ra));
            SB();
            BARRIER();
            __builtin_amdgcn_s_setprio(TAIL_PRIO);
            if (lane < 16) *(LAS unsigned*)(abuf + 64 + lane * 4) = 0u;
            __builtin_amdgcn_s_sleep(STAT_DELAY);
            if (wp == 0) {
                float s1, s2, s3;
#pragma unroll 1
                for (int spin = 0;; ++spin) {
                    asm volatile("" ::: "memory");
                    u32x3 sv[16];
#pragma unroll
                    for (int j = 0; j < 16; ++j) { sv[j] = __builtin_amdgcn_raw_buffer_load_b96(rS, vs, (j >> 3) * 16384 + (j & 7) * 1024, 16); SB(); }
#pragma unroll
                    for (int j = 0; j < 16; ++j) {
                        const unsigned u0 = sv[j][0], u1 = sv[j][1], u2 = sv[j][2];
                        s1 = j ? s1 + __uint_as_float(u0) : __uint_as_float(u0);
                        s2 = j ? s2 + __uint_as_float(u1) : __uint_as_float(u1);
                        s3 = j ? s3 + __uint_as_float(u2) : __uint_as_float(u2);
                    }
                    float z = s1 + s3;
                    asm volatile("" : "+v"(z));
                    if (__builtin_expect(__builtin_amdgcn_ballot_w64(__builtin_isunordered(z, s2)) == 0, 1)) break;
                    unsigned mx = 0u;
#pragma unroll
                    for (int j = 0; j < 16; ++j) mx = max(max(mx, sv[j][0]), max(sv[j][1], sv[j][2]));
                    if (__all(mx != SENT) || spin > SPIN_CAP) break;
                }
                if (wid == 4) STAMP(4);
                const float mean = s1 * (1.0f / D_LEN);
                const float var = fmaxf(s2 * (1.0f / D_LEN) - mean * mean, 0.f);
                const float mb = rbf(mean), rden = __builtin_amdgcn_rcpf(rbf(__builtin_amdgcn_sqrtf(rbf(rbf(var) + 1e-5f))));
                const unsigned kb = wave_max_s(r2key((s3 - mb * wsum) * rden, code));
                __builtin_amdgcn_raw_buffer_store_b32(kb, rKy, vk, 0, 16);
                SB();
                {
                    const unsigned cr_ = ~kb & 63u;
                    u32x2 c = *(const LAS u32x2*)(size_t)(ht + cr_ * 256 + (hb ^ ((cr_ & 15) * 16)));
                    const float m_ = __int_as_float(__builtin_amdgcn_readlane(__float_as_int(mb), cr_));
                    const float d_ = __int_as_float(__builtin_amdgcn_readlane(__float_as_int(rden), cr_));
#pragma unroll
                    for (int i = 0; i < 2; ++i) {
                        const unsigned u = c[i];
                        const unsigned dd = pk_bf16(bflo(u) - m_, bfhi(u) - m_);
                        const unsigned q = pk_bf16(bflo(dd) * d_, bfhi(dd) * d_);
                        c[i] = min(__float_as_uint(pick(lo, __uint_as_float(u), __uint_as_float(q))), SENT - 1u);
                    }
                    __builtin_amdgcn_raw_buffer_store_b64(c, rC, vc, 0, 16);
                    SB();
                    *(LAS u32x2*)(size_t)ra = (u32x2){__float_as_uint(mb), __float_as_uint(rden)};
                    if (lane == 0) ctl[2 + wq] = cr_;
                }
                WAIT_LGKM0();
                if (wid == 4) STAMP(5);
            }
            BARRIER();
#pragma unroll
            for (int i = WD_EARLY; i < 16; ++i) load_wd(i);
            if (slot < 112 && wid < 8 - BQ) {
                WAIT_VM0();
                if (wid == 4) STAMP(7);
                stage2(w2, slot, lw, lds + L_WD0 + lw * 8192, lds + L_WD1 + lw * 8192, abuf, 64, lane);
                if (wid == 4) STAMP(11);
            }
            FLUSH();
            return;
        }
    }

    P2_DEFS;
    LAS float* red = (LAS float*)(lds + L_RED);
    if (sidx < 112) {
        const int slot = sidx, grp = slot >> 3, kq = slot & 7;
        const __amdgpu_buffer_rsrc_t rG = mk_rsrc(a.wg, D_LEN * F_LEN * 2), rU = mk_rsrc(a.wu, D_LEN * F_LEN * 2);
        auto pair = [&](int i) {
            const int d = wid * 16 + i * 2 + (lane >> 5), ky = (d & 3) | ((d >> 1) & 4), c = lane & 31;
            const int vgu = ((kq * 128 + d) * F_LEN + grp * 256 + (((c >> 1) ^ ky) * 16) + (c & 1) * 8) * 2;
            if (i >= G_N || wid >= 4) dma_nt(rG, (unsigned)(size_t)lds + wid * 8192 + i * 1024, vgu, 0);
            dma_nt(rU, (unsigned)(size_t)lds + 65536 + wid * 8192 + i * 1024, vgu ^ 512, 0);
        };
        if (wid >= 4) {
            if (W_PRE) __builtin_amdgcn_s_sleep(W_PRE_SL);
#pragma unroll
            for (int i = 0; i < W_PRE; ++i) pair(i);
        }
        BARRIER();
        if (wid < 4) {
#pragma unroll
            for (int i = 0; i < W_PRE; ++i) pair(i);
        }
#pragma unroll
        for (int i = W_PRE; i < 8; ++i) pair(i);
        if (wid >= 8 - BQ) {
            const __amdgpu_buffer_rsrc_t rD = mk_rsrc(a.wd + (size_t)slot * 32 * D_LEN, 32 * D_LEN * 2);
            const int vd = wd_off(wid - 4, 0, lane);
#pragma unroll
            for (int i = 0; i < 16; ++i)
                dma_nt(rD, (unsigned)(size_t)lds + L_YB + (wid & 1) * 16384 + i * 1024, vd ^ ((2 * (i & 1) | (i & 4)) * 32), i * (4 * D_LEN));
        }
        const __amdgpu_buffer_rsrc_t rK = mk_rsrc(w2, wid == 0 ? (unsigned)(WS_CAND + 32 * 4096) : 0u);
        const int ok_ = lane < 40 ? (int)WS_KEYS + (xcd >> 2) * 8192 + (xcd & 3) * (KEY_S * 16) + (lane - 32) * KEY_S : (int)(WS_CAND + 32 * 4096);
        const int oc = (int)WS_CAND + 2048 + kq * 256 + n * 16;
        int best = 0;
        u32x4 v = {0u, 0u, 0u, 0u};
        auto request = [&]() {
            asm volatile("" ::: "memory");
            v = LD16_SC1(rK, lane < 32 ? oc + best * 4096 : ok_);
            SB();
        };
        auto settled = [&]() {
            const unsigned mx = umax4(v), mn = min(min(v[0], v[1]), min(v[2], v[3]));
            unsigned fv = lane < 32 ? ~mx : mn | (unsigned)(lane >> 3 ^ 4);
            asm volatile("" : "+v"(fv));
            const bool fine = fv != 0u;
            unsigned km = max(mx, dppu<0xB1>(mx));
            km = max(km, dppu<0x4E>(km));
            km = max(km, dppu<0x141>(km));
            const int nb = (int)(((0xffffu - ((unsigned)__builtin_amdgcn_readlane((int)km, 32) & 0xffffu)) >> 6) & 31u);
            const bool same = nb == best;
            best = nb;
            return __all(fine) && same;
        };
        if (wid == 0) {
            request();
            WAIT_VM(0);
            settled();
            request();
        } else if (wid >= 8 - BQ) WAIT_VM(16);
        else WAIT_VM(0);
        BARRIER();
        if (wid == 0) STAMP(8);
        bf16x8 wfr[4][4];
        {
            const int q = (lane >> 2) & 3, ky = q | ((g & 1) << 2);
#pragma unroll
            for (int u = 0; u < 4; ++u) {
                const int tro = (u >> 1) * 65536 + (8 * g + q) * 512 + (((wid * 2 + (u & 1)) ^ ky) * 32) + (lane & 3) * 8;
#pragma unroll
                for (int ks = 0; ks < 4; ++ks) {
                    const bf16x4 w0 = __builtin_amdgcn_ds_read_tr16_b64_v4i16((LAS bf16x4*)(lds + tro + ks * 16384));
                    const bf16x4 w1 = __builtin_amdgcn_ds_read_tr16_b64_v4i16((LAS bf16x4*)(lds + tro + ks * 16384 + 2048));
                    wfr[u][ks] = __builtin_shufflevector(w0, w1, 0, 1, 2, 3, 4, 5, 6, 7);
                }
            }
            SB();
#pragma unroll
            for (int i = 0; i < 16; ++i) asm volatile("" : "+v"(wfr[i >> 2][i & 3]));
        }
        if (wid >= 8 - BQ && lane < 16) *(LAS unsigned*)(lds + 8192 + wid * 64 + lane * 4) = 0u;
        __amdgpu_buffer_rsrc_t rPo = mk_rsrc(w2 + WS_PRE, 112 * 2048);
        int po = (n & 3) == 0 ? pre_off((grp ^ (g >> 1)) * 8 + wid) + kq * 256 + g * 64 + n * 4 : 112 * 2048;
        unsigned xw = (unsigned)(size_t)lds + (n >> 3) * 16384 + (g & 1) * 8192 + (n & 7) * 8, xr = (unsigned)(size_t)lds + (n & 1) * 8192 + g * 16;
        unsigned ix = (n & 1) ? 0xEEEEEEEEu : 0x44444444u;
        asm volatile("" : "+v"(po), "+v"(xw), "+v"(xr), "+v"(ix));
        u64 m1 = 0xFFFF0000FFFF0000ull, m2 = 0xFFFFFFFF00000000ull;
        f32x4 ac[4];
#pragma unroll
        for (int u = 0; u < 4; ++u) { ac[u] = (f32x4){0.f, 0.f, 0.f, 0.f}; asm volatile("" : "+v"(ac[u])); }
        asm volatile("" : "+s"(rPo), "+s"(m1), "+s"(m2));
        if (wid == 0) {
#pragma unroll 1
            for (int spin = 0; spin <= SPIN_CAP && !settled(); ++spin) {
                __builtin_amdgcn_s_sleep(1);
                request();
            }
            __builtin_amdgcn_s_waitcnt(0x0F70);
            if (lane < 32) *(LAS u32x2*)(size_t)xw = (u32x2){lane < 16 ? v[0] : v[1], lane < 16 ? v[2] : v[3]};
            WAIT_LGKM0();
            STAMP(9);
        }
        BARRIER();
        {
            bf16x8 xf[2];
#pragma unroll
            for (int j = 0; j < 2; ++j) xf[j] = *(const LAS bf16x8*)((const LAS unsigned char*)(size_t)xr + j * 16384);
            SB();
            float rs[3];
#pragma unroll
            for (int i = 0; i < 8; ++i) {
                const int u = i >> 1, j = i & 1;
                ac[u] = smf(xf[j], wfr[u][2 * j], wfr[u][2 * j + 1], ac[u], ix);
                SB();
                if (i == 4) { asm volatile("" : "+v"(ac[0])); rs[0] = ac[0][0] + ac[0][1]; asm volatile("" : "+v"(rs[0])); }
                if (i == 6) { asm volatile("" : "+v"(ac[1])); rs[0] = pick(m1, rs[0], ac[1][0] + ac[1][1]); asm volatile("" : "+v"(rs[0])); }
                if (i == 7) { asm volatile("" : "+v"(ac[2])); rs[2] = ac[2][0] + ac[2][1]; asm volatile("" : "+v"(rs[2])); }
                SB();
            }
            const unsigned e = min(__float_as_uint(pick(m2, rs[0], pick(m1, rs[2], ac[3][0] + ac[3][1]))), SENT - 1u);
            const u32x4 sv = {e, dppu<0x55>(e), dppu<0xAA>(e), dppu<0xFF>(e)};
            ST16_SC1(sv, rPo, po);
            SB();
            asm volatile("" :: "v"(v));
        }
        if (wid == 0) STAMP(14);
        if (wid >= 8 - BQ) {
            WAIT_VM(1);
            if (wid == 7) STAMP(15);
            stage2(w2, slot, wid - 4, lds + L_YB + (wid & 1) * 16384, lds + L_YB + (wid & 1) * 16384 + 8192, lds + wid * 64, 8192, lane);
            if (wid == 7) STAMP(10);
        }
    } else {
        const int rj = sidx - 112;
        if (wid == 4) {
            unsigned kk;
            const unsigned k = poll_winner(w2, xcd, lane, kk);
            if (lane == 0) ctl[4] = k;
            if (lane < 32) ctl[8 + lane] = kk;
            WAIT_LGKM0();
        }
        BARRIER();
        const unsigned kw = CTL(4);
        if (wid == 0) STAMP(16);
        {
            const unsigned kc = ctl[8 + wid * 4 + g];
            const __amdgpu_buffer_rsrc_t rL = mk_rsrc(w2 + WS_CAND + (size_t)wid * 16384, 16384);
            u32x2 hv2, nv2;
            for (int spin = 0;; ++spin) {
                asm volatile("" ::: "memory");
                hv2 = __builtin_amdgcn_raw_buffer_load_b64(rL, g * 4096 + (rj * 64 + n * 4) * 2, 0, 16);
                nv2 = __builtin_amdgcn_raw_buffer_load_b64(rL, g * 4096 + 2048 + (rj * 64 + n * 4) * 2, 0, 16);
                if (__all(max(max(hv2[0], hv2[1]), max(nv2[0], nv2[1])) != SENT) || spin > SPIN_CAP) break;
                __builtin_amdgcn_s_sleep(1);
            }
            u32x2 ov;
#pragma unroll
            for (int i = 0; i < 2; ++i) {
                const unsigned u = hv2[i], q = nv2[i];
                ov[i] = pk_bf16(bflo(u) + bflo(q), bfhi(u) + bfhi(q));
            }
            const int crow = (int)(0xffffu - (kc & 0xffffu)) & (S_LEN - 1);
            if (kc != kw) *(u32x2*)(a.out + (size_t)crow * D_LEN + rj * 64 + n * 4) = ov;
        }
        const int wrow = (int)(0xffffu - (kw & 0xffffu)) & (S_LEN - 1);
        const __amdgpu_buffer_rsrc_t rC = mk_rsrc(w2 + WS_CAND + (size_t)(wrow >> 6) * 4096, 4096);
        const __amdgpu_buffer_rsrc_t rPt = mk_rsrc(w2 + WS_PART, 112 * 4096);
        const int ocol = (rj >> 2) * 256 + ((lane >> 1) & 1) * 128 + (rj & 3) * 32 + (lane & 1) * 16 + (lane >> 2);
        unsigned hv, hnv;
        for (int spin = 0;; ++spin) {
            asm volatile("" ::: "memory");
            hv = __builtin_amdgcn_raw_buffer_load_b32(rC, (ocol & ~1) * 2, 0, 16);
            hnv = __builtin_amdgcn_raw_buffer_load_b32(rC, 2048 + (ocol & ~1) * 2, 0, 16);
            if (__all(max(hv, hnv) != SENT) || spin > SPIN_CAP) break;
        }
        float sgm = rbf(1.0f / (1.0f + __expf(-key2r(kw))));
        float hf = (ocol & 1) ? bfhi(hv) : bflo(hv), hnf = (ocol & 1) ? bfhi(hnv) : bflo(hnv);
        GAS unsigned short* optr = (GAS unsigned short*)(a.out + (size_t)wrow * D_LEN + ocol);
        unsigned rw = (unsigned)(size_t)red + (wid >> 2) * 1024 + (n * 4 + g) * 16 + (wid & 3) * 4, rr = (unsigned)(size_t)red + lane * 16;
        int po = wid * 16384 + (rj >> 3) * 8192 + g * 2048 + ((rj & 7) * 64 + n * 4) * 4;
        int po3 = po + 3 * 131072;
        asm volatile("" : "+v"(sgm), "+v"(optr), "+v"(hf), "+v"(hnf), "+v"(rw), "+v"(rr), "+v"(po), "+v"(po3));
        float sm;
#pragma unroll 1
        for (int spin = 0;; ++spin) {
            asm volatile("" ::: "memory");
            u32x4 pv[4];
#pragma unroll
            for (int i = 0; i < 4; ++i) pv[i] = __builtin_amdgcn_raw_buffer_load_b128(rPt, i < 3 ? po : po3, i < 3 ? i * 131072 : 0, 16);
            SB();
            unsigned t[4];
#pragma unroll
            for (int j = 0; j < 4; ++j) {
                const unsigned u0 = pv[0][j], u1 = pv[1][j], u2 = pv[2][j], u3 = pv[3][j];
                t[j] = __float_as_uint(((__uint_as_float(u0) + __uint_as_float(u1)) + __uint_as_float(u2)) + __uint_as_float(u3));
            }
            const auto r0 = __builtin_amdgcn_permlane32_swap(t[0], t[2], false, false), r1 = __builtin_amdgcn_permlane32_swap(t[1], t[3], false, false);
            const unsigned a0 = r0[0], a1 = r0[1], b0 = r1[0], b1 = r1[1];
            const auto r2 = __builtin_amdgcn_permlane16_swap(__float_as_uint(__uint_as_float(a0) + __uint_as_float(a1)),
                                                             __float_as_uint(__uint_as_float(b0) + __uint_as_float(b1)), false, false);
            const unsigned c0 = r2[0], c1 = r2[1];
            sm = __uint_as_float(c0) + __uint_as_float(c1);
            if (__builtin_expect(__builtin_amdgcn_ballot_w64(sm != sm) == 0, 1)) break;
            if (__all(max(max(umax4(pv[0]), umax4(pv[1])), max(umax4(pv[2]), umax4(pv[3]))) != SENT) || spin > SPIN_CAP) break;
            if (FIN_SLEEP) __builtin_amdgcn_s_sleep(FIN_SLEEP);
        }
        if (wid == 0) STAMP(17);
        *(LAS float*)(size_t)rw = sm;
        WAIT_LGKM0();
        BARRIER();
        if (wid == 0) {
            const f32x4 u = *(const LAS f32x4*)(size_t)rr, v = *(const LAS f32x4*)((const LAS unsigned char*)(size_t)rr + 1024);
            const f32x4 w = u + v;
            const float t = (w[0] + w[2]) + (w[1] + w[3]);
            *optr = (unsigned short)pk_bf16(hf + rbf(hnf + rbf(rbf(t) * sgm)), 0.f);
            STAMP(18);
        }
        __builtin_amdgcn_s_waitcnt(0x0F70);
    }
    FLUSH();
}

extern "C" void kernel_launch(void* const* d_in, const int* in_sizes, int n_in, void* d_out, int out_size, void* d_ws,
                              size_t ws_size, hipStream_t stream) {
    Args a;
    a.x = (const bf16_t*)d_in[0];
    a.cr = (const bf16_t*)d_in[1];
    a.ci = (const bf16_t*)d_in[2];
    a.wre = (const bf16_t*)d_in[3];
    a.wim = (const bf16_t*)d_in[4];
    a.w_r = (const bf16_t*)d_in[6];
    a.wg = (const bf16_t*)d_in[7];
    a.wu = (const bf16_t*)d_in[8];
    a.wd = (const bf16_t*)d_in[9];
    a.out = (bf16_t*)d_out;
    a.ws = (char*)d_ws + (WS_END ? ((ws_size - ((size_t)24 << 20)) & ~(size_t)255) : 0);
    (void)hipFuncSetAttribute((const void*)fused_kernel, hipFuncAttributeMaxDynamicSharedMemorySize, LDS_BYTES);
    fused_kernel<<<256, 768, LDS_BYTES, stream>>>(a);
}
```

```cpp
#include <hip/hip_runtime.h>
#include <hip/hip_bf16.h>
#include <stdint.h>

typedef unsigned short bf16_t;
typedef short bf16x8 __attribute__((ext_vector_type(8)));
typedef short bf16x4 __attribute__((ext_vector_type(4)));
typedef float f32x4 __attribute__((ext_vector_type(4)));
typedef unsigned u32x4 __attribute__((ext_vector_type(4)));
typedef unsigned u32x2 __attribute__((ext_vector_type(2)));
typedef unsigned u32x3 __attribute__((ext_vector_type(3)));
typedef unsigned long long u64;
#define LAS __attribute__((address_space(3)))
#define GAS __attribute__((address_space(1)))

constexpr int S_LEN = 2048, D_LEN = 1024, F_LEN = 3584;
constexpr unsigned SENT = 0xFFFFFFFFu;
constexpr int SPIN_CAP = 1 << 17;

constexpr size_t WS_KEYS = 0;
constexpr size_t WS_FLAGS = 16384;
constexpr size_t WS_XRD = 65536;
constexpr size_t WS_FLAG1 = 196608;
constexpr size_t WS_TAG = 131072;
constexpr size_t WS_VOTE = WS_TAG + 4096;
constexpr size_t WS_PROBE = (size_t)20 << 20;
constexpr size_t WS_AT = (size_t)4 << 20;
constexpr size_t WS_Y = (size_t)12 << 20;
constexpr size_t WS_STATS = (size_t)16 << 20;
constexpr size_t WS_PART = WS_STATS + (512u << 10);
constexpr size_t WS_CAND = WS_STATS + (1024u << 10);
constexpr size_t WS_PRE = WS_STATS + (1152u << 10);
constexpr size_t FILL_BYTES = (size_t)1408 << 10;

__device__ __forceinline__ float bflo(unsigned v) { return __uint_as_float(v << 16); }
__device__ __forceinline__ float bfhi(unsigned v) { return __uint_as_float(v & 0xffff0000u); }
__device__ __forceinline__ unsigned pk_bf16(float lo, float hi) {
    typedef __bf16 bf2 __attribute__((ext_vector_type(2)));
    typedef float f2 __attribute__((ext_vector_type(2)));
    f2 v = {lo, hi};
    bf2 r = __builtin_convertvector(v, bf2);
    return __builtin_bit_cast(unsigned, r);
}
__device__ __forceinline__ float rbf(float x) { return __uint_as_float(pk_bf16(0.f, x)); }

template <int CTRL> __device__ __forceinline__ unsigned dppu(unsigned v) {
    return (unsigned)__builtin_amdgcn_update_dpp(0, (int)v, CTRL, 0xf, 0xf, true);
}
template <int CTRL> __device__ __forceinline__ float dppf(float v) { return __uint_as_float(dppu<CTRL>(__float_as_uint(v))); }
__device__ __forceinline__ void swap16(unsigned v, unsigned& a, unsigned& b) {
    auto r = __builtin_amdgcn_permlane16_swap(v, v, false, false);
    a = r[0]; b = r[1];
}
__device__ __forceinline__ void swap32(unsigned v, unsigned& a, unsigned& b) {
    auto r = __builtin_amdgcn_permlane32_swap(v, v, false, false);
    a = r[0]; b = r[1];
}
__device__ __forceinline__ float sum_rows(float v) {
    unsigned a, b;
    swap16(__float_as_uint(v), a, b); v = __uint_as_float(a) + __uint_as_float(b);
    swap32(__float_as_uint(v), a, b); v = __uint_as_float(a) + __uint_as_float(b);
    return v;
}
__device__ __forceinline__ float wave_sum(float v) {
    v += dppf<0xB1>(v);
    v += dppf<0x4E>(v);
    v += dppf<0x141>(v);
    v += dppf<0x140>(v);
    return sum_rows(v);
}
__device__ __forceinline__ unsigned wave_max_u32(unsigned v) {
    v = max(v, dppu<0xB1>(v));
    v = max(v, dppu<0x4E>(v));
    v = max(v, dppu<0x141>(v));
    v = max(v, dppu<0x140>(v));
    unsigned a, b;
    swap16(v, a, b); v = max(a, b);
    swap32(v, a, b); v = max(a, b);
    return v;
}
__device__ __forceinline__ unsigned wave_max_s(unsigned v) {
    v = max(v, dppu<0xB1>(v));
    v = max(v, dppu<0x4E>(v));
    v = max(v, dppu<0x141>(v));
    v = max(v, dppu<0x140>(v));
    v = max(v, dppu<0x142>(v));
    v = max(v, dppu<0x143>(v));
    return (unsigned)__builtin_amdgcn_readlane((int)v, 63);
}
__device__ __forceinline__ float sel4(int g, float a, float b, float c, float d) {
    const float t0 = (g & 1) ? b : a, t1 = (g & 1) ? d : c;
    return (g & 2) ? t1 : t0;
}
__device__ __forceinline__ float pick(u64 m, float a, float b) {
    float r;
    asm("v_cndmask_b32 %0, %1, %2, %3" : "=v"(r) : "v"(a), "v"(b), "s"(m));
    return r;
}
__device__ __forceinline__ unsigned umax4(u32x4 v) { return max(max(v[0], v[1]), max(v[2], v[3])); }

__device__ __forceinline__ void touch_line(const void* p, unsigned lds_dst) {
    unsigned keep;
    asm volatile("s_mov_b32 %0, m0\n\ts_mov_b32 m0, %2\n\ts_nop 0\n\tglobal_load_lds_dword %1, off\n\ts_mov_b32 m0, %0"
                 : "=&s"(keep) : "v"(p), "s"(lds_dst) : "memory");
}
__device__ __forceinline__ __amdgpu_buffer_rsrc_t mk_rsrc(const void* p, unsigned bytes) {
    return __builtin_amdgcn_make_buffer_rsrc((void*)p, 0, (int)bytes, 0x00020000);
}
#define LDS_LOAD(w, v, o, pol) "buffer_load_" w " " v ", %0, %2 offen" o pol " lds"
#define LDS_M0 "s_mov_b32 m0, %1\n\ts_nop 0\n\t"
__device__ __forceinline__ void dma_nt(__amdgpu_buffer_rsrc_t r, unsigned dst, int voff, int soff) {
    asm volatile(LDS_M0 LDS_LOAD("dwordx4", "%3", "", " nt") :: "s"(r), "s"(dst), "s"(soff), "v"(voff) : "memory", "m0");
}
template <bool SC1> __device__ __forceinline__ void dma_word(__amdgpu_buffer_rsrc_t r, unsigned dst, int voff) {
    if constexpr (SC1) asm volatile(LDS_M0 LDS_LOAD("dword", "%3", "", " sc1") :: "s"(r), "s"(dst), "n"(0), "v"(voff) : "memory", "m0");
    else asm volatile(LDS_M0 LDS_LOAD("dword", "%3", "", "") :: "s"(r), "s"(dst), "n"(0), "v"(voff) : "memory", "m0");
}
template <bool SC1> __device__ __forceinline__ void dma_2k(__amdgpu_buffer_rsrc_t r, unsigned dst, int v0, int v1, int soff) {
    if constexpr (SC1) asm volatile(LDS_M0 LDS_LOAD("dwordx4", "%3", "", " sc1") "\n\t" LDS_LOAD("dwordx4", "%4", " offset:1024", " sc1")
                 :: "s"(r), "s"(dst), "s"(soff), "v"(v0), "v"(v1) : "memory", "m0");
    else asm volatile(LDS_M0 LDS_LOAD("dwordx4", "%3", "", "") "\n\t" LDS_LOAD("dwordx4", "%4", " offset:1024", "")
                 :: "s"(r), "s"(dst), "s"(soff), "v"(v0), "v"(v1) : "memory", "m0");
}
template <bool SC1> __device__ __forceinline__ void dma_4k(__amdgpu_buffer_rsrc_t r, unsigned dst, const int (&v)[4], int soff) {
#define FOUR(pol) asm volatile(LDS_M0 LDS_LOAD("dwordx4", "%3", "", pol) "\n\t" LDS_LOAD("dwordx4", "%4", " offset:1024", pol) "\n\t" \
        LDS_LOAD("dwordx4", "%5", " offset:2048", pol) "\n\t" LDS_LOAD("dwordx4", "%6", " offset:3072", pol) \
        :: "s"(r), "s"(dst), "s"(soff), "v"(v[0]), "v"(v[1]), "v"(v[2]), "v"(v[3]) : "memory", "m0")
    if constexpr (SC1) FOUR(" sc1"); else FOUR("");
#undef FOUR
}
#define CODE_BYTES 26112
__device__ __forceinline__ __amdgpu_buffer_rsrc_t code_rsrc() {
    unsigned long long p; unsigned d;
    asm volatile("s_getpc_b64 %0\n1:\n\ts_mov_b32 %1, 1b-_Z12fused_kernel4Args" : "=s"(p), "=s"(d));
    return __builtin_amdgcn_make_buffer_rsrc((void*)(p - d), 0, CODE_BYTES, 0x00020000);
}
#define LD16_SC1(rs, off) __builtin_amdgcn_raw_buffer_load_b128(rs, off, 0, 16)
#define ST16_SC1(v, rs, off) __builtin_amdgcn_raw_buffer_store_b128(v, rs, off, 0, 16)

constexpr int BT = 128, BK = 64, NSTAGE = 4;
constexpr int TILE_BYTES = BT * BK * 2;
constexpr int STAGE_BYTES = 2 * TILE_BYTES;
constexpr int RING_BYTES = NSTAGE * STAGE_BYTES;
constexpr int L_DUMMY = RING_BYTES;
constexpr int L_CTL = RING_BYTES + 256;
constexpr int L_RED = RING_BYTES + 512;

#define WAIT_VM(n) asm volatile("s_waitcnt vmcnt(" #n ")" ::: "memory")
#define WAIT_VM0() do { asm volatile("" ::: "memory"); __builtin_amdgcn_s_waitcnt(0x0F70); asm volatile("s_waitcnt vmcnt(0)" ::: "memory"); } while (0)
#define WAIT_LGKM0() asm volatile("s_waitcnt lgkmcnt(0)" ::: "memory")
#define BARRIER() do { asm volatile("" ::: "memory"); __builtin_amdgcn_s_barrier(); asm volatile("" ::: "memory"); } while (0)

__device__ __forceinline__ int prow(int rho) {
    const int w = rho >> 6, m = (rho >> 4) & 3, q = (rho >> 2) & 3, r = rho & 3;
    return w * 64 + (m >> 1) * 32 + q * 8 + (m & 1) * 4 + r;
}
__device__ __forceinline__ unsigned r2key(float r, unsigned code) {
    const unsigned u = pk_bf16(0.f, r);
    return ((u ^ ((unsigned)((int)u >> 31) | 0x80000000u)) & 0xffff0000u) | code;
}
__device__ __forceinline__ float key2r(unsigned k) {
    const unsigned o = k >> 16;
    return bflo((o & 0x8000u) ? (o & 0x7fffu) : (~o & 0xffffu));
}
#define CK_T -1, -1, -1, -1, 2, 2, 2, 2
#define STAT_M 5
#define W_PRE 2
#define W_PRE_SL 20
#define BQ 2
#define POLL_T (STAT_M + 5)
#define AUX_SLEEP 12
#define WD_EARLY 2
#define C2_START 0
#define C2_STAG 7
#define TAIL_PRIO 3
#define STAT_DELAY 12
#define CODE_WG 1
#define AUX_STAY 1
#define T_DELAY 0
#define WS_END 1
#define CATCH1 4
#define CPF_MASK1 0x1080u
#define CPF_MASK2 0x1080u
#define CPF_STAG 5
#define CPF_FIX 0x1000u
#define OUT_AUX 0
#define KEY_S 128
#define G2_N 2
#define G3_N 2
#define FIN_SLEEP 1
#define G_N 8
#define POLL_SLEEP 4
__device__ __forceinline__ f32x4 smf(bf16x8 a, bf16x8 b0, bf16x8 b1, f32x4 d, unsigned idx) {
    typedef __bf16 bf8 __attribute__((ext_vector_type(8)));
    typedef __bf16 bf16v __attribute__((ext_vector_type(16)));
    const auto b = __builtin_shufflevector(b0, b1, 0, 1, 2, 3, 4, 5, 6, 7, 8, 9, 10, 11, 12, 13, 14, 15);
    return __builtin_amdgcn_smfmac_f32_16x16x64_bf16(__builtin_bit_cast(bf8, a), __builtin_bit_cast(bf16v, b), d, (int)idx, 0, 0);
}
__device__ __forceinline__ float dot2bf(unsigned a, unsigned b, float c) {
    typedef __bf16 bf2 __attribute__((ext_vector_type(2)));
    return __builtin_amdgcn_fdot2_f32_bf16(__builtin_bit_cast(bf2, a), __builtin_bit_cast(bf2, b), c, false);
}

#define STAMP(i) do { } while (0)
#define CSTAMP(i) do { } while (0)
#define FLUSH() do { } while (0)
#define TSV do { } while (0)

#define KSHIFT 4

constexpr int L_HT = ((32 + KSHIFT) % NSTAGE) * STAGE_BYTES;
constexpr int L_WD0 = ((33 + KSHIFT) % NSTAGE) * STAGE_BYTES;
constexpr int L_WD1 = ((34 + KSHIFT) % NSTAGE) * STAGE_BYTES;
constexpr int L_YB = RING_BYTES;
constexpr int L_ATAB = L_YB;
constexpr int LDS_BYTES = RING_BYTES + 32768;

#define SB() __builtin_amdgcn_sched_barrier(0)
template <int N> __device__ __forceinline__ void wait_vm() { asm volatile("s_waitcnt vmcnt(%0)" :: "n"(N) : "memory"); }
__device__ __forceinline__ int at_tile(int c, int par) { return ((c << 8) & 0x78000) | (((c << 2) & 0x2000) ^ par); }
__device__ __forceinline__ int at_panel(int ep) { return ep << 19; }
__device__ __forceinline__ int pre_off(int slot) { const int s_ = slot >= 56, l = slot - 56 * s_; return (l >> 2) * 16384 + s_ * 8192 + (l & 3) * 2048; }
static_assert(KSHIFT == 4, "flag samples must have replaced older contents of their slot; even tile counts");
struct Ring {
    __amdgpu_buffer_rsrc_t rP, rQ, rF;
    int voP[4], voQ[4], voF;
    LAS unsigned char* fl;
    unsigned lb;
    int sp, sq;
    int poff, par;
    bool img;
    template <int LD, bool PIMG> __device__ __forceinline__ void setup(int lw, int lane) {
        const int r = lane >> 3, v0 = (PIMG ? r : (r & 3) + (r & 4) * 2) * (LD * 2) + (((lane & 7) ^ (lane >> 4)) << 4);
#pragma unroll
        for (int j = 0; j < 4; ++j) {
            voQ[j] = (v0 ^ ((j & 1) * 64)) + (PIMG ? j * 8 : (j & 1) * 16 + (j >> 1) * 4) * (LD * 2) - j * 1024;
            voP[j] = PIMG ? (lw >> 1) * 16384 + (lw & 1) * 4096 + lane * 16 : voQ[j];
        }
        img = PIMG;
    }
    __device__ __forceinline__ void seek(int col, int pof) {
        sq = col; poff = pof;
        sp = (img ? at_tile(sq, par) : sq) + poff;
    }
    __device__ __forceinline__ void step() {
        sq = (sq + BK * 2) & 4095;
        sp = (img ? at_tile(sq, par) : sq) + poff;
        asm volatile("" : "+s"(sq), "+s"(sp));
    }
    __device__ __forceinline__ void sample() { dma_word<true>(rF, (unsigned)(size_t)fl, voF); }
    __device__ __forceinline__ unsigned peek(unsigned addr) {
        unsigned v;
        asm volatile("ds_read_b32 %0, %1\n\ts_waitcnt lgkmcnt(0)" : "=v"(v) : "v"(addr) : "memory");
        return v;
    }
    __device__ __forceinline__ bool seen() { return __builtin_amdgcn_readfirstlane(peek((unsigned)(size_t)fl)) == 1u; }
    __device__ __forceinline__ void gate(unsigned want, unsigned sa, unsigned need, unsigned& have) {
#pragma unroll 1
        for (int spin = 0; spin <= SPIN_CAP && (need & ~have) != 0u; ++spin) {
            if (spin) { sample(); WAIT_VM(0); __builtin_amdgcn_s_sleep(1); }
            const unsigned long long m = __ballot(peek(sa) == want);
            have = (unsigned)m & (unsigned)(m >> 32);
        }
    }
    template <int AUX> __device__ __forceinline__ void issueP(LAS unsigned char* base) { dma_4k<AUX != 0>(rP, (unsigned)(size_t)base, voP, sp); }
    __device__ __forceinline__ void issueQ(LAS unsigned char* base) { dma_4k<false>(rQ, (unsigned)(size_t)base + TILE_BYTES, voQ, sq); }
    template <bool P2> __device__ __forceinline__ void issue(LAS unsigned char* lds) {
        issueP<P2 ? 16 : 0>(lds + lb);
        issueQ(lds + lb);
        SB();
        if (P2) sample();
        lb = (lb + STAGE_BYTES) & (RING_BYTES - 1);
        asm volatile("" : "+s"(lb));
        step();
        SB();
    }
    template <int CAT> __device__ __forceinline__ void ramp(LAS unsigned char* lds) {
#pragma unroll 1
        for (int i = 1; i <= CAT + 1; ++i) {
            issue<false>(lds);
            if (i == CAT + 1) continue;
            WAIT_VM(8);
            BARRIER();
        }
    }
    template <bool P2, bool SAMP, int W = 16> __device__ __forceinline__ void steady(int cnt) {
        int t, u;
#define LD(v, r, s, o) "buffer_load_dwordx4 " v ", " r ", " s " offen" o " lds\n\t"
        if constexpr (P2) {
#define BODY(SMP) asm volatile("s_mov_b32 m0, %0\n\ts_branch 2f\n1:\n\ts_waitcnt vmcnt(%17)\n\ts_barrier\n2:\n\t" \
            LD("%8", "%6", "%2", " sc1") LD("%8", "%6", "%2", " offset:1024 sc1") LD("%8", "%6", "%2", " offset:2048 sc1") LD("%8", "%6", "%2", " offset:3072 sc1") \
            SMP "s_mov_b32 %4, %1\n\ts_add_i32 %1, %1, 0x80\n\ts_add_i32 m0, %0, 0x4000\n\ts_and_b32 %1, %1, 0xfff\n\t" \
            LD("%9", "%7", "%4", "") "s_lshl_b32 %2, %1, 8\n\ts_lshl_b32 %5, %1, 2\n\ts_and_b32 %2, %2, 0x78000\n\t" \
            LD("%10", "%7", "%4", " offset:1024") "s_and_b32 %5, %5, 0x2000\n\ts_xor_b32 %5, %5, %13\n\ts_or_b32 %2, %2, %5\n\t" \
            LD("%11", "%7", "%4", " offset:2048") "s_add_i32 %0, %0, 0x8000\n\ts_and_b32 %0, %0, 0x1ffff\n\ts_add_i32 %3, %3, -1\n\ts_cmp_lg_u32 %3, 0\n\t" \
            LD("%12", "%7", "%4", " offset:3072") "s_mov_b32 m0, %0\n\ts_cbranch_scc1 1b\n\ts_waitcnt vmcnt(%17)\n\ts_barrier" \
            : "+s"(lb), "+s"(sq), "+s"(sp), "+s"(cnt), "=&s"(t), "=&s"(u) \
            : "s"(rP), "s"(rQ), "v"(voP[0]), "v"(voQ[0]), "v"(voQ[1]), "v"(voQ[2]), "v"(voQ[3]), "s"(par | poff), "s"(rF), "v"(voF), "s"((unsigned)(size_t)fl), "n"(W) \
            : "memory", "scc", "m0")
            if constexpr (SAMP) BODY("s_mov_b32 m0, %16\n\ts_nop 0\n\tbuffer_load_dword %15, %14, 0 offen sc1 lds\n\t");
            else BODY("");
#undef BODY
        } else {
            asm volatile("s_mov_b32 m0, %0\n\ts_branch 2f\n1:\n\ts_waitcnt vmcnt(16)\n\ts_barrier\n2:\n\t"
                LD("%6", "%4", "%1", "") LD("%7", "%4", "%1", " offset:1024") LD("%8", "%4", "%1", " offset:2048") LD("%9", "%4", "%1", " offset:3072")
                "s_add_i32 m0, %0, 0x4000\n\ts_mov_b32 %3, %1\n\t"
                LD("%6", "%5", "%3", "") "s_add_i32 %1, %1, 0x80\n\ts_add_i32 %0, %0, 0x8000\n\t"
                LD("%7", "%5", "%3", " offset:1024") "s_and_b32 %0, %0, 0x1ffff\n\ts_add_i32 %2, %2, -1\n\t"
                LD("%8", "%5", "%3", " offset:2048") "s_cmp_lg_u32 %2, 0\n\t"
                LD("%9", "%5", "%3", " offset:3072") "s_mov_b32 m0, %0\n\ts_cbranch_scc1 1b\n\ts_waitcnt vmcnt(16)\n\ts_barrier"
                : "+s"(lb), "+s"(sq), "+s"(cnt), "=&s"(t)
                : "s"(rP), "s"(rQ), "v"(voQ[0]), "v"(voQ[1]), "v"(voQ[2]), "v"(voQ[3])
                : "memory", "scc", "m0");
            (void)u;
        }
#undef LD
    }
};

template <bool PLIN, bool FIRST, class Pre>
__device__ __forceinline__ void compute_loop(const LAS unsigned char* lds, f32x4 (&acc)[4][4], int NT, int wp, int wq, int lane, Pre pre,
                                             bf16x8 (&p0)[4], bf16x8 (&p1)[4], unsigned last) {
    const int fr = lane & 15, fq = lane >> 4, key = (fr >> 1) & 7;
    const unsigned l0 = (unsigned)(size_t)lds;
    const unsigned oP0 = l0 + (PLIN ? wp * 8192 + lane * 16 : (wp * 64 + fr) * 128 + ((fq ^ key) * 16));
    const unsigned oP1 = l0 + (PLIN ? wp * 8192 + 1024 + lane * 16 : (wp * 64 + fr) * 128 + (((4 + fq) ^ key) * 16));
    const unsigned oQ0 = l0 + TILE_BYTES + (wq * 64 + fr) * 128 + ((fq ^ key) * 16);
    const unsigned oQ1 = l0 + TILE_BYTES + (wq * 64 + fr) * 128 + (((4 + fq) ^ key) * 16);
    unsigned aP0 = oP0, aQ0 = oQ0, aP1 = oP1, aQ1 = oQ1;
    bf16x8 r0[4], r1[4], q0[4], q1[4];
    unsigned xp = 2 * STAGE_BYTES;
#pragma unroll
    for (int i = 0; i < 16; ++i) asm volatile("" : "+v"(acc[i >> 2][i & 3]));
    asm volatile("" : "+v"(aP0), "+v"(aQ0), "+v"(aP1), "+v"(aQ1));
#define MM(pp, qq, m, n) do { acc[m][n] = __builtin_amdgcn_mfma_f32_16x16x32_bf16(pp[m], qq[n], acc[m][n], 0, 0, 0); SB(); } while (0)
#define RD(dst, i, a, st) do { dst[i] = *(const LAS bf16x8*)((const LAS unsigned char*)(size_t)(a) + (st) * STAGE_BYTES + (i) * 2048); SB(); } while (0)
#define ADP(a) do { a ^= xp; asm volatile("" : "+v"(a)); SB(); } while (0)
#define ADR(a) do { a ^= 2 * STAGE_BYTES; asm volatile("" : "+v"(a)); SB(); } while (0)
    __builtin_amdgcn_s_waitcnt(0xC07F);
    SB();
    if (FIRST) BARRIER();
    SB();
    RD(p0, 0, aP0, 0); RD(q0, 0, aQ0, 0); RD(p1, 0, aP1, 0); RD(q1, 0, aQ1, 0); RD(p1, 1, aP1, 0); RD(p0, 1, aP0, 0);
    RD(p0, 2, aP0, 0); RD(p1, 2, aP1, 0); RD(p1, 3, aP1, 0); RD(p0, 3, aP0, 0);
    pre();
    SB();
    int t = NT;
#pragma unroll 1
    do {
        MM(p0, q0, 0, 0); MM(p1, q1, 0, 0); RD(q0, 1, aQ0, 0);
        MM(p1, q1, 1, 0); MM(p0, q0, 1, 0); RD(q1, 1, aQ1, 0);
        MM(p0, q0, 2, 0); MM(p1, q1, 2, 0); RD(q0, 2, aQ0, 0);
        MM(p1, q1, 3, 0); MM(p0, q0, 3, 0); RD(q1, 2, aQ1, 0);
        MM(p0, q0, 3, 1); MM(p1, q1, 3, 1); RD(q0, 3, aQ0, 0);
        MM(p1, q1, 2, 1); MM(p0, q0, 2, 1); RD(q1, 3, aQ1, 0);
        MM(p0, q0, 1, 1); MM(p1, q1, 1, 1);
        MM(p1, q1, 0, 1); MM(p0, q0, 0, 1);
        MM(p0, q0, 0, 2); MM(p1, q1, 0, 2); __builtin_amdgcn_s_waitcnt(0xC07F); SB(); BARRIER(); SB();
        MM(p1, q1, 1, 2); RD(r0, 0, aP0, 1); MM(p0, q0, 1, 2); RD(q0, 0, aQ0, 1);
        MM(p0, q0, 2, 2); RD(r1, 0, aP1, 1); MM(p1, q1, 2, 2); RD(q1, 0, aQ1, 1);
        MM(p1, q1, 3, 2); MM(p0, q0, 3, 2); RD(r1, 1, aP1, 1);
        MM(p0, q0, 3, 3); RD(r0, 1, aP0, 1); MM(p1, q1, 3, 3);
        MM(p1, q1, 2, 3); RD(r0, 2, aP0, 1); MM(p0, q0, 2, 3); RD(r1, 2, aP1, 1);
        MM(p0, q0, 1, 3); MM(p1, q1, 1, 3); RD(r1, 3, aP1, 1);
        MM(p1, q1, 0, 3); RD(r0, 3, aP0, 1); MM(p0, q0, 0, 3);

        MM(r0, q0, 0, 0); MM(r1, q1, 0, 0); RD(q0, 1, aQ0, 1);
        MM(r1, q1, 1, 0); MM(r0, q0, 1, 0); RD(q1, 1, aQ1, 1);
        MM(r0, q0, 2, 0); MM(r1, q1, 2, 0); RD(q0, 2, aQ0, 1);
        MM(r1, q1, 3, 0); MM(r0, q0, 3, 0); RD(q1, 2, aQ1, 1);
        MM(r0, q0, 3, 1); MM(r1, q1, 3, 1); RD(q0, 3, aQ0, 1);
        MM(r1, q1, 2, 1); MM(r0, q0, 2, 1); RD(q1, 3, aQ1, 1);
        MM(r0, q0, 1, 1); t -= 2; if (PLIN) xp = t == 0 ? last : 2 * STAGE_BYTES; asm volatile("" : "+s"(t), "+s"(xp)); SB(); MM(r1, q1, 1, 1); ADP(aP0);
        MM(r1, q1, 0, 1); ADP(aP1); MM(r0, q0, 0, 1); ADR(aQ0);
        MM(r0, q0, 0, 2); ADR(aQ1); MM(r1, q1, 0, 2); __builtin_amdgcn_s_waitcnt(0xC07F); SB(); BARRIER(); SB();
        MM(r1, q1, 1, 2); RD(p0, 0, aP0, 0); MM(r0, q0, 1, 2); RD(q0, 0, aQ0, 0);
        MM(r0, q0, 2, 2); RD(p1, 0, aP1, 0); MM(r1, q1, 2, 2); RD(q1, 0, aQ1, 0);
        MM(r1, q1, 3, 2); MM(r0, q0, 3, 2); RD(p1, 1, aP1, 0);
        MM(r0, q0, 3, 3); RD(p0, 1, aP0, 0); MM(r1, q1, 3, 3);
        MM(r1, q1, 2, 3); RD(p0, 2, aP0, 0); MM(r0, q0, 2, 3); RD(p1, 2, aP1, 0);
        MM(r0, q0, 1, 3); MM(r1, q1, 1, 3); RD(p1, 3, aP1, 0);
        MM(r1, q1, 0, 3); RD(p0, 3, aP0, 0); MM(r0, q0, 0, 3);
    } while (t != 0);
    SB();
#undef MM
#undef RD
#undef ADR
#undef ADP
}

struct Args {
    const bf16_t* x; const bf16_t* wre; const bf16_t* wim; char* ws; const bf16_t* cr; const bf16_t* ci;
    const bf16_t* w_r; const bf16_t* wg; const bf16_t* wu; const bf16_t* wd; bf16_t* out;
};

__device__ __forceinline__ unsigned poll_winner(const char* ws, int rep, int lane, unsigned& mine) {
    GAS unsigned* kp = (GAS unsigned*)(ws + WS_KEYS + (size_t)(rep >> 2) * 8192 + (rep & 3) * (KEY_S * 16) + ((lane >> 2) & 7) * KEY_S) + (lane & 3);
    unsigned kw;
    for (int spin = 0;; ++spin) {
            asm volatile("" ::: "memory");
        kw = __hip_atomic_load(kp, __ATOMIC_RELAXED, __HIP_MEMORY_SCOPE_AGENT);
        if (__all(kw != 0u) || spin > SPIN_CAP) break;
        __builtin_amdgcn_s_sleep(POLL_SLEEP);
    }
    mine = kw;
    return __builtin_amdgcn_readfirstlane(wave_max_u32(kw));
}

__device__ __forceinline__ void stage2(char* w2, int slot, int lw, LAS unsigned char* img0, LAS unsigned char* img1, LAS unsigned char* abuf, int zoff, int lane) {
    const int g = lane >> 4, n = lane & 15;
    const __amdgpu_buffer_rsrc_t rPi = mk_rsrc(w2 + WS_PRE + pre_off(slot), 2048);
    unsigned pp[8];
    auto request = [&]() {
        asm volatile("" ::: "memory");
#pragma unroll
        for (int k = 0; k < 8; ++k) pp[k] = __builtin_amdgcn_raw_buffer_load_b32(rPi, k * 256 + lane * 4, 0, 16);
        SB();
    };
    request();
    bf16x8 wf[16];
    {
        const int q = (lane >> 2) & 3, ky = q | ((g & 1) << 2);
#pragma unroll
        for (int j = 0; j < 16; ++j) {
            LAS unsigned char* t = (g < 2 ? img0 : img1) + (8 * (g & 1) + q) * 512 + (((j & 7) ^ ky) + (j & 8)) * 32 + (lane & 3) * 8;
            const bf16x4 w0 = __builtin_amdgcn_ds_read_tr16_b64_v4i16((LAS bf16x4*)t);
            const bf16x4 w1 = __builtin_amdgcn_ds_read_tr16_b64_v4i16((LAS bf16x4*)(t + 2048));
            wf[j] = __builtin_shufflevector(w0, w1, 0, 1, 2, 3, 4, 5, 6, 7);
        }
        SB();
#pragma unroll
        for (int j = 0; j < 16; ++j) asm volatile("" : "+v"(wf[j]));
    }
    unsigned aa[5], pto = lane * 16;
#pragma unroll
    for (int m = 0; m < 4; ++m) aa[m] = (unsigned)(size_t)abuf + ((n >> 2) == m && ((n >> 1) & 1) == (g >> 1) ? (n & 1) * 32 + (g & 1) * 16 : zoff + g * 16);
    aa[4] = (unsigned)(size_t)abuf + ((lane >> 1) & 1) * 32 + ((lane >> 4) & 1) * 16 + ((lane >> 2) & 3) * 4 + (lane & 1) * 2;
    unsigned ix = (n & 1) ? 0xEEEEEEEEu : 0x44444444u;
    __amdgpu_buffer_rsrc_t rPt = mk_rsrc(w2 + WS_PART + __builtin_amdgcn_readfirstlane((slot >> 2) * 16384 + (lw >> 1) * 8192 + (slot & 3) * 2048 + (lw & 1) * 1024), 1024);
    asm volatile("" : "+v"(aa[0]), "+v"(aa[1]), "+v"(aa[2]), "+v"(aa[3]), "+v"(aa[4]), "+v"(pto), "+v"(ix));
    asm volatile("" : "+s"(rPt));
    f32x4 d[2] = {{0.f, 0.f, 0.f, 0.f}, {0.f, 0.f, 0.f, 0.f}};
    asm volatile("" : "+v"(d[0]), "+v"(d[1]));
    SB();
    float sv;
#pragma unroll 1
    for (int spin = 0;; ++spin) {
        sv = ((__uint_as_float(pp[0]) + __uint_as_float(pp[1])) + (__uint_as_float(pp[2]) + __uint_as_float(pp[3]))) +
             ((__uint_as_float(pp[4]) + __uint_as_float(pp[5])) + (__uint_as_float(pp[6]) + __uint_as_float(pp[7])));
        if (__builtin_expect(__builtin_amdgcn_ballot_w64(sv != sv) == 0, 1)) break;
        if (__all(max(max(max(pp[0], pp[1]), max(pp[2], pp[3])), max(max(pp[4], pp[5]), max(pp[6], pp[7]))) != SENT) || spin > SPIN_CAP) break;
        request();
    }
    unsigned ug, uu;
    swap32(__float_as_uint(sv), ug, uu);
    const float gg = rbf(__uint_as_float(ug)), up = rbf(__uint_as_float(uu));
    const unsigned act = pk_bf16(rbf(gg * __builtin_amdgcn_rcpf(1.0f + __expf(-gg))) * up, 0.f);
    *(volatile LAS unsigned short*)(size_t)aa[4] = (unsigned short)act;
    bf16x8 af[4];
#pragma unroll
    for (int m = 0; m < 4; ++m) af[m] = *(volatile LAS bf16x8*)(size_t)aa[m];
    SB();
#pragma unroll
    for (int i = 0; i < 8; ++i) {
        const int j = 8 * (i & 1) + 2 * (i >> 1);
        d[i & 1] = smf(af[i >> 1], wf[j], wf[j + 1], d[i & 1], ix);
        SB();
    }
    u32x4 pv;
#pragma unroll
    for (int q = 0; q < 4; ++q) {
        pv[q] = min(__float_as_uint(d[q >> 1][(q & 1) * 2] + d[q >> 1][(q & 1) * 2 + 1]), SENT - 1u);
        asm volatile("" : "+v"(pv[q]));
    }
    ST16_SC1(pv, rPt, pto);
}
__device__ __forceinline__ int wd_off(int lw, int i, int lane) {
    const int r = 2 * (i & 5) + (lane >> 5), ky = (r & 3) | ((r >> 1) & 4), c = lane & 31;
    return (lane >> 5) * (D_LEN * 2) + lw * 512 + (((c >> 1) ^ ky) * 2 + (c & 1)) * 16;
}

__global__ __launch_bounds__(768, 3) void fused_kernel(const Args a) {
    extern __shared__ __attribute__((aligned(16))) unsigned char smem[];
    constexpr int K = S_LEN, NT1 = D_LEN / BK;
    const int tid = threadIdx.x, wid = __builtin_amdgcn_readfirstlane(tid >> 6), lane = tid & 63;
    const int xcd = blockIdx.x & 7, idx = blockIdx.x >> 3;
    TSV;
    LAS unsigned char* lds = (LAS unsigned char*)smem;
    if (idx < CODE_WG && wid < 4) dma_word<false>(code_rsrc(), (unsigned)(size_t)lds + L_DUMMY, tid * 128);
    SB();
    const int h = xcd & 1, xq = xcd >> 1, rt = xq * 4 + (idx & 3), ct = idx >> 2;
    volatile LAS unsigned* ctl1 = (volatile LAS unsigned*)(lds + L_CTL);
#define NBR(k) ((rt + (k)) & 3)
    unsigned kx;
    Ring ring;
    if (__builtin_expect((wid & 12) == 4, 1)) {
        __builtin_amdgcn_s_setprio(3);
        ring.template setup<D_LEN, false>(wid - 4, lane);
        ring.rP = mk_rsrc(a.x + (size_t)(rt * 4 + wid - 4) * 32 * D_LEN, 32 * D_LEN * 2);
        ring.rQ = mk_rsrc((h == 0 ? a.wre : a.wim) + (size_t)(ct * 4 + wid - 4) * 32 * D_LEN, 32 * D_LEN * 2);
        ring.lb = (wid - 4) * 4096;
        ring.sp = ring.sq = ring.poff = 0;
        ring.template issue<false>(lds);
        __builtin_amdgcn_s_setprio(0);
        ring.par = (ct & 4) << 11;
        ring.template ramp<CATCH1>(lds);
        ring.template steady<false, false>(NT1 - 2 - CATCH1);
        ring.template setup<K, true>(wid - 4, lane);
        ring.rQ = mk_rsrc((h == 0 ? a.cr : a.ci) + (size_t)(rt * 4 + wid - 4) * 32 * K, 32 * K * 2);
        ring.seek(rt * 256, 0);
        LAS unsigned char* l0 = lds + (wid - 4) * 4096;
        ring.issueQ(l0); ring.step();
        WAIT_VM(12); BARRIER();
        ring.issueQ(l0 + STAGE_BYTES);
        ring.seek((xq * 4 + NBR(1)) * 256, 0);
        WAIT_VM(8); BARRIER();
        ring.issueQ(l0 + 2 * STAGE_BYTES);
        ring.seek((xq * 4 + NBR(2)) * 256, 0);
        const int xr = (wid & 1) * 8 + (lane >> 4);
        int vx0 = ((((wid >> 1) & 1) * 64 + xr) * D_LEN + (((lane & 15) ^ xr) * 8)) * 2;
        int vx1 = ((((wid >> 1) & 1) * 64 + xr + 4) * D_LEN + (((lane & 15) ^ (xr + 4)) * 8)) * 2 - 1024;
        __amdgpu_buffer_rsrc_t rX = mk_rsrc(a.x + (size_t)rt * BT * D_LEN + ct * BT, BT * D_LEN * 2 - ct * BT * 2);
        unsigned xd = (unsigned)(size_t)lds + ((wid & 2) ? L_YB + 16384 : 3 * STAGE_BYTES) + (wid & 1) * 2048;
        asm volatile("" : "+v"(vx0), "+v"(vx1));
        asm volatile("" : "+s"(rX), "+s"(xd));
        BARRIER();
        if (h != 0) {
#pragma unroll
            for (int k = 0; k < 4; ++k) dma_2k<false>(rX, xd + k * 4096, vx0, vx1, k * (16 * D_LEN * 2));
        }
        ring.issueQ(l0 + 3 * STAGE_BYTES);
        kx = (unsigned)__builtin_amdgcn_readfirstlane((int)ctl1[4]);
        SB();
    }
    char* ws = a.ws + ((0ull - (unsigned long long)a.ws) & 0x1fffffull);
    const int g = lane >> 4, n = lane & 15;
    LAS float* tab = (LAS float*)(lds + L_RED);
    if (wid >= 8) {
        const int aw = wid - 8;
        GAS u64* tagp = (GAS u64*)(ws + WS_TAG);
        const int r0 = xq * 512 + h * 256 + idx * 8 + aw * 2;
        const int w0 = xq * 256 + idx * 8 + aw * 2;
        const u32x4* src = (const u32x4*)((n < 8 ? a.x + (size_t)(r0 + (n >> 2)) * D_LEN
                                                 : (h == 0 ? a.wre : a.wim) + (size_t)(w0 + (n >> 2) - 2) * D_LEN) + g * 32 + (n & 3) * 8);
        if (AUX_SLEEP) __builtin_amdgcn_s_sleep(AUX_SLEEP);
        u64 tag0 = __hip_atomic_load(tagp, __ATOMIC_RELAXED, __HIP_MEMORY_SCOPE_AGENT);
        u32x4 v[8];
        constexpr int CKT[8] = {CK_T};
#pragma unroll
        for (int j = 0; j < 8; ++j) if (CKT[j] < 0) v[j] = src[j * 16];
        __builtin_amdgcn_s_setprio(3);
        SB();
        const char* cb = (const char*)(h ? a.ci : a.cr) + (size_t)rt * (512u << 10) + ct * 65536 + (lane >> 2) * 4096 + (lane & 3) * 128;
        const GAS u32x4* wrp = (const GAS u32x4*)a.w_r + lane;
        asm volatile("" : "+v"(cb), "+v"(wrp));
        unsigned mask = aw == 1 ? CPF_MASK1 : aw == 2 ? CPF_MASK2 : 0u;
        mask = (mask & CPF_FIX) | ((mask & ~CPF_FIX) << (idx % CPF_STAG));
        int ti = 2 * xq + aw - 1;
        auto warm = [&](int t) { if (t >= 4 && ((mask >> t) & 1)) { touch_line(cb + (ti & 7) * 512, L_DUMMY); ti += 2; } };
#pragma unroll
        for (int t = 0; t <= STAT_M; ++t) {
            SB(); BARRIER(); SB();
            if (aw == 1) STAMP(40 + t);
#pragma unroll
            for (int j = 0; j < 8; ++j) if (CKT[j] == t) v[j] = src[j * 16];
            warm(t);
        }
        SB();
        f32x4 d1 = {0.f, 0.f, 0.f, 0.f}, d2 = {0.f, 0.f, 0.f, 0.f};
        {
            const u32x4 ones = {0x3F803F80u, 0x3F803F80u, 0x3F803F80u, 0x3F803F80u};
#pragma unroll
            for (int j = 0; j < 8; ++j) {
                const bf16x8 f = __builtin_bit_cast(bf16x8, v[j]);
                d1 = __builtin_amdgcn_mfma_f32_16x16x32_bf16(f, __builtin_bit_cast(bf16x8, ones), d1, 0, 0, 0);
                d2 = __builtin_amdgcn_mfma_f32_16x16x32_bf16(f, f, d2, 0, 0, 0);
            }
        }
        asm volatile("" : "+v"(tag0), "+v"(d1), "+v"(d2));
        SB(); BARRIER(); SB();
        warm(STAT_M + 1);
        const u64 want = tag0 * 6364136223846793005ull + 1442695040888963407ull;
        const unsigned wl = (unsigned)want, wh = (unsigned)(want >> 32);
        const __amdgpu_buffer_rsrc_t rS = mk_rsrc(ws + WS_XRD, 65536);
        {
            const float sm = (d1[0] + d1[1]) + (d1[2] + d1[3]);
            float sq = sel4(n, d2[0], d2[1], d2[2], d2[3]);
            sq += dppf<0xB1>(sq);
            sq += dppf<0x4E>(sq);
            const float mean = sm * (1.0f / D_LEN);
            const float var = fmaxf(sq * (1.0f / D_LEN) - mean * mean, 0.f);
            const float rden = __builtin_amdgcn_rcpf(rbf(__builtin_amdgcn_sqrtf(rbf(rbf(var) + 1e-5f))));
            const u32x4 rec = {__float_as_uint(g < 2 ? rden : sm), wl, __float_as_uint(rbf(mean) * rden), wh};
            ST16_SC1(rec, rS, n == g * 4 ? (g < 2 ? r0 + g : 2048 - 2 + h * 1024 + w0 + g) * 16 : 65536);
        }
        const __amdgpu_buffer_rsrc_t rV = mk_rsrc(ws + WS_VOTE, 512);
        if (aw == 3 && idx < 8) __builtin_amdgcn_raw_buffer_store_b64((u32x2){ctl1[5], wl}, rV, blockIdx.x * 8 + lane * 512, 0, 16);
        SB(); BARRIER(); SB();
        warm(STAT_M + 2);
        if (aw == 1 || aw == 2) {
#pragma unroll 1
            for (int t = STAT_M + 3; t <= NT1 + AUX_STAY; ++t) {
                BARRIER();
                if (aw == 1 && t <= 16) STAMP(40 + t);
                warm(t);
            }
            FLUSH();
            return;
        }
        if (aw == 3) {
            if (h == 0) {
                const u32x4 w0v = wrp[0], w1v = wrp[64];
                BARRIER(); BARRIER(); BARRIER(); BARRIER();
                SB();
                float t = 0.f;
#pragma unroll
                for (int i = 0; i < 4; ++i) { t = dot2bf(w0v[i], 0x3F803F80u, t); t = dot2bf(w1v[i], 0x3F803F80u, t); }
                SB(); BARRIER(); SB();
                t = wave_sum(t);
                if (lane == 0) ctl1[1] = __float_as_uint(t);
                WAIT_LGKM0();
            }
            return;
        }
#pragma unroll
        for (int t = STAT_M + 3; t <= POLL_T; ++t) BARRIER();
        SB();
        const int o0 = (rt * BT + lane) * 16, o1 = (2048 + (h * 8 + ct) * BT + lane) * 16, os = 1024;
        u32x4 q0, q1, q2, q3;
        u32x2 qv;
        auto request = [&]() {
            asm volatile("" ::: "memory");
            q0 = LD16_SC1(rS, o0); q1 = LD16_SC1(rS, o0 + os); q2 = LD16_SC1(rS, o1); q3 = LD16_SC1(rS, o1 + os);
            qv = __builtin_amdgcn_raw_buffer_load_b64(rV, lane * 8, 0, 16);
            SB();
        };
        request();
        int b = POLL_T + 1;
        for (int spin = 0; spin <= SPIN_CAP; ++spin) {
            if (b < NT1) { BARRIER(); ++b; }
            if (b < NT1) { BARRIER(); ++b; }
            SB();
            unsigned d = ((q0[1] ^ wl) | (q1[1] ^ wl)) | ((q2[1] ^ wl) | (q3[1] ^ wl)) | ((q0[3] ^ wh) | (q1[3] ^ wh)) | ((q2[3] ^ wh) | (q3[3] ^ wh)) | (qv[1] ^ wl);
            asm volatile("" : "+v"(d));
            if (__all(d == 0u)) break;
            request();
        }
        if (b < NT1) BARRIER();
        SB();
        LAS unsigned* tb = (LAS unsigned*)tab;
        const unsigned sg = h == 0 ? 0u : 0x80000000u;
        tb[lane] = q0[0] ^ sg; tb[64 + lane] = q1[0] ^ sg; tb[128 + lane] = q0[2] ^ sg; tb[192 + lane] = q1[2] ^ sg;
        tb[256 + lane] = q2[0]; tb[320 + lane] = q3[0];
        const unsigned vt = qv[0];
        const unsigned kx = __popcll(__ballot(vt == 2u)) > __popcll(__ballot(vt == 1u)) ? 8192u : 0u;
        if (lane == 0) { ctl1[0] = wl | 2u; ctl1[2] = wl; ctl1[3] = wh; ctl1[4] = kx; }
        *(LAS u32x2*)(lds + L_YB + 4096 + 256 + lane * 8) = (u32x2){0u, 0u};
        WAIT_LGKM0();
        return;
    }
#define CTL(i) ((unsigned)__builtin_amdgcn_readfirstlane((int)ctl[i]))
    const int wp = wid & 1, wq = (wid >> 1) & 1;
#define Y_OFF ((int)WS_Y + ((rt & 7) * 8 + ct) * 65536 + (wid & 3) * 16384 + (((rt >> 3) ^ (rt >> 2)) & 1) * 8192)
#define P2_DEFS \
    char* w2 = ws + kx; \
    const int sidx = idx < 28 ? xq * 28 + idx : 112 + xq * 4 + idx - 28;     \
    volatile LAS unsigned* ctl = (volatile LAS unsigned*)(lds + (h == 0 ? L_ATAB : L_CTL)); \
    const int NT = h == 0 ? 32 + KSHIFT : 32 - KSHIFT; \
    LAS float* rstat = (LAS float*)(lds + L_ATAB + 256);     \
    const int hp_ = rt * 2 + wq;                             \
    (void)sidx; (void)ctl; (void)NT; (void)rstat; (void)hp_; (void)w2
    if (__builtin_expect(wid < 4, 1)) {
        {
            const u32x4 sent4 = {SENT, SENT, SENT, SENT}, zero4 = {0u, 0u, 0u, 0u};
            const __amdgpu_buffer_rsrc_t rI = mk_rsrc(ws + WS_STATS + (size_t)blockIdx.x * (FILL_BYTES / 256), FILL_BYTES / 256);
            const __amdgpu_buffer_rsrc_t rZ = mk_rsrc(ws + (size_t)blockIdx.x * 160, 160);
            ST16_SC1(sent4, rI, tid * 16);
            if (wid < 2) ST16_SC1(sent4, rI, 4096 + tid * 16);
            if (wid == 2) ST16_SC1(zero4, rZ, lane * 16);
        }
        if (wid == 3 && idx < 8) {
            const unsigned r = (((unsigned)__builtin_amdgcn_s_memrealtime() * 2654435761u + blockIdx.x * 40503u) >> 9) & 0x1fdf80u;
            const unsigned first = (idx & 1) << 13;
            const char* pb = ws + WS_PROBE;
            unsigned va, vb, left;
            asm volatile("s_waitcnt lgkmcnt(0)\n\ts_load_dword %0, %3, %4\n\ts_load_dword %1, %3, %5\n\ts_waitcnt lgkmcnt(1)\n\t"
                         "s_nop 15\n\ts_getreg_b32 %2, hwreg(HW_REG_IB_STS, 8, 4)\n\ts_waitcnt lgkmcnt(0)"
                         : "=&s"(va), "=&s"(vb), "=&s"(left) : "s"(pb), "s"(r | first), "s"(r | (first ^ 8192u)) : "memory");
            const unsigned nearbit = (left != 0u ? first : first ^ 8192u) >> 13;
            if (lane == 0) ctl1[5] = nearbit == (unsigned)(xcd >> 2) ? 1u : 2u;
        }
        f32x4 acc[4][4];
#pragma unroll
        for (int i = 0; i < 16; ++i) acc[i >> 2][i & 3] = (f32x4){0.f, 0.f, 0.f, 0.f};
        unsigned tc = (unsigned)(size_t)(tab + wp * 64 + g * 8), tw = (unsigned)(size_t)(tab + 256 + prow(wq * 64 + n));
        unsigned ta = (unsigned)(size_t)lds + wp * STAGE_BYTES + wq * 8192 + lane * 16;
        constexpr int NIL = 1 << 30;
        __amdgpu_buffer_rsrc_t rW = mk_rsrc(ws, 18u << 20);
        const int hpan = rt * 2 + wq;
        int sk = (int)WS_AT + at_panel(h * 8 + ct) + at_tile((rt * 2 + wp) * 128, (ct & 4) << 11) + wq * 16384 + lane * 16;
        int st = lane != 0 ? NIL : (int)WS_FLAG1 + (1 - wp) * 32768 + h * 16384 + (rt >> 3) * 8192 + ct * 1024 + (rt & 7) * 128 + wq * 4;
        int so = h != 0 ? Y_OFF + lane * 16 : g != (n >> 2) ? NIL
               : (int)WS_STATS + (hpan & 15) * 32768 + (ct >> 2) * 16384 + (hpan >> 4) * 8192 + ((ct & 3) * 2 + wp) * 1024 + n * 16;
        int sf = lane != 0 ? NIL : (int)WS_FLAGS + (rt * 8 + ct) * 128 + wid * 4;
        u32x4 sel[2], aw[2];
        unsigned one[2];
#pragma unroll
        for (int i = 0; i < 4; ++i) {
            one[i & 1] = g == (n >> 2) && (i & 1) == ((n >> 1) & 1) ? ((n & 1) ? 0x3F800000u : 0x00003F80u) : 0u;
            aw[0][i] = aw[1][i] = (n & 3) == 0 ? 0x3F803F80u : 0u;
        }
        if ((n & 3) == 1) {
            const bf16_t* wr = a.w_r + ct * BT + wp * 64 + g * 8;
            aw[0] = *(const u32x4*)wr; aw[1] = *(const u32x4*)(wr + 32);
        }
        unsigned ea[2];
#pragma unroll
        for (int mp = 0; mp < 2; ++mp)
            ea[mp] = (unsigned)(size_t)lds + (wq ? L_YB + 16384 : 3 * STAGE_BYTES) + n * 256 + (((wp * 8 + mp * 4 + g) ^ n) * 16);
        asm volatile("" : "+v"(tc), "+v"(tw), "+v"(ta), "+v"(sk), "+v"(st), "+v"(so), "+v"(sf), "+v"(one[0]), "+v"(one[1]), "+v"(ea[0]), "+v"(ea[1]));
        asm volatile("" : "+s"(rW));
        unsigned tg;
        bf16x8 e0[4], e1[4];
        auto extra = [&]() __attribute__((always_inline)) {
            bf16x8 ef[4][2];
#pragma unroll
            for (int i = 0; i < 8; ++i) ef[i >> 1][i & 1] = *(const LAS bf16x8*)((const LAS unsigned char*)(size_t)ea[i & 1] + (i >> 1) * 4096);
            SB();
#pragma unroll
            for (int i = 0; i < 16; ++i)
                acc[i & 3][i >> 2] = __builtin_amdgcn_mfma_f32_16x16x32_bf16(__builtin_bit_cast(bf16x8, sel[i & 1]), ef[i >> 2][(i >> 1) & 1], acc[i & 3][i >> 2], 0, 0, 0);
        };
        if (wid == 0) STAMP(0);
        compute_loop<false, true>(lds, acc, NT1, wp, wq, lane, [&]() { if (wid == 0) STAMP(24); WAIT_VM0(); if (wid == 0) STAMP(31); }, e0, e1, 0u);
        if (wid == 0) STAMP(25);
        {
            f32x4 rd[4], mr[4];
            float wsm[4];
#pragma unroll
            for (int m = 0; m < 4; ++m) {
                rd[m] = *(const LAS f32x4*)((const LAS float*)(size_t)tc + (m >> 1) * 32 + (m & 1) * 4);
                mr[m] = *(const LAS f32x4*)((const LAS float*)(size_t)tc + 128 + (m >> 1) * 32 + (m & 1) * 4);
                wsm[m] = *((const LAS float*)(size_t)tw + (m >> 1) * 32 + (m & 1) * 4);
            }
            const unsigned kv = ctl1[4];
            tg = ctl1[0];
            SB();
            sk += (int)kv; st += (int)kv; so += (int)kv; sf += (int)kv;
            kx = (unsigned)__builtin_amdgcn_readfirstlane((int)kv);
#pragma unroll
            for (int nn = 0; nn < 4; ++nn)
#pragma unroll
                for (int mp = 0; mp < 2; ++mp) {
                    const f32x4 u = acc[2 * mp][nn] * rd[2 * mp] - mr[2 * mp] * wsm[nn];
                    const f32x4 v = acc[2 * mp + 1][nn] * rd[2 * mp + 1] - mr[2 * mp + 1] * wsm[nn];
                    u32x4 w;
                    w[0] = pk_bf16(u[0], u[1]); w[1] = pk_bf16(u[2], u[3]);
                    w[2] = pk_bf16(v[0], v[1]); w[3] = pk_bf16(v[2], v[3]);
                    ST16_SC1(w, rW, sk + (nn * 2 + mp) * 1024);
                    *(LAS u32x4*)((LAS unsigned char*)(size_t)ta + (nn * 2 + mp) * 1024) = w;
                }
        }
        if (wid == 0) STAMP(26);
        WAIT_LGKM0();
        BARRIER();
        SB();
        {
#pragma unroll
            for (int i = 0; i < 16; ++i) { acc[i >> 2][i & 3] = (f32x4){0.f, 0.f, 0.f, 0.f}; asm volatile("" : "+v"(acc[i >> 2][i & 3])); }
            sel[0] = (u32x4){one[0], one[1], 0u, 0u}; sel[1] = (u32x4){0u, 0u, one[0], one[1]};
            asm volatile("" : "+v"(st), "+v"(so), "+v"(sf), "+v"(tg), "+v"(sel[0]), "+v"(sel[1]));
            SB();
            if (h != 0) extra();
            SB();
            WAIT_VM(0);
            __builtin_amdgcn_raw_buffer_store_b32(tg, rW, st, 0, 16);
            SB();
            if (wid == 0) STAMP(27);
        }
        compute_loop<true, false>(lds, acc, h == 0 ? 32 + KSHIFT : 32 - KSHIFT, wp, wq, lane, [&]() {
            if (wid == 0) { STAMP(h == 0 ? 1 : 19); CSTAMP(22); }
        }, e0, e1, (unsigned)((wp * 8192 + 2 * STAGE_BYTES) ^ (L_YB + wid * 8192)));
        P2_DEFS;
        if (wid == 0) { STAMP(h == 0 ? 2 : 12); CSTAMP(23); }
        u32x4 hq[4][2];
        auto cvt = [&](int nn, int j) __attribute__((always_inline)) {
            const f32x4 c = acc[j][nn];
            hq[nn][j >> 1][(j & 1) * 2] = pk_bf16(c[0], c[1]);
            hq[nn][j >> 1][(j & 1) * 2 + 1] = pk_bf16(c[2], c[3]);
        };
        int hs = h;
        asm volatile("" : "+s"(hs));
        if (__builtin_expect(hs == 0, 1)) {
            f32x4 d1[4], d2[4];
            u32x3 sv;
            auto add = [&](int nn, int j) __attribute__((always_inline)) {
                acc[j][nn] = __builtin_amdgcn_mfma_f32_16x16x32_bf16(__builtin_bit_cast(bf16x8, sel[j & 1]), j < 2 ? e0[nn] : e1[nn], acc[j][nn], 0, 0, 0);
            };
            auto sum = [&](int nn, int j) __attribute__((always_inline)) {
                const bf16x8 hf = __builtin_bit_cast(bf16x8, hq[nn][j >> 1]);
                const f32x4 z = {0.f, 0.f, 0.f, 0.f};
                if (j & 1) d1[nn] = __builtin_amdgcn_mfma_f32_16x16x32_bf16(__builtin_bit_cast(bf16x8, aw[j >> 1]), hf, j < 2 ? z : d1[nn], 0, 0, 0);
                else d2[nn] = __builtin_amdgcn_mfma_f32_16x16x32_bf16(hf, hf, j < 2 ? z : d2[nn], 0, 0, 0);
            };
            auto out = [&](int nn, int j) __attribute__((always_inline)) {
                if (j == 1) asm volatile("" : "+v"(d2[nn]));
                if (j == 2) asm volatile("" : "+v"(d1[nn]));
                if (j == 1) sv[1] = min(__float_as_uint(sel4(n, d2[nn][0], d2[nn][1], d2[nn][2], d2[nn][3])), SENT - 1u);
                if (j == 2) { sv[0] = min(__float_as_uint(d1[nn][0]), SENT - 1u); sv[2] = min(__float_as_uint(d1[nn][1]), SENT - 1u); }
                if (j == 3) __builtin_amdgcn_raw_buffer_store_b96(sv, rW, so + nn * 256, 0, 16);
            };
#define FOUR(m, v) _Pragma("unroll") for (int j = 0; j < 4; ++j) { m; SB(); v; SB(); }
            FOUR(add(0, j), (void)0) FOUR(add(1, j), cvt(0, j)) FOUR(sum(0, j), cvt(1, j)) FOUR(add(2, j), out(0, j))
            FOUR(sum(1, j), cvt(2, j)) FOUR(add(3, j), out(1, j)) FOUR(sum(2, j), cvt(3, j)) FOUR(sum(3, j), out(2, j))
            FOUR((void)0, out(3, j))
#undef FOUR
            if (wid == 0) STAMP(3);
#pragma unroll
            for (int nn = 0; nn < 4; ++nn)
#pragma unroll
                for (int mp = 0; mp < 2; ++mp)
                    *(LAS u32x4*)(lds + L_HT + (wq * 64 + nn * 16 + n) * 256 + (((wp * 8 + mp * 4 + g) ^ n) * 16)) = hq[nn][mp];
            WAIT_LGKM0();
            BARRIER();
            BARRIER();
            if (wid == 0) STAMP(6);
            const int crow = __builtin_amdgcn_readfirstlane((int)((volatile LAS unsigned*)(lds + L_ATAB))[2 + wq]) + wq * 64;
            const __amdgpu_buffer_rsrc_t rOut = mk_rsrc(a.out + (size_t)rt * BT * D_LEN, BT * D_LEN * 2);
#pragma unroll 2
            for (int j = 0; j < 8; ++j) {
                const int r = wq * 64 + wp * 32 + j * 4 + g;
                const float m_ = rstat[r * 2], d_ = rstat[r * 2 + 1];
                const u32x4 hh = *(const LAS u32x4*)(lds + L_HT + r * 256 + ((n ^ (r & 15)) * 16));
                u32x4 ov;
#pragma unroll
                for (int i = 0; i < 4; ++i) {
                    const unsigned u = hh[i];
                    const float h0 = bflo(u), h1 = bfhi(u);
                    const unsigned dd = pk_bf16(h0 - m_, h1 - m_);
                    const unsigned q = pk_bf16(bflo(dd) * d_, bfhi(dd) * d_);
                    ov[i] = pk_bf16(h0 + bflo(q), h1 + bfhi(q));
                }
                __builtin_amdgcn_raw_buffer_store_b128(ov, rOut, r == crow ? BT * D_LEN * 2 : (r * D_LEN + ct * BT + n * 8) * 2, 0, OUT_AUX);
            }
            FLUSH();
            return;
        }
#pragma unroll
        for (int i = 0; i < 8; ++i) {
            cvt(i >> 1, (i & 1) * 2); cvt(i >> 1, (i & 1) * 2 + 1);
            __builtin_amdgcn_raw_buffer_store_b128(hq[i >> 1][i & 1], rW, so, i * 1024, 16);
            SB();
        }
        WAIT_VM0();
        __builtin_amdgcn_raw_buffer_store_b32(1u, rW, sf, 0, 16);
        if (wid == 0) STAMP(13);
    } else {
        LAS unsigned char* l0 = lds + (wid - 4) * 4096;
        P2_DEFS;
        const __amdgpu_buffer_rsrc_t rD = mk_rsrc(h == 0 ? a.wd + (size_t)sidx * 32 * D_LEN : a.wg,
                                                  sidx >= 112 ? 0 : h != 0 ? D_LEN * F_LEN * 2 : wid < 8 - BQ ? 32 * D_LEN * 2 : 0);
        int vGe = h == 0 ? wd_off(wid & 3, 0, lane)
                : (((sidx & 7) * 128 + (wid & 3) * 16 + (lane >> 5)) * F_LEN + (sidx >> 3) * 256 + ((((lane & 31) >> 1) ^ (lane >> 5)) * 16) + (lane & 1) * 8) * 2;
        auto load_wd = [&](int i) __attribute__((always_inline)) {
            dma_nt(rD, (unsigned)(size_t)lds + (i < 8 ? L_WD0 : L_WD1) + (wid & 3) * 8192 + (i & 7) * 1024, vGe ^ ((2 * (i & 1) | (i & 4)) * 32), i * (4 * D_LEN));
        };
        const int lw = wid - 4;
        constexpr int NIL = 1 << 30;
        ring.rP = ring.rF = mk_rsrc(w2, (unsigned)WS_STATS);
        ring.voP[0] += (int)WS_AT + at_panel(h * 8 + ct);
        ring.voP[1] = ring.voP[2] = ring.voP[3] = ring.voP[0];
#define FLINE(k) (((k) >> 3) * 8192 + ((k) & 7) * 128)
        const int ft = (g & 1) * 32768 + (g >> 1) * 4 + (int)WS_FLAG1 + (lw == 0 ? ct * 1024 : NIL);
        int vall = FLINE(n) + ft + h * 16384;
        ring.voF = (n >> 2) == xq ? vall : NIL;
        ring.fl = lds + L_YB + lw * (h == 0 ? 8192 : 1024) + 4096 + 256;
        unsigned sa = (unsigned)(size_t)lds + L_YB + 4096 + 256 + lane * 4;
        const unsigned tg = (unsigned)__builtin_amdgcn_readfirstlane((int)ctl1[0]);
        float wsum = __uint_as_float(ctl1[1]);
        ring.lb = lw * 4096;
        int cq[3], cp[3];
        unsigned mk[3];
#pragma unroll
        for (int k = 0; k < 3; ++k) {
            const int nb = xq * 4 + NBR(k + 1);
            cq[k] = nb * 256; cp[k] = at_tile(cq[k], ring.par); mk[k] = 0x10000u << nb;
            asm volatile("" : "+s"(cq[k]), "+s"(cp[k]), "+s"(mk[k]));
        }
        unsigned m2 = (mk[0] | mk[1] | mk[2]) >> 16;
        asm volatile("" : "+s"(m2), "+s"(ring.rP));
        asm volatile("" : "+v"(vall), "+v"(ring.voF), "+v"(ring.voP[0]), "+v"(sa), "+v"(wsum));
        ring.rF = ring.rP;
        ring.voP[1] = ring.voP[2] = ring.voP[3] = ring.voP[0];
        WAIT_VM(4);
        BARRIER();
        SB();
        int ve = Y_OFF + lane * 16;
        auto fetch = [&]() __attribute__((always_inline)) {
            if (wid == 4) STAMP(21);
            const int v4[4] = {ve, ve, ve, ve};
#pragma unroll
            for (int k = 0; k < 2; ++k) dma_4k<true>(ring.rP, (unsigned)(size_t)lds + L_YB + lw * 8192 + k * 4096, v4, k * 4096);
        };
        __amdgpu_buffer_rsrc_t rQ2 = mk_rsrc(a.ci + (size_t)(rt * 4 + lw) * 32 * K, 32 * K * 2);
        int vC2 = (ct * 4 + g) * (K * 2) + ((xq * 1024 + 2048 + n * 128) & 4095);
        int vT2 = h == 0 && n < (KSHIFT + 1) / 2 ? FLINE((xq * 4 - 1 - n) & 15) + ft + 16384 : NIL;
        int vY = lane == 0 ? (int)WS_FLAGS + (rt * 8 + ct) * 128 + lw * 4 : NIL;
        int n0 = C2_START + idx % C2_STAG, n1 = (h == 0 ? 32 : NT) - 11 - n0;
        int c8 = (xq * 1024 + 1024) & 4095, p8 = at_tile(c8, ring.par);
        int cK = (xq * 1024 + (32 - KSHIFT) * 128) & 4095, pK = at_tile(cK, ring.par) + at_panel(8);
        auto early = [&](int st, int i0, int i1) __attribute__((always_inline)) {
#pragma unroll
            for (int i = i0; i < i1; ++i)
                dma_nt(rD, (unsigned)(size_t)lds + (st * 4 + lw) * 8192 + i * 1024, vGe ^ ((2 * (i & 1) | (i & 4)) * 32), i * (4 * F_LEN) + st * (128 * F_LEN));
        };
        asm volatile("" : "+v"(vGe));
        asm volatile("" : "+s"(rQ2), "+s"(n0), "+s"(n1), "+s"(c8), "+s"(p8), "+s"(cK), "+s"(pK));
        asm volatile("" : "+v"(ve), "+v"(vC2), "+v"(vT2), "+v"(vY));
        unsigned have = 0u;
        SB();
        if (T_DELAY) __builtin_amdgcn_s_sleep(T_DELAY);
#define GATE(m) do { if (__builtin_expect(((m) & ~have) != 0u, 0)) ring.gate(tg, sa, m, have); SB(); } while (0)
#define TILE(k, o, st) do { ring.sp = cp[k] + (o) * 256; ring.sq = cq[k] + (o); ring.template issueP<16>(l0 + (st) * STAGE_BYTES); \
                            ring.issueQ(l0 + (st) * STAGE_BYTES); ring.sample(); SB(); } while (0)
        ring.gate(tg, sa, mk[0], have);
        if (wid == 4) STAMP(28);
        ring.sp = cp[0]; ring.template issueP<16>(l0 + 2 * STAGE_BYTES); ring.sample();
        BARRIER();
        GATE(mk[1]);
        ring.sp = cp[1]; ring.template issueP<16>(l0 + 3 * STAGE_BYTES); ring.sample();
        WAIT_VM(6); BARRIER();
        if (wid == 4) STAMP(30);
        GATE(mk[2]);
        TILE(2, 0, 0);
        GATE(m2);
        ring.voF = vall;
        TILE(0, 128, 1);
        WAIT_VM(19); BARRIER();
        TILE(1, 128, 2);
        WAIT_VM(19); BARRIER();
        TILE(2, 128, 3);
        WAIT_VM(19); BARRIER();
#undef GATE
#undef TILE
        if (wid == 4) STAMP(29);
        if (have != 0xffffffffu) ring.gate(tg, sa, 0xffffffffu, have);
        ring.fl -= 256;
        ring.sq = c8; ring.sp = p8; ring.poff = 0;
        if (n0 > 0) ring.template steady<true, false>(n0);
        ring.voF = vC2;
        ring.rF = ring.rQ;
        ring.template issue<true>(lds);
        if (__builtin_expect(blockIdx.x == 255, 0) && tid == 256) {
            const u64 want = ((u64)ctl1[3] << 32) | ctl1[2];
            __hip_atomic_store((GAS u64*)(ws + WS_TAG), want, __ATOMIC_RELAXED, __HIP_MEMORY_SCOPE_AGENT);
        }
        ring.voF = vT2;
        ring.rF = ring.rP;
        ring.fl += 512;
        ring.sample();
        WAIT_VM(18); BARRIER();
        if (wid == 4) { CSTAMP(34); STAMP(36); }
        ring.template steady<true, false, 18>(2);
        ring.template steady<true, false>(n1);
        if (wid == 4) { CSTAMP(35); STAMP(37); }
        if (h == 0) {
            have = 0u;
            ring.gate(tg, sa + 256, ((1u << ((KSHIFT + 1) / 2)) - 1u) * 0x10001u, have);
            ring.voF = vY;
            ring.fl -= 512;
            ring.rQ = rQ2;
            ring.sq = cK; ring.sp = pK; ring.poff = at_panel(8);
            ring.template steady<true, true>(KSHIFT);
        }
        if (h != 0) {
            early(0, 0, G_N);
            wait_vm<8 + G_N>(); BARRIER();
            early(1, 0, G2_N);
            wait_vm<G_N + G2_N>(); BARRIER();
            early(1, G2_N, G2_N + G3_N);
        } else if (ring.seen()) {
            fetch();
            WAIT_VM(16); BARRIER();
            WAIT_VM(8); BARRIER();
        } else {
            ring.sample();
            WAIT_VM(10); BARRIER();
            const bool have = ring.seen();
            if (have) { fetch(); WAIT_VM(9); } else { ring.sample(); WAIT_VM(2); }
            BARRIER();
            if (!have) {
#pragma unroll 1
                for (int spin = 0; spin <= SPIN_CAP && !ring.seen(); ++spin) { ring.sample(); WAIT_VM(0); }
                fetch();
            }
        }
        if (wid == 4 && h == 0) STAMP(20);
        if (h == 0) WAIT_VM0();
        BARRIER();
        if (h == 0) {
            const int slot = sidx;
            LAS unsigned char* abuf = lds + L_ATAB + 2048 + lw * 128;
#pragma unroll
            for (int i = 0; i < WD_EARLY; ++i) load_wd(i);
            SB();
            __amdgpu_buffer_rsrc_t rS = mk_rsrc(w2 + WS_STATS + (hp_ & 15) * 32768 + (hp_ >> 4) * 8192, 24576);
            __amdgpu_buffer_rsrc_t rC = mk_rsrc(w2 + WS_CAND + (size_t)hp_ * 4096, 4096);
            __amdgpu_buffer_rsrc_t rKy = mk_rsrc(w2 + WS_KEYS + (hp_ >> 2) * KEY_S + (hp_ & 3) * 4, ct == 0 ? 8192 + 3 * KEY_S * 16 + 4 : 0);
            int vs = lane * 16, vk = !(lane & 1) ? 1 << 20 : (lane >> 2) * 8192 + (lane & 3) * (KEY_S * 16);
            int vc = (lane < 32 ? 2048 : 0) + ct * 256 + (lane & 31) * 8;
            unsigned code = 0xffffu - (unsigned)(hp_ * 64 + lane), hb = (lane & 31) * 8;
            unsigned ra = (unsigned)(size_t)(rstat + (wq * 64 + lane) * 2), ht = (unsigned)(size_t)lds + L_HT + wq * 16384;
            u64 lo = 0xFFFFFFFFull;
            asm volatile("" : "+s"(rS), "+s"(rC), "+s"(rKy), "+s"(ht), "+s"(lo));
            asm volatile("" : "+v"(vs), "+v"(vk), "+v"(vc), "+v"(code), "+v"(hb), "+v"(ra));
            SB();
            BARRIER();
            __builtin_amdgcn_s_setprio(TAIL_PRIO);
            if (lane < 16) *(LAS unsigned*)(abuf + 64 + lane * 4) = 0u;
            __builtin_amdgcn_s_sleep(STAT_DELAY);
            if (wp == 0) {
                float s1, s2, s3;
#pragma unroll 1
                for (int spin = 0;; ++spin) {
                    asm volatile("" ::: "memory");
                    u32x3 sv[16];
#pragma unroll
                    for (int j = 0; j < 16; ++j) { sv[j] = __builtin_amdgcn_raw_buffer_load_b96(rS, vs, (j >> 3) * 16384 + (j & 7) * 1024, 16); SB(); }
#pragma unroll
                    for (int j = 0; j < 16; ++j) {
                        const unsigned u0 = sv[j][0], u1 = sv[j][1], u2 = sv[j][2];
                        s1 = j ? s1 + __uint_as_float(u0) : __uint_as_float(u0);
                        s2 = j ? s2 + __uint_as_float(u1) : __uint_as_float(u1);
                        s3 = j ? s3 + __uint_as_float(u2) : __uint_as_float(u2);
                    }
                    float z = s1 + s3;
                    asm volatile("" : "+v"(z));
                    if (__builtin_expect(__builtin_amdgcn_ballot_w64(__builtin_isunordered(z, s2)) == 0, 1)) break;
                    unsigned mx = 0u;
#pragma unroll
                    for (int j = 0; j < 16; ++j) mx = max(max(mx, sv[j][0]), max(sv[j][1], sv[j][2]));
                    if (__all(mx != SENT) || spin > SPIN_CAP) break;
                }
                if (wid == 4) STAMP(4);
                const float mean = s1 * (1.0f / D_LEN);
                const float var = fmaxf(s2 * (1.0f / D_LEN) - mean * mean, 0.f);
                const float mb = rbf(mean), rden = __builtin_amdgcn_rcpf(rbf(__builtin_amdgcn_sqrtf(rbf(rbf(var) + 1e-5f))));
                const unsigned kb = wave_max_s(r2key((s3 - mb * wsum) * rden, code));
                __builtin_amdgcn_raw_buffer_store_b32(kb, rKy, vk, 0, 16);
                SB();
                {
                    const unsigned cr_ = ~kb & 63u;
                    u32x2 c = *(const LAS u32x2*)(size_t)(ht + cr_ * 256 + (hb ^ ((cr_ & 15) * 16)));
                    const float m_ = __int_as_float(__builtin_amdgcn_readlane(__float_as_int(mb), cr_));
                    const float d_ = __int_as_float(__builtin_amdgcn_readlane(__float_as_int(rden), cr_));
#pragma unroll
                    for (int i = 0; i < 2; ++i) {
                        const unsigned u = c[i];
                        const unsigned dd = pk_bf16(bflo(u) - m_, bfhi(u) - m_);
                        const unsigned q = pk_bf16(bflo(dd) * d_, bfhi(dd) * d_);
                        c[i] = min(__float_as_uint(pick(lo, __uint_as_float(u), __uint_as_float(q))), SENT - 1u);
                    }
                    __builtin_amdgcn_raw_buffer_store_b64(c, rC, vc, 0, 16);
                    SB();
                    *(LAS u32x2*)(size_t)ra = (u32x2){__float_as_uint(mb), __float_as_uint(rden)};
                    if (lane == 0) ctl[2 + wq] = cr_;
                }
                WAIT_LGKM0();
                if (wid == 4) STAMP(5);
            }
            BARRIER();
#pragma unroll
            for (int i = WD_EARLY; i < 16; ++i) load_wd(i);
            if (slot < 112 && wid < 8 - BQ) {
                WAIT_VM0();
                if (wid == 4) STAMP(7);
                stage2(w2, slot, lw, lds + L_WD0 + lw * 8192, lds + L_WD1 + lw * 8192, abuf, 64, lane);
                if (wid == 4) STAMP(11);
            }
            FLUSH();
            return;
        }
    }

    P2_DEFS;
    LAS float* red = (LAS float*)(lds + L_RED);
    if (sidx < 112) {
        const int slot = sidx, grp = slot >> 3, kq = slot & 7;
        const __amdgpu_buffer_rsrc_t rG = mk_rsrc(a.wg, D_LEN * F_LEN * 2), rU = mk_rsrc(a.wu, D_LEN * F_LEN * 2);
        auto pair = [&](int i) {
            const int d = wid * 16 + i * 2 + (lane >> 5), ky = (d & 3) | ((d >> 1) & 4), c = lane & 31;
            const int vgu = ((kq * 128 + d) * F_LEN + grp * 256 + (((c >> 1) ^ ky) * 16) + (c & 1) * 8) * 2;
            if (i >= (wid < 4 ? G_N : G2_N + G3_N)) dma_nt(rG, (unsigned)(size_t)lds + wid * 8192 + i * 1024, vgu, 0);
            dma_nt(rU, (unsigned)(size_t)lds + 65536 + wid * 8192 + i * 1024, vgu ^ 512, 0);
        };
        if (wid >= 4) {
            if (W_PRE) __builtin_amdgcn_s_sleep(W_PRE_SL);
#pragma unroll
            for (int i = 0; i < W_PRE; ++i) pair(i);
        }
        BARRIER();
        if (wid < 4) {
#pragma unroll
            for (int i = 0; i < W_PRE; ++i) pair(i);
        }
#pragma unroll
        for (int i = W_PRE; i < 8; ++i) pair(i);
        if (wid >= 8 - BQ) {
            const __amdgpu_buffer_rsrc_t rD = mk_rsrc(a.wd + (size_t)slot * 32 * D_LEN, 32 * D_LEN * 2);
            const int vd = wd_off(wid - 4, 0, lane);
#pragma unroll
            for (int i = 0; i < 16; ++i)
                dma_nt(rD, (unsigned)(size_t)lds + L_YB + (wid & 1) * 16384 + i * 1024, vd ^ ((2 * (i & 1) | (i & 4)) * 32), i * (4 * D_LEN));
        }
        const __amdgpu_buffer_rsrc_t rK = mk_rsrc(w2, wid == 0 ? (unsigned)(WS_CAND + 32 * 4096) : 0u);
        const int ok_ = lane < 40 ? (int)WS_KEYS + (xcd >> 2) * 8192 + (xcd & 3) * (KEY_S * 16) + (lane - 32) * KEY_S : (int)(WS_CAND + 32 * 4096);
        const int oc = (int)WS_CAND + 2048 + kq * 256 + n * 16;
        int best = 0;
        u32x4 v = {0u, 0u, 0u, 0u};
        auto request = [&]() {
            asm volatile("" ::: "memory");
            v = LD16_SC1(rK, lane < 32 ? oc + best * 4096 : ok_);
            SB();
        };
        auto settled = [&]() {
            const unsigned mx = umax4(v), mn = min(min(v[0], v[1]), min(v[2], v[3]));
            unsigned fv = lane < 32 ? ~mx : mn | (unsigned)(lane >> 3 ^ 4);
            asm volatile("" : "+v"(fv));
            const bool fine = fv != 0u;
            unsigned km = max(mx, dppu<0xB1>(mx));
            km = max(km, dppu<0x4E>(km));
            km = max(km, dppu<0x141>(km));
            const int nb = (int)(((0xffffu - ((unsigned)__builtin_amdgcn_readlane((int)km, 32) & 0xffffu)) >> 6) & 31u);
            const bool same = nb == best;
            best = nb;
            return __all(fine) && same;
        };
        if (wid == 0) {
            request();
            WAIT_VM(0);
            settled();
            request();
        } else if (wid >= 8 - BQ) WAIT_VM(16);
        else WAIT_VM(0);
        BARRIER();
        if (wid == 0) STAMP(8);
        bf16x8 wfr[4][4];
        {
            const int q = (lane >> 2) & 3, ky = q | ((g & 1) << 2);
#pragma unroll
            for (int u = 0; u < 4; ++u) {
                const int tro = (u >> 1) * 65536 + (8 * g + q) * 512 + (((wid * 2 + (u & 1)) ^ ky) * 32) + (lane & 3) * 8;
#pragma unroll
                for (int ks = 0; ks < 4; ++ks) {
                    const bf16x4 w0 = __builtin_amdgcn_ds_read_tr16_b64_v4i16((LAS bf16x4*)(lds + tro + ks * 16384));
                    const bf16x4 w1 = __builtin_amdgcn_ds_read_tr16_b64_v4i16((LAS bf16x4*)(lds + tro + ks * 16384 + 2048));
                    wfr[u][ks] = __builtin_shufflevector(w0, w1, 0, 1, 2, 3, 4, 5, 6, 7);
                }
            }
            SB();
#pragma unroll
            for (int i = 0; i < 16; ++i) asm volatile("" : "+v"(wfr[i >> 2][i & 3]));
        }
        if (wid >= 8 - BQ && lane < 16) *(LAS unsigned*)(lds + 8192 + wid * 64 + lane * 4) = 0u;
        __amdgpu_buffer_rsrc_t rPo = mk_rsrc(w2 + WS_PRE, 112 * 2048);
        int po = (n & 3) == 0 ? pre_off((grp ^ (g >> 1)) * 8 + wid) + kq * 256 + g * 64 + n * 4 : 112 * 2048;
        unsigned xw = (unsigned)(size_t)lds + (n >> 3) * 16384 + (g & 1) * 8192 + (n & 7) * 8, xr = (unsigned)(size_t)lds + (n & 1) * 8192 + g * 16;
        unsigned ix = (n & 1) ? 0xEEEEEEEEu : 0x44444444u;
        asm volatile("" : "+v"(po), "+v"(xw), "+v"(xr), "+v"(ix));
        u64 m1 = 0xFFFF0000FFFF0000ull, m2 = 0xFFFFFFFF00000000ull;
        f32x4 ac[4];
#pragma unroll
        for (int u = 0; u < 4; ++u) { ac[u] = (f32x4){0.f, 0.f, 0.f, 0.f}; asm volatile("" : "+v"(ac[u])); }
        asm volatile("" : "+s"(rPo), "+s"(m1), "+s"(m2));
        if (wid == 0) {
#pragma unroll 1
            for (int spin = 0; spin <= SPIN_CAP && !settled(); ++spin) {
                __builtin_amdgcn_s_sleep(1);
                request();
            }
            __builtin_amdgcn_s_waitcnt(0x0F70);
            if (lane < 32) *(LAS u32x2*)(size_t)xw = (u32x2){lane < 16 ? v[0] : v[1], lane < 16 ? v[2] : v[3]};
            WAIT_LGKM0();
            STAMP(9);
        }
        BARRIER();
        {
            bf16x8 xf[2];
#pragma unroll
            for (int j = 0; j < 2; ++j) xf[j] = *(const LAS bf16x8*)((const LAS unsigned char*)(size_t)xr + j * 16384);
            SB();
            float rs[3];
#pragma unroll
            for (int i = 0; i < 8; ++i) {
                const int u = i >> 1, j = i & 1;
                ac[u] = smf(xf[j], wfr[u][2 * j], wfr[u][2 * j + 1], ac[u], ix);
                SB();
                if (i == 4) { asm volatile("" : "+v"(ac[0])); rs[0] = ac[0][0] + ac[0][1]; asm volatile("" : "+v"(rs[0])); }
                if (i == 6) { asm volatile("" : "+v"(ac[1])); rs[0] = pick(m1, rs[0], ac[1][0] + ac[1][1]); asm volatile("" : "+v"(rs[0])); }
                if (i == 7) { asm volatile("" : "+v"(ac[2])); rs[2] = ac[2][0] + ac[2][1]; asm volatile("" : "+v"(rs[2])); }
                SB();
            }
            const unsigned e = min(__float_as_uint(pick(m2, rs[0], pick(m1, rs[2], ac[3][0] + ac[3][1]))), SENT - 1u);
            const u32x4 sv = {e, dppu<0x55>(e), dppu<0xAA>(e), dppu<0xFF>(e)};
            ST16_SC1(sv, rPo, po);
            SB();
            asm volatile("" :: "v"(v));
        }
        if (wid == 0) STAMP(14);
        if (wid >= 8 - BQ) {
            WAIT_VM(1);
            if (wid == 7) STAMP(15);
            stage2(w2, slot, wid - 4, lds + L_YB + (wid & 1) * 16384, lds + L_YB + (wid & 1) * 16384 + 8192, lds + wid * 64, 8192, lane);
            if (wid == 7) STAMP(10);
        }
    } else {
        const int rj = sidx - 112;
        if (wid == 4) {
            unsigned kk;
            const unsigned k = poll_winner(w2, xcd, lane, kk);
            if (lane == 0) ctl[4] = k;
            if (lane < 32) ctl[8 + lane] = kk;
            WAIT_LGKM0();
        }
        BARRIER();
        const unsigned kw = CTL(4);
        if (wid == 0) STAMP(16);
        {
            const unsigned kc = ctl[8 + wid * 4 + g];
            const __amdgpu_buffer_rsrc_t rL = mk_rsrc(w2 + WS_CAND + (size_t)wid * 16384, 16384);
            u32x2 hv2, nv2;
            for (int spin = 0;; ++spin) {
                asm volatile("" ::: "memory");
                hv2 = __builtin_amdgcn_raw_buffer_load_b64(rL, g * 4096 + (rj * 64 + n * 4) * 2, 0, 16);
                nv2 = __builtin_amdgcn_raw_buffer_load_b64(rL, g * 4096 + 2048 + (rj * 64 + n * 4) * 2, 0, 16);
                if (__all(max(max(hv2[0], hv2[1]), max(nv2[0], nv2[1])) != SENT) || spin > SPIN_CAP) break;
                __builtin_amdgcn_s_sleep(1);
            }
            u32x2 ov;
#pragma unroll
            for (int i = 0; i < 2; ++i) {
                const unsigned u = hv2[i], q = nv2[i];
                ov[i] = pk_bf16(bflo(u) + bflo(q), bfhi(u) + bfhi(q));
            }
            const int crow = (int)(0xffffu - (kc & 0xffffu)) & (S_LEN - 1);
            if (kc != kw) *(u32x2*)(a.out + (size_t)crow * D_LEN + rj * 64 + n * 4) = ov;
        }
        const int wrow = (int)(0xffffu - (kw & 0xffffu)) & (S_LEN - 1);
        const __amdgpu_buffer_rsrc_t rC = mk_rsrc(w2 + WS_CAND + (size_t)(wrow >> 6) * 4096, 4096);
        const __amdgpu_buffer_rsrc_t rPt = mk_rsrc(w2 + WS_PART, 112 * 4096);
        const int ocol = (rj >> 2) * 256 + ((lane >> 1) & 1) * 128 + (rj & 3) * 32 + (lane & 1) * 16 + (lane >> 2);
        unsigned hv, hnv;
        for (int spin = 0;; ++spin) {
            asm volatile("" ::: "memory");
            hv = __builtin_amdgcn_raw_buffer_load_b32(rC, (ocol & ~1) * 2, 0, 16);
            hnv = __builtin_amdgcn_raw_buffer_load_b32(rC, 2048 + (ocol & ~1) * 2, 0, 16);
            if (__all(max(hv, hnv) != SENT) || spin > SPIN_CAP) break;
        }
        float sgm = rbf(1.0f / (1.0f + __expf(-key2r(kw))));
        float hf = (ocol & 1) ? bfhi(hv) : bflo(hv), hnf = (ocol & 1) ? bfhi(hnv) : bflo(hnv);
        GAS unsigned short* optr = (GAS unsigned short*)(a.out + (size_t)wrow * D_LEN + ocol);
        unsigned rw = (unsigned)(size_t)red + (wid >> 2) * 1024 + (n * 4 + g) * 16 + (wid & 3) * 4, rr = (unsigned)(size_t)red + lane * 16;
        int po = wid * 16384 + (rj >> 3) * 8192 + g * 2048 + ((rj & 7) * 64 + n * 4) * 4;
        int po3 = po + 3 * 131072;
        asm volatile("" : "+v"(sgm), "+v"(optr), "+v"(hf), "+v"(hnf), "+v"(rw), "+v"(rr), "+v"(po), "+v"(po3));
        float sm;
#pragma unroll 1
        for (int spin = 0;; ++spin) {
            asm volatile("" ::: "memory");
            u32x4 pv[4];
#pragma unroll
            for (int i = 0; i < 4; ++i) pv[i] = __builtin_amdgcn_raw_buffer_load_b128(rPt, i < 3 ? po : po3, i < 3 ? i * 131072 : 0, 16);
            SB();
            unsigned t[4];
#pragma unroll
            for (int j = 0; j < 4; ++j) {
                const unsigned u0 = pv[0][j], u1 = pv[1][j], u2 = pv[2][j], u3 = pv[3][j];
                t[j] = __float_as_uint(((__uint_as_float(u0) + __uint_as_float(u1)) + __uint_as_float(u2)) + __uint_as_float(u3));
            }
            const auto r0 = __builtin_amdgcn_permlane32_swap(t[0], t[2], false, false), r1 = __builtin_amdgcn_permlane32_swap(t[1], t[3], false, false);
            const unsigned a0 = r0[0], a1 = r0[1], b0 = r1[0], b1 = r1[1];
            const auto r2 = __builtin_amdgcn_permlane16_swap(__float_as_uint(__uint_as_float(a0) + __uint_as_float(a1)),
                                                             __float_as_uint(__uint_as_float(b0) + __uint_as_float(b1)), false, false);
            const unsigned c0 = r2[0], c1 = r2[1];
            sm = __uint_as_float(c0) + __uint_as_float(c1);
            if (__builtin_expect(__builtin_amdgcn_ballot_w64(sm != sm) == 0, 1)) break;
            if (__all(max(max(umax4(pv[0]), umax4(pv[1])), max(umax4(pv[2]), umax4(pv[3]))) != SENT) || spin > SPIN_CAP) break;
            if (FIN_SLEEP) __builtin_amdgcn_s_sleep(FIN_SLEEP);
        }
        if (wid == 0) STAMP(17);
        *(LAS float*)(size_t)rw = sm;
        WAIT_LGKM0();
        BARRIER();
        if (wid == 0) {
            const f32x4 u = *(const LAS f32x4*)(size_t)rr, v = *(const LAS f32x4*)((const LAS unsigned char*)(size_t)rr + 1024);
            const f32x4 w = u + v;
            const float t = (w[0] + w[2]) + (w[1] + w[3]);
            *optr = (unsigned short)pk_bf16(hf + rbf(hnf + rbf(rbf(t) * sgm)), 0.f);
            STAMP(18);
        }
        __builtin_amdgcn_s_waitcnt(0x0F70);
    }
    FLUSH();
}

extern "C" void kernel_launch(void* const* d_in, const int* in_sizes, int n_in, void* d_out, int out_size, void* d_ws,
                              size_t ws_size, hipStream_t stream) {
    Args a;
    a.x = (const bf16_t*)d_in[0];
    a.cr = (const bf16_t*)d_in[1];
    a.ci = (const bf16_t*)d_in[2];
    a.wre = (const bf16_t*)d_in[3];
    a.wim = (const bf16_t*)d_in[4];
    a.w_r = (const bf16_t*)d_in[6];
    a.wg = (const bf16_t*)d_in[7];
    a.wu = (const bf16_t*)d_in[8];
    a.wd = (const bf16_t*)d_in[9];
    a.out = (bf16_t*)d_out;
    a.ws = (char*)d_ws + (WS_END ? ((ws_size - ((size_t)24 << 20)) & ~(size_t)255) : 0);
    (void)hipFuncSetAttribute((const void*)fused_kernel, hipFuncAttributeMaxDynamicSharedMemorySize, LDS_BYTES);
    fused_kernel<<<256, 768, LDS_BYTES, stream>>>(a);
}
```

```cpp
#include <hip/hip_runtime.h>
#include <hip/hip_bf16.h>
#include <stdint.h>

typedef unsigned short bf16_t;
typedef short bf16x8 __attribute__((ext_vector_type(8)));
typedef short bf16x4 __attribute__((ext_vector_type(4)));
typedef float f32x4 __attribute__((ext_vector_type(4)));
typedef unsigned u32x4 __attribute__((ext_vector_type(4)));
typedef unsigned u32x2 __attribute__((ext_vector_type(2)));
typedef unsigned u32x3 __attribute__((ext_vector_type(3)));
typedef unsigned long long u64;
#define LAS __attribute__((address_space(3)))
#define GAS __attribute__((address_space(1)))

constexpr int S_LEN = 2048, D_LEN = 1024, F_LEN = 3584;
constexpr unsigned SENT = 0xFFFFFFFFu;
constexpr int SPIN_CAP = 1 << 17;

constexpr size_t WS_KEYS = 0;
constexpr size_t WS_FLAGS = 16384;
constexpr size_t WS_XRD = 65536;
constexpr size_t WS_FLAG1 = 196608;
constexpr size_t WS_TAG = 131072;
constexpr size_t WS_VOTE = WS_TAG + 4096;
constexpr size_t WS_PROBE = (size_t)20 << 20;
constexpr size_t WS_AT = (size_t)4 << 20;
constexpr size_t WS_Y = (size_t)12 << 20;
constexpr size_t WS_STATS = (size_t)16 << 20;
constexpr size_t WS_PART = WS_STATS + (512u << 10);
constexpr size_t WS_CAND = WS_STATS + (1024u << 10);
constexpr size_t WS_PRE = WS_STATS + (1152u << 10);
constexpr size_t FILL_BYTES = (size_t)1408 << 10;

__device__ __forceinline__ float bflo(unsigned v) { return __uint_as_float(v << 16); }
__device__ __forceinline__ float bfhi(unsigned v) { return __uint_as_float(v & 0xffff0000u); }
__device__ __forceinline__ unsigned pk_bf16(float lo, float hi) {
    typedef __bf16 bf2 __attribute__((ext_vector_type(2)));
    typedef float f2 __attribute__((ext_vector_type(2)));
    f2 v = {lo, hi};
    bf2 r = __builtin_convertvector(v, bf2);
    return __builtin_bit_cast(unsigned, r);
}
__device__ __forceinline__ float rbf(float x) { return __uint_as_float(pk_bf16(0.f, x)); }

template <int CTRL> __device__ __forceinline__ unsigned dppu(unsigned v) {
    return (unsigned)__builtin_amdgcn_update_dpp(0, (int)v, CTRL, 0xf, 0xf, true);
}
template <int CTRL> __device__ __forceinline__ float dppf(float v) { return __uint_as_float(dppu<CTRL>(__float_as_uint(v))); }
__device__ __forceinline__ void swap16(unsigned v, unsigned& a, unsigned& b) {
    auto r = __builtin_amdgcn_permlane16_swap(v, v, false, false);
    a = r[0]; b = r[1];
}
__device__ __forceinline__ void swap32(unsigned v, unsigned& a, unsigned& b) {
    auto r = __builtin_amdgcn_permlane32_swap(v, v, false, false);
    a = r[0]; b = r[1];
}
__device__ __forceinline__ float sum_rows(float v) {
    unsigned a, b;
    swap16(__float_as_uint(v), a, b); v = __uint_as_float(a) + __uint_as_float(b);
    swap32(__float_as_uint(v), a, b); v = __uint_as_float(a) + __uint_as_float(b);
    return v;
}
__device__ __forceinline__ float wave_sum(float v) {
    v += dppf<0xB1>(v);
    v += dppf<0x4E>(v);
    v += dppf<0x141>(v);
    v += dppf<0x140>(v);
    return sum_rows(v);
}
__device__ __forceinline__ unsigned wave_max_u32(unsigned v) {
    v = max(v, dppu<0xB1>(v));
    v = max(v, dppu<0x4E>(v));
    v = max(v, dppu<0x141>(v));
    v = max(v, dppu<0x140>(v));
    unsigned a, b;
    swap16(v, a, b); v = max(a, b);
    swap32(v, a, b); v = max(a, b);
    return v;
}
__device__ __forceinline__ unsigned wave_max_s(unsigned v) {
    v = max(v, dppu<0xB1>(v));
    v = max(v, dppu<0x4E>(v));
    v = max(v, dppu<0x141>(v));
    v = max(v, dppu<0x140>(v));
    v = max(v, dppu<0x142>(v));
    v = max(v, dppu<0x143>(v));
    return (unsigned)__builtin_amdgcn_readlane((int)v, 63);
}
__device__ __forceinline__ float sel4(int g, float a, float b, float c, float d) {
    const float t0 = (g & 1) ? b : a, t1 = (g & 1) ? d : c;
    return (g & 2) ? t1 : t0;
}
__device__ __forceinline__ float pick(u64 m, float a, float b) {
    float r;
    asm("v_cndmask_b32 %0, %1, %2, %3" : "=v"(r) : "v"(a), "v"(b), "s"(m));
    return r;
}
__device__ __forceinline__ unsigned umax4(u32x4 v) { return max(max(v[0], v[1]), max(v[2], v[3])); }

__device__ __forceinline__ void touch_line(const void* p, unsigned lds_dst) {
    unsigned keep;
    asm volatile("s_mov_b32 %0, m0\n\ts_mov_b32 m0, %2\n\ts_nop 0\n\tglobal_load_lds_dword %1, off\n\ts_mov_b32 m0, %0"
                 : "=&s"(keep) : "v"(p), "s"(lds_dst) : "memory");
}
__device__ __forceinline__ __amdgpu_buffer_rsrc_t mk_rsrc(const void* p, unsigned bytes) {
    return __builtin_amdgcn_make_buffer_rsrc((void*)p, 0, (int)bytes, 0x00020000);
}
#define LDS_LOAD(w, v, o, pol) "buffer_load_" w " " v ", %0, %2 offen" o pol " lds"
#define LDS_M0 "s_mov_b32 m0, %1\n\ts_nop 0\n\t"
__device__ __forceinline__ void dma_nt(__amdgpu_buffer_rsrc_t r, unsigned dst, int voff, int soff) {
    asm volatile(LDS_M0 LDS_LOAD("dwordx4", "%3", "", " nt") :: "s"(r), "s"(dst), "s"(soff), "v"(voff) : "memory", "m0");
}
template <bool SC1> __device__ __forceinline__ void dma_word(__amdgpu_buffer_rsrc_t r, unsigned dst, int voff) {
    if constexpr (SC1) asm volatile(LDS_M0 LDS_LOAD("dword", "%3", "", " sc1") :: "s"(r), "s"(dst), "n"(0), "v"(voff) : "memory", "m0");
    else asm volatile(LDS_M0 LDS_LOAD("dword", "%3", "", "") :: "s"(r), "s"(dst), "n"(0), "v"(voff) : "memory", "m0");
}
template <bool SC1> __device__ __forceinline__ void dma_2k(__amdgpu_buffer_rsrc_t r, unsigned dst, int v0, int v1, int soff) {
    if constexpr (SC1) asm volatile(LDS_M0 LDS_LOAD("dwordx4", "%3", "", " sc1") "\n\t" LDS_LOAD("dwordx4", "%4", " offset:1024", " sc1")
                 :: "s"(r), "s"(dst), "s"(soff), "v"(v0), "v"(v1) : "memory", "m0");
    else asm volatile(LDS_M0 LDS_LOAD("dwordx4", "%3", "", "") "\n\t" LDS_LOAD("dwordx4", "%4", " offset:1024", "")
                 :: "s"(r), "s"(dst), "s"(soff), "v"(v0), "v"(v1) : "memory", "m0");
}
template <bool SC1> __device__ __forceinline__ void dma_4k(__amdgpu_buffer_rsrc_t r, unsigned dst, const int (&v)[4], int soff) {
#define FOUR(pol) asm volatile(LDS_M0 LDS_LOAD("dwordx4", "%3", "", pol) "\n\t" LDS_LOAD("dwordx4", "%4", " offset:1024", pol) "\n\t" \
        LDS_LOAD("dwordx4", "%5", " offset:2048", pol) "\n\t" LDS_LOAD("dwordx4", "%6", " offset:3072", pol) \
        :: "s"(r), "s"(dst), "s"(soff), "v"(v[0]), "v"(v[1]), "v"(v[2]), "v"(v[3]) : "memory", "m0")
    if constexpr (SC1) FOUR(" sc1"); else FOUR("");
#undef FOUR
}
#define CODE_BYTES 25728
__device__ __forceinline__ __amdgpu_buffer_rsrc_t code_rsrc() {
    unsigned long long p; unsigned d;
    asm volatile("s_getpc_b64 %0\n1:\n\ts_mov_b32 %1, 1b-_Z12fused_kernel4Args" : "=s"(p), "=s"(d));
    return __builtin_amdgcn_make_buffer_rsrc((void*)(p - d), 0, CODE_BYTES, 0x00020000);
}
#define LD16_SC1(rs, off) __builtin_amdgcn_raw_buffer_load_b128(rs, off, 0, 16)
#define ST16_SC1(v, rs, off) __builtin_amdgcn_raw_buffer_store_b128(v, rs, off, 0, 16)

constexpr int BT = 128, BK = 64, NSTAGE = 4;
constexpr int TILE_BYTES = BT * BK * 2;
constexpr int STAGE_BYTES = 2 * TILE_BYTES;
constexpr int RING_BYTES = NSTAGE * STAGE_BYTES;
constexpr int L_DUMMY = RING_BYTES;
constexpr int L_CTL = RING_BYTES + 256;
constexpr int L_RED = RING_BYTES + 512;

#define WAIT_VM(n) asm volatile("s_waitcnt vmcnt(" #n ")" ::: "memory")
#define WAIT_VM0() do { asm volatile("" ::: "memory"); __builtin_amdgcn_s_waitcnt(0x0F70); asm volatile("s_waitcnt vmcnt(0)" ::: "memory"); } while (0)
#define WAIT_LGKM0() asm volatile("s_waitcnt lgkmcnt(0)" ::: "memory")
#define BARRIER() do { asm volatile("" ::: "memory"); __builtin_amdgcn_s_barrier(); asm volatile("" ::: "memory"); } while (0)

__device__ __forceinline__ int prow(int rho) {
    const int w = rho >> 6, m = (rho >> 4) & 3, q = (rho >> 2) & 3, r = rho & 3;
    return w * 64 + (m >> 1) * 32 + q * 8 + (m & 1) * 4 + r;
}
__device__ __forceinline__ unsigned r2key(float r, unsigned code) {
    const unsigned u = pk_bf16(0.f, r);
    return ((u ^ ((unsigned)((int)u >> 31) | 0x80000000u)) & 0xffff0000u) | code;
}
__device__ __forceinline__ float key2r(unsigned k) {
    const unsigned o = k >> 16;
    return bflo((o & 0x8000u) ? (o & 0x7fffu) : (~o & 0xffffu));
}
#define CK_T -1, -1, -1, -1, 2, 2, 2, 2
#define STAT_M 5
#define W_PRE 2
#define W_PRE_SL 20
#define BQ 2
#define POLL_T (STAT_M + 5)
#define AUX_SLEEP 12
#define WD_EARLY 2
#define C2_START 0
#define C2_STAG 7
#define TAIL_PRIO 3
#define STAT_DELAY 12
#define CODE_WG 1
#define AUX_STAY 1
#define T_DELAY 0
#define WS_END 1
#define CATCH1 4
#define CPF_MASK1 0x1080u
#define CPF_MASK2 0x1080u
#define CPF_STAG 5
#define CPF_FIX 0x1000u
#define OUT_AUX 0
#define KEY_S 128
#define G2_N 2
#define G3_N 2
#define XMODE 1
#define FIN_SLEEP 1
#define G_N 8
#define POLL_SLEEP 4
__device__ __forceinline__ f32x4 smf(bf16x8 a, bf16x8 b0, bf16x8 b1, f32x4 d, unsigned idx) {
    typedef __bf16 bf8 __attribute__((ext_vector_type(8)));
    typedef __bf16 bf16v __attribute__((ext_vector_type(16)));
    const auto b = __builtin_shufflevector(b0, b1, 0, 1, 2, 3, 4, 5, 6, 7, 8, 9, 10, 11, 12, 13, 14, 15);
    return __builtin_amdgcn_smfmac_f32_16x16x64_bf16(__builtin_bit_cast(bf8, a), __builtin_bit_cast(bf16v, b), d, (int)idx, 0, 0);
}
__device__ __forceinline__ float dot2bf(unsigned a, unsigned b, float c) {
    typedef __bf16 bf2 __attribute__((ext_vector_type(2)));
    return __builtin_amdgcn_fdot2_f32_bf16(__builtin_bit_cast(bf2, a), __builtin_bit_cast(bf2, b), c, false);
}

#define STAMP(i) do { } while (0)
#define CSTAMP(i) do { } while (0)
#define FLUSH() do { } while (0)
#define TSV do { } while (0)

#define KSHIFT 4

constexpr int L_HT = ((32 + KSHIFT) % NSTAGE) * STAGE_BYTES;
constexpr int L_WD0 = ((33 + KSHIFT) % NSTAGE) * STAGE_BYTES;
constexpr int L_WD1 = ((34 + KSHIFT) % NSTAGE) * STAGE_BYTES;
constexpr int L_YB = RING_BYTES;
constexpr int L_ATAB = L_YB;
constexpr int LDS_BYTES = RING_BYTES + 32768;

#define SB() __builtin_amdgcn_sched_barrier(0)
template <int N> __device__ __forceinline__ void wait_vm() { asm volatile("s_waitcnt vmcnt(%0)" :: "n"(N) : "memory"); }
__device__ __forceinline__ int at_tile(int c, int par) { return ((c << 8) & 0x78000) | (((c << 2) & 0x2000) ^ par); }
__device__ __forceinline__ int at_panel(int ep) { return ep << 19; }
__device__ __forceinline__ int pre_off(int slot) { const int s_ = slot >= 56, l = slot - 56 * s_; return (l >> 2) * 16384 + s_ * 8192 + (l & 3) * 2048; }
static_assert(KSHIFT == 4, "flag samples must have replaced older contents of their slot; even tile counts");
struct Ring {
    __amdgpu_buffer_rsrc_t rP, rQ, rF;
    int voP[4], voQ[4], voF;
    LAS unsigned char* fl;
    unsigned lb;
    int sp, sq;
    int poff, par, xm;
    bool img;
    template <int LD, bool PIMG> __device__ __forceinline__ void setup(int lw, int lane) {
        const int r = lane >> 3, v0 = (PIMG ? r : (r & 3) + (r & 4) * 2) * (LD * 2) + (((lane & 7) ^ (lane >> 4)) << 4);
#pragma unroll
        for (int j = 0; j < 4; ++j) {
            voQ[j] = (v0 ^ ((j & 1) * 64)) + (PIMG ? j * 8 : (j & 1) * 16 + (j >> 1) * 4) * (LD * 2) - j * 1024;
            voP[j] = PIMG ? (lw >> 1) * 16384 + (lw & 1) * 4096 + lane * 16 : voQ[j];
        }
        img = PIMG;
    }
    __device__ __forceinline__ void seek(int col, int pof) {
        sq = col; poff = pof;
        sp = (img ? at_tile(sq, par) : sq) + poff;
    }
    __device__ __forceinline__ void step() {
        sq = (sq + BK * 2) & 4095;
        sp = (img ? at_tile(sq ^ xm, par) : sq) + poff;
        asm volatile("" : "+s"(sq), "+s"(sp));
    }
    __device__ __forceinline__ void sample() { dma_word<true>(rF, (unsigned)(size_t)fl, voF); }
    __device__ __forceinline__ unsigned peek(unsigned addr) {
        unsigned v;
        asm volatile("ds_read_b32 %0, %1\n\ts_waitcnt lgkmcnt(0)" : "=v"(v) : "v"(addr) : "memory");
        return v;
    }
    __device__ __forceinline__ void gate(unsigned want, unsigned sa, unsigned need, unsigned& have) {
#pragma unroll 1
        for (int spin = 0; spin <= SPIN_CAP && (need & ~have) != 0u; ++spin) {
            if (spin) { sample(); WAIT_VM(0); __builtin_amdgcn_s_sleep(1); }
            const unsigned long long m = __ballot(peek(sa) == want);
            have = (unsigned)m & (unsigned)(m >> 32);
        }
    }
    template <int AUX> __device__ __forceinline__ void issueP(LAS unsigned char* base) { dma_4k<AUX != 0>(rP, (unsigned)(size_t)base, voP, sp); }
    __device__ __forceinline__ void issueQ(LAS unsigned char* base) { dma_4k<false>(rQ, (unsigned)(size_t)base + TILE_BYTES, voQ, sq ^ xm); }
    template <bool P2> __device__ __forceinline__ void issue(LAS unsigned char* lds) {
        issueP<P2 ? 16 : 0>(lds + lb);
        issueQ(lds + lb);
        SB();
        if (P2) sample();
        lb = (lb + STAGE_BYTES) & (RING_BYTES - 1);
        asm volatile("" : "+s"(lb));
        step();
        SB();
    }
    template <int CAT> __device__ __forceinline__ void ramp(LAS unsigned char* lds) {
#pragma unroll 1
        for (int i = 1; i <= CAT + 1; ++i) {
            issue<false>(lds);
            if (i == CAT + 1) continue;
            WAIT_VM(8);
            BARRIER();
        }
    }
    template <bool P2, bool SAMP, int W0 = 16> __device__ __forceinline__ void steady(int cnt, int trig = 0, int v2 = 0) {
        int t, u;
#define LD(v, r, s, o) "buffer_load_dwordx4 " v ", " r ", " s " offen" o " lds\n\t"
        if constexpr (P2) {
#define BODY(SMP, TST, ENT) asm volatile("s_mov_b32 m0, %0\n" ENT "1:\n\ts_waitcnt vmcnt(16)\n2:\n\ts_barrier\n\t" \
            LD("%8", "%6", "%2", " sc1") LD("%8", "%6", "%2", " offset:1024 sc1") LD("%8", "%6", "%2", " offset:2048 sc1") LD("%8", "%6", "%2", " offset:3072 sc1") \
            SMP "s_xor_b32 %4, %1, %18\n\ts_add_i32 %1, %1, 0x80\n\ts_add_i32 m0, %0, 0x4000\n\ts_and_b32 %1, %1, 0xfff\n\t" \
            LD("%9", "%7", "%4", "") "s_xor_b32 %2, %1, %18\n\ts_lshl_b32 %5, %1, 2\n\ts_lshl_b32 %2, %2, 8\n\ts_and_b32 %2, %2, 0x78000\n\t" \
            LD("%10", "%7", "%4", " offset:1024") "s_and_b32 %5, %5, 0x2000\n\ts_xor_b32 %5, %5, %13\n\ts_or_b32 %2, %2, %5\n\ts_add_i32 %3, %3, -1\n\t" \
            LD("%11", "%7", "%4", " offset:2048") "s_add_i32 %0, %0, 0x8000\n\ts_and_b32 %0, %0, 0x1ffff\n\t" TST "s_cmp_lg_u32 %3, 0\n\t" \
            LD("%12", "%7", "%4", " offset:3072") "\n4:\n\ts_mov_b32 m0, %0\n\ts_cbranch_scc1 1b\n\ts_waitcnt vmcnt(16)\n\ts_barrier" \
            : "+s"(lb), "+s"(sq), "+s"(sp), "+s"(cnt), "=&s"(t), "=&s"(u) \
            : "s"(rP), "s"(rQ), "v"(voP[0]), "v"(voQ[0]), "v"(voQ[1]), "v"(voQ[2]), "v"(voQ[3]), "s"(par | poff), "s"(rF), "v"(voF), "s"((unsigned)(size_t)fl), "n"(W0), "s"(xm), \
              "s"(trig), "v"(v2) \
            : "memory", "scc", "m0")
            if constexpr (SAMP) BODY("s_mov_b32 m0, %16\n\ts_nop 0\n\tbuffer_load_dword %15, %14, 0 offen sc1 lds\n\t", "", "");
            else BODY("", "s_cmp_eq_u32 %3, %19\n\ts_cbranch_scc1 3b\n\t",
                      "\ts_waitcnt vmcnt(%17)\n\ts_branch 2f\n3:\n\t" LD("%12", "%7", "%4", " offset:3072") "s_mov_b32 m0, %16\n\ts_nop 0\n\tbuffer_load_dword %20, %7, 0 offen lds\n\t"
                      "s_add_i32 m0, %16, 0x200\n\ts_cmp_lg_u32 %3, 0\n\tbuffer_load_dword %15, %14, 0 offen sc1 lds\n\ts_branch 4f\n");
#undef BODY
        } else {
            asm volatile("s_mov_b32 m0, %0\n\ts_branch 2f\n1:\n\ts_waitcnt vmcnt(16)\n\ts_barrier\n2:\n\t"
                LD("%6", "%4", "%1", "") LD("%7", "%4", "%1", " offset:1024") LD("%8", "%4", "%1", " offset:2048") LD("%9", "%4", "%1", " offset:3072")
                "s_add_i32 m0, %0, 0x4000\n\ts_mov_b32 %3, %1\n\t"
                LD("%6", "%5", "%3", "") "s_add_i32 %1, %1, 0x80\n\ts_add_i32 %0, %0, 0x8000\n\t"
                LD("%7", "%5", "%3", " offset:1024") "s_and_b32 %0, %0, 0x1ffff\n\ts_add_i32 %2, %2, -1\n\t"
                LD("%8", "%5", "%3", " offset:2048") "s_cmp_lg_u32 %2, 0\n\t"
                LD("%9", "%5", "%3", " offset:3072") "s_mov_b32 m0, %0\n\ts_cbranch_scc1 1b\n\ts_waitcnt vmcnt(16)\n\ts_barrier"
                : "+s"(lb), "+s"(sq), "+s"(cnt), "=&s"(t)
                : "s"(rP), "s"(rQ), "v"(voQ[0]), "v"(voQ[1]), "v"(voQ[2]), "v"(voQ[3])
                : "memory", "scc", "m0");
            (void)u;
        }
#undef LD
    }
};

template <bool PLIN, bool FIRST, class Pre>
__device__ __forceinline__ void compute_loop(const LAS unsigned char* lds, f32x4 (&acc)[4][4], int NT, int wp, int wq, int lane, Pre pre,
                                             bf16x8 (&p0)[4], bf16x8 (&p1)[4], unsigned last) {
    const int fr = lane & 15, fq = lane >> 4, key = (fr >> 1) & 7;
    const unsigned l0 = (unsigned)(size_t)lds;
    const unsigned oP0 = l0 + (PLIN ? wp * 8192 + lane * 16 : (wp * 64 + fr) * 128 + ((fq ^ key) * 16));
    const unsigned oP1 = l0 + (PLIN ? wp * 8192 + 1024 + lane * 16 : (wp * 64 + fr) * 128 + (((4 + fq) ^ key) * 16));
    const unsigned oQ0 = l0 + TILE_BYTES + (wq * 64 + fr) * 128 + ((fq ^ key) * 16);
    const unsigned oQ1 = l0 + TILE_BYTES + (wq * 64 + fr) * 128 + (((4 + fq) ^ key) * 16);
    unsigned aP0 = oP0, aQ0 = oQ0, aP1 = oP1, aQ1 = oQ1;
    bf16x8 r0[4], r1[4], q0[4], q1[4];
    unsigned xp = 2 * STAGE_BYTES;
#pragma unroll
    for (int i = 0; i < 16; ++i) asm volatile("" : "+v"(acc[i >> 2][i & 3]));
    asm volatile("" : "+v"(aP0), "+v"(aQ0), "+v"(aP1), "+v"(aQ1));
#define MM(pp, qq, m, n) do { acc[m][n] = __builtin_amdgcn_mfma_f32_16x16x32_bf16(pp[m], qq[n], acc[m][n], 0, 0, 0); SB(); } while (0)
#define RD(dst, i, a, st) do { dst[i] = *(const LAS bf16x8*)((const LAS unsigned char*)(size_t)(a) + (st) * STAGE_BYTES + (i) * 2048); SB(); } while (0)
#define ADP(a) do { a ^= xp; asm volatile("" : "+v"(a)); SB(); } while (0)
#define ADR(a) do { a ^= 2 * STAGE_BYTES; asm volatile("" : "+v"(a)); SB(); } while (0)
    __builtin_amdgcn_s_waitcnt(0xC07F);
    SB();
    if (FIRST) BARRIER();
    SB();
    RD(p0, 0, aP0, 0); RD(q0, 0, aQ0, 0); RD(p1, 0, aP1, 0); RD(q1, 0, aQ1, 0); RD(p1, 1, aP1, 0); RD(p0, 1, aP0, 0);
    RD(p0, 2, aP0, 0); RD(p1, 2, aP1, 0); RD(p1, 3, aP1, 0); RD(p0, 3, aP0, 0);
    pre();
    SB();
    int t = NT;
#pragma unroll 1
    do {
        MM(p0, q0, 0, 0); MM(p1, q1, 0, 0); RD(q0, 1, aQ0, 0);
        MM(p1, q1, 1, 0); MM(p0, q0, 1, 0); RD(q1, 1, aQ1, 0);
        MM(p0, q0, 2, 0); MM(p1, q1, 2, 0); RD(q0, 2, aQ0, 0);
        MM(p1, q1, 3, 0); MM(p0, q0, 3, 0); RD(q1, 2, aQ1, 0);
        MM(p0, q0, 3, 1); MM(p1, q1, 3, 1); RD(q0, 3, aQ0, 0);
        MM(p1, q1, 2, 1); MM(p0, q0, 2, 1); RD(q1, 3, aQ1, 0);
        MM(p0, q0, 1, 1); MM(p1, q1, 1, 1);
        MM(p1, q1, 0, 1); MM(p0, q0, 0, 1);
        MM(p0, q0, 0, 2); MM(p1, q1, 0, 2); __builtin_amdgcn_s_waitcnt(0xC07F); SB(); BARRIER(); SB();
        MM(p1, q1, 1, 2); RD(r0, 0, aP0, 1); MM(p0, q0, 1, 2); RD(q0, 0, aQ0, 1);
        MM(p0, q0, 2, 2); RD(r1, 0, aP1, 1); MM(p1, q1, 2, 2); RD(q1, 0, aQ1, 1);
        MM(p1, q1, 3, 2); MM(p0, q0, 3, 2); RD(r1, 1, aP1, 1);
        MM(p0, q0, 3, 3); RD(r0, 1, aP0, 1); MM(p1, q1, 3, 3);
        MM(p1, q1, 2, 3); RD(r0, 2, aP0, 1); MM(p0, q0, 2, 3); RD(r1, 2, aP1, 1);
        MM(p0, q0, 1, 3); MM(p1, q1, 1, 3); RD(r1, 3, aP1, 1);
        MM(p1, q1, 0, 3); RD(r0, 3, aP0, 1); MM(p0, q0, 0, 3);

        MM(r0, q0, 0, 0); MM(r1, q1, 0, 0); RD(q0, 1, aQ0, 1);
        MM(r1, q1, 1, 0); MM(r0, q0, 1, 0); RD(q1, 1, aQ1, 1);
        MM(r0, q0, 2, 0); MM(r1, q1, 2, 0); RD(q0, 2, aQ0, 1);
        MM(r1, q1, 3, 0); MM(r0, q0, 3, 0); RD(q1, 2, aQ1, 1);
        MM(r0, q0, 3, 1); MM(r1, q1, 3, 1); RD(q0, 3, aQ0, 1);
        MM(r1, q1, 2, 1); MM(r0, q0, 2, 1); RD(q1, 3, aQ1, 1);
        MM(r0, q0, 1, 1); t -= 2; if (PLIN) xp = t == 0 ? last : 2 * STAGE_BYTES; asm volatile("" : "+s"(t), "+s"(xp)); SB(); MM(r1, q1, 1, 1); ADP(aP0);
        MM(r1, q1, 0, 1); ADP(aP1); MM(r0, q0, 0, 1); ADR(aQ0);
        MM(r0, q0, 0, 2); ADR(aQ1); MM(r1, q1, 0, 2); __builtin_amdgcn_s_waitcnt(0xC07F); SB(); BARRIER(); SB();
        MM(r1, q1, 1, 2); RD(p0, 0, aP0, 0); MM(r0, q0, 1, 2); RD(q0, 0, aQ0, 0);
        MM(r0, q0, 2, 2); RD(p1, 0, aP1, 0); MM(r1, q1, 2, 2); RD(q1, 0, aQ1, 0);
        MM(r1, q1, 3, 2); MM(r0, q0, 3, 2); RD(p1, 1, aP1, 0);
        MM(r0, q0, 3, 3); RD(p0, 1, aP0, 0); MM(r1, q1, 3, 3);
        MM(r1, q1, 2, 3); RD(p0, 2, aP0, 0); MM(r0, q0, 2, 3); RD(p1, 2, aP1, 0);
        MM(r0, q0, 1, 3); MM(r1, q1, 1, 3); RD(p1, 3, aP1, 0);
        MM(r1, q1, 0, 3); RD(p0, 3, aP0, 0); MM(r0, q0, 0, 3);
    } while (t != 0);
    SB();
#undef MM
#undef RD
#undef ADR
#undef ADP
}

struct Args {
    const bf16_t* x; const bf16_t* wre; const bf16_t* wim; char* ws; const bf16_t* cr; const bf16_t* ci;
    const bf16_t* w_r; const bf16_t* wg; const bf16_t* wu; const bf16_t* wd; bf16_t* out;
};

__device__ __forceinline__ unsigned poll_winner(const char* ws, int rep, int lane, unsigned& mine) {
    GAS unsigned* kp = (GAS unsigned*)(ws + WS_KEYS + (size_t)(rep >> 2) * 8192 + (rep & 3) * (KEY_S * 16) + ((lane >> 2) & 7) * KEY_S) + (lane & 3);
    unsigned kw;
    for (int spin = 0;; ++spin) {
            asm volatile("" ::: "memory");
        kw = __hip_atomic_load(kp, __ATOMIC_RELAXED, __HIP_MEMORY_SCOPE_AGENT);
        if (__all(kw != 0u) || spin > SPIN_CAP) break;
        __builtin_amdgcn_s_sleep(POLL_SLEEP);
    }
    mine = kw;
    return __builtin_amdgcn_readfirstlane(wave_max_u32(kw));
}

__device__ __forceinline__ void stage2(char* w2, int slot, int lw, LAS unsigned char* img0, LAS unsigned char* img1, LAS unsigned char* abuf, int zoff, int lane) {
    const int g = lane >> 4, n = lane & 15;
    const __amdgpu_buffer_rsrc_t rPi = mk_rsrc(w2 + WS_PRE + pre_off(slot), 2048);
    unsigned pp[8];
    auto request = [&]() {
        asm volatile("" ::: "memory");
#pragma unroll
        for (int k = 0; k < 8; ++k) pp[k] = __builtin_amdgcn_raw_buffer_load_b32(rPi, k * 256 + lane * 4, 0, 16);
        SB();
    };
    request();
    bf16x8 wf[16];
    {
        const int q = (lane >> 2) & 3, ky = q | ((g & 1) << 2);
#pragma unroll
        for (int j = 0; j < 16; ++j) {
            LAS unsigned char* t = (g < 2 ? img0 : img1) + (8 * (g & 1) + q) * 512 + (((j & 7) ^ ky) + (j & 8)) * 32 + (lane & 3) * 8;
            const bf16x4 w0 = __builtin_amdgcn_ds_read_tr16_b64_v4i16((LAS bf16x4*)t);
            const bf16x4 w1 = __builtin_amdgcn_ds_read_tr16_b64_v4i16((LAS bf16x4*)(t + 2048));
            wf[j] = __builtin_shufflevector(w0, w1, 0, 1, 2, 3, 4, 5, 6, 7);
        }
        SB();
#pragma unroll
        for (int j = 0; j < 16; ++j) asm volatile("" : "+v"(wf[j]));
    }
    unsigned aa[5], pto = lane * 16;
#pragma unroll
    for (int m = 0; m < 4; ++m) aa[m] = (unsigned)(size_t)abuf + ((n >> 2) == m && ((n >> 1) & 1) == (g >> 1) ? (n & 1) * 32 + (g & 1) * 16 : zoff + g * 16);
    aa[4] = (unsigned)(size_t)abuf + ((lane >> 1) & 1) * 32 + ((lane >> 4) & 1) * 16 + ((lane >> 2) & 3) * 4 + (lane & 1) * 2;
    unsigned ix = (n & 1) ? 0xEEEEEEEEu : 0x44444444u;
    __amdgpu_buffer_rsrc_t rPt = mk_rsrc(w2 + WS_PART + __builtin_amdgcn_readfirstlane((slot >> 2) * 16384 + (lw >> 1) * 8192 + (slot & 3) * 2048 + (lw & 1) * 1024), 1024);
    asm volatile("" : "+v"(aa[0]), "+v"(aa[1]), "+v"(aa[2]), "+v"(aa[3]), "+v"(aa[4]), "+v"(pto), "+v"(ix));
    asm volatile("" : "+s"(rPt));
    f32x4 d[2] = {{0.f, 0.f, 0.f, 0.f}, {0.f, 0.f, 0.f, 0.f}};
    asm volatile("" : "+v"(d[0]), "+v"(d[1]));
    SB();
    float sv;
#pragma unroll 1
    for (int spin = 0;; ++spin) {
        sv = ((__uint_as_float(pp[0]) + __uint_as_float(pp[1])) + (__uint_as_float(pp[2]) + __uint_as_float(pp[3]))) +
             ((__uint_as_float(pp[4]) + __uint_as_float(pp[5])) + (__uint_as_float(pp[6]) + __uint_as_float(pp[7])));
        if (__builtin_expect(__builtin_amdgcn_ballot_w64(sv != sv) == 0, 1)) break;
        if (__all(max(max(max(pp[0], pp[1]), max(pp[2], pp[3])), max(max(pp[4], pp[5]), max(pp[6], pp[7]))) != SENT) || spin > SPIN_CAP) break;
        request();
    }
    unsigned ug, uu;
    swap32(__float_as_uint(sv), ug, uu);
    const float gg = rbf(__uint_as_float(ug)), up = rbf(__uint_as_float(uu));
    const unsigned act = pk_bf16(rbf(gg * __builtin_amdgcn_rcpf(1.0f + __expf(-gg))) * up, 0.f);
    *(volatile LAS unsigned short*)(size_t)aa[4] = (unsigned short)act;
    bf16x8 af[4];
#pragma unroll
    for (int m = 0; m < 4; ++m) af[m] = *(volatile LAS bf16x8*)(size_t)aa[m];
    SB();
#pragma unroll
    for (int i = 0; i < 8; ++i) {
        const int j = 8 * (i & 1) + 2 * (i >> 1);
        d[i & 1] = smf(af[i >> 1], wf[j], wf[j + 1], d[i & 1], ix);
        SB();
    }
    u32x4 pv;
#pragma unroll
    for (int q = 0; q < 4; ++q) {
        pv[q] = min(__float_as_uint(d[q >> 1][(q & 1) * 2] + d[q >> 1][(q & 1) * 2 + 1]), SENT - 1u);
        asm volatile("" : "+v"(pv[q]));
    }
    ST16_SC1(pv, rPt, pto);
}
__device__ __forceinline__ int wd_off(int lw, int i, int lane) {
    const int r = 2 * (i & 5) + (lane >> 5), ky = (r & 3) | ((r >> 1) & 4), c = lane & 31;
    return (lane >> 5) * (D_LEN * 2) + lw * 512 + (((c >> 1) ^ ky) * 2 + (c & 1)) * 16;
}

__global__ __launch_bounds__(768, 3) void fused_kernel(const Args a) {
    extern __shared__ __attribute__((aligned(16))) unsigned char smem[];
    constexpr int K = S_LEN, NT1 = D_LEN / BK;
    const int tid = threadIdx.x, wid = __builtin_amdgcn_readfirstlane(tid >> 6), lane = tid & 63;
    const int xcd = blockIdx.x & 7, idx = blockIdx.x >> 3;
    TSV;
    LAS unsigned char* lds = (LAS unsigned char*)smem;
    if (idx < CODE_WG && wid < 4) dma_word<false>(code_rsrc(), (unsigned)(size_t)lds + L_DUMMY, tid * 128);
    SB();
    const int h = xcd & 1, xq = xcd >> 1, rt = xq * 4 + (idx & 3), ct = idx >> 2;
    volatile LAS unsigned* ctl1 = (volatile LAS unsigned*)(lds + L_CTL);
#define NBR(k) ((rt + (k)) & 3)
    unsigned kx;
    Ring ring;
    if (__builtin_expect((wid & 12) == 4, 1)) {
        __builtin_amdgcn_s_setprio(3);
        ring.template setup<D_LEN, false>(wid - 4, lane);
        ring.rP = mk_rsrc(a.x + (size_t)(rt * 4 + wid - 4) * 32 * D_LEN, 32 * D_LEN * 2);
        ring.rQ = mk_rsrc((h == 0 ? a.wre : a.wim) + (size_t)(ct * 4 + wid - 4) * 32 * D_LEN, 32 * D_LEN * 2);
        ring.lb = (wid - 4) * 4096;
        ring.sp = ring.sq = ring.poff = ring.xm = 0;
        ring.template issue<false>(lds);
        __builtin_amdgcn_s_setprio(0);
        ring.par = (ct & 4) << 11;
        ring.template ramp<CATCH1>(lds);
        ring.template steady<false, false>(NT1 - 2 - CATCH1);
        ring.template setup<K, true>(wid - 4, lane);
        ring.rQ = mk_rsrc((h == 0 ? a.cr : a.ci) + (size_t)(rt * 4 + wid - 4) * 32 * K, 32 * K * 2);
        ring.seek(rt * 256, 0);
        LAS unsigned char* l0 = lds + (wid - 4) * 4096;
        ring.issueQ(l0); ring.step();
        WAIT_VM(12); BARRIER();
        ring.issueQ(l0 + STAGE_BYTES);
        ring.seek((xq * 4 + NBR(1)) * 256, 0);
        WAIT_VM(8); BARRIER();
        ring.issueQ(l0 + 2 * STAGE_BYTES);
        ring.seek((xq * 4 + NBR(2)) * 256, 0);
        const int xr = (wid & 1) * 8 + (lane >> 4);
        int vx0 = ((((wid >> 1) & 1) * 64 + xr) * D_LEN + (((lane & 15) ^ xr) * 8)) * 2;
        int vx1 = ((((wid >> 1) & 1) * 64 + xr + 4) * D_LEN + (((lane & 15) ^ (xr + 4)) * 8)) * 2 - 1024;
        __amdgpu_buffer_rsrc_t rX = mk_rsrc(a.x + (size_t)rt * BT * D_LEN + ct * BT, BT * D_LEN * 2 - ct * BT * 2);
        unsigned xd = (unsigned)(size_t)lds + ((wid & 2) ? L_YB + 16384 : 3 * STAGE_BYTES) + (wid & 1) * 2048;
        asm volatile("" : "+v"(vx0), "+v"(vx1));
        asm volatile("" : "+s"(rX), "+s"(xd));
        BARRIER();
        if (h != 0) {
#pragma unroll
            for (int k = 0; k < 4; ++k) dma_2k<false>(rX, xd + k * 4096, vx0, vx1, k * (16 * D_LEN * 2));
        }
        ring.issueQ(l0 + 3 * STAGE_BYTES);
        kx = (unsigned)__builtin_amdgcn_readfirstlane((int)ctl1[4]);
        SB();
    }
    char* ws = a.ws + ((0ull - (unsigned long long)a.ws) & 0x1fffffull);
    const int g = lane >> 4, n = lane & 15;
    LAS float* tab = (LAS float*)(lds + L_RED);
    if (wid >= 8) {
        const int aw = wid - 8;
        GAS u64* tagp = (GAS u64*)(ws + WS_TAG);
        const int r0 = xq * 512 + h * 256 + idx * 8 + aw * 2;
        const int w0 = xq * 256 + idx * 8 + aw * 2;
        const u32x4* src = (const u32x4*)((n < 8 ? a.x + (size_t)(r0 + (n >> 2)) * D_LEN
                                                 : (h == 0 ? a.wre : a.wim) + (size_t)(w0 + (n >> 2) - 2) * D_LEN) + g * 32 + (n & 3) * 8);
        if (AUX_SLEEP) __builtin_amdgcn_s_sleep(AUX_SLEEP);
        u64 tag0 = __hip_atomic_load(tagp, __ATOMIC_RELAXED, __HIP_MEMORY_SCOPE_AGENT);
        u32x4 v[8];
        constexpr int CKT[8] = {CK_T};
#pragma unroll
        for (int j = 0; j < 8; ++j) if (CKT[j] < 0) v[j] = src[j * 16];
        __builtin_amdgcn_s_setprio(3);
        SB();
        const char* cb = (const char*)(h ? a.ci : a.cr) + (size_t)rt * (512u << 10) + ct * 65536 + (lane >> 2) * 4096 + (lane & 3) * 128;
        const GAS u32x4* wrp = (const GAS u32x4*)a.w_r + lane;
        asm volatile("" : "+v"(cb), "+v"(wrp));
        unsigned mask = aw == 1 ? CPF_MASK1 : aw == 2 ? CPF_MASK2 : 0u;
        mask = (mask & CPF_FIX) | ((mask & ~CPF_FIX) << (idx % CPF_STAG));
        int ti = 2 * xq + aw - 1;
        auto warm = [&](int t) { if (t >= 4 && ((mask >> t) & 1)) { touch_line(cb + (ti & 7) * 512, L_DUMMY); ti += 2; } };
#pragma unroll
        for (int t = 0; t <= STAT_M; ++t) {
            SB(); BARRIER(); SB();
            if (aw == 1) STAMP(40 + t);
#pragma unroll
            for (int j = 0; j < 8; ++j) if (CKT[j] == t) v[j] = src[j * 16];
            warm(t);
        }
        SB();
        f32x4 d1 = {0.f, 0.f, 0.f, 0.f}, d2 = {0.f, 0.f, 0.f, 0.f};
        {
            const u32x4 ones = {0x3F803F80u, 0x3F803F80u, 0x3F803F80u, 0x3F803F80u};
#pragma unroll
            for (int j = 0; j < 8; ++j) {
                const bf16x8 f = __builtin_bit_cast(bf16x8, v[j]);
                d1 = __builtin_amdgcn_mfma_f32_16x16x32_bf16(f, __builtin_bit_cast(bf16x8, ones), d1, 0, 0, 0);
                d2 = __builtin_amdgcn_mfma_f32_16x16x32_bf16(f, f, d2, 0, 0, 0);
            }
        }
        asm volatile("" : "+v"(tag0), "+v"(d1), "+v"(d2));
        SB(); BARRIER(); SB();
        warm(STAT_M + 1);
        const u64 want = tag0 * 6364136223846793005ull + 1442695040888963407ull;
        const unsigned wl = (unsigned)want, wh = (unsigned)(want >> 32);
        const __amdgpu_buffer_rsrc_t rS = mk_rsrc(ws + WS_XRD, 65536);
        {
            const float sm = (d1[0] + d1[1]) + (d1[2] + d1[3]);
            float sq = sel4(n, d2[0], d2[1], d2[2], d2[3]);
            sq += dppf<0xB1>(sq);
            sq += dppf<0x4E>(sq);
            const float mean = sm * (1.0f / D_LEN);
            const float var = fmaxf(sq * (1.0f / D_LEN) - mean * mean, 0.f);
            const float rden = __builtin_amdgcn_rcpf(rbf(__builtin_amdgcn_sqrtf(rbf(rbf(var) + 1e-5f))));
            const u32x4 rec = {__float_as_uint(g < 2 ? rden : sm), wl, __float_as_uint(rbf(mean) * rden), wh};
            ST16_SC1(rec, rS, n == g * 4 ? (g < 2 ? r0 + g : 2048 - 2 + h * 1024 + w0 + g) * 16 : 65536);
        }
        const __amdgpu_buffer_rsrc_t rV = mk_rsrc(ws + WS_VOTE, 512);
        if (aw == 3 && idx < 8) __builtin_amdgcn_raw_buffer_store_b64((u32x2){ctl1[5], wl}, rV, blockIdx.x * 8 + lane * 512, 0, 16);
        SB(); BARRIER(); SB();
        warm(STAT_M + 2);
        if (aw == 1 || aw == 2) {
#pragma unroll 1
            for (int t = STAT_M + 3; t <= NT1 + AUX_STAY; ++t) {
                BARRIER();
                if (aw == 1 && t <= 16) STAMP(40 + t);
                warm(t);
            }
            FLUSH();
            return;
        }
        if (aw == 3) {
            if (h == 0) {
                const u32x4 w0v = wrp[0], w1v = wrp[64];
                BARRIER(); BARRIER(); BARRIER(); BARRIER();
                SB();
                float t = 0.f;
#pragma unroll
                for (int i = 0; i < 4; ++i) { t = dot2bf(w0v[i], 0x3F803F80u, t); t = dot2bf(w1v[i], 0x3F803F80u, t); }
                SB(); BARRIER(); SB();
                t = wave_sum(t);
                if (lane == 0) ctl1[1] = __float_as_uint(t);
                WAIT_LGKM0();
            }
            return;
        }
#pragma unroll
        for (int t = STAT_M + 3; t <= POLL_T; ++t) BARRIER();
        SB();
        const int o0 = (rt * BT + lane) * 16, o1 = (2048 + (h * 8 + ct) * BT + lane) * 16, os = 1024;
        u32x4 q0, q1, q2, q3;
        u32x2 qv;
        auto request = [&]() {
            asm volatile("" ::: "memory");
            q0 = LD16_SC1(rS, o0); q1 = LD16_SC1(rS, o0 + os); q2 = LD16_SC1(rS, o1); q3 = LD16_SC1(rS, o1 + os);
            qv = __builtin_amdgcn_raw_buffer_load_b64(rV, lane * 8, 0, 16);
            SB();
        };
        request();
        int b = POLL_T + 1;
        for (int spin = 0; spin <= SPIN_CAP; ++spin) {
            if (b < NT1) { BARRIER(); ++b; }
            if (b < NT1) { BARRIER(); ++b; }
            SB();
            unsigned d = ((q0[1] ^ wl) | (q1[1] ^ wl)) | ((q2[1] ^ wl) | (q3[1] ^ wl)) | ((q0[3] ^ wh) | (q1[3] ^ wh)) | ((q2[3] ^ wh) | (q3[3] ^ wh)) | (qv[1] ^ wl);
            asm volatile("" : "+v"(d));
            if (__all(d == 0u)) break;
            request();
        }
        if (b < NT1) BARRIER();
        SB();
        LAS unsigned* tb = (LAS unsigned*)tab;
        const unsigned sg = h == 0 ? 0u : 0x80000000u;
        tb[lane] = q0[0] ^ sg; tb[64 + lane] = q1[0] ^ sg; tb[128 + lane] = q0[2] ^ sg; tb[192 + lane] = q1[2] ^ sg;
        tb[256 + lane] = q2[0]; tb[320 + lane] = q3[0];
        const unsigned vt = qv[0];
        const unsigned kx = __popcll(__ballot(vt == 2u)) > __popcll(__ballot(vt == 1u)) ? 8192u : 0u;
        if (lane == 0) { ctl1[0] = wl | 2u; ctl1[2] = wl; ctl1[3] = wh; ctl1[4] = kx; }
        *(LAS u32x2*)(lds + L_YB + 4096 + 256 + lane * 8) = (u32x2){0u, 0u};
        WAIT_LGKM0();
        return;
    }
#define CTL(i) ((unsigned)__builtin_amdgcn_readfirstlane((int)ctl[i]))
    const int wp = wid & 1, wq = (wid >> 1) & 1;
#define Y_OFF ((int)WS_Y + ((rt & 7) * 8 + ct) * 65536 + (wid & 3) * 16384 + (((rt >> 3) ^ (rt >> 2)) & 1) * 8192)
#define P2_DEFS \
    char* w2 = ws + kx; \
    const int sidx = idx < 28 ? xq * 28 + idx : 112 + xq * 4 + idx - 28;     \
    volatile LAS unsigned* ctl = (volatile LAS unsigned*)(lds + (h == 0 ? L_ATAB : L_CTL)); \
    const int NT = h == 0 ? 32 + KSHIFT : 32 - KSHIFT; \
    LAS float* rstat = (LAS float*)(lds + L_ATAB + 256);     \
    const int hp_ = rt * 2 + wq;                             \
    (void)sidx; (void)ctl; (void)NT; (void)rstat; (void)hp_; (void)w2
    if (__builtin_expect(wid < 4, 1)) {
        {
            const u32x4 sent4 = {SENT, SENT, SENT, SENT}, zero4 = {0u, 0u, 0u, 0u};
            const __amdgpu_buffer_rsrc_t rI = mk_rsrc(ws + WS_STATS + (size_t)blockIdx.x * (FILL_BYTES / 256), FILL_BYTES / 256);
            const __amdgpu_buffer_rsrc_t rZ = mk_rsrc(ws + (size_t)blockIdx.x * 160, 160);
            ST16_SC1(sent4, rI, tid * 16);
            if (wid < 2) ST16_SC1(sent4, rI, 4096 + tid * 16);
            if (wid == 2) ST16_SC1(zero4, rZ, lane * 16);
        }
        if (wid == 3 && idx < 8) {
            const unsigned r = (((unsigned)__builtin_amdgcn_s_memrealtime() * 2654435761u + blockIdx.x * 40503u) >> 9) & 0x1fdf80u;
            const unsigned first = (idx & 1) << 13;
            const char* pb = ws + WS_PROBE;
            unsigned va, vb, left;
            asm volatile("s_waitcnt lgkmcnt(0)\n\ts_load_dword %0, %3, %4\n\ts_load_dword %1, %3, %5\n\ts_waitcnt lgkmcnt(1)\n\t"
                         "s_nop 15\n\ts_getreg_b32 %2, hwreg(HW_REG_IB_STS, 8, 4)\n\ts_waitcnt lgkmcnt(0)"
                         : "=&s"(va), "=&s"(vb), "=&s"(left) : "s"(pb), "s"(r | first), "s"(r | (first ^ 8192u)) : "memory");
            const unsigned nearbit = (left != 0u ? first : first ^ 8192u) >> 13;
            if (lane == 0) ctl1[5] = nearbit == (unsigned)(xcd >> 2) ? 1u : 2u;
        }
        f32x4 acc[4][4];
#pragma unroll
        for (int i = 0; i < 16; ++i) acc[i >> 2][i & 3] = (f32x4){0.f, 0.f, 0.f, 0.f};
        unsigned tc = (unsigned)(size_t)(tab + wp * 64 + g * 8), tw = (unsigned)(size_t)(tab + 256 + prow(wq * 64 + n));
        unsigned ta = (unsigned)(size_t)lds + wp * STAGE_BYTES + wq * 8192 + lane * 16;
        constexpr int NIL = 1 << 30;
        __amdgpu_buffer_rsrc_t rW = mk_rsrc(ws, 18u << 20);
        const int hpan = rt * 2 + wq;
        int sk = (int)WS_AT + at_panel(h * 8 + ct) + at_tile((rt * 2 + wp) * 128, (ct & 4) << 11) + wq * 16384 + lane * 16;
        int st = lane != 0 ? NIL : (int)WS_FLAG1 + (1 - wp) * 32768 + h * 16384 + (rt >> 3) * 8192 + ct * 1024 + (rt & 7) * 128 + wq * 4;
        int so = h != 0 ? Y_OFF + lane * 16 : g != (n >> 2) ? NIL
               : (int)WS_STATS + (hpan & 15) * 32768 + (ct >> 2) * 16384 + (hpan >> 4) * 8192 + ((ct & 3) * 2 + wp) * 1024 + n * 16;
        int sf = lane != 0 ? NIL : (int)WS_FLAGS + (rt * 8 + ct) * 128 + wid * 4;
        u32x4 sel[2], aw[2];
        unsigned one[2];
#pragma unroll
        for (int i = 0; i < 4; ++i) {
            one[i & 1] = g == (n >> 2) && (i & 1) == ((n >> 1) & 1) ? ((n & 1) ? 0x3F800000u : 0x00003F80u) : 0u;
            aw[0][i] = aw[1][i] = (n & 3) == 0 ? 0x3F803F80u : 0u;
        }
        if ((n & 3) == 1) {
            const bf16_t* wr = a.w_r + ct * BT + wp * 64 + g * 8;
            aw[0] = *(const u32x4*)wr; aw[1] = *(const u32x4*)(wr + 32);
        }
        unsigned ea[2];
#pragma unroll
        for (int mp = 0; mp < 2; ++mp)
            ea[mp] = (unsigned)(size_t)lds + (wq ? L_YB + 16384 : 3 * STAGE_BYTES) + n * 256 + (((wp * 8 + mp * 4 + g) ^ n) * 16);
        asm volatile("" : "+v"(tc), "+v"(tw), "+v"(ta), "+v"(sk), "+v"(st), "+v"(so), "+v"(sf), "+v"(one[0]), "+v"(one[1]), "+v"(ea[0]), "+v"(ea[1]));
        asm volatile("" : "+s"(rW));
        unsigned tg;
        bf16x8 e0[4], e1[4];
        auto extra = [&]() __attribute__((always_inline)) {
            bf16x8 ef[4][2];
#pragma unroll
            for (int i = 0; i < 8; ++i) ef[i >> 1][i & 1] = *(const LAS bf16x8*)((const LAS unsigned char*)(size_t)ea[i & 1] + (i >> 1) * 4096);
            SB();
#pragma unroll
            for (int i = 0; i < 16; ++i)
                acc[i & 3][i >> 2] = __builtin_amdgcn_mfma_f32_16x16x32_bf16(__builtin_bit_cast(bf16x8, sel[i & 1]), ef[i >> 2][(i >> 1) & 1], acc[i & 3][i >> 2], 0, 0, 0);
        };
        if (wid == 0) STAMP(0);
        compute_loop<false, true>(lds, acc, NT1, wp, wq, lane, [&]() { if (wid == 0) STAMP(24); WAIT_VM0(); if (wid == 0) STAMP(31); }, e0, e1, 0u);
        if (wid == 0) STAMP(25);
        {
            f32x4 rd[4], mr[4];
            float wsm[4];
#pragma unroll
            for (int m = 0; m < 4; ++m) {
                rd[m] = *(const LAS f32x4*)((const LAS float*)(size_t)tc + (m >> 1) * 32 + (m & 1) * 4);
                mr[m] = *(const LAS f32x4*)((const LAS float*)(size_t)tc + 128 + (m >> 1) * 32 + (m & 1) * 4);
                wsm[m] = *((const LAS float*)(size_t)tw + (m >> 1) * 32 + (m & 1) * 4);
            }
            const unsigned kv = ctl1[4];
            tg = ctl1[0];
            SB();
            sk += (int)kv; st += (int)kv; so += (int)kv; sf += (int)kv;
            kx = (unsigned)__builtin_amdgcn_readfirstlane((int)kv);
#pragma unroll
            for (int nn = 0; nn < 4; ++nn)
#pragma unroll
                for (int mp = 0; mp < 2; ++mp) {
                    const f32x4 u = acc[2 * mp][nn] * rd[2 * mp] - mr[2 * mp] * wsm[nn];
                    const f32x4 v = acc[2 * mp + 1][nn] * rd[2 * mp + 1] - mr[2 * mp + 1] * wsm[nn];
                    u32x4 w;
                    w[0] = pk_bf16(u[0], u[1]); w[1] = pk_bf16(u[2], u[3]);
                    w[2] = pk_bf16(v[0], v[1]); w[3] = pk_bf16(v[2], v[3]);
                    ST16_SC1(w, rW, sk + (nn * 2 + mp) * 1024);
                    *(LAS u32x4*)((LAS unsigned char*)(size_t)ta + (nn * 2 + mp) * 1024) = w;
                }
        }
        if (wid == 0) STAMP(26);
        WAIT_LGKM0();
        BARRIER();
        SB();
        {
#pragma unroll
            for (int i = 0; i < 16; ++i) { acc[i >> 2][i & 3] = (f32x4){0.f, 0.f, 0.f, 0.f}; asm volatile("" : "+v"(acc[i >> 2][i & 3])); }
            sel[0] = (u32x4){one[0], one[1], 0u, 0u}; sel[1] = (u32x4){0u, 0u, one[0], one[1]};
            asm volatile("" : "+v"(st), "+v"(so), "+v"(sf), "+v"(tg), "+v"(sel[0]), "+v"(sel[1]));
            SB();
            if (h != 0) extra();
            SB();
            WAIT_VM(0);
            __builtin_amdgcn_raw_buffer_store_b32(tg, rW, st, 0, 16);
            SB();
            if (wid == 0) STAMP(27);
        }
        compute_loop<true, false>(lds, acc, h == 0 ? 32 + KSHIFT : 32 - KSHIFT, wp, wq, lane, [&]() {
            if (wid == 0) { STAMP(h == 0 ? 1 : 19); CSTAMP(22); }
        }, e0, e1, (unsigned)((wp * 8192 + 2 * STAGE_BYTES) ^ (L_YB + wid * 8192)));
        P2_DEFS;
        if (wid == 0) { STAMP(h == 0 ? 2 : 12); CSTAMP(23); }
        u32x4 hq[4][2];
        auto cvt = [&](int nn, int j) __attribute__((always_inline)) {
            const f32x4 c = acc[j][nn];
            hq[nn][j >> 1][(j & 1) * 2] = pk_bf16(c[0], c[1]);
            hq[nn][j >> 1][(j & 1) * 2 + 1] = pk_bf16(c[2], c[3]);
        };
        int hs = h;
        asm volatile("" : "+s"(hs));
        if (__builtin_expect(hs == 0, 1)) {
            f32x4 d1[4], d2[4];
            u32x3 sv;
            auto add = [&](int nn, int j) __attribute__((always_inline)) {
                acc[j][nn] = __builtin_amdgcn_mfma_f32_16x16x32_bf16(__builtin_bit_cast(bf16x8, sel[j & 1]), j < 2 ? e0[nn] : e1[nn], acc[j][nn], 0, 0, 0);
            };
            auto sum = [&](int nn, int j) __attribute__((always_inline)) {
                const bf16x8 hf = __builtin_bit_cast(bf16x8, hq[nn][j >> 1]);
                const f32x4 z = {0.f, 0.f, 0.f, 0.f};
                if (j & 1) d1[nn] = __builtin_amdgcn_mfma_f32_16x16x32_bf16(__builtin_bit_cast(bf16x8, aw[j >> 1]), hf, j < 2 ? z : d1[nn], 0, 0, 0);
                else d2[nn] = __builtin_amdgcn_mfma_f32_16x16x32_bf16(hf, hf, j < 2 ? z : d2[nn], 0, 0, 0);
            };
            auto out = [&](int nn, int j) __attribute__((always_inline)) {
                if (j == 1) asm volatile("" : "+v"(d2[nn]));
                if (j == 2) asm volatile("" : "+v"(d1[nn]));
                if (j == 1) sv[1] = min(__float_as_uint(sel4(n, d2[nn][0], d2[nn][1], d2[nn][2], d2[nn][3])), SENT - 1u);
                if (j == 2) { sv[0] = min(__float_as_uint(d1[nn][0]), SENT - 1u); sv[2] = min(__float_as_uint(d1[nn][1]), SENT - 1u); }
                if (j == 3) __builtin_amdgcn_raw_buffer_store_b96(sv, rW, so + nn * 256, 0, 16);
            };
#define FOUR(m, v) _Pragma("unroll") for (int j = 0; j < 4; ++j) { m; SB(); v; SB(); }
            FOUR(add(0, j), (void)0) FOUR(add(1, j), cvt(0, j)) FOUR(sum(0, j), cvt(1, j)) FOUR(add(2, j), out(0, j))
            FOUR(sum(1, j), cvt(2, j)) FOUR(add(3, j), out(1, j)) FOUR(sum(2, j), cvt(3, j)) FOUR(sum(3, j), out(2, j))
            FOUR((void)0, out(3, j))
#undef FOUR
            if (wid == 0) STAMP(3);
#pragma unroll
            for (int nn = 0; nn < 4; ++nn)
#pragma unroll
                for (int mp = 0; mp < 2; ++mp)
                    *(LAS u32x4*)(lds + L_HT + (wq * 64 + nn * 16 + n) * 256 + (((wp * 8 + mp * 4 + g) ^ n) * 16)) = hq[nn][mp];
            WAIT_LGKM0();
            BARRIER();
            BARRIER();
            if (wid == 0) STAMP(6);
            const int crow = __builtin_amdgcn_readfirstlane((int)((volatile LAS unsigned*)(lds + L_ATAB))[2 + wq]) + wq * 64;
            const __amdgpu_buffer_rsrc_t rOut = mk_rsrc(a.out + (size_t)rt * BT * D_LEN, BT * D_LEN * 2);
#pragma unroll 2
            for (int j = 0; j < 8; ++j) {
                const int r = wq * 64 + wp * 32 + j * 4 + g;
                const float m_ = rstat[r * 2], d_ = rstat[r * 2 + 1];
                const u32x4 hh = *(const LAS u32x4*)(lds + L_HT + r * 256 + ((n ^ (r & 15)) * 16));
                u32x4 ov;
#pragma unroll
                for (int i = 0; i < 4; ++i) {
                    const unsigned u = hh[i];
                    const float h0 = bflo(u), h1 = bfhi(u);
                    const unsigned dd = pk_bf16(h0 - m_, h1 - m_);
                    const unsigned q = pk_bf16(bflo(dd) * d_, bfhi(dd) * d_);
                    ov[i] = pk_bf16(h0 + bflo(q), h1 + bfhi(q));
                }
                __builtin_amdgcn_raw_buffer_store_b128(ov, rOut, r == crow ? BT * D_LEN * 2 : (r * D_LEN + ct * BT + n * 8) * 2, 0, OUT_AUX);
            }
            FLUSH();
            return;
        }
#pragma unroll
        for (int i = 0; i < 8; ++i) {
            cvt(i >> 1, (i & 1) * 2); cvt(i >> 1, (i & 1) * 2 + 1);
            __builtin_amdgcn_raw_buffer_store_b128(hq[i >> 1][i & 1], rW, so, i * 1024, 16);
            SB();
        }
        WAIT_VM0();
        __builtin_amdgcn_raw_buffer_store_b32(1u, rW, sf, 0, 16);
        if (wid == 0) STAMP(13);
    } else {
        LAS unsigned char* l0 = lds + (wid - 4) * 4096;
        P2_DEFS;
        __amdgpu_buffer_rsrc_t rD = mk_rsrc(h == 0 ? a.wd + (size_t)sidx * 32 * D_LEN : a.wg,
                                                  sidx >= 112 ? 0 : h != 0 ? D_LEN * F_LEN * 2 : wid < 8 - BQ ? 32 * D_LEN * 2 : 0);
        int vGe = h == 0 ? wd_off(wid & 3, 0, lane)
                : (((sidx & 7) * 128 + (wid & 3) * 16 + (lane >> 5)) * F_LEN + (sidx >> 3) * 256 + ((((lane & 31) >> 1) ^ (lane >> 5)) * 16) + (lane & 1) * 8) * 2;
        auto load_wd = [&](int i) __attribute__((always_inline)) {
            dma_nt(rD, (unsigned)(size_t)lds + (i < 8 ? L_WD0 : L_WD1) + (wid & 3) * 8192 + (i & 7) * 1024, vGe ^ ((2 * (i & 1) | (i & 4)) * 32), i * (4 * D_LEN));
        };
        const int lw = wid - 4;
        constexpr int NIL = 1 << 30;
        ring.rP = ring.rF = mk_rsrc(w2, (unsigned)WS_STATS);
        ring.voP[0] += (int)WS_AT + at_panel(h * 8 + ct);
        ring.voP[1] = ring.voP[2] = ring.voP[3] = ring.voP[0];
#define FLINE(k) (((k) >> 3) * 8192 + ((k) & 7) * 128)
        const int ft = (g & 1) * 32768 + (g >> 1) * 4 + (int)WS_FLAG1 + (lw == 0 ? ct * 1024 : NIL);
        int vall = FLINE(n) + ft + h * 16384;
        ring.voF = (n >> 2) == xq ? vall : NIL;
        ring.fl = lds + L_YB + lw * (h == 0 ? 8192 : 1024) + 4096 + 256;
        unsigned sa = (unsigned)(size_t)lds + L_YB + 4096 + 256 + lane * 4;
        const unsigned tg = (unsigned)__builtin_amdgcn_readfirstlane((int)ctl1[0]);
        float wsum = __uint_as_float(ctl1[1]);
        ring.lb = lw * 4096;
        int cq[3], cp[3];
        unsigned mk[3];
#pragma unroll
        for (int k = 0; k < 3; ++k) {
            const int nb = xq * 4 + NBR(k + 1);
            cq[k] = nb * 256; cp[k] = at_tile(cq[k], ring.par); mk[k] = 0x10000u << nb;
            asm volatile("" : "+s"(cq[k]), "+s"(cp[k]), "+s"(mk[k]));
        }
        unsigned m2 = (mk[0] | mk[1] | mk[2]) >> 16;
        asm volatile("" : "+s"(m2), "+s"(ring.rP));
        asm volatile("" : "+v"(vall), "+v"(ring.voF), "+v"(ring.voP[0]), "+v"(sa), "+v"(wsum));
        ring.rF = ring.rP;
        ring.voP[1] = ring.voP[2] = ring.voP[3] = ring.voP[0];
        WAIT_VM(4);
        BARRIER();
        SB();
        int ve = Y_OFF + lane * 16;
        unsigned yd = (unsigned)(size_t)lds + L_YB + lw * 8192, fa = 0u;
        auto fetch = [&]() __attribute__((always_inline)) {
            if (wid == 4) STAMP(21);
            const int v4[4] = {ve, ve, ve, ve};
#pragma unroll
            for (int k = 0; k < 2; ++k) dma_4k<true>(ring.rP, yd + k * 4096, v4, k * 4096);
        };
        __amdgpu_buffer_rsrc_t rQ2 = mk_rsrc(a.ci + (size_t)(rt * 4 + lw) * 32 * K, 32 * K * 2);
        int vC2 = (ct * 4 + g) * (K * 2) + ((xq * 1024 + 2048 + n * 128) & 4095);
        int vT2 = h == 0 && n < (KSHIFT + 1) / 2 ? FLINE((xq * 4 - 1 - n) & 15) + ft + 16384 : NIL;
        int vY = lane == 0 ? (int)WS_FLAGS + (rt * 8 + ct) * 128 + lw * 4 : NIL;
        int n1 = (h == 0 ? 32 : NT) - 9, n0 = n1 - 1 - (C2_START + idx % C2_STAG);
        int xm = XMODE ? (idx & 1) * 128 : 0;
        int c8 = (xq * 1024 + 1024) & 4095, p8 = at_tile(c8 ^ xm, ring.par);
        int cK = (xq * 1024 + (32 - KSHIFT) * 128) & 4095, pK = at_tile(cK ^ xm, ring.par) + at_panel(8);
        auto early = [&](int st, int i0, int i1) __attribute__((always_inline)) {
#pragma unroll
            for (int i = i0; i < i1; ++i)
                dma_nt(rD, (unsigned)(size_t)lds + (st * 4 + lw) * 8192 + i * 1024, vGe ^ ((2 * (i & 1) | (i & 4)) * 32), i * (4 * F_LEN) + st * (128 * F_LEN));
        };
        asm volatile("" : "+v"(vGe));
        asm volatile("" : "+s"(rQ2), "+s"(n0), "+s"(n1), "+s"(c8), "+s"(p8), "+s"(cK), "+s"(pK), "+s"(xm), "+s"(rD));
        asm volatile("" : "+v"(ve), "+v"(vC2), "+v"(vT2), "+v"(vY));
        unsigned have = 0u;
        SB();
        if (T_DELAY) __builtin_amdgcn_s_sleep(T_DELAY);
#define GATE(m) do { if (__builtin_expect(((m) & ~have) != 0u, 0)) ring.gate(tg, sa, m, have); SB(); } while (0)
#define TILE(k, o, st) do { ring.sp = cp[k] + (o) * 256; ring.sq = cq[k] + (o); ring.template issueP<16>(l0 + (st) * STAGE_BYTES); \
                            ring.issueQ(l0 + (st) * STAGE_BYTES); ring.sample(); SB(); } while (0)
        ring.gate(tg, sa, mk[0], have);
        if (wid == 4) STAMP(28);
        ring.sp = cp[0]; ring.template issueP<16>(l0 + 2 * STAGE_BYTES); ring.sample();
        BARRIER();
        GATE(mk[1]);
        ring.sp = cp[1]; ring.template issueP<16>(l0 + 3 * STAGE_BYTES); ring.sample();
        WAIT_VM(6); BARRIER();
        if (wid == 4) STAMP(30);
        GATE(mk[2]);
        TILE(2, 0, 0);
        GATE(m2);
        ring.voF = vall;
        TILE(0, 128, 1);
        WAIT_VM(19); BARRIER();
        TILE(1, 128, 2);
        WAIT_VM(19); BARRIER();
        ring.sp = cp[2] + 128 * 256; ring.template issueP<16>(l0 + 3 * STAGE_BYTES);
        ring.sq = cq[2] + 128;
        SB();
        {
            const unsigned long long m = __ballot(ring.peek(sa) == tg);
            have = (unsigned)m & (unsigned)(m >> 32);
            if (__builtin_expect(have != 0xffffffffu, 0)) ring.gate(tg, sa, 0xffffffffu, have);
        }
        ring.fl -= 256;
        ring.voF = vT2;
        SB();
        ring.issueQ(l0 + 3 * STAGE_BYTES);
#undef GATE
#undef TILE
        ring.sq = c8; ring.sp = p8; ring.poff = 0; ring.xm = xm;
        if (wid == 4) { STAMP(29); CSTAMP(34); STAMP(36); }
        ring.template steady<true, false, 18>(n1, n0, vC2);
        ring.fl += 512;
        if (wid == 4) { CSTAMP(35); STAMP(37); }
        ring.template issueP<16>(lds + ring.lb);
        SB();
        {
            const unsigned long long m = __ballot(ring.peek(sa + 256) == tg);
            have = (unsigned)m & (unsigned)(m >> 32);
            asm volatile("" : "+s"(have));
        }
        int hs = h;
        asm volatile("" : "+s"(hs));
        if (__builtin_expect(hs == 0, 1)) {
            constexpr unsigned MK = ((1u << ((KSHIFT + 1) / 2)) - 1u) * 0x10001u;
            if (__builtin_expect((MK & ~have) != 0u, 0)) ring.gate(tg, sa + 256, MK, have);
            unsigned lq = ring.lb + TILE_BYTES, pp = ring.par | at_panel(8);
            int sq = ring.sq ^ ring.xm, cnt = KSHIFT;
            ring.lb = (ring.lb + STAGE_BYTES) & (RING_BYTES - 1);
            ring.fl -= 512;
            fa = (unsigned)(size_t)ring.fl;
            asm volatile("" : "+s"(lq), "+s"(sq), "+s"(cnt), "+s"(ring.lb), "+s"(pp), "+s"(yd));
            asm volatile("" : "+v"(fa));
            SB();
            dma_4k<false>(ring.rQ, lq, ring.voQ, sq);
            ring.voF = vY;
            ring.rQ = rQ2;
            ring.sq = cK; ring.sp = pK; ring.poff = 0; ring.par = pp;
            ring.template steady<true, true>(cnt);
            auto seen = [&]() __attribute__((always_inline)) { return __builtin_amdgcn_readfirstlane(ring.peek(fa)) == 1u; };
            if (__builtin_expect(seen(), 1)) {
                fetch();
                WAIT_VM(16); BARRIER();
                WAIT_VM(8); BARRIER();
            } else {
                ring.sample();
                WAIT_VM(10); BARRIER();
                const bool have = seen();
                if (have) { fetch(); WAIT_VM(9); } else { ring.sample(); WAIT_VM(2); }
                BARRIER();
                if (!have) {
#pragma unroll 1
                    for (int spin = 0; spin <= SPIN_CAP && !seen(); ++spin) { ring.sample(); WAIT_VM(0); }
                    fetch();
                }
            }
            if (wid == 4) STAMP(20);
            WAIT_VM0();
        } else {
            ring.issueQ(lds + ring.lb);
            WAIT_VM(16); BARRIER();
            early(0, 0, G_N);
            wait_vm<8 + G_N>(); BARRIER();
            early(1, 0, G2_N);
            wait_vm<G_N + G2_N>(); BARRIER();
            early(1, G2_N, G2_N + G3_N);
        }
        BARRIER();
        if (h == 0) {
            const int slot = sidx;
            LAS unsigned char* abuf = lds + L_ATAB + 2048 + lw * 128;
#pragma unroll
            for (int i = 0; i < WD_EARLY; ++i) load_wd(i);
            SB();
            __amdgpu_buffer_rsrc_t rS = mk_rsrc(w2 + WS_STATS + (hp_ & 15) * 32768 + (hp_ >> 4) * 8192, 24576);
            __amdgpu_buffer_rsrc_t rC = mk_rsrc(w2 + WS_CAND + (size_t)hp_ * 4096, 4096);
            __amdgpu_buffer_rsrc_t rKy = mk_rsrc(w2 + WS_KEYS + (hp_ >> 2) * KEY_S + (hp_ & 3) * 4, ct == 0 ? 8192 + 3 * KEY_S * 16 + 4 : 0);
            int vs = lane * 16, vk = !(lane & 1) ? 1 << 20 : (lane >> 2) * 8192 + (lane & 3) * (KEY_S * 16);
            int vc = (lane < 32 ? 2048 : 0) + ct * 256 + (lane & 31) * 8;
            unsigned code = 0xffffu - (unsigned)(hp_ * 64 + lane), hb = (lane & 31) * 8;
            unsigned ra = (unsigned)(size_t)(rstat + (wq * 64 + lane) * 2), ht = (unsigned)(size_t)lds + L_HT + wq * 16384;
            u64 lo = 0xFFFFFFFFull;
            asm volatile("" : "+s"(rS), "+s"(rC), "+s"(rKy), "+s"(ht), "+s"(lo));
            asm volatile("" : "+v"(vs), "+v"(vk), "+v"(vc), "+v"(code), "+v"(hb), "+v"(ra));
            SB();
            BARRIER();
            __builtin_amdgcn_s_setprio(TAIL_PRIO);
            if (lane < 16) *(LAS unsigned*)(abuf + 64 + lane * 4) = 0u;
            __builtin_amdgcn_s_sleep(STAT_DELAY);
            if (wp == 0) {
                float s1, s2, s3;
#pragma unroll 1
                for (int spin = 0;; ++spin) {
                    asm volatile("" ::: "memory");
                    u32x3 sv[16];
#pragma unroll
                    for (int j = 0; j < 16; ++j) { sv[j] = __builtin_amdgcn_raw_buffer_load_b96(rS, vs, (j >> 3) * 16384 + (j & 7) * 1024, 16); SB(); }
#pragma unroll
                    for (int j = 0; j < 16; ++j) {
                        const unsigned u0 = sv[j][0], u1 = sv[j][1], u2 = sv[j][2];
                        s1 = j ? s1 + __uint_as_float(u0) : __uint_as_float(u0);
                        s2 = j ? s2 + __uint_as_float(u1) : __uint_as_float(u1);
                        s3 = j ? s3 + __uint_as_float(u2) : __uint_as_float(u2);
                    }
                    float z = s1 + s3;
                    asm volatile("" : "+v"(z));
                    if (__builtin_expect(__builtin_amdgcn_ballot_w64(__builtin_isunordered(z, s2)) == 0, 1)) break;
                    unsigned mx = 0u;
#pragma unroll
                    for (int j = 0; j < 16; ++j) mx = max(max(mx, sv[j][0]), max(sv[j][1], sv[j][2]));
                    if (__all(mx != SENT) || spin > SPIN_CAP) break;
                }
                if (wid == 4) STAMP(4);
                const float mean = s1 * (1.0f / D_LEN);
                const float var = fmaxf(s2 * (1.0f / D_LEN) - mean * mean, 0.f);
                const float mb = rbf(mean), rden = __builtin_amdgcn_rcpf(rbf(__builtin_amdgcn_sqrtf(rbf(rbf(var) + 1e-5f))));
                const unsigned kb = wave_max_s(r2key((s3 - mb * wsum) * rden, code));
                __builtin_amdgcn_raw_buffer_store_b32(kb, rKy, vk, 0, 16);
                SB();
                {
                    const unsigned cr_ = ~kb & 63u;
                    u32x2 c = *(const LAS u32x2*)(size_t)(ht + cr_ * 256 + (hb ^ ((cr_ & 15) * 16)));
                    const float m_ = __int_as_float(__builtin_amdgcn_readlane(__float_as_int(mb), cr_));
                    const float d_ = __int_as_float(__builtin_amdgcn_readlane(__float_as_int(rden), cr_));
#pragma unroll
                    for (int i = 0; i < 2; ++i) {
                        const unsigned u = c[i];
                        const unsigned dd = pk_bf16(bflo(u) - m_, bfhi(u) - m_);
                        const unsigned q = pk_bf16(bflo(dd) * d_, bfhi(dd) * d_);
                        c[i] = min(__float_as_uint(pick(lo, __uint_as_float(u), __uint_as_float(q))), SENT - 1u);
                    }
                    __builtin_amdgcn_raw_buffer_store_b64(c, rC, vc, 0, 16);
                    SB();
                    *(LAS u32x2*)(size_t)ra = (u32x2){__float_as_uint(mb), __float_as_uint(rden)};
                    if (lane == 0) ctl[2 + wq] = cr_;
                }
                WAIT_LGKM0();
                if (wid == 4) STAMP(5);
            }
            BARRIER();
#pragma unroll
            for (int i = WD_EARLY; i < 16; ++i) load_wd(i);
            if (slot < 112 && wid < 8 - BQ) {
                WAIT_VM0();
                if (wid == 4) STAMP(7);
                stage2(w2, slot, lw, lds + L_WD0 + lw * 8192, lds + L_WD1 + lw * 8192, abuf, 64, lane);
                if (wid == 4) STAMP(11);
            }
            FLUSH();
            return;
        }
    }

    P2_DEFS;
    LAS float* red = (LAS float*)(lds + L_RED);
    if (sidx < 112) {
        const int slot = sidx, grp = slot >> 3, kq = slot & 7;
        const __amdgpu_buffer_rsrc_t rG = mk_rsrc(a.wg, D_LEN * F_LEN * 2), rU = mk_rsrc(a.wu, D_LEN * F_LEN * 2);
        auto pair = [&](int i) {
            const int d = wid * 16 + i * 2 + (lane >> 5), ky = (d & 3) | ((d >> 1) & 4), c = lane & 31;
            const int vgu = ((kq * 128 + d) * F_LEN + grp * 256 + (((c >> 1) ^ ky) * 16) + (c & 1) * 8) * 2;
            if (i >= (wid < 4 ? G_N : G2_N + G3_N)) dma_nt(rG, (unsigned)(size_t)lds + wid * 8192 + i * 1024, vgu, 0);
            dma_nt(rU, (unsigned)(size_t)lds + 65536 + wid * 8192 + i * 1024, vgu ^ 512, 0);
        };
        if (wid >= 4) {
            if (W_PRE) __builtin_amdgcn_s_sleep(W_PRE_SL);
#pragma unroll
            for (int i = 0; i < W_PRE; ++i) pair(i);
        }
        BARRIER();
        if (wid < 4) {
#pragma unroll
            for (int i = 0; i < W_PRE; ++i) pair(i);
        }
#pragma unroll
        for (int i = W_PRE; i < 8; ++i) pair(i);
        if (wid >= 8 - BQ) {
            const __amdgpu_buffer_rsrc_t rD = mk_rsrc(a.wd + (size_t)slot * 32 * D_LEN, 32 * D_LEN * 2);
            const int vd = wd_off(wid - 4, 0, lane);
#pragma unroll
            for (int i = 0; i < 16; ++i)
                dma_nt(rD, (unsigned)(size_t)lds + L_YB + (wid & 1) * 16384 + i * 1024, vd ^ ((2 * (i & 1) | (i & 4)) * 32), i * (4 * D_LEN));
        }
        const __amdgpu_buffer_rsrc_t rK = mk_rsrc(w2, wid == 0 ? (unsigned)(WS_CAND + 32 * 4096) : 0u);
        const int ok_ = lane < 40 ? (int)WS_KEYS + (xcd >> 2) * 8192 + (xcd & 3) * (KEY_S * 16) + (lane - 32) * KEY_S : (int)(WS_CAND + 32 * 4096);
        const int oc = (int)WS_CAND + 2048 + kq * 256 + n * 16;
        int best = 0;
        u32x4 v = {0u, 0u, 0u, 0u};
        auto request = [&]() {
            asm volatile("" ::: "memory");
            v = LD16_SC1(rK, lane < 32 ? oc + best * 4096 : ok_);
            SB();
        };
        auto settled = [&]() {
            const unsigned mx = umax4(v), mn = min(min(v[0], v[1]), min(v[2], v[3]));
            unsigned fv = lane < 32 ? ~mx : mn | (unsigned)(lane >> 3 ^ 4);
            asm volatile("" : "+v"(fv));
            const bool fine = fv != 0u;
            unsigned km = max(mx, dppu<0xB1>(mx));
            km = max(km, dppu<0x4E>(km));
            km = max(km, dppu<0x141>(km));
            const int nb = (int)(((0xffffu - ((unsigned)__builtin_amdgcn_readlane((int)km, 32) & 0xffffu)) >> 6) & 31u);
            const bool same = nb == best;
            best = nb;
            return __all(fine) && same;
        };
        if (wid == 0) {
            request();
            WAIT_VM(0);
            settled();
            request();
        } else if (wid >= 8 - BQ) WAIT_VM(16);
        else WAIT_VM(0);
        BARRIER();
        if (wid == 0) STAMP(8);
        bf16x8 wfr[4][4];
        {
            const int q = (lane >> 2) & 3, ky = q | ((g & 1) << 2);
#pragma unroll
            for (int u = 0; u < 4; ++u) {
                const int tro = (u >> 1) * 65536 + (8 * g + q) * 512 + (((wid * 2 + (u & 1)) ^ ky) * 32) + (lane & 3) * 8;
#pragma unroll
                for (int ks = 0; ks < 4; ++ks) {
                    const bf16x4 w0 = __builtin_amdgcn_ds_read_tr16_b64_v4i16((LAS bf16x4*)(lds + tro + ks * 16384));
                    const bf16x4 w1 = __builtin_amdgcn_ds_read_tr16_b64_v4i16((LAS bf16x4*)(lds + tro + ks * 16384 + 2048));
                    wfr[u][ks] = __builtin_shufflevector(w0, w1, 0, 1, 2, 3, 4, 5, 6, 7);
                }
            }
            SB();
#pragma unroll
            for (int i = 0; i < 16; ++i) asm volatile("" : "+v"(wfr[i >> 2][i & 3]));
        }
        if (wid >= 8 - BQ && lane < 16) *(LAS unsigned*)(lds + 8192 + wid * 64 + lane * 4) = 0u;
        __amdgpu_buffer_rsrc_t rPo = mk_rsrc(w2 + WS_PRE, 112 * 2048);
        int po = (n & 3) == 0 ? pre_off((grp ^ (g >> 1)) * 8 + wid) + kq * 256 + g * 64 + n * 4 : 112 * 2048;
        unsigned xw = (unsigned)(size_t)lds + (n >> 3) * 16384 + (g & 1) * 8192 + (n & 7) * 8, xr = (unsigned)(size_t)lds + (n & 1) * 8192 + g * 16;
        unsigned ix = (n & 1) ? 0xEEEEEEEEu : 0x44444444u;
        asm volatile("" : "+v"(po), "+v"(xw), "+v"(xr), "+v"(ix));
        u64 m1 = 0xFFFF0000FFFF0000ull, m2 = 0xFFFFFFFF00000000ull;
        f32x4 ac[4];
#pragma unroll
        for (int u = 0; u < 4; ++u) { ac[u] = (f32x4){0.f, 0.f, 0.f, 0.f}; asm volatile("" : "+v"(ac[u])); }
        asm volatile("" : "+s"(rPo), "+s"(m1), "+s"(m2));
        if (wid == 0) {
#pragma unroll 1
            for (int spin = 0; spin <= SPIN_CAP && !settled(); ++spin) {
                __builtin_amdgcn_s_sleep(1);
                request();
            }
            __builtin_amdgcn_s_waitcnt(0x0F70);
            if (lane < 32) *(LAS u32x2*)(size_t)xw = (u32x2){lane < 16 ? v[0] : v[1], lane < 16 ? v[2] : v[3]};
            WAIT_LGKM0();
            STAMP(9);
        }
        BARRIER();
        {
            bf16x8 xf[2];
#pragma unroll
            for (int j = 0; j < 2; ++j) xf[j] = *(const LAS bf16x8*)((const LAS unsigned char*)(size_t)xr + j * 16384);
            SB();
            float rs[3];
#pragma unroll
            for (int i = 0; i < 8; ++i) {
                const int u = i >> 1, j = i & 1;
                ac[u] = smf(xf[j], wfr[u][2 * j], wfr[u][2 * j + 1], ac[u], ix);
                SB();
                if (i == 4) { asm volatile("" : "+v"(ac[0])); rs[0] = ac[0][0] + ac[0][1]; asm volatile("" : "+v"(rs[0])); }
                if (i == 6) { asm volatile("" : "+v"(ac[1])); rs[0] = pick(m1, rs[0], ac[1][0] + ac[1][1]); asm volatile("" : "+v"(rs[0])); }
                if (i == 7) { asm volatile("" : "+v"(ac[2])); rs[2] = ac[2][0] + ac[2][1]; asm volatile("" : "+v"(rs[2])); }
                SB();
            }
            const unsigned e = min(__float_as_uint(pick(m2, rs[0], pick(m1, rs[2], ac[3][0] + ac[3][1]))), SENT - 1u);
            const u32x4 sv = {e, dppu<0x55>(e), dppu<0xAA>(e), dppu<0xFF>(e)};
            ST16_SC1(sv, rPo, po);
            SB();
            asm volatile("" :: "v"(v));
        }
        if (wid == 0) STAMP(14);
        if (wid >= 8 - BQ) {
            WAIT_VM(1);
            if (wid == 7) STAMP(15);
            stage2(w2, slot, wid - 4, lds + L_YB + (wid & 1) * 16384, lds + L_YB + (wid & 1) * 16384 + 8192, lds + wid * 64, 8192, lane);
            if (wid == 7) STAMP(10);
        }
    } else {
        const int rj = sidx - 112;
        if (blockIdx.x == 255 && tid == 0) {
            const u64 want = ((u64)ctl1[3] << 32) | ctl1[2];
            __hip_atomic_store((GAS u64*)(ws + WS_TAG), want, __ATOMIC_RELAXED, __HIP_MEMORY_SCOPE_AGENT);
        }
        if (wid == 4) {
            unsigned kk;
            const unsigned k = poll_winner(w2, xcd, lane, kk);
            if (lane == 0) ctl[4] = k;
            if (lane < 32) ctl[8 + lane] = kk;
            WAIT_LGKM0();
        }
        BARRIER();
        const unsigned kw = CTL(4);
        if (wid == 0) STAMP(16);
        {
            const unsigned kc = ctl[8 + wid * 4 + g];
            const __amdgpu_buffer_rsrc_t rL = mk_rsrc(w2 + WS_CAND + (size_t)wid * 16384, 16384);
            u32x2 hv2, nv2;
            for (int spin = 0;; ++spin) {
                asm volatile("" ::: "memory");
                hv2 = __builtin_amdgcn_raw_buffer_load_b64(rL, g * 4096 + (rj * 64 + n * 4) * 2, 0, 16);
                nv2 = __builtin_amdgcn_raw_buffer_load_b64(rL, g * 4096 + 2048 + (rj * 64 + n * 4) * 2, 0, 16);
                if (__all(max(max(hv2[0], hv2[1]), max(nv2[0], nv2[1])) != SENT) || spin > SPIN_CAP) break;
                __builtin_amdgcn_s_sleep(1);
            }
            u32x2 ov;
#pragma unroll
            for (int i = 0; i < 2; ++i) {
                const unsigned u = hv2[i], q = nv2[i];
                ov[i] = pk_bf16(bflo(u) + bflo(q), bfhi(u) + bfhi(q));
            }
            const int crow = (int)(0xffffu - (kc & 0xffffu)) & (S_LEN - 1);
            if (kc != kw) *(u32x2*)(a.out + (size_t)crow * D_LEN + rj * 64 + n * 4) = ov;
        }
        const int wrow = (int)(0xffffu - (kw & 0xffffu)) & (S_LEN - 1);
        const __amdgpu_buffer_rsrc_t rC = mk_rsrc(w2 + WS_CAND + (size_t)(wrow >> 6) * 4096, 4096);
        const __amdgpu_buffer_rsrc_t rPt = mk_rsrc(w2 + WS_PART, 112 * 4096);
        const int ocol = (rj >> 2) * 256 + ((lane >> 1) & 1) * 128 + (rj & 3) * 32 + (lane & 1) * 16 + (lane >> 2);
        unsigned hv, hnv;
        for (int spin = 0;; ++spin) {
            asm volatile("" ::: "memory");
            hv = __builtin_amdgcn_raw_buffer_load_b32(rC, (ocol & ~1) * 2, 0, 16);
            hnv = __builtin_amdgcn_raw_buffer_load_b32(rC, 2048 + (ocol & ~1) * 2, 0, 16);
            if (__all(max(hv, hnv) != SENT) || spin > SPIN_CAP) break;
        }
        float sgm = rbf(1.0f / (1.0f + __expf(-key2r(kw))));
        float hf = (ocol & 1) ? bfhi(hv) : bflo(hv), hnf = (ocol & 1) ? bfhi(hnv) : bflo(hnv);
        GAS unsigned short* optr = (GAS unsigned short*)(a.out + (size_t)wrow * D_LEN + ocol);
        unsigned rw = (unsigned)(size_t)red + (wid >> 2) * 1024 + (n * 4 + g) * 16 + (wid & 3) * 4, rr = (unsigned)(size_t)red + lane * 16;
        int po = wid * 16384 + (rj >> 3) * 8192 + g * 2048 + ((rj & 7) * 64 + n * 4) * 4;
        int po3 = po + 3 * 131072;
        asm volatile("" : "+v"(sgm), "+v"(optr), "+v"(hf), "+v"(hnf), "+v"(rw), "+v"(rr), "+v"(po), "+v"(po3));
        float sm;
#pragma unroll 1
        for (int spin = 0;; ++spin) {
            asm volatile("" ::: "memory");
            u32x4 pv[4];
#pragma unroll
            for (int i = 0; i < 4; ++i) pv[i] = __builtin_amdgcn_raw_buffer_load_b128(rPt, i < 3 ? po : po3, i < 3 ? i * 131072 : 0, 16);
            SB();
            unsigned t[4];
#pragma unroll
            for (int j = 0; j < 4; ++j) {
                const unsigned u0 = pv[0][j], u1 = pv[1][j], u2 = pv[2][j], u3 = pv[3][j];
                t[j] = __float_as_uint(((__uint_as_float(u0) + __uint_as_float(u1)) + __uint_as_float(u2)) + __uint_as_float(u3));
            }
            const auto r0 = __builtin_amdgcn_permlane32_swap(t[0], t[2], false, false), r1 = __builtin_amdgcn_permlane32_swap(t[1], t[3], false, false);
            const unsigned a0 = r0[0], a1 = r0[1], b0 = r1[0], b1 = r1[1];
            const auto r2 = __builtin_amdgcn_permlane16_swap(__float_as_uint(__uint_as_float(a0) + __uint_as_float(a1)),
                                                             __float_as_uint(__uint_as_float(b0) + __uint_as_float(b1)), false, false);
            const unsigned c0 = r2[0], c1 = r2[1];
            sm = __uint_as_float(c0) + __uint_as_float(c1);
            if (__builtin_expect(__builtin_amdgcn_ballot_w64(sm != sm) == 0, 1)) break;
            if (__all(max(max(umax4(pv[0]), umax4(pv[1])), max(umax4(pv[2]), umax4(pv[3]))) != SENT) || spin > SPIN_CAP) break;
            if (FIN_SLEEP) __builtin_amdgcn_s_sleep(FIN_SLEEP);
        }
        if (wid == 0) STAMP(17);
        *(LAS float*)(size_t)rw = sm;
        WAIT_LGKM0();
        BARRIER();
        if (wid == 0) {
            const f32x4 u = *(const LAS f32x4*)(size_t)rr, v = *(const LAS f32x4*)((const LAS unsigned char*)(size_t)rr + 1024);
            const f32x4 w = u + v;
            const float t = (w[0] + w[2]) + (w[1] + w[3]);
            *optr = (unsigned short)pk_bf16(hf + rbf(hnf + rbf(rbf(t) * sgm)), 0.f);
            STAMP(18);
        }
        __builtin_amdgcn_s_waitcnt(0x0F70);
    }
    FLUSH();
}

extern "C" void kernel_launch(void* const* d_in, const int* in_sizes, int n_in, void* d_out, int out_size, void* d_ws,
                              size_t ws_size, hipStream_t stream) {
    Args a;
    a.x = (const bf16_t*)d_in[0];
    a.cr = (const bf16_t*)d_in[1];
    a.ci = (const bf16_t*)d_in[2];
    a.wre = (const bf16_t*)d_in[3];
    a.wim = (const bf16_t*)d_in[4];
    a.w_r = (const bf16_t*)d_in[6];
    a.wg = (const bf16_t*)d_in[7];
    a.wu = (const bf16_t*)d_in[8];
    a.wd = (const bf16_t*)d_in[9];
    a.out = (bf16_t*)d_out;
    a.ws = (char*)d_ws + (WS_END ? ((ws_size - ((size_t)24 << 20)) & ~(size_t)255) : 0);
    (void)hipFuncSetAttribute((const void*)fused_kernel, hipFuncAttributeMaxDynamicSharedMemorySize, LDS_BYTES);
    fused_kernel<<<256, 768, LDS_BYTES, stream>>>(a);
}
```

```cpp
#include <hip/hip_runtime.h>
#include <hip/hip_bf16.h>
#include <stdint.h>

typedef unsigned short bf16_t;
typedef short bf16x8 __attribute__((ext_vector_type(8)));
typedef short bf16x4 __attribute__((ext_vector_type(4)));
typedef float f32x4 __attribute__((ext_vector_type(4)));
typedef unsigned u32x4 __attribute__((ext_vector_type(4)));
typedef unsigned u32x2 __attribute__((ext_vector_type(2)));
typedef unsigned u32x3 __attribute__((ext_vector_type(3)));
typedef unsigned long long u64;
#define LAS __attribute__((address_space(3)))
#define GAS __attribute__((address_space(1)))

constexpr int S_LEN = 2048, D_LEN = 1024, F_LEN = 3584;
constexpr unsigned SENT = 0xFFFFFFFFu;
constexpr int SPIN_CAP = 1 << 17;

constexpr size_t WS_KEYS = 0;
constexpr size_t WS_FLAGS = 16384;
constexpr size_t WS_XRD = 65536;
constexpr size_t WS_FLAG1 = 196608;
constexpr size_t WS_TAG = 131072;
constexpr size_t WS_VOTE = WS_TAG + 4096;
constexpr size_t WS_PROBE = (size_t)20 << 20;
constexpr size_t WS_AT = (size_t)4 << 20;
constexpr size_t WS_Y = (size_t)12 << 20;
constexpr size_t WS_STATS = (size_t)16 << 20;
constexpr size_t WS_PART = WS_STATS + (512u << 10);
constexpr size_t WS_CAND = WS_STATS + (1024u << 10);
constexpr size_t WS_PRE = WS_STATS + (1152u << 10);
constexpr size_t FILL_BYTES = (size_t)1408 << 10;

__device__ __forceinline__ float bflo(unsigned v) { return __uint_as_float(v << 16); }
__device__ __forceinline__ float bfhi(unsigned v) { return __uint_as_float(v & 0xffff0000u); }
__device__ __forceinline__ unsigned pk_bf16(float lo, float hi) {
    typedef __bf16 bf2 __attribute__((ext_vector_type(2)));
    typedef float f2 __attribute__((ext_vector_type(2)));
    f2 v = {lo, hi};
    bf2 r = __builtin_convertvector(v, bf2);
    return __builtin_bit_cast(unsigned, r);
}
__device__ __forceinline__ float rbf(float x) { return __uint_as_float(pk_bf16(0.f, x)); }

template <int CTRL> __device__ __forceinline__ unsigned dppu(unsigned v) {
    return (unsigned)__builtin_amdgcn_update_dpp(0, (int)v, CTRL, 0xf, 0xf, true);
}
template <int CTRL> __device__ __forceinline__ float dppf(float v) { return __uint_as_float(dppu<CTRL>(__float_as_uint(v))); }
__device__ __forceinline__ void swap16(unsigned v, unsigned& a, unsigned& b) {
    auto r = __builtin_amdgcn_permlane16_swap(v, v, false, false);
    a = r[0]; b = r[1];
}
__device__ __forceinline__ void swap32(unsigned v, unsigned& a, unsigned& b) {
    auto r = __builtin_amdgcn_permlane32_swap(v, v, false, false);
    a = r[0]; b = r[1];
}
__device__ __forceinline__ float sum_rows(float v) {
    unsigned a, b;
    swap16(__float_as_uint(v), a, b); v = __uint_as_float(a) + __uint_as_float(b);
    swap32(__float_as_uint(v), a, b); v = __uint_as_float(a) + __uint_as_float(b);
    return v;
}
__device__ __forceinline__ float wave_sum(float v) {
    v += dppf<0xB1>(v);
    v += dppf<0x4E>(v);
    v += dppf<0x141>(v);
    v += dppf<0x140>(v);
    return sum_rows(v);
}
__device__ __forceinline__ unsigned wave_max_u32(unsigned v) {
    v = max(v, dppu<0xB1>(v));
    v = max(v, dppu<0x4E>(v));
    v = max(v, dppu<0x141>(v));
    v = max(v, dppu<0x140>(v));
    unsigned a, b;
    swap16(v, a, b); v = max(a, b);
    swap32(v, a, b); v = max(a, b);
    return v;
}
__device__ __forceinline__ unsigned wave_max_s(unsigned v) {
    v = max(v, dppu<0xB1>(v));
    v = max(v, dppu<0x4E>(v));
    v = max(v, dppu<0x141>(v));
    v = max(v, dppu<0x140>(v));
    v = max(v, dppu<0x142>(v));
    v = max(v, dppu<0x143>(v));
    return (unsigned)__builtin_amdgcn_readlane((int)v, 63);
}
__device__ __forceinline__ float sel4(int g, float a, float b, float c, float d) {
    const float t0 = (g & 1) ? b : a, t1 = (g & 1) ? d : c;
    return (g & 2) ? t1 : t0;
}
__device__ __forceinline__ float pick(u64 m, float a, float b) {
    float r;
    asm("v_cndmask_b32 %0, %1, %2, %3" : "=v"(r) : "v"(a), "v"(b), "s"(m));
    return r;
}
__device__ __forceinline__ unsigned umax4(u32x4 v) { return max(max(v[0], v[1]), max(v[2], v[3])); }

__device__ __forceinline__ void touch_line(const void* p, unsigned lds_dst) {
    unsigned keep;
    asm volatile("s_mov_b32 %0, m0\n\ts_mov_b32 m0, %2\n\ts_nop 0\n\tglobal_load_lds_dword %1, off\n\ts_mov_b32 m0, %0"
                 : "=&s"(keep) : "v"(p), "s"(lds_dst) : "memory");
}
__device__ __forceinline__ __amdgpu_buffer_rsrc_t mk_rsrc(const void* p, unsigned bytes) {
    return __builtin_amdgcn_make_buffer_rsrc((void*)p, 0, (int)bytes, 0x00020000);
}
#define LDS_LOAD(w, v, o, pol) "buffer_load_" w " " v ", %0, %2 offen" o pol " lds"
#define LDS_M0 "s_mov_b32 m0, %1\n\ts_nop 0\n\t"
__device__ __forceinline__ void dma_nt(__amdgpu_buffer_rsrc_t r, unsigned dst, int voff, int soff) {
    asm volatile(LDS_M0 LDS_LOAD("dwordx4", "%3", "", " nt") :: "s"(r), "s"(dst), "s"(soff), "v"(voff) : "memory", "m0");
}
template <bool SC1> __device__ __forceinline__ void dma_word(__amdgpu_buffer_rsrc_t r, unsigned dst, int voff) {
    if constexpr (SC1) asm volatile(LDS_M0 LDS_LOAD("dword", "%3", "", " sc1") :: "s"(r), "s"(dst), "n"(0), "v"(voff) : "memory", "m0");
    else asm volatile(LDS_M0 LDS_LOAD("dword", "%3", "", "") :: "s"(r), "s"(dst), "n"(0), "v"(voff) : "memory", "m0");
}
template <bool SC1> __device__ __forceinline__ void dma_2k(__amdgpu_buffer_rsrc_t r, unsigned dst, int v0, int v1, int soff) {
    if constexpr (SC1) asm volatile(LDS_M0 LDS_LOAD("dwordx4", "%3", "", " sc1") "\n\t" LDS_LOAD("dwordx4", "%4", " offset:1024", " sc1")
                 :: "s"(r), "s"(dst), "s"(soff), "v"(v0), "v"(v1) : "memory", "m0");
    else asm volatile(LDS_M0 LDS_LOAD("dwordx4", "%3", "", "") "\n\t" LDS_LOAD("dwordx4", "%4", " offset:1024", "")
                 :: "s"(r), "s"(dst), "s"(soff), "v"(v0), "v"(v1) : "memory", "m0");
}
template <bool SC1> __device__ __forceinline__ void dma_4k(__amdgpu_buffer_rsrc_t r, unsigned dst, const int (&v)[4], int soff) {
#define FOUR(pol) asm volatile(LDS_M0 LDS_LOAD("dwordx4", "%3", "", pol) "\n\t" LDS_LOAD("dwordx4", "%4", " offset:1024", pol) "\n\t" \
        LDS_LOAD("dwordx4", "%5", " offset:2048", pol) "\n\t" LDS_LOAD("dwordx4", "%6", " offset:3072", pol) \
        :: "s"(r), "s"(dst), "s"(soff), "v"(v[0]), "v"(v[1]), "v"(v[2]), "v"(v[3]) : "memory", "m0")
    if constexpr (SC1) FOUR(" sc1"); else FOUR("");
#undef FOUR
}
#define CODE_BYTES 25856
__device__ __forceinline__ __amdgpu_buffer_rsrc_t code_rsrc() {
    unsigned long long p; unsigned d;
    asm volatile("s_getpc_b64 %0\n1:\n\ts_mov_b32 %1, 1b-_Z12fused_kernel4Args" : "=s"(p), "=s"(d));
    return __builtin_amdgcn_make_buffer_rsrc((void*)(p - d), 0, CODE_BYTES, 0x00020000);
}
#define LD16_SC1(rs, off) __builtin_amdgcn_raw_buffer_load_b128(rs, off, 0, 16)
#define ST16_SC1(v, rs, off) __builtin_amdgcn_raw_buffer_store_b128(v, rs, off, 0, 16)

constexpr int BT = 128, BK = 64, NSTAGE = 4;
constexpr int TILE_BYTES = BT * BK * 2;
constexpr int STAGE_BYTES = 2 * TILE_BYTES;
constexpr int RING_BYTES = NSTAGE * STAGE_BYTES;
constexpr int L_DUMMY = RING_BYTES;
constexpr int L_CTL = RING_BYTES + 256;
constexpr int L_RED = RING_BYTES + 512;

#define WAIT_VM(n) asm volatile("s_waitcnt vmcnt(" #n ")" ::: "memory")
#define WAIT_VM0() do { asm volatile("" ::: "memory"); __builtin_amdgcn_s_waitcnt(0x0F70); asm volatile("s_waitcnt vmcnt(0)" ::: "memory"); } while (0)
#define WAIT_LGKM0() asm volatile("s_waitcnt lgkmcnt(0)" ::: "memory")
#define BARRIER() do { asm volatile("" ::: "memory"); __builtin_amdgcn_s_barrier(); asm volatile("" ::: "memory"); } while (0)

__device__ __forceinline__ int prow(int rho) {
    const int w = rho >> 6, m = (rho >> 4) & 3, q = (rho >> 2) & 3, r = rho & 3;
    return w * 64 + (m >> 1) * 32 + q * 8 + (m & 1) * 4 + r;
}
__device__ __forceinline__ unsigned r2key(float r, unsigned code) {
    const unsigned u = pk_bf16(0.f, r);
    return ((u ^ ((unsigned)((int)u >> 31) | 0x80000000u)) & 0xffff0000u) | code;
}
__device__ __forceinline__ float key2r(unsigned k) {
    const unsigned o = k >> 16;
    return bflo((o & 0x8000u) ? (o & 0x7fffu) : (~o & 0xffffu));
}
#define STAT_M 5
#define W_PRE 2
#define W_PRE_SL 20
#define BQ 2
#define POLL_T (STAT_M + 5)
#define AUX_SLEEP 12
#define WD_EARLY 2
#define C2_START 0
#define C2_STAG 7
#define TAIL_PRIO 3
#define STAT_DELAY 12
#define AUX_STAY 1
#define CATCH1 4
#define CPF_MASK1 0x1080u
#define CPF_MASK2 0x1080u
#define CPF_STAG 5
#define CPF_FIX 0x1000u
#define KEY_S 128
#define G2_N 2
#define G3_N 2
#define FIN_SLEEP 1
#define G_N 8
#define POLL_SLEEP 4
__device__ __forceinline__ f32x4 smf(bf16x8 a, bf16x8 b0, bf16x8 b1, f32x4 d, unsigned idx) {
    typedef __bf16 bf8 __attribute__((ext_vector_type(8)));
    typedef __bf16 bf16v __attribute__((ext_vector_type(16)));
    const auto b = __builtin_shufflevector(b0, b1, 0, 1, 2, 3, 4, 5, 6, 7, 8, 9, 10, 11, 12, 13, 14, 15);
    return __builtin_amdgcn_smfmac_f32_16x16x64_bf16(__builtin_bit_cast(bf8, a), __builtin_bit_cast(bf16v, b), d, (int)idx, 0, 0);
}
__device__ __forceinline__ float dot2bf(unsigned a, unsigned b, float c) {
    typedef __bf16 bf2 __attribute__((ext_vector_type(2)));
    return __builtin_amdgcn_fdot2_f32_bf16(__builtin_bit_cast(bf2, a), __builtin_bit_cast(bf2, b), c, false);
}

#define STAMP(i) do { } while (0)
#define CSTAMP(i) do { } while (0)
#define FLUSH() do { } while (0)
#define TSV do { } while (0)

#define KSHIFT 4

constexpr int L_HT = ((32 + KSHIFT) % NSTAGE) * STAGE_BYTES;
constexpr int L_WD0 = ((33 + KSHIFT) % NSTAGE) * STAGE_BYTES;
constexpr int L_WD1 = ((34 + KSHIFT) % NSTAGE) * STAGE_BYTES;
constexpr int L_YB = RING_BYTES;
constexpr int L_ATAB = L_YB;
constexpr int LDS_BYTES = RING_BYTES + 32768;

#define SB() __builtin_amdgcn_sched_barrier(0)
template <int N> __device__ __forceinline__ void wait_vm() { asm volatile("s_waitcnt vmcnt(%0)" :: "n"(N) : "memory"); }
__device__ __forceinline__ int at_tile(int c, int par) { return ((c << 8) & 0x78000) | (((c << 2) & 0x2000) ^ par); }
__device__ __forceinline__ int at_panel(int ep) { return ep << 19; }
__device__ __forceinline__ int pre_off(int slot) { const int s_ = slot >= 56, l = slot - 56 * s_; return (l >> 2) * 16384 + s_ * 8192 + (l & 3) * 2048; }
static_assert(KSHIFT == 4, "flag samples must have replaced older contents of their slot; even tile counts");
struct Ring {
    __amdgpu_buffer_rsrc_t rP, rQ, rF;
    int voP[4], voQ[4], voF;
    LAS unsigned char* fl;
    unsigned lb;
    int sp, sq;
    int poff, par, xm;
    bool img;
    template <int LD, bool PIMG> __device__ __forceinline__ void setup(int lw, int lane) {
        const int r = lane >> 3, v0 = (PIMG ? r : (r & 3) + (r & 4) * 2) * (LD * 2) + (((lane & 7) ^ (lane >> 4)) << 4);
#pragma unroll
        for (int j = 0; j < 4; ++j) {
            voQ[j] = (v0 ^ ((j & 1) * 64)) + (PIMG ? j * 8 : (j & 1) * 16 + (j >> 1) * 4) * (LD * 2) - j * 1024;
            voP[j] = PIMG ? (lw >> 1) * 16384 + (lw & 1) * 4096 + lane * 16 : voQ[j];
        }
        img = PIMG;
    }
    __device__ __forceinline__ void seek(int col, int pof) {
        sq = col; poff = pof;
        sp = (img ? at_tile(sq, par) : sq) + poff;
    }
    __device__ __forceinline__ void step() {
        sq = (sq + BK * 2) & 4095;
        sp = (img ? at_tile(sq ^ xm, par) : sq) + poff;
        asm volatile("" : "+s"(sq), "+s"(sp));
    }
    __device__ __forceinline__ void sample() { dma_word<true>(rF, (unsigned)(size_t)fl, voF); }
    __device__ __forceinline__ unsigned peek(unsigned addr) {
        unsigned v;
        asm volatile("ds_read_b32 %0, %1\n\ts_waitcnt lgkmcnt(0)" : "=v"(v) : "v"(addr) : "memory");
        return v;
    }
    __device__ __forceinline__ void gate(unsigned want, unsigned sa, unsigned need, unsigned& have) {
#pragma unroll 1
        for (int spin = 0; spin <= SPIN_CAP && (need & ~have) != 0u; ++spin) {
            if (spin) { sample(); WAIT_VM(0); __builtin_amdgcn_s_sleep(1); }
            const unsigned long long m = __ballot(peek(sa) == want);
            have = (unsigned)m & (unsigned)(m >> 32);
        }
    }
    template <int AUX> __device__ __forceinline__ void issueP(LAS unsigned char* base) { dma_4k<AUX != 0>(rP, (unsigned)(size_t)base, voP, sp); }
    __device__ __forceinline__ void issueQ(LAS unsigned char* base) { dma_4k<false>(rQ, (unsigned)(size_t)base + TILE_BYTES, voQ, sq ^ xm); }
    template <bool P2> __device__ __forceinline__ void issue(LAS unsigned char* lds) {
        issueP<P2 ? 16 : 0>(lds + lb);
        issueQ(lds + lb);
        SB();
        if (P2) sample();
        lb = (lb + STAGE_BYTES) & (RING_BYTES - 1);
        asm volatile("" : "+s"(lb));
        step();
        SB();
    }
    template <int CAT> __device__ __forceinline__ void ramp(LAS unsigned char* lds) {
#pragma unroll 1
        for (int i = 1; i <= CAT + 1; ++i) {
            issue<false>(lds);
            if (i == CAT + 1) continue;
            WAIT_VM(8);
            BARRIER();
        }
    }
    template <bool P2, bool SAMP, int W0 = 16> __device__ __forceinline__ void steady(int cnt, int trig = 0, int v2 = 0) {
        int t, u;
#define LD(v, r, s, o) "buffer_load_dwordx4 " v ", " r ", " s " offen" o " lds\n\t"
        if constexpr (P2) {
#define BODY(SMP, TST, ENT) asm volatile("s_mov_b32 m0, %0\n" ENT "1:\n\ts_waitcnt vmcnt(16)\n2:\n\ts_barrier\n\t" \
            LD("%8", "%6", "%2", " sc1") LD("%8", "%6", "%2", " offset:1024 sc1") LD("%8", "%6", "%2", " offset:2048 sc1") LD("%8", "%6", "%2", " offset:3072 sc1") \
            SMP "s_xor_b32 %4, %1, %18\n\ts_add_i32 %1, %1, 0x80\n\ts_add_i32 m0, %0, 0x4000\n\ts_and_b32 %1, %1, 0xfff\n\t" \
            LD("%9", "%7", "%4", "") "s_xor_b32 %2, %1, %18\n\ts_lshl_b32 %5, %1, 2\n\ts_lshl_b32 %2, %2, 8\n\ts_and_b32 %2, %2, 0x78000\n\t" \
            LD("%10", "%7", "%4", " offset:1024") "s_and_b32 %5, %5, 0x2000\n\ts_xor_b32 %5, %5, %13\n\ts_or_b32 %2, %2, %5\n\ts_add_i32 %3, %3, -1\n\t" \
            LD("%11", "%7", "%4", " offset:2048") "s_add_i32 %0, %0, 0x8000\n\ts_and_b32 %0, %0, 0x1ffff\n\t" TST "s_cmp_lg_u32 %3, 0\n\t" \
            LD("%12", "%7", "%4", " offset:3072") "\n4:\n\ts_mov_b32 m0, %0\n\ts_cbranch_scc1 1b\n\ts_waitcnt vmcnt(16)\n\ts_barrier" \
            : "+s"(lb), "+s"(sq), "+s"(sp), "+s"(cnt), "=&s"(t), "=&s"(u) \
            : "s"(rP), "s"(rQ), "v"(voP[0]), "v"(voQ[0]), "v"(voQ[1]), "v"(voQ[2]), "v"(voQ[3]), "s"(par | poff), "s"(rF), "v"(voF), "s"((unsigned)(size_t)fl), "n"(W0), "s"(xm), \
              "s"(trig), "v"(v2) \
            : "memory", "scc", "m0")
            if constexpr (SAMP) BODY("s_mov_b32 m0, %16\n\ts_nop 0\n\tbuffer_load_dword %15, %14, 0 offen sc1 lds\n\t", "", "");
            else BODY("", "s_cmp_eq_u32 %3, %19\n\ts_cbranch_scc1 3b\n\t",
                      "\ts_waitcnt vmcnt(%17)\n\ts_branch 2f\n3:\n\t" LD("%12", "%7", "%4", " offset:3072") "s_mov_b32 m0, %16\n\ts_nop 0\n\tbuffer_load_dword %20, %7, 0 offen lds\n\t"
                      "s_add_i32 m0, %16, 0x200\n\ts_cmp_lg_u32 %3, 0\n\tbuffer_load_dword %15, %14, 0 offen sc1 lds\n\ts_branch 4f\n");
#undef BODY
        } else {
            asm volatile("s_mov_b32 m0, %0\n\ts_branch 2f\n1:\n\ts_waitcnt vmcnt(16)\n\ts_barrier\n2:\n\t"
                LD("%6", "%4", "%1", "") LD("%7", "%4", "%1", " offset:1024") LD("%8", "%4", "%1", " offset:2048") LD("%9", "%4", "%1", " offset:3072")
                "s_add_i32 m0, %0, 0x4000\n\ts_mov_b32 %3, %1\n\t"
                LD("%6", "%5", "%3", "") "s_add_i32 %1, %1, 0x80\n\ts_add_i32 %0, %0, 0x8000\n\t"
                LD("%7", "%5", "%3", " offset:1024") "s_and_b32 %0, %0, 0x1ffff\n\ts_add_i32 %2, %2, -1\n\t"
                LD("%8", "%5", "%3", " offset:2048") "s_cmp_lg_u32 %2, 0\n\t"
                LD("%9", "%5", "%3", " offset:3072") "s_mov_b32 m0, %0\n\ts_cbranch_scc1 1b\n\ts_waitcnt vmcnt(16)\n\ts_barrier"
                : "+s"(lb), "+s"(sq), "+s"(cnt), "=&s"(t)
                : "s"(rP), "s"(rQ), "v"(voQ[0]), "v"(voQ[1]), "v"(voQ[2]), "v"(voQ[3])
                : "memory", "scc", "m0");
            (void)u;
        }
#undef LD
    }
};

template <bool PLIN, bool FIRST, class Pre>
__device__ __forceinline__ void compute_loop(const LAS unsigned char* lds, f32x4 (&acc)[4][4], int NT, int wp, int wq, int lane, Pre pre,
                                             bf16x8 (&p0)[4], bf16x8 (&p1)[4], unsigned last) {
    const int fr = lane & 15, fq = lane >> 4, key = (fr >> 1) & 7;
    const unsigned l0 = (unsigned)(size_t)lds;
    const unsigned oP0 = l0 + (PLIN ? wp * 8192 + lane * 16 : (wp * 64 + fr) * 128 + ((fq ^ key) * 16));
    const unsigned oP1 = l0 + (PLIN ? wp * 8192 + 1024 + lane * 16 : (wp * 64 + fr) * 128 + (((4 + fq) ^ key) * 16));
    const unsigned oQ0 = l0 + TILE_BYTES + (wq * 64 + fr) * 128 + ((fq ^ key) * 16);
    const unsigned oQ1 = l0 + TILE_BYTES + (wq * 64 + fr) * 128 + (((4 + fq) ^ key) * 16);
    unsigned aP0 = oP0, aQ0 = oQ0, aP1 = oP1, aQ1 = oQ1;
    bf16x8 r0[4], r1[4], q0[4], q1[4];
    unsigned xp = 2 * STAGE_BYTES;
#pragma unroll
    for (int i = 0; i < 16; ++i) asm volatile("" : "+v"(acc[i >> 2][i & 3]));
    asm volatile("" : "+v"(aP0), "+v"(aQ0), "+v"(aP1), "+v"(aQ1));
#define MM(pp, qq, m, n) do { acc[m][n] = __builtin_amdgcn_mfma_f32_16x16x32_bf16(pp[m], qq[n], acc[m][n], 0, 0, 0); SB(); } while (0)
#define RD(dst, i, a, st) do { dst[i] = *(const LAS bf16x8*)((const LAS unsigned char*)(size_t)(a) + (st) * STAGE_BYTES + (i) * 2048); SB(); } while (0)
#define ADP(a) do { a ^= xp; asm volatile("" : "+v"(a)); SB(); } while (0)
#define ADR(a) do { a ^= 2 * STAGE_BYTES; asm volatile("" : "+v"(a)); SB(); } while (0)
    __builtin_amdgcn_s_waitcnt(0xC07F);
    SB();
    if (FIRST) BARRIER();
    SB();
    RD(p0, 0, aP0, 0); RD(q0, 0, aQ0, 0); RD(p1, 0, aP1, 0); RD(q1, 0, aQ1, 0); RD(p1, 1, aP1, 0); RD(p0, 1, aP0, 0);
    RD(p0, 2, aP0, 0); RD(p1, 2, aP1, 0); RD(p1, 3, aP1, 0); RD(p0, 3, aP0, 0);
    pre();
    SB();
    int t = NT;
#pragma unroll 1
    do {
        MM(p0, q0, 0, 0); MM(p1, q1, 0, 0); RD(q0, 1, aQ0, 0);
        MM(p1, q1, 1, 0); MM(p0, q0, 1, 0); RD(q1, 1, aQ1, 0);
        MM(p0, q0, 2, 0); MM(p1, q1, 2, 0); RD(q0, 2, aQ0, 0);
        MM(p1, q1, 3, 0); MM(p0, q0, 3, 0); RD(q1, 2, aQ1, 0);
        MM(p0, q0, 3, 1); MM(p1, q1, 3, 1); RD(q0, 3, aQ0, 0);
        MM(p1, q1, 2, 1); MM(p0, q0, 2, 1); RD(q1, 3, aQ1, 0);
        MM(p0, q0, 1, 1); MM(p1, q1, 1, 1);
        MM(p1, q1, 0, 1); MM(p0, q0, 0, 1);
        MM(p0, q0, 0, 2); MM(p1, q1, 0, 2); __builtin_amdgcn_s_waitcnt(0xC07F); SB(); BARRIER(); SB();
        MM(p1, q1, 1, 2); RD(r0, 0, aP0, 1); MM(p0, q0, 1, 2); RD(q0, 0, aQ0, 1);
        MM(p0, q0, 2, 2); RD(r1, 0, aP1, 1); MM(p1, q1, 2, 2); RD(q1, 0, aQ1, 1);
        MM(p1, q1, 3, 2); MM(p0, q0, 3, 2); RD(r1, 1, aP1, 1);
        MM(p0, q0, 3, 3); RD(r0, 1, aP0, 1); MM(p1, q1, 3, 3);
        MM(p1, q1, 2, 3); RD(r0, 2, aP0, 1); MM(p0, q0, 2, 3); RD(r1, 2, aP1, 1);
        MM(p0, q0, 1, 3); MM(p1, q1, 1, 3); RD(r1, 3, aP1, 1);
        MM(p1, q1, 0, 3); RD(r0, 3, aP0, 1); MM(p0, q0, 0, 3);

        MM(r0, q0, 0, 0); MM(r1, q1, 0, 0); RD(q0, 1, aQ0, 1);
        MM(r1, q1, 1, 0); MM(r0, q0, 1, 0); RD(q1, 1, aQ1, 1);
        MM(r0, q0, 2, 0); MM(r1, q1, 2, 0); RD(q0, 2, aQ0, 1);
        MM(r1, q1, 3, 0); MM(r0, q0, 3, 0); RD(q1, 2, aQ1, 1);
        MM(r0, q0, 3, 1); MM(r1, q1, 3, 1); RD(q0, 3, aQ0, 1);
        MM(r1, q1, 2, 1); MM(r0, q0, 2, 1); RD(q1, 3, aQ1, 1);
        MM(r0, q0, 1, 1); t -= 2; if (PLIN) xp = t == 0 ? last : 2 * STAGE_BYTES; asm volatile("" : "+s"(t), "+s"(xp)); SB(); MM(r1, q1, 1, 1); ADP(aP0);
        MM(r1, q1, 0, 1); ADP(aP1); MM(r0, q0, 0, 1); ADR(aQ0);
        MM(r0, q0, 0, 2); ADR(aQ1); MM(r1, q1, 0, 2); __builtin_amdgcn_s_waitcnt(0xC07F); SB(); BARRIER(); SB();
        MM(r1, q1, 1, 2); RD(p0, 0, aP0, 0); MM(r0, q0, 1, 2); RD(q0, 0, aQ0, 0);
        MM(r0, q0, 2, 2); RD(p1, 0, aP1, 0); MM(r1, q1, 2, 2); RD(q1, 0, aQ1, 0);
        MM(r1, q1, 3, 2); MM(r0, q0, 3, 2); RD(p1, 1, aP1, 0);
        MM(r0, q0, 3, 3); RD(p0, 1, aP0, 0); MM(r1, q1, 3, 3);
        MM(r1, q1, 2, 3); RD(p0, 2, aP0, 0); MM(r0, q0, 2, 3); RD(p1, 2, aP1, 0);
        MM(r0, q0, 1, 3); MM(r1, q1, 1, 3); RD(p1, 3, aP1, 0);
        MM(r1, q1, 0, 3); RD(p0, 3, aP0, 0); MM(r0, q0, 0, 3);
    } while (t != 0);
    SB();
#undef MM
#undef RD
#undef ADR
#undef ADP
}

struct Args {
    const bf16_t* x; const bf16_t* wre; const bf16_t* wim; char* ws; const bf16_t* cr; const bf16_t* ci;
    const bf16_t* w_r; const bf16_t* wg; const bf16_t* wu; const bf16_t* wd; bf16_t* out;
};

__device__ __forceinline__ unsigned poll_winner(const char* ws, int rep, int lane, unsigned& mine) {
    GAS unsigned* kp = (GAS unsigned*)(ws + WS_KEYS + (size_t)(rep >> 2) * 8192 + (rep & 3) * (KEY_S * 16) + ((lane >> 2) & 7) * KEY_S) + (lane & 3);
    unsigned kw;
    for (int spin = 0;; ++spin) {
            asm volatile("" ::: "memory");
        kw = __hip_atomic_load(kp, __ATOMIC_RELAXED, __HIP_MEMORY_SCOPE_AGENT);
        if (__all(kw != 0u) || spin > SPIN_CAP) break;
        __builtin_amdgcn_s_sleep(POLL_SLEEP);
    }
    mine = kw;
    return __builtin_amdgcn_readfirstlane(wave_max_u32(kw));
}

__device__ __forceinline__ void stage2(char* w2, int slot, int lw, LAS unsigned char* img0, LAS unsigned char* img1, LAS unsigned char* abuf, int zoff, int lane) {
    const int g = lane >> 4, n = lane & 15;
    const __amdgpu_buffer_rsrc_t rPi = mk_rsrc(w2 + WS_PRE + pre_off(slot), 2048);
    unsigned pp[8];
    auto request = [&]() {
        asm volatile("" ::: "memory");
#pragma unroll
        for (int k = 0; k < 8; ++k) pp[k] = __builtin_amdgcn_raw_buffer_load_b32(rPi, k * 256 + lane * 4, 0, 16);
        SB();
    };
    request();
    bf16x8 wf[16];
    {
        const int q = (lane >> 2) & 3, ky = q | ((g & 1) << 2);
#pragma unroll
        for (int j = 0; j < 16; ++j) {
            LAS unsigned char* t = (g < 2 ? img0 : img1) + (8 * (g & 1) + q) * 512 + (((j & 7) ^ ky) + (j & 8)) * 32 + (lane & 3) * 8;
            const bf16x4 w0 = __builtin_amdgcn_ds_read_tr16_b64_v4i16((LAS bf16x4*)t);
            const bf16x4 w1 = __builtin_amdgcn_ds_read_tr16_b64_v4i16((LAS bf16x4*)(t + 2048));
            wf[j] = __builtin_shufflevector(w0, w1, 0, 1, 2, 3, 4, 5, 6, 7);
        }
        SB();
#pragma unroll
        for (int j = 0; j < 16; ++j) asm volatile("" : "+v"(wf[j]));
    }
    unsigned aa[5], pto = lane * 16;
#pragma unroll
    for (int m = 0; m < 4; ++m) aa[m] = (unsigned)(size_t)abuf + ((n >> 2) == m && ((n >> 1) & 1) == (g >> 1) ? (n & 1) * 32 + (g & 1) * 16 : zoff + g * 16);
    aa[4] = (unsigned)(size_t)abuf + ((lane >> 1) & 1) * 32 + ((lane >> 4) & 1) * 16 + ((lane >> 2) & 3) * 4 + (lane & 1) * 2;
    unsigned ix = (n & 1) ? 0xEEEEEEEEu : 0x44444444u;
    __amdgpu_buffer_rsrc_t rPt = mk_rsrc(w2 + WS_PART + __builtin_amdgcn_readfirstlane((slot >> 2) * 16384 + (lw >> 1) * 8192 + (slot & 3) * 2048 + (lw & 1) * 1024), 1024);
    asm volatile("" : "+v"(aa[0]), "+v"(aa[1]), "+v"(aa[2]), "+v"(aa[3]), "+v"(aa[4]), "+v"(pto), "+v"(ix));
    asm volatile("" : "+s"(rPt));
    f32x4 d[2] = {{0.f, 0.f, 0.f, 0.f}, {0.f, 0.f, 0.f, 0.f}};
    asm volatile("" : "+v"(d[0]), "+v"(d[1]));
    SB();
    float sv;
#pragma unroll 1
    for (int spin = 0;; ++spin) {
        sv = ((__uint_as_float(pp[0]) + __uint_as_float(pp[1])) + (__uint_as_float(pp[2]) + __uint_as_float(pp[3]))) +
             ((__uint_as_float(pp[4]) + __uint_as_float(pp[5])) + (__uint_as_float(pp[6]) + __uint_as_float(pp[7])));
        if (__builtin_expect(__builtin_amdgcn_ballot_w64(sv != sv) == 0, 1)) break;
        if (__all(max(max(max(pp[0], pp[1]), max(pp[2], pp[3])), max(max(pp[4], pp[5]), max(pp[6], pp[7]))) != SENT) || spin > SPIN_CAP) break;
        request();
    }
    unsigned ug, uu;
    swap32(__float_as_uint(sv), ug, uu);
    const float gg = rbf(__uint_as_float(ug)), up = rbf(__uint_as_float(uu));
    const unsigned act = pk_bf16(rbf(gg * __builtin_amdgcn_rcpf(1.0f + __expf(-gg))) * up, 0.f);
    *(volatile LAS unsigned short*)(size_t)aa[4] = (unsigned short)act;
    bf16x8 af[4];
#pragma unroll
    for (int m = 0; m < 4; ++m) af[m] = *(volatile LAS bf16x8*)(size_t)aa[m];
    SB();
#pragma unroll
    for (int i = 0; i < 8; ++i) {
        const int j = 8 * (i & 1) + 2 * (i >> 1);
        d[i & 1] = smf(af[i >> 1], wf[j], wf[j + 1], d[i & 1], ix);
        SB();
    }
    u32x4 pv;
#pragma unroll
    for (int q = 0; q < 4; ++q) {
        pv[q] = min(__float_as_uint(d[q >> 1][(q & 1) * 2] + d[q >> 1][(q & 1) * 2 + 1]), SENT - 1u);
        asm volatile("" : "+v"(pv[q]));
    }
    ST16_SC1(pv, rPt, pto);
}
__device__ __forceinline__ int wd_off(int lw, int i, int lane) {
    const int r = 2 * (i & 5) + (lane >> 5), ky = (r & 3) | ((r >> 1) & 4), c = lane & 31;
    return (lane >> 5) * (D_LEN * 2) + lw * 512 + (((c >> 1) ^ ky) * 2 + (c & 1)) * 16;
}

__global__ __launch_bounds__(768, 3) void fused_kernel(const Args a) {
    extern __shared__ __attribute__((aligned(16))) unsigned char smem[];
    constexpr int K = S_LEN, NT1 = D_LEN / BK;
    const int tid = threadIdx.x, wid = __builtin_amdgcn_readfirstlane(tid >> 6), lane = tid & 63;
    const int xcd = blockIdx.x & 7, idx = blockIdx.x >> 3;
    TSV;
    LAS unsigned char* lds = (LAS unsigned char*)smem;
    if (idx < 1 && wid < 4) dma_word<false>(code_rsrc(), (unsigned)(size_t)lds + L_DUMMY, tid * 128);
    SB();
    const int h = xcd & 1, xq = xcd >> 1, rt = xq * 4 + (idx & 3), ct = idx >> 2;
    volatile LAS unsigned* ctl1 = (volatile LAS unsigned*)(lds + L_CTL);
#define NBR(k) ((rt + (k)) & 3)
    unsigned kx;
    Ring ring;
    if (__builtin_expect((wid & 12) == 4, 1)) {
        __builtin_amdgcn_s_setprio(3);
        ring.template setup<D_LEN, false>(wid - 4, lane);
        ring.rP = mk_rsrc(a.x + (size_t)(rt * 4 + wid - 4) * 32 * D_LEN, 32 * D_LEN * 2);
        ring.rQ = mk_rsrc((h == 0 ? a.wre : a.wim) + (size_t)(ct * 4 + wid - 4) * 32 * D_LEN, 32 * D_LEN * 2);
        ring.lb = (wid - 4) * 4096;
        ring.sp = ring.sq = ring.poff = ring.xm = 0;
        ring.template issue<false>(lds);
        __builtin_amdgcn_s_setprio(0);
        ring.par = (ct & 4) << 11;
        ring.template ramp<CATCH1>(lds);
        ring.template steady<false, false>(NT1 - 2 - CATCH1);
        ring.template setup<K, true>(wid - 4, lane);
        ring.rQ = mk_rsrc((h == 0 ? a.cr : a.ci) + (size_t)(rt * 4 + wid - 4) * 32 * K, 32 * K * 2);
        ring.seek(rt * 256, 0);
        LAS unsigned char* l0 = lds + (wid - 4) * 4096;
        ring.issueQ(l0); ring.step();
        WAIT_VM(12); BARRIER();
        ring.issueQ(l0 + STAGE_BYTES);
        ring.seek((xq * 4 + NBR(1)) * 256, 0);
        WAIT_VM(8); BARRIER();
        ring.issueQ(l0 + 2 * STAGE_BYTES);
        ring.seek((xq * 4 + NBR(2)) * 256, 0);
        const int xr = (wid & 1) * 8 + (lane >> 4);
        int vx0 = ((((wid >> 1) & 1) * 64 + xr) * D_LEN + (((lane & 15) ^ xr) * 8)) * 2;
        int vx1 = ((((wid >> 1) & 1) * 64 + xr + 4) * D_LEN + (((lane & 15) ^ (xr + 4)) * 8)) * 2 - 1024;
        __amdgpu_buffer_rsrc_t rX = mk_rsrc(a.x + (size_t)rt * BT * D_LEN + ct * BT, BT * D_LEN * 2 - ct * BT * 2);
        unsigned xd = (unsigned)(size_t)lds + ((wid & 2) ? L_YB + 16384 : 3 * STAGE_BYTES) + (wid & 1) * 2048;
        asm volatile("" : "+v"(vx0), "+v"(vx1));
        asm volatile("" : "+s"(rX), "+s"(xd));
        BARRIER();
        if (h != 0) {
#pragma unroll
            for (int k = 0; k < 4; ++k) dma_2k<false>(rX, xd + k * 4096, vx0, vx1, k * (16 * D_LEN * 2));
        }
        ring.issueQ(l0 + 3 * STAGE_BYTES);
        kx = (unsigned)__builtin_amdgcn_readfirstlane((int)ctl1[4]);
        SB();
    }
    char* ws = a.ws + ((0ull - (unsigned long long)a.ws) & 0x1fffffull);
    const int g = lane >> 4, n = lane & 15;
    LAS float* tab = (LAS float*)(lds + L_RED);
    if (wid >= 8) {
        const int aw = wid - 8;
#define T1(t) do { if (aw == 1 && (t) <= 16) STAMP(40 + (t)); } while (0)
        GAS u64* tagp = (GAS u64*)(ws + WS_TAG);
        const int r0 = xq * 512 + h * 256 + idx * 8 + aw * 2;
        const int w0 = xq * 256 + idx * 8 + aw * 2;
        const u32x4* src = (const u32x4*)((n < 8 ? a.x + (size_t)(r0 + (n >> 2)) * D_LEN
                                                 : (h == 0 ? a.wre : a.wim) + (size_t)(w0 + (n >> 2) - 2) * D_LEN) + g * 32 + (n & 3) * 8);
        if (AUX_SLEEP) __builtin_amdgcn_s_sleep(AUX_SLEEP);
        u64 tag0 = __hip_atomic_load(tagp, __ATOMIC_RELAXED, __HIP_MEMORY_SCOPE_AGENT);
        u32x4 v[8];
#pragma unroll
        for (int j = 0; j < 4; ++j) v[j] = src[j * 16];
        __builtin_amdgcn_s_setprio(3);
        SB();
        const char* cb = (const char*)(h ? a.ci : a.cr) + (size_t)rt * (512u << 10) + ct * 65536 + (lane >> 2) * 4096 + (lane & 3) * 128;
        const GAS u32x4* wrp = (const GAS u32x4*)a.w_r + lane;
        asm volatile("" : "+v"(cb), "+v"(wrp));
        unsigned mask = aw == 1 ? CPF_MASK1 : aw == 2 ? CPF_MASK2 : 0u;
        mask = (mask & CPF_FIX) | ((mask & ~CPF_FIX) << (idx % CPF_STAG));
        int ti = 2 * xq + aw - 1;
        auto warm = [&](int t) { if (t >= 4 && ((mask >> t) & 1)) { touch_line(cb + (ti & 7) * 512, L_DUMMY); ti += 2; } };
        f32x4 d1 = {0.f, 0.f, 0.f, 0.f}, d2 = {0.f, 0.f, 0.f, 0.f};
        u32x4 ones = {0x3F803F80u, 0x3F803F80u, 0x3F803F80u, 0x3F803F80u};
        auto sums = [&](int j0) __attribute__((always_inline)) {
#pragma unroll
            for (int j = j0; j < j0 + 4; ++j) {
                const bf16x8 f = __builtin_bit_cast(bf16x8, v[j]);
                d1 = __builtin_amdgcn_mfma_f32_16x16x32_bf16(f, __builtin_bit_cast(bf16x8, ones), d1, 0, 0, 0);
                d2 = __builtin_amdgcn_mfma_f32_16x16x32_bf16(f, f, d2, 0, 0, 0);
            }
            asm volatile("" : "+v"(d1), "+v"(d2));
        };
        unsigned wl, wh;
        int ro;
        __amdgpu_buffer_rsrc_t rS;
        u64 m1 = 0xAAAAAAAAAAAAAAAAull, m2 = 0xCCCCCCCCCCCCCCCCull, mh = 0xFFFFFFFF00000000ull;
#pragma unroll
        for (int t = 0; t <= 5; ++t) {
            SB(); BARRIER(); SB();
            T1(t);
            if (t == 2) {
#pragma unroll
                for (int j = 4; j < 8; ++j) v[j] = src[j * 16];
            }
            if (t == 3) {
                asm volatile("" : "+v"(ones));
                sums(0);
                asm volatile("" : "+v"(tag0));
                const u64 want = tag0 * 6364136223846793005ull + 1442695040888963407ull;
                wl = (unsigned)want; wh = (unsigned)(want >> 32);
                asm volatile("" : "+v"(wl), "+v"(wh));
            }
            if (t == 4) { sums(4); asm volatile("s_nop 7\n\ts_nop 3" : "+v"(d2)); }
            if (t == 1) {
                rS = mk_rsrc(ws + WS_XRD, 65536);
                ro = n == g * 4 ? (g < 2 ? r0 + g : 2048 - 2 + h * 1024 + w0 + g) * 16 : 65536;
                asm volatile("" : "+v"(ro));
                asm volatile("" : "+s"(rS), "+s"(m1), "+s"(m2), "+s"(mh), "+s"(mask), "+s"(ti));
            }
        }
        float sm = (d1[0] + d1[1]) + (d1[2] + d1[3]);
        float sq = pick(m2, pick(m1, d2[0], d2[1]), pick(m1, d2[2], d2[3]));
        asm volatile("s_nop 1" : "+v"(sq));
        sq += dppf<0xB1>(sq);
        sq += dppf<0x4E>(sq);
        float mean = sm * (1.0f / D_LEN), var = rbf(rbf(fmaxf(sq * (1.0f / D_LEN) - mean * mean, 0.f)) + 1e-5f);
        asm volatile("" : "+v"(sm), "+v"(mean), "+v"(var));
        SB(); BARRIER(); SB();
        T1(6);
        {
            float rden = __builtin_amdgcn_rcpf(rbf(__builtin_amdgcn_sqrtf(var)));
            asm volatile("s_nop 1" : "+v"(rden));
            const u32x4 rec = {__float_as_uint(pick(mh, rden, sm)), wl, __float_as_uint(rbf(mean) * rden), wh};
            ST16_SC1(rec, rS, ro);
        }
        const __amdgpu_buffer_rsrc_t rV = mk_rsrc(ws + WS_VOTE, 512);
        if (aw == 3 && idx < 8) __builtin_amdgcn_raw_buffer_store_b64((u32x2){ctl1[5], wl}, rV, blockIdx.x * 8 + lane * 512, 0, 16);
        SB(); BARRIER(); SB();
        T1(STAT_M + 2);
        warm(STAT_M + 2);
        if (aw == 1 || aw == 2) {
#pragma unroll 1
            for (int t = STAT_M + 3; t <= NT1 + AUX_STAY; ++t) {
                BARRIER();
                T1(t);
                warm(t);
            }
            FLUSH();
            return;
        }
        if (aw == 3) {
            if (h == 0) {
                const u32x4 w0v = wrp[0], w1v = wrp[64];
                BARRIER(); BARRIER(); BARRIER(); BARRIER();
                SB();
                float t = 0.f;
#pragma unroll
                for (int i = 0; i < 4; ++i) { t = dot2bf(w0v[i], 0x3F803F80u, t); t = dot2bf(w1v[i], 0x3F803F80u, t); }
                SB(); BARRIER(); SB();
                t = wave_sum(t);
                ctl1[1] = __float_as_uint(t);
                WAIT_LGKM0();
            }
            return;
        }
#pragma unroll
        for (int t = STAT_M + 3; t <= POLL_T; ++t) BARRIER();
        SB();
        const int o0 = (rt * BT + lane) * 16, o1 = (2048 + (h * 8 + ct) * BT + lane) * 16, os = 1024;
        u32x4 q0, q1, q2, q3;
        u32x2 qv;
        auto request = [&]() {
            asm volatile("" ::: "memory");
            q0 = LD16_SC1(rS, o0); q1 = LD16_SC1(rS, o0 + os); q2 = LD16_SC1(rS, o1); q3 = LD16_SC1(rS, o1 + os);
            qv = __builtin_amdgcn_raw_buffer_load_b64(rV, lane * 8, 0, 16);
            SB();
        };
        request();
        int b = POLL_T + 1;
        for (int spin = 0; spin <= SPIN_CAP; ++spin) {
            if (b < NT1) { BARRIER(); ++b; }
            if (b < NT1) { BARRIER(); ++b; }
            SB();
            unsigned d = ((q0[1] ^ wl) | (q1[1] ^ wl)) | ((q2[1] ^ wl) | (q3[1] ^ wl)) | ((q0[3] ^ wh) | (q1[3] ^ wh)) | ((q2[3] ^ wh) | (q3[3] ^ wh)) | (qv[1] ^ wl);
            asm volatile("" : "+v"(d));
            if (__all(d == 0u)) break;
            request();
        }
        if (b < NT1) BARRIER();
        SB();
        LAS unsigned* tb = (LAS unsigned*)tab;
        const unsigned sg = h == 0 ? 0u : 0x80000000u;
        tb[lane] = q0[0] ^ sg; tb[64 + lane] = q1[0] ^ sg; tb[128 + lane] = q0[2] ^ sg; tb[192 + lane] = q1[2] ^ sg;
        tb[256 + lane] = q2[0]; tb[320 + lane] = q3[0];
        const unsigned vt = qv[0];
        const unsigned kx = __popcll(__ballot(vt == 2u)) > __popcll(__ballot(vt == 1u)) ? 8192u : 0u;
        if (lane == 0) { ctl1[0] = wl | 2u; ctl1[2] = wl; ctl1[3] = wh; ctl1[4] = kx; }
        *(LAS u32x2*)(lds + L_YB + 4096 + 256 + lane * 8) = (u32x2){0u, 0u};
        WAIT_LGKM0();
        return;
    }
#define CTL(i) ((unsigned)__builtin_amdgcn_readfirstlane((int)ctl[i]))
    const int wp = wid & 1, wq = (wid >> 1) & 1;
#define Y_OFF ((int)WS_Y + ((rt & 7) * 8 + ct) * 65536 + (wid & 3) * 16384 + (((rt >> 3) ^ (rt >> 2)) & 1) * 8192)
#define P2_DEFS \
    char* w2 = ws + kx; \
    const int sidx = idx < 28 ? xq * 28 + idx : 112 + xq * 4 + idx - 28;     \
    volatile LAS unsigned* ctl = (volatile LAS unsigned*)(lds + (h == 0 ? L_ATAB : L_CTL)); \
    const int NT = h == 0 ? 32 + KSHIFT : 32 - KSHIFT; \
    LAS float* rstat = (LAS float*)(lds + L_ATAB + 256);     \
    const int hp_ = rt * 2 + wq;                             \
    (void)sidx; (void)ctl; (void)NT; (void)rstat; (void)hp_; (void)w2
    if (__builtin_expect(wid < 4, 1)) {
        {
            const u32x4 sent4 = {SENT, SENT, SENT, SENT}, zero4 = {0u, 0u, 0u, 0u};
            const __amdgpu_buffer_rsrc_t rI = mk_rsrc(ws + WS_STATS + (size_t)blockIdx.x * (FILL_BYTES / 256), FILL_BYTES / 256);
            const __amdgpu_buffer_rsrc_t rZ = mk_rsrc(ws + (size_t)blockIdx.x * 160, 160);
            ST16_SC1(sent4, rI, tid * 16);
            if (wid < 2) ST16_SC1(sent4, rI, 4096 + tid * 16);
            if (wid == 2) ST16_SC1(zero4, rZ, lane * 16);
        }
        if (wid == 3 && idx < 8) {
            const unsigned r = (((unsigned)__builtin_amdgcn_s_memrealtime() * 2654435761u + blockIdx.x * 40503u) >> 9) & 0x1fdf80u;
            const unsigned first = (idx & 1) << 13;
            const char* pb = ws + WS_PROBE;
            unsigned va, vb, left;
            asm volatile("s_waitcnt lgkmcnt(0)\n\ts_load_dword %0, %3, %4\n\ts_load_dword %1, %3, %5\n\ts_waitcnt lgkmcnt(1)\n\t"
                         "s_nop 15\n\ts_getreg_b32 %2, hwreg(HW_REG_IB_STS, 8, 4)\n\ts_waitcnt lgkmcnt(0)"
                         : "=&s"(va), "=&s"(vb), "=&s"(left) : "s"(pb), "s"(r | first), "s"(r | (first ^ 8192u)) : "memory");
            const unsigned nearbit = (left != 0u ? first : first ^ 8192u) >> 13;
            if (lane == 0) ctl1[5] = nearbit == (unsigned)(xcd >> 2) ? 1u : 2u;
        }
        f32x4 acc[4][4];
#pragma unroll
        for (int i = 0; i < 16; ++i) acc[i >> 2][i & 3] = (f32x4){0.f, 0.f, 0.f, 0.f};
        unsigned tc = (unsigned)(size_t)(tab + wp * 64 + g * 8), tw = (unsigned)(size_t)(tab + 256 + prow(wq * 64 + n));
        unsigned ta = (unsigned)(size_t)lds + wp * STAGE_BYTES + wq * 8192 + lane * 16;
        constexpr int NIL = 1 << 30;
        __amdgpu_buffer_rsrc_t rW = mk_rsrc(ws, 18u << 20);
        const int hpan = rt * 2 + wq;
        int sk = (int)WS_AT + at_panel(h * 8 + ct) + at_tile((rt * 2 + wp) * 128, (ct & 4) << 11) + wq * 16384 + lane * 16;
        int st = lane != 0 ? NIL : (int)WS_FLAG1 + (1 - wp) * 32768 + h * 16384 + (rt >> 3) * 8192 + ct * 1024 + (rt & 7) * 128 + wq * 4;
        int so = h != 0 ? Y_OFF + lane * 16 : g != (n >> 2) ? NIL
               : (int)WS_STATS + (hpan & 15) * 32768 + (ct >> 2) * 16384 + (hpan >> 4) * 8192 + ((ct & 3) * 2 + wp) * 1024 + n * 16;
        int sf = lane != 0 ? NIL : (int)WS_FLAGS + (rt * 8 + ct) * 128 + wid * 4;
        u32x4 sel[2], aw[2];
        unsigned one[2];
#pragma unroll
        for (int i = 0; i < 4; ++i) {
            one[i & 1] = g == (n >> 2) && (i & 1) == ((n >> 1) & 1) ? ((n & 1) ? 0x3F800000u : 0x00003F80u) : 0u;
            aw[0][i] = aw[1][i] = (n & 3) == 0 ? 0x3F803F80u : 0u;
        }
        if ((n & 3) == 1) {
            const bf16_t* wr = a.w_r + ct * BT + wp * 64 + g * 8;
            aw[0] = *(const u32x4*)wr; aw[1] = *(const u32x4*)(wr + 32);
        }
        unsigned ea[2];
#pragma unroll
        for (int mp = 0; mp < 2; ++mp)
            ea[mp] = (unsigned)(size_t)lds + (wq ? L_YB + 16384 : 3 * STAGE_BYTES) + n * 256 + (((wp * 8 + mp * 4 + g) ^ n) * 16);
        asm volatile("" : "+v"(tc), "+v"(tw), "+v"(ta), "+v"(sk), "+v"(st), "+v"(so), "+v"(sf), "+v"(one[0]), "+v"(one[1]), "+v"(ea[0]), "+v"(ea[1]));
        asm volatile("" : "+s"(rW));
        unsigned tg;
        bf16x8 e0[4], e1[4];
        auto extra = [&]() __attribute__((always_inline)) {
            bf16x8 ef[4][2];
#pragma unroll
            for (int i = 0; i < 8; ++i) ef[i >> 1][i & 1] = *(const LAS bf16x8*)((const LAS unsigned char*)(size_t)ea[i & 1] + (i >> 1) * 4096);
            SB();
#pragma unroll
            for (int i = 0; i < 16; ++i)
                acc[i & 3][i >> 2] = __builtin_amdgcn_mfma_f32_16x16x32_bf16(__builtin_bit_cast(bf16x8, sel[i & 1]), ef[i >> 2][(i >> 1) & 1], acc[i & 3][i >> 2], 0, 0, 0);
        };
        if (wid == 0) STAMP(0);
        compute_loop<false, true>(lds, acc, NT1, wp, wq, lane, [&]() { if (wid == 0) STAMP(24); WAIT_VM0(); if (wid == 0) STAMP(31); }, e0, e1, 0u);
        if (wid == 0) STAMP(25);
        {
            f32x4 rd[4], mr[4];
            float wsm[4];
#pragma unroll
            for (int m = 0; m < 4; ++m) {
                rd[m] = *(const LAS f32x4*)((const LAS float*)(size_t)tc + (m >> 1) * 32 + (m & 1) * 4);
                mr[m] = *(const LAS f32x4*)((const LAS float*)(size_t)tc + 128 + (m >> 1) * 32 + (m & 1) * 4);
                wsm[m] = *((const LAS float*)(size_t)tw + (m >> 1) * 32 + (m & 1) * 4);
            }
            const unsigned kv = ctl1[4];
            tg = ctl1[0];
            SB();
            sk += (int)kv; st += (int)kv; so += (int)kv; sf += (int)kv;
            kx = (unsigned)__builtin_amdgcn_readfirstlane((int)kv);
#pragma unroll
            for (int nn = 0; nn < 4; ++nn)
#pragma unroll
                for (int mp = 0; mp < 2; ++mp) {
                    const f32x4 u = acc[2 * mp][nn] * rd[2 * mp] - mr[2 * mp] * wsm[nn];
                    const f32x4 v = acc[2 * mp + 1][nn] * rd[2 * mp + 1] - mr[2 * mp + 1] * wsm[nn];
                    u32x4 w;
                    w[0] = pk_bf16(u[0], u[1]); w[1] = pk_bf16(u[2], u[3]);
                    w[2] = pk_bf16(v[0], v[1]); w[3] = pk_bf16(v[2], v[3]);
                    ST16_SC1(w, rW, sk + (nn * 2 + mp) * 1024);
                    *(LAS u32x4*)((LAS unsigned char*)(size_t)ta + (nn * 2 + mp) * 1024) = w;
                }
        }
        if (wid == 0) STAMP(26);
        WAIT_LGKM0();
        BARRIER();
        SB();
        {
#pragma unroll
            for (int i = 0; i < 16; ++i) { acc[i >> 2][i & 3] = (f32x4){0.f, 0.f, 0.f, 0.f}; asm volatile("" : "+v"(acc[i >> 2][i & 3])); }
            sel[0] = (u32x4){one[0], one[1], 0u, 0u}; sel[1] = (u32x4){0u, 0u, one[0], one[1]};
            asm volatile("" : "+v"(st), "+v"(so), "+v"(sf), "+v"(tg), "+v"(sel[0]), "+v"(sel[1]));
            SB();
            if (h != 0) extra();
            SB();
            WAIT_VM(0);
            __builtin_amdgcn_raw_buffer_store_b32(tg, rW, st, 0, 16);
            SB();
            if (wid == 0) STAMP(27);
        }
        compute_loop<true, false>(lds, acc, h == 0 ? 32 + KSHIFT : 32 - KSHIFT, wp, wq, lane, [&]() {
            if (wid == 0) { STAMP(h == 0 ? 1 : 19); CSTAMP(22); }
        }, e0, e1, (unsigned)((wp * 8192 + 2 * STAGE_BYTES) ^ (L_YB + wid * 8192)));
        P2_DEFS;
        if (wid == 0) { STAMP(h == 0 ? 2 : 12); CSTAMP(23); }
        u32x4 hq[4][2];
        auto cvt = [&](int nn, int j) __attribute__((always_inline)) {
            const f32x4 c = acc[j][nn];
            hq[nn][j >> 1][(j & 1) * 2] = pk_bf16(c[0], c[1]);
            hq[nn][j >> 1][(j & 1) * 2 + 1] = pk_bf16(c[2], c[3]);
        };
        int hs = h;
        asm volatile("" : "+s"(hs));
        if (__builtin_expect(hs == 0, 1)) {
            f32x4 d1[4], d2[4];
            u32x3 sv;
            auto add = [&](int nn, int j) __attribute__((always_inline)) {
                acc[j][nn] = __builtin_amdgcn_mfma_f32_16x16x32_bf16(__builtin_bit_cast(bf16x8, sel[j & 1]), j < 2 ? e0[nn] : e1[nn], acc[j][nn], 0, 0, 0);
            };
            auto sum = [&](int nn, int j) __attribute__((always_inline)) {
                const bf16x8 hf = __builtin_bit_cast(bf16x8, hq[nn][j >> 1]);
                const f32x4 z = {0.f, 0.f, 0.f, 0.f};
                if (j & 1) d1[nn] = __builtin_amdgcn_mfma_f32_16x16x32_bf16(__builtin_bit_cast(bf16x8, aw[j >> 1]), hf, j < 2 ? z : d1[nn], 0, 0, 0);
                else d2[nn] = __builtin_amdgcn_mfma_f32_16x16x32_bf16(hf, hf, j < 2 ? z : d2[nn], 0, 0, 0);
            };
            auto out = [&](int nn, int j) __attribute__((always_inline)) {
                if (j == 1) asm volatile("" : "+v"(d2[nn]));
                if (j == 2) asm volatile("" : "+v"(d1[nn]));
                if (j == 1) sv[1] = min(__float_as_uint(sel4(n, d2[nn][0], d2[nn][1], d2[nn][2], d2[nn][3])), SENT - 1u);
                if (j == 2) { sv[0] = min(__float_as_uint(d1[nn][0]), SENT - 1u); sv[2] = min(__float_as_uint(d1[nn][1]), SENT - 1u); }
                if (j == 3) __builtin_amdgcn_raw_buffer_store_b96(sv, rW, so + nn * 256, 0, 16);
            };
#define FOUR(m, v) _Pragma("unroll") for (int j = 0; j < 4; ++j) { m; SB(); v; SB(); }
            FOUR(add(0, j), (void)0) FOUR(add(1, j), cvt(0, j)) FOUR(sum(0, j), cvt(1, j)) FOUR(add(2, j), out(0, j))
            FOUR(sum(1, j), cvt(2, j)) FOUR(add(3, j), out(1, j)) FOUR(sum(2, j), cvt(3, j)) FOUR(sum(3, j), out(2, j))
            FOUR((void)0, out(3, j))
#undef FOUR
            if (wid == 0) STAMP(3);
#pragma unroll
            for (int nn = 0; nn < 4; ++nn)
#pragma unroll
                for (int mp = 0; mp < 2; ++mp)
                    *(LAS u32x4*)(lds + L_HT + (wq * 64 + nn * 16 + n) * 256 + (((wp * 8 + mp * 4 + g) ^ n) * 16)) = hq[nn][mp];
            WAIT_LGKM0();
            BARRIER();
            BARRIER();
            if (wid == 0) STAMP(6);
            const int crow = __builtin_amdgcn_readfirstlane((int)((volatile LAS unsigned*)(lds + L_ATAB))[2 + wq]) + wq * 64;
            const __amdgpu_buffer_rsrc_t rOut = mk_rsrc(a.out + (size_t)rt * BT * D_LEN, BT * D_LEN * 2);
#pragma unroll 2
            for (int j = 0; j < 8; ++j) {
                const int r = wq * 64 + wp * 32 + j * 4 + g;
                const float m_ = rstat[r * 2], d_ = rstat[r * 2 + 1];
                const u32x4 hh = *(const LAS u32x4*)(lds + L_HT + r * 256 + ((n ^ (r & 15)) * 16));
                u32x4 ov;
#pragma unroll
                for (int i = 0; i < 4; ++i) {
                    const unsigned u = hh[i];
                    const float h0 = bflo(u), h1 = bfhi(u);
                    const unsigned dd = pk_bf16(h0 - m_, h1 - m_);
                    const unsigned q = pk_bf16(bflo(dd) * d_, bfhi(dd) * d_);
                    ov[i] = pk_bf16(h0 + bflo(q), h1 + bfhi(q));
                }
                __builtin_amdgcn_raw_buffer_store_b128(ov, rOut, r == crow ? BT * D_LEN * 2 : (r * D_LEN + ct * BT + n * 8) * 2, 0, 0);
            }
            FLUSH();
            return;
        }
#pragma unroll
        for (int i = 0; i < 8; ++i) {
            cvt(i >> 1, (i & 1) * 2); cvt(i >> 1, (i & 1) * 2 + 1);
            __builtin_amdgcn_raw_buffer_store_b128(hq[i >> 1][i & 1], rW, so, i * 1024, 16);
            SB();
        }
        WAIT_VM0();
        __builtin_amdgcn_raw_buffer_store_b32(1u, rW, sf, 0, 16);
        if (wid == 0) STAMP(13);
    } else {
        LAS unsigned char* l0 = lds + (wid - 4) * 4096;
        P2_DEFS;
        __amdgpu_buffer_rsrc_t rD = mk_rsrc(h == 0 ? a.wd + (size_t)sidx * 32 * D_LEN : a.wg,
                                                  sidx >= 112 ? 0 : h != 0 ? D_LEN * F_LEN * 2 : wid < 8 - BQ ? 32 * D_LEN * 2 : 0);
        int vGe = h == 0 ? wd_off(wid & 3, 0, lane)
                : (((sidx & 7) * 128 + (wid & 3) * 16 + (lane >> 5)) * F_LEN + (sidx >> 3) * 256 + ((((lane & 31) >> 1) ^ (lane >> 5)) * 16) + (lane & 1) * 8) * 2;
        auto load_wd = [&](int i) __attribute__((always_inline)) {
            dma_nt(rD, (unsigned)(size_t)lds + (i < 8 ? L_WD0 : L_WD1) + (wid & 3) * 8192 + (i & 7) * 1024, vGe ^ ((2 * (i & 1) | (i & 4)) * 32), i * (4 * D_LEN));
        };
        const int lw = wid - 4;
        constexpr int NIL = 1 << 30;
        ring.rP = ring.rF = mk_rsrc(w2, (unsigned)WS_STATS);
        ring.voP[0] += (int)WS_AT + at_panel(h * 8 + ct);
        ring.voP[1] = ring.voP[2] = ring.voP[3] = ring.voP[0];
#define FLINE(k) (((k) >> 3) * 8192 + ((k) & 7) * 128)
        const int ft = (g & 1) * 32768 + (g >> 1) * 4 + (int)WS_FLAG1 + (lw == 0 ? ct * 1024 : NIL);
        int vall = FLINE(n) + ft + h * 16384;
        ring.voF = (n >> 2) == xq ? vall : NIL;
        ring.fl = lds + L_YB + lw * (h == 0 ? 8192 : 1024) + 4096 + 256;
        unsigned sa = (unsigned)(size_t)lds + L_YB + 4096 + 256 + lane * 4;
        const unsigned tg = (unsigned)__builtin_amdgcn_readfirstlane((int)ctl1[0]);
        float wsum = __uint_as_float(ctl1[1]);
        ring.lb = lw * 4096;
        int cq[3], cp[3];
        unsigned mk[3];
#pragma unroll
        for (int k = 0; k < 3; ++k) {
            const int nb = xq * 4 + NBR(k + 1);
            cq[k] = nb * 256; cp[k] = at_tile(cq[k], ring.par); mk[k] = 0x10000u << nb;
            asm volatile("" : "+s"(cq[k]), "+s"(cp[k]), "+s"(mk[k]));
        }
        unsigned m2 = (mk[0] | mk[1] | mk[2]) >> 16;
        asm volatile("" : "+s"(m2), "+s"(ring.rP));
        asm volatile("" : "+v"(vall), "+v"(ring.voF), "+v"(ring.voP[0]), "+v"(sa), "+v"(wsum));
        ring.rF = ring.rP;
        ring.voP[1] = ring.voP[2] = ring.voP[3] = ring.voP[0];
        WAIT_VM(4);
        BARRIER();
        SB();
        int ve = Y_OFF + lane * 16;
        unsigned yd = (unsigned)(size_t)lds + L_YB + lw * 8192, fa = 0u;
        auto fetch = [&]() __attribute__((always_inline)) {
            if (wid == 4) STAMP(21);
            const int v4[4] = {ve, ve, ve, ve};
#pragma unroll
            for (int k = 0; k < 2; ++k) dma_4k<true>(ring.rP, yd + k * 4096, v4, k * 4096);
        };
        __amdgpu_buffer_rsrc_t rQ2 = mk_rsrc(a.ci + (size_t)(rt * 4 + lw) * 32 * K, 32 * K * 2);
        int vC2 = (ct * 4 + g) * (K * 2) + ((xq * 1024 + 2048 + n * 128) & 4095);
        int vT2 = h == 0 && n < (KSHIFT + 1) / 2 ? FLINE((xq * 4 - 1 - n) & 15) + ft + 16384 : NIL;
        int vY = lane == 0 ? (int)WS_FLAGS + (rt * 8 + ct) * 128 + lw * 4 : NIL;
        int n1 = (h == 0 ? 32 : NT) - 9, n0 = n1 - 1 - (C2_START + idx % C2_STAG);
        int xm = (idx & 1) * 128;
        int c8 = (xq * 1024 + 1024) & 4095, p8 = at_tile(c8 ^ xm, ring.par);
        int cK = (xq * 1024 + (32 - KSHIFT) * 128) & 4095, pK = at_tile(cK ^ xm, ring.par) + at_panel(8);
        auto early = [&](int st, int i0, int i1) __attribute__((always_inline)) {
#pragma unroll
            for (int i = i0; i < i1; ++i)
                dma_nt(rD, (unsigned)(size_t)lds + (st * 4 + lw) * 8192 + i * 1024, vGe ^ ((2 * (i & 1) | (i & 4)) * 32), i * (4 * F_LEN) + st * (128 * F_LEN));
        };
        asm volatile("" : "+v"(vGe));
        asm volatile("" : "+s"(rQ2), "+s"(n0), "+s"(n1), "+s"(c8), "+s"(p8), "+s"(cK), "+s"(pK), "+s"(xm), "+s"(rD));
        asm volatile("" : "+v"(ve), "+v"(vC2), "+v"(vT2), "+v"(vY));
        unsigned have = 0u;
        SB();
#define GATE(m) do { if (__builtin_expect(((m) & ~have) != 0u, 0)) ring.gate(tg, sa, m, have); SB(); } while (0)
#define TILE(k, o, st) do { ring.sp = cp[k] + (o) * 256; ring.sq = cq[k] + (o); ring.template issueP<16>(l0 + (st) * STAGE_BYTES); \
                            ring.issueQ(l0 + (st) * STAGE_BYTES); ring.sample(); SB(); } while (0)
        ring.gate(tg, sa, mk[0], have);
        if (wid == 4) STAMP(28);
        ring.sp = cp[0]; ring.template issueP<16>(l0 + 2 * STAGE_BYTES); ring.sample();
        BARRIER();
        GATE(mk[1]);
        ring.sp = cp[1]; ring.template issueP<16>(l0 + 3 * STAGE_BYTES); ring.sample();
        WAIT_VM(6); BARRIER();
        if (wid == 4) STAMP(30);
        GATE(mk[2]);
        TILE(2, 0, 0);
        GATE(m2);
        ring.voF = vall;
        TILE(0, 128, 1);
        WAIT_VM(19); BARRIER();
        TILE(1, 128, 2);
        WAIT_VM(19); BARRIER();
        ring.sp = cp[2] + 128 * 256; ring.template issueP<16>(l0 + 3 * STAGE_BYTES);
        ring.sq = cq[2] + 128;
        SB();
        {
            const unsigned long long m = __ballot(ring.peek(sa) == tg);
            have = (unsigned)m & (unsigned)(m >> 32);
            if (__builtin_expect(have != 0xffffffffu, 0)) ring.gate(tg, sa, 0xffffffffu, have);
        }
        ring.fl -= 256;
        ring.voF = vT2;
        SB();
        ring.issueQ(l0 + 3 * STAGE_BYTES);
#undef GATE
#undef TILE
        ring.sq = c8; ring.sp = p8; ring.poff = 0; ring.xm = xm;
        if (wid == 4) { STAMP(29); CSTAMP(34); STAMP(36); }
        ring.template steady<true, false, 18>(n1, n0, vC2);
        ring.fl += 512;
        if (wid == 4) { CSTAMP(35); STAMP(37); }
        ring.template issueP<16>(lds + ring.lb);
        SB();
        {
            const unsigned long long m = __ballot(ring.peek(sa + 256) == tg);
            have = (unsigned)m & (unsigned)(m >> 32);
            asm volatile("" : "+s"(have));
        }
        int hs = h;
        asm volatile("" : "+s"(hs));
        if (__builtin_expect(hs == 0, 1)) {
            constexpr unsigned MK = ((1u << ((KSHIFT + 1) / 2)) - 1u) * 0x10001u;
            if (__builtin_expect((MK & ~have) != 0u, 0)) ring.gate(tg, sa + 256, MK, have);
            unsigned lq = ring.lb + TILE_BYTES, pp = ring.par | at_panel(8);
            int sq = ring.sq ^ ring.xm, cnt = KSHIFT;
            ring.lb = (ring.lb + STAGE_BYTES) & (RING_BYTES - 1);
            ring.fl -= 512;
            fa = (unsigned)(size_t)ring.fl;
            asm volatile("" : "+s"(lq), "+s"(sq), "+s"(cnt), "+s"(ring.lb), "+s"(pp), "+s"(yd));
            asm volatile("" : "+v"(fa));
            SB();
            dma_4k<false>(ring.rQ, lq, ring.voQ, sq);
            ring.voF = vY;
            ring.rQ = rQ2;
            ring.sq = cK; ring.sp = pK; ring.poff = 0; ring.par = pp;
            ring.template steady<true, true>(cnt);
            auto seen = [&]() __attribute__((always_inline)) { return __builtin_amdgcn_readfirstlane(ring.peek(fa)) == 1u; };
            if (__builtin_expect(seen(), 1)) {
                fetch();
                WAIT_VM(16); BARRIER();
                WAIT_VM(8); BARRIER();
            } else {
                ring.sample();
                WAIT_VM(10); BARRIER();
                const bool have = seen();
                if (have) { fetch(); WAIT_VM(9); } else { ring.sample(); WAIT_VM(2); }
                BARRIER();
                if (!have) {
#pragma unroll 1
                    for (int spin = 0; spin <= SPIN_CAP && !seen(); ++spin) { ring.sample(); WAIT_VM(0); }
                    fetch();
                }
            }
            if (wid == 4) STAMP(20);
            WAIT_VM0();
        } else {
            ring.issueQ(lds + ring.lb);
            WAIT_VM(16); BARRIER();
            early(0, 0, G_N);
            wait_vm<8 + G_N>(); BARRIER();
            early(1, 0, G2_N);
            wait_vm<G_N + G2_N>(); BARRIER();
            early(1, G2_N, G2_N + G3_N);
        }
        BARRIER();
        if (h == 0) {
            const int slot = sidx;
            LAS unsigned char* abuf = lds + L_ATAB + 2048 + lw * 128;
#pragma unroll
            for (int i = 0; i < WD_EARLY; ++i) load_wd(i);
            SB();
            __amdgpu_buffer_rsrc_t rS = mk_rsrc(w2 + WS_STATS + (hp_ & 15) * 32768 + (hp_ >> 4) * 8192, 24576);
            __amdgpu_buffer_rsrc_t rC = mk_rsrc(w2 + WS_CAND + (size_t)hp_ * 4096, 4096);
            __amdgpu_buffer_rsrc_t rKy = mk_rsrc(w2 + WS_KEYS + (hp_ >> 2) * KEY_S + (hp_ & 3) * 4, ct == 0 ? 8192 + 3 * KEY_S * 16 + 4 : 0);
            int vs = lane * 16, vk = !(lane & 1) ? 1 << 20 : (lane >> 2) * 8192 + (lane & 3) * (KEY_S * 16);
            int vc = (lane < 32 ? 2048 : 0) + ct * 256 + (lane & 31) * 8;
            unsigned code = 0xffffu - (unsigned)(hp_ * 64 + lane), hb = (lane & 31) * 8;
            unsigned ra = (unsigned)(size_t)(rstat + (wq * 64 + lane) * 2), ht = (unsigned)(size_t)lds + L_HT + wq * 16384;
            u64 lo = 0xFFFFFFFFull;
            asm volatile("" : "+s"(rS), "+s"(rC), "+s"(rKy), "+s"(ht), "+s"(lo));
            asm volatile("" : "+v"(vs), "+v"(vk), "+v"(vc), "+v"(code), "+v"(hb), "+v"(ra));
            SB();
            BARRIER();
            __builtin_amdgcn_s_setprio(TAIL_PRIO);
            if (lane < 16) *(LAS unsigned*)(abuf + 64 + lane * 4) = 0u;
            __builtin_amdgcn_s_sleep(STAT_DELAY);
            if (wp == 0) {
                float s1, s2, s3;
#pragma unroll 1
                for (int spin = 0;; ++spin) {
                    asm volatile("" ::: "memory");
                    u32x3 sv[16];
#pragma unroll
                    for (int j = 0; j < 16; ++j) { sv[j] = __builtin_amdgcn_raw_buffer_load_b96(rS, vs, (j >> 3) * 16384 + (j & 7) * 1024, 16); SB(); }
#pragma unroll
                    for (int j = 0; j < 16; ++j) {
                        const unsigned u0 = sv[j][0], u1 = sv[j][1], u2 = sv[j][2];
                        s1 = j ? s1 + __uint_as_float(u0) : __uint_as_float(u0);
                        s2 = j ? s2 + __uint_as_float(u1) : __uint_as_float(u1);
                        s3 = j ? s3 + __uint_as_float(u2) : __uint_as_float(u2);
                    }
                    float z = s1 + s3;
                    asm volatile("" : "+v"(z));
                    if (__builtin_expect(__builtin_amdgcn_ballot_w64(__builtin_isunordered(z, s2)) == 0, 1)) break;
                    unsigned mx = 0u;
#pragma unroll
                    for (int j = 0; j < 16; ++j) mx = max(max(mx, sv[j][0]), max(sv[j][1], sv[j][2]));
                    if (__all(mx != SENT) || spin > SPIN_CAP) break;
                }
                if (wid == 4) STAMP(4);
                const float mean = s1 * (1.0f / D_LEN);
                const float var = fmaxf(s2 * (1.0f / D_LEN) - mean * mean, 0.f);
                const float mb = rbf(mean), rden = __builtin_amdgcn_rcpf(rbf(__builtin_amdgcn_sqrtf(rbf(rbf(var) + 1e-5f))));
                const unsigned kb = wave_max_s(r2key((s3 - mb * wsum) * rden, code));
                __builtin_amdgcn_raw_buffer_store_b32(kb, rKy, vk, 0, 16);
                SB();
                {
                    const unsigned cr_ = ~kb & 63u;
                    u32x2 c = *(const LAS u32x2*)(size_t)(ht + cr_ * 256 + (hb ^ ((cr_ & 15) * 16)));
                    const float m_ = __int_as_float(__builtin_amdgcn_readlane(__float_as_int(mb), cr_));
                    const float d_ = __int_as_float(__builtin_amdgcn_readlane(__float_as_int(rden), cr_));
#pragma unroll
                    for (int i = 0; i < 2; ++i) {
                        const unsigned u = c[i];
                        const unsigned dd = pk_bf16(bflo(u) - m_, bfhi(u) - m_);
                        const unsigned q = pk_bf16(bflo(dd) * d_, bfhi(dd) * d_);
                        c[i] = min(__float_as_uint(pick(lo, __uint_as_float(u), __uint_as_float(q))), SENT - 1u);
                    }
                    __builtin_amdgcn_raw_buffer_store_b64(c, rC, vc, 0, 16);
                    SB();
                    *(LAS u32x2*)(size_t)ra = (u32x2){__float_as_uint(mb), __float_as_uint(rden)};
                    if (lane == 0) ctl[2 + wq] = cr_;
                }
                WAIT_LGKM0();
                if (wid == 4) STAMP(5);
            }
            BARRIER();
#pragma unroll
            for (int i = WD_EARLY; i < 16; ++i) load_wd(i);
            if (slot < 112 && wid < 8 - BQ) {
                WAIT_VM0();
                if (wid == 4) STAMP(7);
                stage2(w2, slot, lw, lds + L_WD0 + lw * 8192, lds + L_WD1 + lw * 8192, abuf, 64, lane);
                if (wid == 4) STAMP(11);
            }
            FLUSH();
            return;
        }
    }

    P2_DEFS;
    LAS float* red = (LAS float*)(lds + L_RED);
    if (sidx < 112) {
        const int slot = sidx, grp = slot >> 3, kq = slot & 7;
        const __amdgpu_buffer_rsrc_t rG = mk_rsrc(a.wg, D_LEN * F_LEN * 2), rU = mk_rsrc(a.wu, D_LEN * F_LEN * 2);
        auto pair = [&](int i) {
            const int d = wid * 16 + i * 2 + (lane >> 5), ky = (d & 3) | ((d >> 1) & 4), c = lane & 31;
            const int vgu = ((kq * 128 + d) * F_LEN + grp * 256 + (((c >> 1) ^ ky) * 16) + (c & 1) * 8) * 2;
            if (i >= (wid < 4 ? G_N : G2_N + G3_N)) dma_nt(rG, (unsigned)(size_t)lds + wid * 8192 + i * 1024, vgu, 0);
            dma_nt(rU, (unsigned)(size_t)lds + 65536 + wid * 8192 + i * 1024, vgu ^ 512, 0);
        };
        if (wid >= 4) {
            if (W_PRE) __builtin_amdgcn_s_sleep(W_PRE_SL);
#pragma unroll
            for (int i = 0; i < W_PRE; ++i) pair(i);
        }
        BARRIER();
        if (wid < 4) {
#pragma unroll
            for (int i = 0; i < W_PRE; ++i) pair(i);
        }
#pragma unroll
        for (int i = W_PRE; i < 8; ++i) pair(i);
        if (wid >= 8 - BQ) {
            const __amdgpu_buffer_rsrc_t rD = mk_rsrc(a.wd + (size_t)slot * 32 * D_LEN, 32 * D_LEN * 2);
            const int vd = wd_off(wid - 4, 0, lane);
#pragma unroll
            for (int i = 0; i < 16; ++i)
                dma_nt(rD, (unsigned)(size_t)lds + L_YB + (wid & 1) * 16384 + i * 1024, vd ^ ((2 * (i & 1) | (i & 4)) * 32), i * (4 * D_LEN));
        }
        const __amdgpu_buffer_rsrc_t rK = mk_rsrc(w2, wid == 0 ? (unsigned)(WS_CAND + 32 * 4096) : 0u);
        const int ok_ = lane < 40 ? (int)WS_KEYS + (xcd >> 2) * 8192 + (xcd & 3) * (KEY_S * 16) + (lane - 32) * KEY_S : (int)(WS_CAND + 32 * 4096);
        const int oc = (int)WS_CAND + 2048 + kq * 256 + n * 16;
        int best = 0;
        u32x4 v = {0u, 0u, 0u, 0u};
        auto request = [&]() {
            asm volatile("" ::: "memory");
            v = LD16_SC1(rK, lane < 32 ? oc + best * 4096 : ok_);
            SB();
        };
        auto settled = [&]() {
            const unsigned mx = umax4(v), mn = min(min(v[0], v[1]), min(v[2], v[3]));
            unsigned fv = lane < 32 ? ~mx : mn | (unsigned)(lane >> 3 ^ 4);
            asm volatile("" : "+v"(fv));
            const bool fine = fv != 0u;
            unsigned km = max(mx, dppu<0xB1>(mx));
            km = max(km, dppu<0x4E>(km));
            km = max(km, dppu<0x141>(km));
            const int nb = (int)(((0xffffu - ((unsigned)__builtin_amdgcn_readlane((int)km, 32) & 0xffffu)) >> 6) & 31u);
            const bool same = nb == best;
            best = nb;
            return __all(fine) && same;
        };
        if (wid == 0) {
            request();
            WAIT_VM(0);
            settled();
            request();
        } else if (wid >= 8 - BQ) WAIT_VM(16);
        else WAIT_VM(0);
        BARRIER();
        if (wid == 0) STAMP(8);
        bf16x8 wfr[4][4];
        {
            const int q = (lane >> 2) & 3, ky = q | ((g & 1) << 2);
#pragma unroll
            for (int u = 0; u < 4; ++u) {
                const int tro = (u >> 1) * 65536 + (8 * g + q) * 512 + (((wid * 2 + (u & 1)) ^ ky) * 32) + (lane & 3) * 8;
#pragma unroll
                for (int ks = 0; ks < 4; ++ks) {
                    const bf16x4 w0 = __builtin_amdgcn_ds_read_tr16_b64_v4i16((LAS bf16x4*)(lds + tro + ks * 16384));
                    const bf16x4 w1 = __builtin_amdgcn_ds_read_tr16_b64_v4i16((LAS bf16x4*)(lds + tro + ks * 16384 + 2048));
                    wfr[u][ks] = __builtin_shufflevector(w0, w1, 0, 1, 2, 3, 4, 5, 6, 7);
                }
            }
            SB();
#pragma unroll
            for (int i = 0; i < 16; ++i) asm volatile("" : "+v"(wfr[i >> 2][i & 3]));
        }
        if (wid >= 8 - BQ && lane < 16) *(LAS unsigned*)(lds + 8192 + wid * 64 + lane * 4) = 0u;
        __amdgpu_buffer_rsrc_t rPo = mk_rsrc(w2 + WS_PRE, 112 * 2048);
        int po = (n & 3) == 0 ? pre_off((grp ^ (g >> 1)) * 8 + wid) + kq * 256 + g * 64 + n * 4 : 112 * 2048;
        unsigned xw = (unsigned)(size_t)lds + (n >> 3) * 16384 + (g & 1) * 8192 + (n & 7) * 8, xr = (unsigned)(size_t)lds + (n & 1) * 8192 + g * 16;
        unsigned ix = (n & 1) ? 0xEEEEEEEEu : 0x44444444u;
        asm volatile("" : "+v"(po), "+v"(xw), "+v"(xr), "+v"(ix));
        u64 m1 = 0xFFFF0000FFFF0000ull, m2 = 0xFFFFFFFF00000000ull;
        f32x4 ac[4];
#pragma unroll
        for (int u = 0; u < 4; ++u) { ac[u] = (f32x4){0.f, 0.f, 0.f, 0.f}; asm volatile("" : "+v"(ac[u])); }
        asm volatile("" : "+s"(rPo), "+s"(m1), "+s"(m2));
        if (wid == 0) {
#pragma unroll 1
            for (int spin = 0; spin <= SPIN_CAP && !settled(); ++spin) {
                __builtin_amdgcn_s_sleep(1);
                request();
            }
            __builtin_amdgcn_s_waitcnt(0x0F70);
            if (lane < 32) *(LAS u32x2*)(size_t)xw = (u32x2){lane < 16 ? v[0] : v[1], lane < 16 ? v[2] : v[3]};
            WAIT_LGKM0();
            STAMP(9);
        }
        BARRIER();
        {
            bf16x8 xf[2];
#pragma unroll
            for (int j = 0; j < 2; ++j) xf[j] = *(const LAS bf16x8*)((const LAS unsigned char*)(size_t)xr + j * 16384);
            SB();
            float rs[3];
#pragma unroll
            for (int i = 0; i < 8; ++i) {
                const int u = i >> 1, j = i & 1;
                ac[u] = smf(xf[j], wfr[u][2 * j], wfr[u][2 * j + 1], ac[u], ix);
                SB();
                if (i == 4) { asm volatile("" : "+v"(ac[0])); rs[0] = ac[0][0] + ac[0][1]; asm volatile("" : "+v"(rs[0])); }
                if (i == 6) { asm volatile("" : "+v"(ac[1])); rs[0] = pick(m1, rs[0], ac[1][0] + ac[1][1]); asm volatile("" : "+v"(rs[0])); }
                if (i == 7) { asm volatile("" : "+v"(ac[2])); rs[2] = ac[2][0] + ac[2][1]; asm volatile("" : "+v"(rs[2])); }
                SB();
            }
            const unsigned e = min(__float_as_uint(pick(m2, rs[0], pick(m1, rs[2], ac[3][0] + ac[3][1]))), SENT - 1u);
            const u32x4 sv = {e, dppu<0x55>(e), dppu<0xAA>(e), dppu<0xFF>(e)};
            ST16_SC1(sv, rPo, po);
            SB();
            asm volatile("" :: "v"(v));
        }
        if (wid == 0) STAMP(14);
        if (wid >= 8 - BQ) {
            WAIT_VM(1);
            if (wid == 7) STAMP(15);
            stage2(w2, slot, wid - 4, lds + L_YB + (wid & 1) * 16384, lds + L_YB + (wid & 1) * 16384 + 8192, lds + wid * 64, 8192, lane);
            if (wid == 7) STAMP(10);
        }
    } else {
        const int rj = sidx - 112;
        if (blockIdx.x == 255 && tid == 0) {
            const u64 want = ((u64)ctl1[3] << 32) | ctl1[2];
            __hip_atomic_store((GAS u64*)(ws + WS_TAG), want, __ATOMIC_RELAXED, __HIP_MEMORY_SCOPE_AGENT);
        }
        if (wid == 4) {
            unsigned kk;
            const unsigned k = poll_winner(w2, xcd, lane, kk);
            if (lane == 0) ctl[4] = k;
            if (lane < 32) ctl[8 + lane] = kk;
            WAIT_LGKM0();
        }
        BARRIER();
        const unsigned kw = CTL(4);
        if (wid == 0) STAMP(16);
        {
            const unsigned kc = ctl[8 + wid * 4 + g];
            const __amdgpu_buffer_rsrc_t rL = mk_rsrc(w2 + WS_CAND + (size_t)wid * 16384, 16384);
            u32x2 hv2, nv2;
            for (int spin = 0;; ++spin) {
                asm volatile("" ::: "memory");
                hv2 = __builtin_amdgcn_raw_buffer_load_b64(rL, g * 4096 + (rj * 64 + n * 4) * 2, 0, 16);
                nv2 = __builtin_amdgcn_raw_buffer_load_b64(rL, g * 4096 + 2048 + (rj * 64 + n * 4) * 2, 0, 16);
                if (__all(max(max(hv2[0], hv2[1]), max(nv2[0], nv2[1])) != SENT) || spin > SPIN_CAP) break;
                __builtin_amdgcn_s_sleep(1);
            }
            u32x2 ov;
#pragma unroll
            for (int i = 0; i < 2; ++i) {
                const unsigned u = hv2[i], q = nv2[i];
                ov[i] = pk_bf16(bflo(u) + bflo(q), bfhi(u) + bfhi(q));
            }
            const int crow = (int)(0xffffu - (kc & 0xffffu)) & (S_LEN - 1);
            if (kc != kw) *(u32x2*)(a.out + (size_t)crow * D_LEN + rj * 64 + n * 4) = ov;
        }
        const int wrow = (int)(0xffffu - (kw & 0xffffu)) & (S_LEN - 1);
        const __amdgpu_buffer_rsrc_t rC = mk_rsrc(w2 + WS_CAND + (size_t)(wrow >> 6) * 4096, 4096);
        const __amdgpu_buffer_rsrc_t rPt = mk_rsrc(w2 + WS_PART, 112 * 4096);
        const int ocol = (rj >> 2) * 256 + ((lane >> 1) & 1) * 128 + (rj & 3) * 32 + (lane & 1) * 16 + (lane >> 2);
        unsigned hv, hnv;
        for (int spin = 0;; ++spin) {
            asm volatile("" ::: "memory");
            hv = __builtin_amdgcn_raw_buffer_load_b32(rC, (ocol & ~1) * 2, 0, 16);
            hnv = __builtin_amdgcn_raw_buffer_load_b32(rC, 2048 + (ocol & ~1) * 2, 0, 16);
            if (__all(max(hv, hnv) != SENT) || spin > SPIN_CAP) break;
        }
        float sgm = rbf(1.0f / (1.0f + __expf(-key2r(kw))));
        float hf = (ocol & 1) ? bfhi(hv) : bflo(hv), hnf = (ocol & 1) ? bfhi(hnv) : bflo(hnv);
        GAS unsigned short* optr = (GAS unsigned short*)(a.out + (size_t)wrow * D_LEN + ocol);
        unsigned rw = (unsigned)(size_t)red + (wid >> 2) * 1024 + (n * 4 + g) * 16 + (wid & 3) * 4, rr = (unsigned)(size_t)red + lane * 16;
        int po = wid * 16384 + (rj >> 3) * 8192 + g * 2048 + ((rj & 7) * 64 + n * 4) * 4;
        int po3 = po + 3 * 131072;
        asm volatile("" : "+v"(sgm), "+v"(optr), "+v"(hf), "+v"(hnf), "+v"(rw), "+v"(rr), "+v"(po), "+v"(po3));
        float sm;
#pragma unroll 1
        for (int spin = 0;; ++spin) {
            asm volatile("" ::: "memory");
            u32x4 pv[4];
#pragma unroll
            for (int i = 0; i < 4; ++i) pv[i] = __builtin_amdgcn_raw_buffer_load_b128(rPt, i < 3 ? po : po3, i < 3 ? i * 131072 : 0, 16);
            SB();
            unsigned t[4];
#pragma unroll
            for (int j = 0; j < 4; ++j) {
                const unsigned u0 = pv[0][j], u1 = pv[1][j], u2 = pv[2][j], u3 = pv[3][j];
                t[j] = __float_as_uint(((__uint_as_float(u0) + __uint_as_float(u1)) + __uint_as_float(u2)) + __uint_as_float(u3));
            }
            const auto r0 = __builtin_amdgcn_permlane32_swap(t[0], t[2], false, false), r1 = __builtin_amdgcn_permlane32_swap(t[1], t[3], false, false);
            const unsigned a0 = r0[0], a1 = r0[1], b0 = r1[0], b1 = r1[1];
            const auto r2 = __builtin_amdgcn_permlane16_swap(__float_as_uint(__uint_as_float(a0) + __uint_as_float(a1)),
                                                             __float_as_uint(__uint_as_float(b0) + __uint_as_float(b1)), false, false);
            const unsigned c0 = r2[0], c1 = r2[1];
            sm = __uint_as_float(c0) + __uint_as_float(c1);
            if (__builtin_expect(__builtin_amdgcn_ballot_w64(sm != sm) == 0, 1)) break;
            if (__all(max(max(umax4(pv[0]), umax4(pv[1])), max(umax4(pv[2]), umax4(pv[3]))) != SENT) || spin > SPIN_CAP) break;
            if (FIN_SLEEP) __builtin_amdgcn_s_sleep(FIN_SLEEP);
        }
        if (wid == 0) STAMP(17);
        *(LAS float*)(size_t)rw = sm;
        WAIT_LGKM0();
        BARRIER();
        if (wid == 0) {
            const f32x4 u = *(const LAS f32x4*)(size_t)rr, v = *(const LAS f32x4*)((const LAS unsigned char*)(size_t)rr + 1024);
            const f32x4 w = u + v;
            const float t = (w[0] + w[2]) + (w[1] + w[3]);
            *optr = (unsigned short)pk_bf16(hf + rbf(hnf + rbf(rbf(t) * sgm)), 0.f);
            STAMP(18);
        }
        __builtin_amdgcn_s_waitcnt(0x0F70);
    }
    FLUSH();
}

extern "C" void kernel_launch(void* const* d_in, const int* in_sizes, int n_in, void* d_out, int out_size, void* d_ws,
                              size_t ws_size, hipStream_t stream) {
    Args a;
    a.x = (const bf16_t*)d_in[0];
    a.cr = (const bf16_t*)d_in[1];
    a.ci = (const bf16_t*)d_in[2];
    a.wre = (const bf16_t*)d_in[3];
    a.wim = (const bf16_t*)d_in[4];
    a.w_r = (const bf16_t*)d_in[6];
    a.wg = (const bf16_t*)d_in[7];
    a.wu = (const bf16_t*)d_in[8];
    a.wd = (const bf16_t*)d_in[9];
    a.out = (bf16_t*)d_out;
    a.ws = (char*)d_ws + ((ws_size - ((size_t)24 << 20)) & ~(size_t)255);
    (void)hipFuncSetAttribute((const void*)fused_kernel, hipFuncAttributeMaxDynamicSharedMemorySize, LDS_BYTES);
    fused_kernel<<<256, 768, LDS_BYTES, stream>>>(a);
}
```

```cpp
#include <hip/hip_runtime.h>
#include <hip/hip_bf16.h>
#include <stdint.h>

typedef unsigned short bf16_t;
typedef short bf16x8 __attribute__((ext_vector_type(8)));
typedef short bf16x4 __attribute__((ext_vector_type(4)));
typedef float f32x4 __attribute__((ext_vector_type(4)));
typedef unsigned u32x4 __attribute__((ext_vector_type(4)));
typedef unsigned u32x2 __attribute__((ext_vector_type(2)));
typedef unsigned u32x3 __attribute__((ext_vector_type(3)));
typedef unsigned long long u64;
#define LAS __attribute__((address_space(3)))
#define GAS __attribute__((address_space(1)))

constexpr int S_LEN = 2048, D_LEN = 1024, F_LEN = 3584;
constexpr unsigned SENT = 0xFFFFFFFFu;
constexpr int SPIN_CAP = 1 << 17;

constexpr size_t WS_KEYS = 0;
constexpr size_t WS_FLAGS = 16384;
constexpr size_t WS_XRD = 65536;
constexpr size_t WS_FLAG1 = 196608;
constexpr size_t WS_TAG = 131072;
constexpr size_t WS_VOTE = WS_TAG + 4096;
constexpr size_t WS_PROBE = (size_t)20 << 20;
constexpr size_t WS_AT = (size_t)4 << 20;
constexpr size_t WS_Y = (size_t)12 << 20;
constexpr size_t WS_STATS = (size_t)16 << 20;
constexpr size_t WS_PART = WS_STATS + (512u << 10);
constexpr size_t WS_CAND = WS_STATS + (1024u << 10);
constexpr size_t WS_PRE = WS_STATS + (1152u << 10);
constexpr size_t FILL_BYTES = (size_t)1408 << 10;

__device__ __forceinline__ float bflo(unsigned v) { return __uint_as_float(v << 16); }
__device__ __forceinline__ float bfhi(unsigned v) { return __uint_as_float(v & 0xffff0000u); }
__device__ __forceinline__ unsigned pk_bf16(float lo, float hi) {
    typedef __bf16 bf2 __attribute__((ext_vector_type(2)));
    typedef float f2 __attribute__((ext_vector_type(2)));
    f2 v = {lo, hi};
    bf2 r = __builtin_convertvector(v, bf2);
    return __builtin_bit_cast(unsigned, r);
}
__device__ __forceinline__ float rbf(float x) { return __uint_as_float(pk_bf16(0.f, x)); }

template <int CTRL> __device__ __forceinline__ unsigned dppu(unsigned v) {
    return (unsigned)__builtin_amdgcn_update_dpp(0, (int)v, CTRL, 0xf, 0xf, true);
}
template <int CTRL> __device__ __forceinline__ float dppf(float v) { return __uint_as_float(dppu<CTRL>(__float_as_uint(v))); }
__device__ __forceinline__ void swap16(unsigned v, unsigned& a, unsigned& b) {
    auto r = __builtin_amdgcn_permlane16_swap(v, v, false, false);
    a = r[0]; b = r[1];
}
__device__ __forceinline__ void swap32(unsigned v, unsigned& a, unsigned& b) {
    auto r = __builtin_amdgcn_permlane32_swap(v, v, false, false);
    a = r[0]; b = r[1];
}
__device__ __forceinline__ float sum_rows(float v) {
    unsigned a, b;
    swap16(__float_as_uint(v), a, b); v = __uint_as_float(a) + __uint_as_float(b);
    swap32(__float_as_uint(v), a, b); v = __uint_as_float(a) + __uint_as_float(b);
    return v;
}
__device__ __forceinline__ float wave_sum(float v) {
    v += dppf<0xB1>(v);
    v += dppf<0x4E>(v);
    v += dppf<0x141>(v);
    v += dppf<0x140>(v);
    return sum_rows(v);
}
__device__ __forceinline__ unsigned wave_max_u32(unsigned v) {
    v = max(v, dppu<0xB1>(v));
    v = max(v, dppu<0x4E>(v));
    v = max(v, dppu<0x141>(v));
    v = max(v, dppu<0x140>(v));
    unsigned a, b;
    swap16(v, a, b); v = max(a, b);
    swap32(v, a, b); v = max(a, b);
    return v;
}
__device__ __forceinline__ unsigned wave_max_s(unsigned v) {
    v = max(v, dppu<0xB1>(v));
    v = max(v, dppu<0x4E>(v));
    v = max(v, dppu<0x141>(v));
    v = max(v, dppu<0x140>(v));
    v = max(v, dppu<0x142>(v));
    v = max(v, dppu<0x143>(v));
    return (unsigned)__builtin_amdgcn_readlane((int)v, 63);
}
__device__ __forceinline__ float sel4(int g, float a, float b, float c, float d) {
    const float t0 = (g & 1) ? b : a, t1 = (g & 1) ? d : c;
    return (g & 2) ? t1 : t0;
}
__device__ __forceinline__ float pick(u64 m, float a, float b) {
    float r;
    asm("v_cndmask_b32 %0, %1, %2, %3" : "=v"(r) : "v"(a), "v"(b), "s"(m));
    return r;
}
__device__ __forceinline__ unsigned umax4(u32x4 v) { return max(max(v[0], v[1]), max(v[2], v[3])); }

__device__ __forceinline__ void touch_line(const void* p, unsigned lds_dst) {
    unsigned keep;
    asm volatile("s_mov_b32 %0, m0\n\ts_mov_b32 m0, %2\n\ts_nop 0\n\tglobal_load_lds_dword %1, off\n\ts_mov_b32 m0, %0"
                 : "=&s"(keep) : "v"(p), "s"(lds_dst) : "memory");
}
__device__ __forceinline__ __amdgpu_buffer_rsrc_t mk_rsrc(const void* p, unsigned bytes) {
    return __builtin_amdgcn_make_buffer_rsrc((void*)p, 0, (int)bytes, 0x00020000);
}
#define LDS_LOAD(w, v, o, pol) "buffer_load_" w " " v ", %0, %2 offen" o pol " lds"
#define LDS_M0 "s_mov_b32 m0, %1\n\ts_nop 0\n\t"
__device__ __forceinline__ void dma_nt(__amdgpu_buffer_rsrc_t r, unsigned dst, int voff, int soff) {
    asm volatile(LDS_M0 LDS_LOAD("dwordx4", "%3", "", " nt") :: "s"(r), "s"(dst), "s"(soff), "v"(voff) : "memory", "m0");
}
template <bool SC1> __device__ __forceinline__ void dma_word(__amdgpu_buffer_rsrc_t r, unsigned dst, int voff) {
    if constexpr (SC1) asm volatile(LDS_M0 LDS_LOAD("dword", "%3", "", " sc1") :: "s"(r), "s"(dst), "n"(0), "v"(voff) : "memory", "m0");
    else asm volatile(LDS_M0 LDS_LOAD("dword", "%3", "", "") :: "s"(r), "s"(dst), "n"(0), "v"(voff) : "memory", "m0");
}
template <bool SC1> __device__ __forceinline__ void dma_2k(__amdgpu_buffer_rsrc_t r, unsigned dst, int v0, int v1, int soff) {
    if constexpr (SC1) asm volatile(LDS_M0 LDS_LOAD("dwordx4", "%3", "", " sc1") "\n\t" LDS_LOAD("dwordx4", "%4", " offset:1024", " sc1")
                 :: "s"(r), "s"(dst), "s"(soff), "v"(v0), "v"(v1) : "memory", "m0");
    else asm volatile(LDS_M0 LDS_LOAD("dwordx4", "%3", "", "") "\n\t" LDS_LOAD("dwordx4", "%4", " offset:1024", "")
                 :: "s"(r), "s"(dst), "s"(soff), "v"(v0), "v"(v1) : "memory", "m0");
}
template <bool SC1> __device__ __forceinline__ void dma_4k(__amdgpu_buffer_rsrc_t r, unsigned dst, const int (&v)[4], int soff) {
#define FOUR(pol) asm volatile(LDS_M0 LDS_LOAD("dwordx4", "%3", "", pol) "\n\t" LDS_LOAD("dwordx4", "%4", " offset:1024", pol) "\n\t" \
        LDS_LOAD("dwordx4", "%5", " offset:2048", pol) "\n\t" LDS_LOAD("dwordx4", "%6", " offset:3072", pol) \
        :: "s"(r), "s"(dst), "s"(soff), "v"(v[0]), "v"(v[1]), "v"(v[2]), "v"(v[3]) : "memory", "m0")
    if constexpr (SC1) FOUR(" sc1"); else FOUR("");
#undef FOUR
}
#define CODE_BYTES 25984
__device__ __forceinline__ __amdgpu_buffer_rsrc_t code_rsrc() {
    unsigned long long p; unsigned d;
    asm volatile("s_getpc_b64 %0\n1:\n\ts_mov_b32 %1, 1b-_Z12fused_kernel4Args" : "=s"(p), "=s"(d));
    return __builtin_amdgcn_make_buffer_rsrc((void*)(p - d), 0, CODE_BYTES, 0x00020000);
}
#define LD16_SC1(rs, off) __builtin_amdgcn_raw_buffer_load_b128(rs, off, 0, 16)
#define ST16_SC1(v, rs, off) __builtin_amdgcn_raw_buffer_store_b128(v, rs, off, 0, 16)

constexpr int BT = 128, BK = 64, NSTAGE = 4;
constexpr int TILE_BYTES = BT * BK * 2;
constexpr int STAGE_BYTES = 2 * TILE_BYTES;
constexpr int RING_BYTES = NSTAGE * STAGE_BYTES;
constexpr int L_DUMMY = RING_BYTES;
constexpr int L_CTL = RING_BYTES + 256;
constexpr int L_RED = RING_BYTES + 512;

#define WAIT_VM(n) asm volatile("s_waitcnt vmcnt(" #n ")" ::: "memory")
#define WAIT_VM0() do { asm volatile("" ::: "memory"); __builtin_amdgcn_s_waitcnt(0x0F70); asm volatile("s_waitcnt vmcnt(0)" ::: "memory"); } while (0)
#define WAIT_LGKM0() asm volatile("s_waitcnt lgkmcnt(0)" ::: "memory")
#define BARRIER() do { asm volatile("" ::: "memory"); __builtin_amdgcn_s_barrier(); asm volatile("" ::: "memory"); } while (0)

__device__ __forceinline__ int prow(int rho) {
    const int w = rho >> 6, m = (rho >> 4) & 3, q = (rho >> 2) & 3, r = rho & 3;
    return w * 64 + (m >> 1) * 32 + q * 8 + (m & 1) * 4 + r;
}
__device__ __forceinline__ unsigned r2key(float r, unsigned code) {
    const unsigned u = pk_bf16(0.f, r);
    return ((u ^ ((unsigned)((int)u >> 31) | 0x80000000u)) & 0xffff0000u) | code;
}
__device__ __forceinline__ float key2r(unsigned k) {
    const unsigned o = k >> 16;
    return bflo((o & 0x8000u) ? (o & 0x7fffu) : (~o & 0xffffu));
}
#define STAT_M 5
#define W_PRE 4
#define W_PRE_SL 12
#define BQ 2
#define POLL_T (STAT_M + 5)
#define AUX_SLEEP 12
#define WD_EARLY 2
#define C2_START 0
#define C2_STAG 7
#define TAIL_PRIO 3
#define STAT_DELAY 12
#define AUX_STAY 1
#define CATCH1 4
#define CPF_MASK1 0x1080u
#define CPF_MASK2 0x1080u
#define CPF_STAG 5
#define CPF_FIX 0x1000u
#define KEY_S 128
#define G2_N 2
#define G3_N 2
#define FIN_SLEEP 1
#define G_N 8
#define POLL_SLEEP 4
__device__ __forceinline__ f32x4 smf(bf16x8 a, bf16x8 b0, bf16x8 b1, f32x4 d, unsigned idx) {
    typedef __bf16 bf8 __attribute__((ext_vector_type(8)));
    typedef __bf16 bf16v __attribute__((ext_vector_type(16)));
    const auto b = __builtin_shufflevector(b0, b1, 0, 1, 2, 3, 4, 5, 6, 7, 8, 9, 10, 11, 12, 13, 14, 15);
    return __builtin_amdgcn_smfmac_f32_16x16x64_bf16(__builtin_bit_cast(bf8, a), __builtin_bit_cast(bf16v, b), d, (int)idx, 0, 0);
}
__device__ __forceinline__ float dot2bf(unsigned a, unsigned b, float c) {
    typedef __bf16 bf2 __attribute__((ext_vector_type(2)));
    return __builtin_amdgcn_fdot2_f32_bf16(__builtin_bit_cast(bf2, a), __builtin_bit_cast(bf2, b), c, false);
}

#define STAMP(i) do { } while (0)
#define CSTAMP(i) do { } while (0)
#define FLUSH() do { } while (0)
#define TSV do { } while (0)

#define KSHIFT 4

constexpr int L_HT = ((32 + KSHIFT) % NSTAGE) * STAGE_BYTES;
constexpr int L_WD0 = ((33 + KSHIFT) % NSTAGE) * STAGE_BYTES;
constexpr int L_WD1 = ((34 + KSHIFT) % NSTAGE) * STAGE_BYTES;
constexpr int L_YB = RING_BYTES;
constexpr int L_ATAB = L_YB;
constexpr int LDS_BYTES = RING_BYTES + 32768;

#define SB() __builtin_amdgcn_sched_barrier(0)
template <int N> __device__ __forceinline__ void wait_vm() { asm volatile("s_waitcnt vmcnt(%0)" :: "n"(N) : "memory"); }
__device__ __forceinline__ int at_tile(int c, int par) { return ((c << 8) & 0x78000) | (((c << 2) & 0x2000) ^ par); }
__device__ __forceinline__ int at_panel(int ep) { return ep << 19; }
__device__ __forceinline__ int pre_off(int slot) { const int s_ = slot >= 56, l = slot - 56 * s_; return (l >> 2) * 16384 + s_ * 8192 + (l & 3) * 2048; }
static_assert(KSHIFT == 4, "flag samples must have replaced older contents of their slot; even tile counts");
struct Ring {
    __amdgpu_buffer_rsrc_t rP, rQ, rF;
    int voP[4], voQ[4], voF;
    LAS unsigned char* fl;
    unsigned lb;
    int sp, sq;
    int poff, par, xm;
    bool img;
    template <int LD, bool PIMG> __device__ __forceinline__ void setup(int lw, int lane) {
        const int r = lane >> 3, v0 = (PIMG ? r : (r & 3) + (r & 4) * 2) * (LD * 2) + (((lane & 7) ^ (lane >> 4)) << 4);
#pragma unroll
        for (int j = 0; j < 4; ++j) {
            voQ[j] = (v0 ^ ((j & 1) * 64)) + (PIMG ? j * 8 : (j & 1) * 16 + (j >> 1) * 4) * (LD * 2) - j * 1024;
            voP[j] = PIMG ? (lw >> 1) * 16384 + (lw & 1) * 4096 + lane * 16 : voQ[j];
        }
        img = PIMG;
    }
    __device__ __forceinline__ void seek(int col, int pof) {
        sq = col; poff = pof;
        sp = (img ? at_tile(sq, par) : sq) + poff;
    }
    __device__ __forceinline__ void step() {
        sq = (sq + BK * 2) & 4095;
        sp = (img ? at_tile(sq ^ xm, par) : sq) + poff;
        asm volatile("" : "+s"(sq), "+s"(sp));
    }
    __device__ __forceinline__ void sample() { dma_word<true>(rF, (unsigned)(size_t)fl, voF); }
    __device__ __forceinline__ unsigned peek(unsigned addr) {
        unsigned v;
        asm volatile("ds_read_b32 %0, %1\n\ts_waitcnt lgkmcnt(0)" : "=v"(v) : "v"(addr) : "memory");
        return v;
    }
    __device__ __forceinline__ void gate(unsigned want, unsigned sa, unsigned need, unsigned& have) {
#pragma unroll 1
        for (int spin = 0; spin <= SPIN_CAP && (need & ~have) != 0u; ++spin) {
            if (spin) { sample(); WAIT_VM(0); __builtin_amdgcn_s_sleep(1); }
            const unsigned long long m = __ballot(peek(sa) == want);
            have = (unsigned)m & (unsigned)(m >> 32);
        }
    }
    template <int AUX> __device__ __forceinline__ void issueP(LAS unsigned char* base) { dma_4k<AUX != 0>(rP, (unsigned)(size_t)base, voP, sp); }
    __device__ __forceinline__ void issueQ(LAS unsigned char* base) { dma_4k<false>(rQ, (unsigned)(size_t)base + TILE_BYTES, voQ, sq ^ xm); }
    template <bool P2> __device__ __forceinline__ void issue(LAS unsigned char* lds) {
        issueP<P2 ? 16 : 0>(lds + lb);
        issueQ(lds + lb);
        SB();
        if (P2) sample();
        lb = (lb + STAGE_BYTES) & (RING_BYTES - 1);
        asm volatile("" : "+s"(lb));
        step();
        SB();
    }
    template <int CAT> __device__ __forceinline__ void ramp(LAS unsigned char* lds) {
#pragma unroll 1
        for (int i = 1; i <= CAT + 1; ++i) {
            issue<false>(lds);
            if (i == CAT + 1) continue;
            WAIT_VM(8);
            BARRIER();
        }
    }
    template <bool P2, bool SAMP, int W0 = 16> __device__ __forceinline__ void steady(int cnt, int trig = 0, int v2 = 0) {
        int t, u;
#define LD(v, r, s, o) "buffer_load_dwordx4 " v ", " r ", " s " offen" o " lds\n\t"
        if constexpr (P2) {
#define BODY(SMP, TST, ENT) asm volatile("s_mov_b32 m0, %0\n" ENT "1:\n\ts_waitcnt vmcnt(16)\n2:\n\ts_barrier\n\t" \
            LD("%8", "%6", "%2", " sc1") LD("%8", "%6", "%2", " offset:1024 sc1") LD("%8", "%6", "%2", " offset:2048 sc1") LD("%8", "%6", "%2", " offset:3072 sc1") \
            SMP "s_xor_b32 %4, %1, %18\n\ts_add_i32 %1, %1, 0x80\n\ts_add_i32 m0, %0, 0x4000\n\ts_and_b32 %1, %1, 0xfff\n\t" \
            LD("%9", "%7", "%4", "") "s_xor_b32 %2, %1, %18\n\ts_lshl_b32 %5, %1, 2\n\ts_lshl_b32 %2, %2, 8\n\ts_and_b32 %2, %2, 0x78000\n\t" \
            LD("%10", "%7", "%4", " offset:1024") "s_and_b32 %5, %5, 0x2000\n\ts_xor_b32 %5, %5, %13\n\ts_or_b32 %2, %2, %5\n\ts_add_i32 %3, %3, -1\n\t" \
            LD("%11", "%7", "%4", " offset:2048") "s_add_i32 %0, %0, 0x8000\n\ts_and_b32 %0, %0, 0x1ffff\n\t" TST "s_cmp_lg_u32 %3, 0\n\t" \
            LD("%12", "%7", "%4", " offset:3072") "\n4:\n\ts_mov_b32 m0, %0\n\ts_cbranch_scc1 1b\n\ts_waitcnt vmcnt(16)\n\ts_barrier" \
            : "+s"(lb), "+s"(sq), "+s"(sp), "+s"(cnt), "=&s"(t), "=&s"(u) \
            : "s"(rP), "s"(rQ), "v"(voP[0]), "v"(voQ[0]), "v"(voQ[1]), "v"(voQ[2]), "v"(voQ[3]), "s"(par | poff), "s"(rF), "v"(voF), "s"((unsigned)(size_t)fl), "n"(W0), "s"(xm), \
              "s"(trig), "v"(v2) \
            : "memory", "scc", "m0")
            if constexpr (SAMP) BODY("s_mov_b32 m0, %16\n\ts_nop 0\n\tbuffer_load_dword %15, %14, 0 offen sc1 lds\n\t", "", "");
            else BODY("", "s_cmp_eq_u32 %3, %19\n\ts_cbranch_scc1 3b\n\t",
                      "\ts_waitcnt vmcnt(%17)\n\ts_branch 2f\n3:\n\t" LD("%12", "%7", "%4", " offset:3072") "s_mov_b32 m0, %16\n\ts_nop 0\n\tbuffer_load_dword %20, %7, 0 offen lds\n\t"
                      "s_add_i32 m0, %16, 0x200\n\ts_cmp_lg_u32 %3, 0\n\tbuffer_load_dword %15, %14, 0 offen sc1 lds\n\ts_branch 4f\n");
#undef BODY
        } else {
            asm volatile("s_mov_b32 m0, %0\n\ts_branch 2f\n1:\n\ts_waitcnt vmcnt(16)\n\ts_barrier\n2:\n\t"
                LD("%6", "%4", "%1", "") LD("%7", "%4", "%1", " offset:1024") LD("%8", "%4", "%1", " offset:2048") LD("%9", "%4", "%1", " offset:3072")
                "s_add_i32 m0, %0, 0x4000\n\ts_mov_b32 %3, %1\n\t"
                LD("%6", "%5", "%3", "") "s_add_i32 %1, %1, 0x80\n\ts_add_i32 %0, %0, 0x8000\n\t"
                LD("%7", "%5", "%3", " offset:1024") "s_and_b32 %0, %0, 0x1ffff\n\ts_add_i32 %2, %2, -1\n\t"
                LD("%8", "%5", "%3", " offset:2048") "s_cmp_lg_u32 %2, 0\n\t"
                LD("%9", "%5", "%3", " offset:3072") "s_mov_b32 m0, %0\n\ts_cbranch_scc1 1b\n\ts_waitcnt vmcnt(16)\n\ts_barrier"
                : "+s"(lb), "+s"(sq), "+s"(cnt), "=&s"(t)
                : "s"(rP), "s"(rQ), "v"(voQ[0]), "v"(voQ[1]), "v"(voQ[2]), "v"(voQ[3])
                : "memory", "scc", "m0");
            (void)u;
        }
#undef LD
    }
};

template <bool PLIN, bool FIRST, class Pre>
__device__ __forceinline__ void compute_loop(const LAS unsigned char* lds, f32x4 (&acc)[4][4], int NT, int wp, int wq, int lane, Pre pre,
                                             bf16x8 (&p0)[4], bf16x8 (&p1)[4], unsigned last) {
    const int fr = lane & 15, fq = lane >> 4, key = (fr >> 1) & 7;
    const unsigned l0 = (unsigned)(size_t)lds;
    const unsigned oP0 = l0 + (PLIN ? wp * 8192 + lane * 16 : (wp * 64 + fr) * 128 + ((fq ^ key) * 16));
    const unsigned oP1 = l0 + (PLIN ? wp * 8192 + 1024 + lane * 16 : (wp * 64 + fr) * 128 + (((4 + fq) ^ key) * 16));
    const unsigned oQ0 = l0 + TILE_BYTES + (wq * 64 + fr) * 128 + ((fq ^ key) * 16);
    const unsigned oQ1 = l0 + TILE_BYTES + (wq * 64 + fr) * 128 + (((4 + fq) ^ key) * 16);
    unsigned aP0 = oP0, aQ0 = oQ0, aP1 = oP1, aQ1 = oQ1;
    bf16x8 r0[4], r1[4], q0[4], q1[4];
    unsigned xp = 2 * STAGE_BYTES;
#pragma unroll
    for (int i = 0; i < 16; ++i) asm volatile("" : "+v"(acc[i >> 2][i & 3]));
    asm volatile("" : "+v"(aP0), "+v"(aQ0), "+v"(aP1), "+v"(aQ1));
#define MM(pp, qq, m, n) do { acc[m][n] = __builtin_amdgcn_mfma_f32_16x16x32_bf16(pp[m], qq[n], acc[m][n], 0, 0, 0); SB(); } while (0)
#define RD(dst, i, a, st) do { dst[i] = *(const LAS bf16x8*)((const LAS unsigned char*)(size_t)(a) + (st) * STAGE_BYTES + (i) * 2048); SB(); } while (0)
#define ADP(a) do { a ^= xp; asm volatile("" : "+v"(a)); SB(); } while (0)
#define ADR(a) do { a ^= 2 * STAGE_BYTES; asm volatile("" : "+v"(a)); SB(); } while (0)
    __builtin_amdgcn_s_waitcnt(0xC07F);
    SB();
    if (FIRST) BARRIER();
    SB();
    RD(p0, 0, aP0, 0); RD(q0, 0, aQ0, 0); RD(p1, 0, aP1, 0); RD(q1, 0, aQ1, 0); RD(p1, 1, aP1, 0); RD(p0, 1, aP0, 0);
    RD(p0, 2, aP0, 0); RD(p1, 2, aP1, 0); RD(p1, 3, aP1, 0); RD(p0, 3, aP0, 0);
    pre();
    SB();
    int t = NT;
#pragma unroll 1
    do {
        MM(p0, q0, 0, 0); MM(p1, q1, 0, 0); RD(q0, 1, aQ0, 0);
        MM(p1, q1, 1, 0); MM(p0, q0, 1, 0); RD(q1, 1, aQ1, 0);
        MM(p0, q0, 2, 0); MM(p1, q1, 2, 0); RD(q0, 2, aQ0, 0);
        MM(p1, q1, 3, 0); MM(p0, q0, 3, 0); RD(q1, 2, aQ1, 0);
        MM(p0, q0, 3, 1); MM(p1, q1, 3, 1); RD(q0, 3, aQ0, 0);
        MM(p1, q1, 2, 1); MM(p0, q0, 2, 1); RD(q1, 3, aQ1, 0);
        MM(p0, q0, 1, 1); MM(p1, q1, 1, 1);
        MM(p1, q1, 0, 1); MM(p0, q0, 0, 1);
        MM(p0, q0, 0, 2); MM(p1, q1, 0, 2); __builtin_amdgcn_s_waitcnt(0xC07F); SB(); BARRIER(); SB();
        MM(p1, q1, 1, 2); RD(r0, 0, aP0, 1); MM(p0, q0, 1, 2); RD(q0, 0, aQ0, 1);
        MM(p0, q0, 2, 2); RD(r1, 0, aP1, 1); MM(p1, q1, 2, 2); RD(q1, 0, aQ1, 1);
        MM(p1, q1, 3, 2); MM(p0, q0, 3, 2); RD(r1, 1, aP1, 1);
        MM(p0, q0, 3, 3); RD(r0, 1, aP0, 1); MM(p1, q1, 3, 3);
        MM(p1, q1, 2, 3); RD(r0, 2, aP0, 1); MM(p0, q0, 2, 3); RD(r1, 2, aP1, 1);
        MM(p0, q0, 1, 3); MM(p1, q1, 1, 3); RD(r1, 3, aP1, 1);
        MM(p1, q1, 0, 3); RD(r0, 3, aP0, 1); MM(p0, q0, 0, 3);

        MM(r0, q0, 0, 0); MM(r1, q1, 0, 0); RD(q0, 1, aQ0, 1);
        MM(r1, q1, 1, 0); MM(r0, q0, 1, 0); RD(q1, 1, aQ1, 1);
        MM(r0, q0, 2, 0); MM(r1, q1, 2, 0); RD(q0, 2, aQ0, 1);
        MM(r1, q1, 3, 0); MM(r0, q0, 3, 0); RD(q1, 2, aQ1, 1);
        MM(r0, q0, 3, 1); MM(r1, q1, 3, 1); RD(q0, 3, aQ0, 1);
        MM(r1, q1, 2, 1); MM(r0, q0, 2, 1); RD(q1, 3, aQ1, 1);
        MM(r0, q0, 1, 1); t -= 2; if (PLIN) xp = t == 0 ? last : 2 * STAGE_BYTES; asm volatile("" : "+s"(t), "+s"(xp)); SB(); MM(r1, q1, 1, 1); ADP(aP0);
        MM(r1, q1, 0, 1); ADP(aP1); MM(r0, q0, 0, 1); ADR(aQ0);
        MM(r0, q0, 0, 2); ADR(aQ1); MM(r1, q1, 0, 2); __builtin_amdgcn_s_waitcnt(0xC07F); SB(); BARRIER(); SB();
        MM(r1, q1, 1, 2); RD(p0, 0, aP0, 0); MM(r0, q0, 1, 2); RD(q0, 0, aQ0, 0);
        MM(r0, q0, 2, 2); RD(p1, 0, aP1, 0); MM(r1, q1, 2, 2); RD(q1, 0, aQ1, 0);
        MM(r1, q1, 3, 2); MM(r0, q0, 3, 2); RD(p1, 1, aP1, 0);
        MM(r0, q0, 3, 3); RD(p0, 1, aP0, 0); MM(r1, q1, 3, 3);
        MM(r1, q1, 2, 3); RD(p0, 2, aP0, 0); MM(r0, q0, 2, 3); RD(p1, 2, aP1, 0);
        MM(r0, q0, 1, 3); MM(r1, q1, 1, 3); RD(p1, 3, aP1, 0);
        MM(r1, q1, 0, 3); RD(p0, 3, aP0, 0); MM(r0, q0, 0, 3);
    } while (t != 0);
    SB();
#undef MM
#undef RD
#undef ADR
#undef ADP
}

struct Args {
    const bf16_t* x; const bf16_t* wre; const bf16_t* wim; char* ws; const bf16_t* cr; const bf16_t* ci;
    const bf16_t* w_r; const bf16_t* wg; const bf16_t* wu; const bf16_t* wd; bf16_t* out;
};

__device__ __forceinline__ unsigned poll_winner(const char* ws, int rep, int lane, unsigned& mine) {
    GAS unsigned* kp = (GAS unsigned*)(ws + WS_KEYS + (size_t)(rep >> 2) * 8192 + (rep & 3) * (KEY_S * 16) + ((lane >> 2) & 7) * KEY_S) + (lane & 3);
    unsigned kw;
    for (int spin = 0;; ++spin) {
            asm volatile("" ::: "memory");
        kw = __hip_atomic_load(kp, __ATOMIC_RELAXED, __HIP_MEMORY_SCOPE_AGENT);
        if (__all(kw != 0u) || spin > SPIN_CAP) break;
        __builtin_amdgcn_s_sleep(POLL_SLEEP);
    }
    mine = kw;
    return __builtin_amdgcn_readfirstlane(wave_max_u32(kw));
}

__device__ __forceinline__ void stage2(char* w2, int slot, int lw, LAS unsigned char* img0, LAS unsigned char* img1, LAS unsigned char* abuf, int zoff, int lane) {
    const int g = lane >> 4, n = lane & 15;
    const __amdgpu_buffer_rsrc_t rPi = mk_rsrc(w2 + WS_PRE + pre_off(slot), 2048);
    unsigned pp[8];
    auto request = [&]() {
        asm volatile("" ::: "memory");
#pragma unroll
        for (int k = 0; k < 8; ++k) pp[k] = __builtin_amdgcn_raw_buffer_load_b32(rPi, k * 256 + lane * 4, 0, 16);
        SB();
    };
    request();
    bf16x8 wf[16];
    {
        const int q = (lane >> 2) & 3, ky = q | ((g & 1) << 2);
#pragma unroll
        for (int j = 0; j < 16; ++j) {
            LAS unsigned char* t = (g < 2 ? img0 : img1) + (8 * (g & 1) + q) * 512 + (((j & 7) ^ ky) + (j & 8)) * 32 + (lane & 3) * 8;
            const bf16x4 w0 = __builtin_amdgcn_ds_read_tr16_b64_v4i16((LAS bf16x4*)t);
            const bf16x4 w1 = __builtin_amdgcn_ds_read_tr16_b64_v4i16((LAS bf16x4*)(t + 2048));
            wf[j] = __builtin_shufflevector(w0, w1, 0, 1, 2, 3, 4, 5, 6, 7);
        }
        SB();
#pragma unroll
        for (int j = 0; j < 16; ++j) asm volatile("" : "+v"(wf[j]));
    }
    unsigned aa[5], pto = lane * 16;
#pragma unroll
    for (int m = 0; m < 4; ++m) aa[m] = (unsigned)(size_t)abuf + ((n >> 2) == m && ((n >> 1) & 1) == (g >> 1) ? (n & 1) * 32 + (g & 1) * 16 : zoff + g * 16);
    aa[4] = (unsigned)(size_t)abuf + ((lane >> 1) & 1) * 32 + ((lane >> 4) & 1) * 16 + ((lane >> 2) & 3) * 4 + (lane & 1) * 2;
    unsigned ix = (n & 1) ? 0xEEEEEEEEu : 0x44444444u;
    __amdgpu_buffer_rsrc_t rPt = mk_rsrc(w2 + WS_PART + __builtin_amdgcn_readfirstlane((slot >> 2) * 16384 + (lw >> 1) * 8192 + (slot & 3) * 2048 + (lw & 1) * 1024), 1024);
    asm volatile("" : "+v"(aa[0]), "+v"(aa[1]), "+v"(aa[2]), "+v"(aa[3]), "+v"(aa[4]), "+v"(pto), "+v"(ix));
    asm volatile("" : "+s"(rPt));
    f32x4 d[2] = {{0.f, 0.f, 0.f, 0.f}, {0.f, 0.f, 0.f, 0.f}};
    asm volatile("" : "+v"(d[0]), "+v"(d[1]));
    SB();
    float sv;
#pragma unroll 1
    for (int spin = 0;; ++spin) {
        sv = ((__uint_as_float(pp[0]) + __uint_as_float(pp[1])) + (__uint_as_float(pp[2]) + __uint_as_float(pp[3]))) +
             ((__uint_as_float(pp[4]) + __uint_as_float(pp[5])) + (__uint_as_float(pp[6]) + __uint_as_float(pp[7])));
        if (__builtin_expect(__builtin_amdgcn_ballot_w64(sv != sv) == 0, 1)) break;
        if (__all(max(max(max(pp[0], pp[1]), max(pp[2], pp[3])), max(max(pp[4], pp[5]), max(pp[6], pp[7]))) != SENT) || spin > SPIN_CAP) break;
        request();
    }
    unsigned ug, uu;
    swap32(__float_as_uint(sv), ug, uu);
    const float gg = rbf(__uint_as_float(ug)), up = rbf(__uint_as_float(uu));
    const unsigned act = pk_bf16(rbf(gg * __builtin_amdgcn_rcpf(1.0f + __expf(-gg))) * up, 0.f);
    *(volatile LAS unsigned short*)(size_t)aa[4] = (unsigned short)act;
    bf16x8 af[4];
#pragma unroll
    for (int m = 0; m < 4; ++m) af[m] = *(volatile LAS bf16x8*)(size_t)aa[m];
    SB();
#pragma unroll
    for (int i = 0; i < 8; ++i) {
        const int j = 8 * (i & 1) + 2 * (i >> 1);
        d[i & 1] = smf(af[i >> 1], wf[j], wf[j + 1], d[i & 1], ix);
        SB();
    }
    u32x4 pv;
#pragma unroll
    for (int q = 0; q < 4; ++q) {
        pv[q] = min(__float_as_uint(d[q >> 1][(q & 1) * 2] + d[q >> 1][(q & 1) * 2 + 1]), SENT - 1u);
        asm volatile("" : "+v"(pv[q]));
    }
    ST16_SC1(pv, rPt, pto);
}
__device__ __forceinline__ int wd_off(int lw, int i, int lane) {
    const int r = 2 * (i & 5) + (lane >> 5), ky = (r & 3) | ((r >> 1) & 4), c = lane & 31;
    return (lane >> 5) * (D_LEN * 2) + lw * 512 + (((c >> 1) ^ ky) * 2 + (c & 1)) * 16;
}

__global__ __launch_bounds__(768, 3) void fused_kernel(const Args a) {
    extern __shared__ __attribute__((aligned(16))) unsigned char smem[];
    constexpr int K = S_LEN, NT1 = D_LEN / BK;
    const int tid = threadIdx.x, wid = __builtin_amdgcn_readfirstlane(tid >> 6), lane = tid & 63;
    const int xcd = blockIdx.x & 7, idx = blockIdx.x >> 3;
    TSV;
    LAS unsigned char* lds = (LAS unsigned char*)smem;
    if (idx < 1 && wid < 4) dma_word<false>(code_rsrc(), (unsigned)(size_t)lds + L_DUMMY, tid * 128);
    SB();
    const int h = xcd & 1, xq = xcd >> 1, rt = xq * 4 + (idx & 3), ct = idx >> 2;
    volatile LAS unsigned* ctl1 = (volatile LAS unsigned*)(lds + L_CTL);
#define NBR(k) ((rt + (k)) & 3)
    unsigned kx;
    Ring ring;
    if (__builtin_expect((wid & 12) == 4, 1)) {
        __builtin_amdgcn_s_setprio(3);
        ring.template setup<D_LEN, false>(wid - 4, lane);
        ring.rP = mk_rsrc(a.x + (size_t)(rt * 4 + wid - 4) * 32 * D_LEN, 32 * D_LEN * 2);
        ring.rQ = mk_rsrc((h == 0 ? a.wre : a.wim) + (size_t)(ct * 4 + wid - 4) * 32 * D_LEN, 32 * D_LEN * 2);
        ring.lb = (wid - 4) * 4096;
        ring.sp = ring.sq = ring.poff = ring.xm = 0;
        ring.template issue<false>(lds);
        __builtin_amdgcn_s_setprio(0);
        ring.par = (ct & 4) << 11;
        ring.template ramp<CATCH1>(lds);
        ring.template steady<false, false>(NT1 - 2 - CATCH1);
        ring.template setup<K, true>(wid - 4, lane);
        ring.rQ = mk_rsrc((h == 0 ? a.cr : a.ci) + (size_t)(rt * 4 + wid - 4) * 32 * K, 32 * K * 2);
        ring.seek(rt * 256, 0);
        LAS unsigned char* l0 = lds + (wid - 4) * 4096;
        ring.issueQ(l0); ring.step();
        WAIT_VM(12); BARRIER();
        ring.issueQ(l0 + STAGE_BYTES);
        ring.seek((xq * 4 + NBR(1)) * 256, 0);
        const int xr = (wid & 1) * 8 + (lane >> 4);
        int vx0 = ((((wid >> 1) & 1) * 64 + xr) * D_LEN + (((lane & 15) ^ xr) * 8)) * 2;
        int vx1 = ((((wid >> 1) & 1) * 64 + xr + 4) * D_LEN + (((lane & 15) ^ (xr + 4)) * 8)) * 2 - 1024;
        __amdgpu_buffer_rsrc_t rX = mk_rsrc(a.x + (size_t)rt * BT * D_LEN + ct * BT, BT * D_LEN * 2 - ct * BT * 2);
        unsigned xd = (unsigned)(size_t)lds + L_YB + ((wid & 2) ? 16384 : 4096) + (wid & 1) * 2048;
        unsigned xe = (wid & 2) ? xd + 12288 : (unsigned)(size_t)lds + 3 * STAGE_BYTES + (wid & 1) * 2048;
        asm volatile("" : "+v"(vx0), "+v"(vx1));
        asm volatile("" : "+s"(rX), "+s"(xd), "+s"(xe));
        WAIT_VM(8); BARRIER();
        ring.issueQ(l0 + 2 * STAGE_BYTES);
        ring.seek((xq * 4 + NBR(2)) * 256, 0);
        if (h != 0) {
#pragma unroll
            for (int k = 0; k < 3; ++k) dma_2k<false>(rX, xd + k * 4096, vx0, vx1, k * (16 * D_LEN * 2));
        }
        BARRIER();
        if (h != 0) dma_2k<false>(rX, xe, vx0, vx1, 3 * (16 * D_LEN * 2));
        ring.issueQ(l0 + 3 * STAGE_BYTES);
        kx = (unsigned)__builtin_amdgcn_readfirstlane((int)ctl1[4]);
        SB();
    }
    char* ws = a.ws + ((0ull - (unsigned long long)a.ws) & 0x1fffffull);
    const int g = lane >> 4, n = lane & 15;
    LAS float* tab = (LAS float*)(lds + L_RED);
    if (wid >= 8) {
        const int aw = wid - 8;
#define T1(t) do { if (aw == 1 && (t) <= 16) STAMP(40 + (t)); } while (0)
        GAS u64* tagp = (GAS u64*)(ws + WS_TAG);
        const int r0 = xq * 512 + h * 256 + idx * 8 + aw * 2;
        const int w0 = xq * 256 + idx * 8 + aw * 2;
        const u32x4* src = (const u32x4*)((n < 8 ? a.x + (size_t)(r0 + (n >> 2)) * D_LEN
                                                 : (h == 0 ? a.wre : a.wim) + (size_t)(w0 + (n >> 2) - 2) * D_LEN) + g * 32 + (n & 3) * 8);
        if (AUX_SLEEP) __builtin_amdgcn_s_sleep(AUX_SLEEP);
        u64 tag0 = __hip_atomic_load(tagp, __ATOMIC_RELAXED, __HIP_MEMORY_SCOPE_AGENT);
        u32x4 v[8];
#pragma unroll
        for (int j = 0; j < 4; ++j) v[j] = src[j * 16];
        __builtin_amdgcn_s_setprio(3);
        SB();
        const char* cb = (const char*)(h ? a.ci : a.cr) + (size_t)rt * (512u << 10) + ct * 65536 + (lane >> 2) * 4096 + (lane & 3) * 128;
        const GAS u32x4* wrp = (const GAS u32x4*)a.w_r + lane;
        asm volatile("" : "+v"(cb), "+v"(wrp));
        unsigned mask = aw == 1 ? CPF_MASK1 : aw == 2 ? CPF_MASK2 : 0u;
        mask = (mask & CPF_FIX) | ((mask & ~CPF_FIX) << (idx % CPF_STAG));
        int ti = 2 * xq + aw - 1;
        auto warm = [&](int t) { if (t >= 4 && ((mask >> t) & 1)) { touch_line(cb + (ti & 7) * 512, L_DUMMY); ti += 2; } };
        f32x4 d1 = {0.f, 0.f, 0.f, 0.f}, d2 = {0.f, 0.f, 0.f, 0.f};
        u32x4 ones = {0x3F803F80u, 0x3F803F80u, 0x3F803F80u, 0x3F803F80u};
        auto sums = [&](int j0) __attribute__((always_inline)) {
#pragma unroll
            for (int j = j0; j < j0 + 4; ++j) {
                const bf16x8 f = __builtin_bit_cast(bf16x8, v[j]);
                d1 = __builtin_amdgcn_mfma_f32_16x16x32_bf16(f, __builtin_bit_cast(bf16x8, ones), d1, 0, 0, 0);
                d2 = __builtin_amdgcn_mfma_f32_16x16x32_bf16(f, f, d2, 0, 0, 0);
            }
            asm volatile("" : "+v"(d1), "+v"(d2));
        };
        unsigned wl, wh;
        int ro;
        __amdgpu_buffer_rsrc_t rS;
        u64 m1 = 0xAAAAAAAAAAAAAAAAull, m2 = 0xCCCCCCCCCCCCCCCCull, mh = 0xFFFFFFFF00000000ull;
#pragma unroll
        for (int t = 0; t <= 5; ++t) {
            SB(); BARRIER(); SB();
            T1(t);
            if (t == 2) {
#pragma unroll
                for (int j = 4; j < 8; ++j) v[j] = src[j * 16];
            }
            if (t == 3) {
                asm volatile("" : "+v"(ones));
                sums(0);
                asm volatile("" : "+v"(tag0));
                const u64 want = tag0 * 6364136223846793005ull + 1442695040888963407ull;
                wl = (unsigned)want; wh = (unsigned)(want >> 32);
                asm volatile("" : "+v"(wl), "+v"(wh));
            }
            if (t == 4) { sums(4); asm volatile("s_nop 7\n\ts_nop 3" : "+v"(d2)); }
            if (t == 1) {
                rS = mk_rsrc(ws + WS_XRD, 65536);
                ro = n == g * 4 ? (g < 2 ? r0 + g : 2048 - 2 + h * 1024 + w0 + g) * 16 : 65536;
                asm volatile("" : "+v"(ro));
                asm volatile("" : "+s"(rS), "+s"(m1), "+s"(m2), "+s"(mh), "+s"(mask), "+s"(ti));
            }
        }
        float sm = (d1[0] + d1[1]) + (d1[2] + d1[3]);
        float sq = pick(m2, pick(m1, d2[0], d2[1]), pick(m1, d2[2], d2[3]));
        asm volatile("s_nop 1" : "+v"(sq));
        sq += dppf<0xB1>(sq);
        sq += dppf<0x4E>(sq);
        float mean = sm * (1.0f / D_LEN), var = rbf(rbf(fmaxf(sq * (1.0f / D_LEN) - mean * mean, 0.f)) + 1e-5f);
        asm volatile("" : "+v"(sm), "+v"(mean), "+v"(var));
        SB(); BARRIER(); SB();
        T1(6);
        {
            float rden = __builtin_amdgcn_rcpf(rbf(__builtin_amdgcn_sqrtf(var)));
            asm volatile("s_nop 1" : "+v"(rden));
            const u32x4 rec = {__float_as_uint(pick(mh, rden, sm)), wl, __float_as_uint(rbf(mean) * rden), wh};
            ST16_SC1(rec, rS, ro);
        }
        const __amdgpu_buffer_rsrc_t rV = mk_rsrc(ws + WS_VOTE, 512);
        if (aw == 3 && idx < 8) __builtin_amdgcn_raw_buffer_store_b64((u32x2){ctl1[5], wl}, rV, blockIdx.x * 8 + lane * 512, 0, 16);
        SB(); BARRIER(); SB();
        T1(STAT_M + 2);
        warm(STAT_M + 2);
        if (aw == 1 || aw == 2) {
#pragma unroll 1
            for (int t = STAT_M + 3; t <= NT1 + AUX_STAY; ++t) {
                BARRIER();
                T1(t);
                warm(t);
            }
            FLUSH();
            return;
        }
        if (aw == 3) {
            if (h == 0) {
                const u32x4 w0v = wrp[0], w1v = wrp[64];
                BARRIER(); BARRIER(); BARRIER(); BARRIER();
                SB();
                float t = 0.f;
#pragma unroll
                for (int i = 0; i < 4; ++i) { t = dot2bf(w0v[i], 0x3F803F80u, t); t = dot2bf(w1v[i], 0x3F803F80u, t); }
                SB(); BARRIER(); SB();
                t = wave_sum(t);
                ctl1[1] = __float_as_uint(t);
                WAIT_LGKM0();
            }
            return;
        }
#pragma unroll
        for (int t = STAT_M + 3; t <= POLL_T; ++t) BARRIER();
        SB();
        const int o0 = (rt * BT + lane) * 16, o1 = (2048 + (h * 8 + ct) * BT + lane) * 16, os = 1024;
        u32x4 q0, q1, q2, q3;
        u32x2 qv;
        auto request = [&]() {
            asm volatile("" ::: "memory");
            q0 = LD16_SC1(rS, o0); q1 = LD16_SC1(rS, o0 + os); q2 = LD16_SC1(rS, o1); q3 = LD16_SC1(rS, o1 + os);
            qv = __builtin_amdgcn_raw_buffer_load_b64(rV, lane * 8, 0, 16);
            SB();
        };
        request();
        int b = POLL_T + 1;
        for (int spin = 0; spin <= SPIN_CAP; ++spin) {
            if (b < NT1) { BARRIER(); ++b; }
            if (b < NT1) { BARRIER(); ++b; }
            SB();
            unsigned d = ((q0[1] ^ wl) | (q1[1] ^ wl)) | ((q2[1] ^ wl) | (q3[1] ^ wl)) | ((q0[3] ^ wh) | (q1[3] ^ wh)) | ((q2[3] ^ wh) | (q3[3] ^ wh)) | (qv[1] ^ wl);
            asm volatile("" : "+v"(d));
            if (__all(d == 0u)) break;
            request();
        }
        if (b < NT1) BARRIER();
        SB();
        LAS unsigned* tb = (LAS unsigned*)tab;
        const unsigned sg = h == 0 ? 0u : 0x80000000u;
        tb[lane] = q0[0] ^ sg; tb[64 + lane] = q1[0] ^ sg; tb[128 + lane] = q0[2] ^ sg; tb[192 + lane] = q1[2] ^ sg;
        tb[256 + lane] = q2[0]; tb[320 + lane] = q3[0];
        const unsigned vt = qv[0];
        const unsigned kx = __popcll(__ballot(vt == 2u)) > __popcll(__ballot(vt == 1u)) ? 8192u : 0u;
        if (lane == 0) { ctl1[0] = wl | 2u; ctl1[2] = wl; ctl1[3] = wh; ctl1[4] = kx; }
        *(LAS u32x2*)(lds + L_YB + 2560 + 256 + lane * 8) = (u32x2){0u, 0u};
        WAIT_LGKM0();
        return;
    }
#define CTL(i) ((unsigned)__builtin_amdgcn_readfirstlane((int)ctl[i]))
    const int wp = wid & 1, wq = (wid >> 1) & 1;
#define Y_OFF ((int)WS_Y + ((rt & 7) * 8 + ct) * 65536 + (wid & 3) * 16384 + (((rt >> 3) ^ (rt >> 2)) & 1) * 8192)
#define P2_DEFS \
    char* w2 = ws + kx; \
    const int sidx = idx < 28 ? xq * 28 + idx : 112 + xq * 4 + idx - 28;     \
    volatile LAS unsigned* ctl = (volatile LAS unsigned*)(lds + (h == 0 ? L_ATAB : L_CTL)); \
    const int NT = h == 0 ? 32 + KSHIFT : 32 - KSHIFT; \
    LAS float* rstat = (LAS float*)(lds + L_ATAB + 256);     \
    const int hp_ = rt * 2 + wq;                             \
    (void)sidx; (void)ctl; (void)NT; (void)rstat; (void)hp_; (void)w2
    if (__builtin_expect(wid < 4, 1)) {
        {
            const u32x4 sent4 = {SENT, SENT, SENT, SENT}, zero4 = {0u, 0u, 0u, 0u};
            const __amdgpu_buffer_rsrc_t rI = mk_rsrc(ws + WS_STATS + (size_t)blockIdx.x * (FILL_BYTES / 256), FILL_BYTES / 256);
            const __amdgpu_buffer_rsrc_t rZ = mk_rsrc(ws + (size_t)blockIdx.x * 160, 160);
            ST16_SC1(sent4, rI, tid * 16);
            if (wid < 2) ST16_SC1(sent4, rI, 4096 + tid * 16);
            if (wid == 2) ST16_SC1(zero4, rZ, lane * 16);
        }
        if (wid == 3 && idx < 8) {
            const unsigned r = (((unsigned)__builtin_amdgcn_s_memrealtime() * 2654435761u + blockIdx.x * 40503u) >> 9) & 0x1fdf80u;
            const unsigned first = (idx & 1) << 13;
            const char* pb = ws + WS_PROBE;
            unsigned va, vb, left;
            asm volatile("s_waitcnt lgkmcnt(0)\n\ts_load_dword %0, %3, %4\n\ts_load_dword %1, %3, %5\n\ts_waitcnt lgkmcnt(1)\n\t"
                         "s_nop 15\n\ts_getreg_b32 %2, hwreg(HW_REG_IB_STS, 8, 4)\n\ts_waitcnt lgkmcnt(0)"
                         : "=&s"(va), "=&s"(vb), "=&s"(left) : "s"(pb), "s"(r | first), "s"(r | (first ^ 8192u)) : "memory");
            const unsigned nearbit = (left != 0u ? first : first ^ 8192u) >> 13;
            if (lane == 0) ctl1[5] = nearbit == (unsigned)(xcd >> 2) ? 1u : 2u;
        }
        f32x4 acc[4][4];
#pragma unroll
        for (int i = 0; i < 16; ++i) acc[i >> 2][i & 3] = (f32x4){0.f, 0.f, 0.f, 0.f};
        unsigned tc = (unsigned)(size_t)(tab + wp * 64 + g * 8), tw = (unsigned)(size_t)(tab + 256 + prow(wq * 64 + n));
        unsigned ta = (unsigned)(size_t)lds + wp * STAGE_BYTES + wq * 8192 + lane * 16;
        constexpr int NIL = 1 << 30;
        __amdgpu_buffer_rsrc_t rW = mk_rsrc(ws, 18u << 20);
        const int hpan = rt * 2 + wq;
        int sk = (int)WS_AT + at_panel(h * 8 + ct) + at_tile((rt * 2 + wp) * 128, (ct & 4) << 11) + wq * 16384 + lane * 16;
        int st = lane != 0 ? NIL : (int)WS_FLAG1 + (1 - wp) * 32768 + h * 16384 + (rt >> 3) * 8192 + ct * 1024 + (rt & 7) * 128 + wq * 4;
        int so = h != 0 ? Y_OFF + lane * 16 : g != (n >> 2) ? NIL
               : (int)WS_STATS + (hpan & 15) * 32768 + (ct >> 2) * 16384 + (hpan >> 4) * 8192 + ((ct & 3) * 2 + wp) * 1024 + n * 16;
        int sf = lane != 0 ? NIL : (int)WS_FLAGS + (rt * 8 + ct) * 128 + wid * 4;
        u32x4 sel[2], aw[2];
        unsigned one[2];
#pragma unroll
        for (int i = 0; i < 4; ++i) {
            one[i & 1] = g == (n >> 2) && (i & 1) == ((n >> 1) & 1) ? ((n & 1) ? 0x3F800000u : 0x00003F80u) : 0u;
            aw[0][i] = aw[1][i] = (n & 3) == 0 ? 0x3F803F80u : 0u;
        }
        if ((n & 3) == 1) {
            const bf16_t* wr = a.w_r + ct * BT + wp * 64 + g * 8;
            aw[0] = *(const u32x4*)wr; aw[1] = *(const u32x4*)(wr + 32);
        }
        unsigned ea[2];
#pragma unroll
        for (int mp = 0; mp < 2; ++mp)
            ea[mp] = (unsigned)(size_t)lds + L_YB + (wq ? 16384 : 4096) + n * 256 + (((wp * 8 + mp * 4 + g) ^ n) * 16);
        asm volatile("" : "+v"(tc), "+v"(tw), "+v"(ta), "+v"(sk), "+v"(st), "+v"(so), "+v"(sf), "+v"(one[0]), "+v"(one[1]), "+v"(ea[0]), "+v"(ea[1]));
        asm volatile("" : "+s"(rW));
        unsigned tg;
        bf16x8 e0[4], e1[4];
        auto extra = [&]() __attribute__((always_inline)) {
            bf16x8 ef[4][2];
#pragma unroll
            for (int i = 0; i < 8; ++i)
                ef[i >> 1][i & 1] = *(const LAS bf16x8*)((const LAS unsigned char*)(size_t)(ea[i & 1] + (i < 6 || wq ? 0 : 3 * STAGE_BYTES - L_YB - 16384)) + (i >> 1) * 4096);
            SB();
#pragma unroll
            for (int i = 0; i < 16; ++i)
                acc[i & 3][i >> 2] = __builtin_amdgcn_mfma_f32_16x16x32_bf16(__builtin_bit_cast(bf16x8, sel[i & 1]), ef[i >> 2][(i >> 1) & 1], acc[i & 3][i >> 2], 0, 0, 0);
        };
        if (wid == 0) STAMP(0);
        compute_loop<false, true>(lds, acc, NT1, wp, wq, lane, [&]() { if (wid == 0) STAMP(24); WAIT_VM0(); if (wid == 0) STAMP(31); }, e0, e1, 0u);
        if (wid == 0) STAMP(25);
        {
            f32x4 rd[4], mr[4];
            float wsm[4];
#pragma unroll
            for (int m = 0; m < 4; ++m) {
                rd[m] = *(const LAS f32x4*)((const LAS float*)(size_t)tc + (m >> 1) * 32 + (m & 1) * 4);
                mr[m] = *(const LAS f32x4*)((const LAS float*)(size_t)tc + 128 + (m >> 1) * 32 + (m & 1) * 4);
                wsm[m] = *((const LAS float*)(size_t)tw + (m >> 1) * 32 + (m & 1) * 4);
            }
            const unsigned kv = ctl1[4];
            tg = ctl1[0];
            SB();
            sk += (int)kv; st += (int)kv; so += (int)kv; sf += (int)kv;
            kx = (unsigned)__builtin_amdgcn_readfirstlane((int)kv);
#pragma unroll
            for (int nn = 0; nn < 4; ++nn)
#pragma unroll
                for (int mp = 0; mp < 2; ++mp) {
                    const f32x4 u = acc[2 * mp][nn] * rd[2 * mp] - mr[2 * mp] * wsm[nn];
                    const f32x4 v = acc[2 * mp + 1][nn] * rd[2 * mp + 1] - mr[2 * mp + 1] * wsm[nn];
                    u32x4 w;
                    w[0] = pk_bf16(u[0], u[1]); w[1] = pk_bf16(u[2], u[3]);
                    w[2] = pk_bf16(v[0], v[1]); w[3] = pk_bf16(v[2], v[3]);
                    ST16_SC1(w, rW, sk + (nn * 2 + mp) * 1024);
                    *(LAS u32x4*)((LAS unsigned char*)(size_t)ta + (nn * 2 + mp) * 1024) = w;
                }
        }
        if (wid == 0) STAMP(26);
        WAIT_LGKM0();
        BARRIER();
        SB();
        {
#pragma unroll
            for (int i = 0; i < 16; ++i) { acc[i >> 2][i & 3] = (f32x4){0.f, 0.f, 0.f, 0.f}; asm volatile("" : "+v"(acc[i >> 2][i & 3])); }
            sel[0] = (u32x4){one[0], one[1], 0u, 0u}; sel[1] = (u32x4){0u, 0u, one[0], one[1]};
            asm volatile("" : "+v"(st), "+v"(so), "+v"(sf), "+v"(tg), "+v"(sel[0]), "+v"(sel[1]));
            SB();
            if (h != 0) extra();
            SB();
            WAIT_VM(0);
            __builtin_amdgcn_raw_buffer_store_b32(tg, rW, st, 0, 16);
            SB();
            if (wid == 0) STAMP(27);
        }
        compute_loop<true, false>(lds, acc, h == 0 ? 32 + KSHIFT : 32 - KSHIFT, wp, wq, lane, [&]() {
            if (wid == 0) { STAMP(h == 0 ? 1 : 19); CSTAMP(22); }
        }, e0, e1, (unsigned)((wp * 8192 + 2 * STAGE_BYTES) ^ (L_YB + wid * 8192)));
        P2_DEFS;
        if (wid == 0) { STAMP(h == 0 ? 2 : 12); CSTAMP(23); }
        u32x4 hq[4][2];
        auto cvt = [&](int nn, int j) __attribute__((always_inline)) {
            const f32x4 c = acc[j][nn];
            hq[nn][j >> 1][(j & 1) * 2] = pk_bf16(c[0], c[1]);
            hq[nn][j >> 1][(j & 1) * 2 + 1] = pk_bf16(c[2], c[3]);
        };
        int hs = h;
        asm volatile("" : "+s"(hs));
        if (__builtin_expect(hs == 0, 1)) {
            f32x4 d1[4], d2[4];
            u32x3 sv;
            auto add = [&](int nn, int j) __attribute__((always_inline)) {
                acc[j][nn] = __builtin_amdgcn_mfma_f32_16x16x32_bf16(__builtin_bit_cast(bf16x8, sel[j & 1]), j < 2 ? e0[nn] : e1[nn], acc[j][nn], 0, 0, 0);
            };
            auto sum = [&](int nn, int j) __attribute__((always_inline)) {
                const bf16x8 hf = __builtin_bit_cast(bf16x8, hq[nn][j >> 1]);
                const f32x4 z = {0.f, 0.f, 0.f, 0.f};
                if (j & 1) d1[nn] = __builtin_amdgcn_mfma_f32_16x16x32_bf16(__builtin_bit_cast(bf16x8, aw[j >> 1]), hf, j < 2 ? z : d1[nn], 0, 0, 0);
                else d2[nn] = __builtin_amdgcn_mfma_f32_16x16x32_bf16(hf, hf, j < 2 ? z : d2[nn], 0, 0, 0);
            };
            auto out = [&](int nn, int j) __attribute__((always_inline)) {
                if (j == 1) asm volatile("" : "+v"(d2[nn]));
                if (j == 2) asm volatile("" : "+v"(d1[nn]));
                if (j == 1) sv[1] = min(__float_as_uint(sel4(n, d2[nn][0], d2[nn][1], d2[nn][2], d2[nn][3])), SENT - 1u);
                if (j == 2) { sv[0] = min(__float_as_uint(d1[nn][0]), SENT - 1u); sv[2] = min(__float_as_uint(d1[nn][1]), SENT - 1u); }
                if (j == 3) __builtin_amdgcn_raw_buffer_store_b96(sv, rW, so + nn * 256, 0, 16);
            };
#define FOUR(m, v) _Pragma("unroll") for (int j = 0; j < 4; ++j) { m; SB(); v; SB(); }
            FOUR(add(0, j), (void)0) FOUR(add(1, j), cvt(0, j)) FOUR(sum(0, j), cvt(1, j)) FOUR(add(2, j), out(0, j))
            FOUR(sum(1, j), cvt(2, j)) FOUR(add(3, j), out(1, j)) FOUR(sum(2, j), cvt(3, j)) FOUR(sum(3, j), out(2, j))
            FOUR((void)0, out(3, j))
#undef FOUR
            if (wid == 0) STAMP(3);
#pragma unroll
            for (int nn = 0; nn < 4; ++nn)
#pragma unroll
                for (int mp = 0; mp < 2; ++mp)
                    *(LAS u32x4*)(lds + L_HT + (wq * 64 + nn * 16 + n) * 256 + (((wp * 8 + mp * 4 + g) ^ n) * 16)) = hq[nn][mp];
            WAIT_LGKM0();
            BARRIER();
            BARRIER();
            if (wid == 0) STAMP(6);
            const int crow = __builtin_amdgcn_readfirstlane((int)((volatile LAS unsigned*)(lds + L_ATAB))[2 + wq]) + wq * 64;
            const __amdgpu_buffer_rsrc_t rOut = mk_rsrc(a.out + (size_t)rt * BT * D_LEN, BT * D_LEN * 2);
#pragma unroll 2
            for (int j = 0; j < 8; ++j) {
                const int r = wq * 64 + wp * 32 + j * 4 + g;
                const float m_ = rstat[r * 2], d_ = rstat[r * 2 + 1];
                const u32x4 hh = *(const LAS u32x4*)(lds + L_HT + r * 256 + ((n ^ (r & 15)) * 16));
                u32x4 ov;
#pragma unroll
                for (int i = 0; i < 4; ++i) {
                    const unsigned u = hh[i];
                    const float h0 = bflo(u), h1 = bfhi(u);
                    const unsigned dd = pk_bf16(h0 - m_, h1 - m_);
                    const unsigned q = pk_bf16(bflo(dd) * d_, bfhi(dd) * d_);
                    ov[i] = pk_bf16(h0 + bflo(q), h1 + bfhi(q));
                }
                __builtin_amdgcn_raw_buffer_store_b128(ov, rOut, r == crow ? BT * D_LEN * 2 : (r * D_LEN + ct * BT + n * 8) * 2, 0, 0);
            }
            FLUSH();
            return;
        }
#pragma unroll
        for (int i = 0; i < 8; ++i) {
            cvt(i >> 1, (i & 1) * 2); cvt(i >> 1, (i & 1) * 2 + 1);
            __builtin_amdgcn_raw_buffer_store_b128(hq[i >> 1][i & 1], rW, so, i * 1024, 16);
            SB();
        }
        WAIT_VM0();
        __builtin_amdgcn_raw_buffer_store_b32(1u, rW, sf, 0, 16);
        if (wid == 0) STAMP(13);
    } else {
        LAS unsigned char* l0 = lds + (wid - 4) * 4096;
        P2_DEFS;
        __amdgpu_buffer_rsrc_t rD = mk_rsrc(h == 0 ? a.wd + (size_t)sidx * 32 * D_LEN : a.wg,
                                                  sidx >= 112 ? 0 : h != 0 ? D_LEN * F_LEN * 2 : wid < 8 - BQ ? 32 * D_LEN * 2 : 0);
        int vGe = h == 0 ? wd_off(wid & 3, 0, lane)
                : (((sidx & 7) * 128 + (wid & 3) * 16 + (lane >> 5)) * F_LEN + (sidx >> 3) * 256 + ((((lane & 31) >> 1) ^ (lane >> 5)) * 16) + (lane & 1) * 8) * 2;
        auto load_wd = [&](int i) __attribute__((always_inline)) {
            dma_nt(rD, (unsigned)(size_t)lds + (i < 8 ? L_WD0 : L_WD1) + (wid & 3) * 8192 + (i & 7) * 1024, vGe ^ ((2 * (i & 1) | (i & 4)) * 32), i * (4 * D_LEN));
        };
        const int lw = wid - 4;
        constexpr int NIL = 1 << 30;
        ring.rP = ring.rF = mk_rsrc(w2, (unsigned)WS_STATS);
        ring.voP[0] += (int)WS_AT + at_panel(h * 8 + ct);
        ring.voP[1] = ring.voP[2] = ring.voP[3] = ring.voP[0];
#define FLINE(k) (((k) >> 3) * 8192 + ((k) & 7) * 128)
        const int ft = (g & 1) * 32768 + (g >> 1) * 4 + (int)WS_FLAG1 + (lw == 0 ? ct * 1024 : NIL);
        int vall = FLINE(n) + ft + h * 16384;
        ring.voF = (n >> 2) == xq ? vall : NIL;
        ring.fl = lds + L_YB + (h == 0 ? lw * 8192 : lw ? 768 : 0) + 2560 + 256;
        unsigned sa = (unsigned)(size_t)lds + L_YB + 2560 + 256 + lane * 4;
        const unsigned tg = (unsigned)__builtin_amdgcn_readfirstlane((int)ctl1[0]);
        float wsum = __uint_as_float(ctl1[1]);
        ring.lb = lw * 4096;
        int cq[3], cp[3];
        unsigned mk[3];
#pragma unroll
        for (int k = 0; k < 3; ++k) {
            const int nb = xq * 4 + NBR(k + 1);
            cq[k] = nb * 256; cp[k] = at_tile(cq[k], ring.par); mk[k] = 0x10000u << nb;
            asm volatile("" : "+s"(cq[k]), "+s"(cp[k]), "+s"(mk[k]));
        }
        unsigned m2 = (mk[0] | mk[1] | mk[2]) >> 16;
        asm volatile("" : "+s"(m2), "+s"(ring.rP));
        asm volatile("" : "+v"(vall), "+v"(ring.voF), "+v"(ring.voP[0]), "+v"(sa), "+v"(wsum));
        ring.rF = ring.rP;
        ring.voP[1] = ring.voP[2] = ring.voP[3] = ring.voP[0];
        WAIT_VM(4);
        BARRIER();
        SB();
        int ve = Y_OFF + lane * 16;
        unsigned yd = (unsigned)(size_t)lds + L_YB + lw * 8192, fa = 0u;
        auto fetch = [&]() __attribute__((always_inline)) {
            if (wid == 4) STAMP(21);
            const int v4[4] = {ve, ve, ve, ve};
#pragma unroll
            for (int k = 0; k < 2; ++k) dma_4k<true>(ring.rP, yd + k * 4096, v4, k * 4096);
        };
        __amdgpu_buffer_rsrc_t rQ2 = mk_rsrc(a.ci + (size_t)(rt * 4 + lw) * 32 * K, 32 * K * 2);
        int vC2 = (ct * 4 + g) * (K * 2) + ((xq * 1024 + 2048 + n * 128) & 4095);
        int vT2 = h == 0 && n < (KSHIFT + 1) / 2 ? FLINE((xq * 4 - 1 - n) & 15) + ft + 16384 : NIL;
        int vY = lane == 0 ? (int)WS_FLAGS + (rt * 8 + ct) * 128 + lw * 4 : NIL;
        int n1 = (h == 0 ? 32 : NT) - 9, n0 = n1 - 1 - (C2_START + idx % C2_STAG);
        int xm = (idx & 1) * 128;
        int c8 = (xq * 1024 + 1024) & 4095, p8 = at_tile(c8 ^ xm, ring.par);
        int cK = (xq * 1024 + (32 - KSHIFT) * 128) & 4095, pK = at_tile(cK ^ xm, ring.par) + at_panel(8);
        auto early = [&](int st, int i0, int i1) __attribute__((always_inline)) {
#pragma unroll
            for (int i = i0; i < i1; ++i)
                dma_nt(rD, (unsigned)(size_t)lds + (st * 4 + lw) * 8192 + i * 1024, vGe ^ ((2 * (i & 1) | (i & 4)) * 32), i * (4 * F_LEN) + st * (128 * F_LEN));
        };
        asm volatile("" : "+v"(vGe));
        asm volatile("" : "+s"(rQ2), "+s"(n0), "+s"(n1), "+s"(c8), "+s"(p8), "+s"(cK), "+s"(pK), "+s"(xm), "+s"(rD));
        asm volatile("" : "+v"(ve), "+v"(vC2), "+v"(vT2), "+v"(vY));
        unsigned have = 0u;
        SB();
#define GATE(m) do { if (__builtin_expect(((m) & ~have) != 0u, 0)) ring.gate(tg, sa, m, have); SB(); } while (0)
#define TILE(k, o, st) do { ring.sp = cp[k] + (o) * 256; ring.sq = cq[k] + (o); ring.template issueP<16>(l0 + (st) * STAGE_BYTES); \
                            ring.issueQ(l0 + (st) * STAGE_BYTES); ring.sample(); SB(); } while (0)
        ring.gate(tg, sa, mk[0], have);
        if (wid == 4) STAMP(28);
        ring.sp = cp[0]; ring.template issueP<16>(l0 + 2 * STAGE_BYTES); ring.sample();
        BARRIER();
        GATE(mk[1]);
        ring.sp = cp[1]; ring.template issueP<16>(l0 + 3 * STAGE_BYTES); ring.sample();
        GATE(mk[2]);
        TILE(2, 0, 0);
        WAIT_VM(15); BARRIER();
        if (wid == 4) STAMP(30);
        GATE(m2);
        ring.voF = vall;
        TILE(0, 128, 1);
        WAIT_VM(19); BARRIER();
        TILE(1, 128, 2);
        WAIT_VM(19); BARRIER();
        ring.sp = cp[2] + 128 * 256; ring.template issueP<16>(l0 + 3 * STAGE_BYTES);
        ring.sq = cq[2] + 128;
        SB();
        {
            const unsigned long long m = __ballot(ring.peek(sa) == tg);
            have = (unsigned)m & (unsigned)(m >> 32);
            if (__builtin_expect(have != 0xffffffffu, 0)) ring.gate(tg, sa, 0xffffffffu, have);
        }
        ring.fl -= 256;
        ring.voF = vT2;
        SB();
        ring.issueQ(l0 + 3 * STAGE_BYTES);
#undef GATE
#undef TILE
        ring.sq = c8; ring.sp = p8; ring.poff = 0; ring.xm = xm;
        if (wid == 4) { STAMP(29); CSTAMP(34); STAMP(36); }
        ring.template steady<true, false, 18>(n1, n0, vC2);
        ring.fl += 512;
        if (wid == 4) { CSTAMP(35); STAMP(37); }
        ring.template issueP<16>(lds + ring.lb);
        SB();
        {
            const unsigned long long m = __ballot(ring.peek(sa + 256) == tg);
            have = (unsigned)m & (unsigned)(m >> 32);
            asm volatile("" : "+s"(have));
        }
        int hs = h;
        asm volatile("" : "+s"(hs));
        if (__builtin_expect(hs == 0, 1)) {
            constexpr unsigned MK = ((1u << ((KSHIFT + 1) / 2)) - 1u) * 0x10001u;
            if (__builtin_expect((MK & ~have) != 0u, 0)) ring.gate(tg, sa + 256, MK, have);
            unsigned lq = ring.lb + TILE_BYTES, pp = ring.par | at_panel(8);
            int sq = ring.sq ^ ring.xm, cnt = KSHIFT;
            ring.lb = (ring.lb + STAGE_BYTES) & (RING_BYTES - 1);
            ring.fl -= 512;
            fa = (unsigned)(size_t)ring.fl;
            asm volatile("" : "+s"(lq), "+s"(sq), "+s"(cnt), "+s"(ring.lb), "+s"(pp), "+s"(yd));
            asm volatile("" : "+v"(fa));
            SB();
            dma_4k<false>(ring.rQ, lq, ring.voQ, sq);
            ring.voF = vY;
            ring.rQ = rQ2;
            ring.sq = cK; ring.sp = pK; ring.poff = 0; ring.par = pp;
            ring.template steady<true, true>(cnt);
            auto seen = [&]() __attribute__((always_inline)) { return __builtin_amdgcn_readfirstlane(ring.peek(fa)) == 1u; };
            if (__builtin_expect(seen(), 1)) {
                fetch();
                WAIT_VM(16); BARRIER();
                WAIT_VM(8); BARRIER();
            } else {
                ring.sample();
                WAIT_VM(10); BARRIER();
                const bool have = seen();
                if (have) { fetch(); WAIT_VM(9); } else { ring.sample(); WAIT_VM(2); }
                BARRIER();
                if (!have) {
#pragma unroll 1
                    for (int spin = 0; spin <= SPIN_CAP && !seen(); ++spin) { ring.sample(); WAIT_VM(0); }
                    fetch();
                }
            }
            if (wid == 4) STAMP(20);
            WAIT_VM0();
        } else {
            ring.issueQ(lds + ring.lb);
            WAIT_VM(16); BARRIER();
            early(0, 0, G_N);
            wait_vm<8 + G_N>(); BARRIER();
            early(1, 0, G2_N);
            wait_vm<G_N + G2_N>(); BARRIER();
            early(1, G2_N, G2_N + G3_N);
        }
        BARRIER();
        if (h == 0) {
            const int slot = sidx;
            LAS unsigned char* abuf = lds + L_ATAB + 2048 + lw * 128;
#pragma unroll
            for (int i = 0; i < WD_EARLY; ++i) load_wd(i);
            SB();
            __amdgpu_buffer_rsrc_t rS = mk_rsrc(w2 + WS_STATS + (hp_ & 15) * 32768 + (hp_ >> 4) * 8192, 24576);
            __amdgpu_buffer_rsrc_t rC = mk_rsrc(w2 + WS_CAND + (size_t)hp_ * 4096, 4096);
            __amdgpu_buffer_rsrc_t rKy = mk_rsrc(w2 + WS_KEYS + (hp_ >> 2) * KEY_S + (hp_ & 3) * 4, ct == 0 ? 8192 + 3 * KEY_S * 16 + 4 : 0);
            int vs = lane * 16, vk = !(lane & 1) ? 1 << 20 : (lane >> 2) * 8192 + (lane & 3) * (KEY_S * 16);
            int vc = (lane < 32 ? 2048 : 0) + ct * 256 + (lane & 31) * 8;
            unsigned code = 0xffffu - (unsigned)(hp_ * 64 + lane), hb = (lane & 31) * 8;
            unsigned ra = (unsigned)(size_t)(rstat + (wq * 64 + lane) * 2), ht = (unsigned)(size_t)lds + L_HT + wq * 16384;
            u64 lo = 0xFFFFFFFFull;
            asm volatile("" : "+s"(rS), "+s"(rC), "+s"(rKy), "+s"(ht), "+s"(lo));
            asm volatile("" : "+v"(vs), "+v"(vk), "+v"(vc), "+v"(code), "+v"(hb), "+v"(ra));
            SB();
            BARRIER();
            __builtin_amdgcn_s_setprio(TAIL_PRIO);
            if (lane < 16) *(LAS unsigned*)(abuf + 64 + lane * 4) = 0u;
            __builtin_amdgcn_s_sleep(STAT_DELAY);
            if (wp == 0) {
                float s1, s2, s3;
#pragma unroll 1
                for (int spin = 0;; ++spin) {
                    asm volatile("" ::: "memory");
                    u32x3 sv[16];
#pragma unroll
                    for (int j = 0; j < 16; ++j) { sv[j] = __builtin_amdgcn_raw_buffer_load_b96(rS, vs, (j >> 3) * 16384 + (j & 7) * 1024, 16); SB(); }
#pragma unroll
                    for (int j = 0; j < 16; ++j) {
                        const unsigned u0 = sv[j][0], u1 = sv[j][1], u2 = sv[j][2];
                        s1 = j ? s1 + __uint_as_float(u0) : __uint_as_float(u0);
                        s2 = j ? s2 + __uint_as_float(u1) : __uint_as_float(u1);
                        s3 = j ? s3 + __uint_as_float(u2) : __uint_as_float(u2);
                    }
                    float z = s1 + s3;
                    asm volatile("" : "+v"(z));
                    if (__builtin_expect(__builtin_amdgcn_ballot_w64(__builtin_isunordered(z, s2)) == 0, 1)) break;
                    unsigned mx = 0u;
#pragma unroll
                    for (int j = 0; j < 16; ++j) mx = max(max(mx, sv[j][0]), max(sv[j][1], sv[j][2]));
                    if (__all(mx != SENT) || spin > SPIN_CAP) break;
                }
                if (wid == 4) STAMP(4);
                const float mean = s1 * (1.0f / D_LEN);
                const float var = fmaxf(s2 * (1.0f / D_LEN) - mean * mean, 0.f);
                const float mb = rbf(mean), rden = __builtin_amdgcn_rcpf(rbf(__builtin_amdgcn_sqrtf(rbf(rbf(var) + 1e-5f))));
                const unsigned kb = wave_max_s(r2key((s3 - mb * wsum) * rden, code));
                __builtin_amdgcn_raw_buffer_store_b32(kb, rKy, vk, 0, 16);
                SB();
                {
                    const unsigned cr_ = ~kb & 63u;
                    u32x2 c = *(const LAS u32x2*)(size_t)(ht + cr_ * 256 + (hb ^ ((cr_ & 15) * 16)));
                    const float m_ = __int_as_float(__builtin_amdgcn_readlane(__float_as_int(mb), cr_));
                    const float d_ = __int_as_float(__builtin_amdgcn_readlane(__float_as_int(rden), cr_));
#pragma unroll
                    for (int i = 0; i < 2; ++i) {
                        const unsigned u = c[i];
                        const unsigned dd = pk_bf16(bflo(u) - m_, bfhi(u) - m_);
                        const unsigned q = pk_bf16(bflo(dd) * d_, bfhi(dd) * d_);
                        c[i] = min(__float_as_uint(pick(lo, __uint_as_float(u), __uint_as_float(q))), SENT - 1u);
                    }
                    __builtin_amdgcn_raw_buffer_store_b64(c, rC, vc, 0, 16);
                    SB();
                    *(LAS u32x2*)(size_t)ra = (u32x2){__float_as_uint(mb), __float_as_uint(rden)};
                    if (lane == 0) ctl[2 + wq] = cr_;
                }
                WAIT_LGKM0();
                if (wid == 4) STAMP(5);
            }
            BARRIER();
#pragma unroll
            for (int i = WD_EARLY; i < 16; ++i) load_wd(i);
            if (slot < 112 && wid < 8 - BQ) {
                WAIT_VM0();
                if (wid == 4) STAMP(7);
                stage2(w2, slot, lw, lds + L_WD0 + lw * 8192, lds + L_WD1 + lw * 8192, abuf, 64, lane);
                if (wid == 4) STAMP(11);
            }
            FLUSH();
            return;
        }
    }

    P2_DEFS;
    LAS float* red = (LAS float*)(lds + L_RED);
    if (sidx < 112) {
        const int slot = sidx, grp = slot >> 3, kq = slot & 7;
        const __amdgpu_buffer_rsrc_t rG = mk_rsrc(a.wg, D_LEN * F_LEN * 2), rU = mk_rsrc(a.wu, D_LEN * F_LEN * 2);
        auto pair = [&](int i) {
            const int d = wid * 16 + i * 2 + (lane >> 5), ky = (d & 3) | ((d >> 1) & 4), c = lane & 31;
            const int vgu = ((kq * 128 + d) * F_LEN + grp * 256 + (((c >> 1) ^ ky) * 16) + (c & 1) * 8) * 2;
            if (i >= (wid < 4 ? G_N : G2_N + G3_N)) dma_nt(rG, (unsigned)(size_t)lds + wid * 8192 + i * 1024, vgu, 0);
            dma_nt(rU, (unsigned)(size_t)lds + 65536 + wid * 8192 + i * 1024, vgu ^ 512, 0);
        };
        if (wid >= 4) {
            if (W_PRE) __builtin_amdgcn_s_sleep(W_PRE_SL);
#pragma unroll
            for (int i = 0; i < W_PRE; ++i) pair(i);
        }
        BARRIER();
        if (wid < 4) {
#pragma unroll
            for (int i = 0; i < W_PRE; ++i) pair(i);
        }
#pragma unroll
        for (int i = W_PRE; i < 8; ++i) pair(i);
        if (wid >= 8 - BQ) {
            const __amdgpu_buffer_rsrc_t rD = mk_rsrc(a.wd + (size_t)slot * 32 * D_LEN, 32 * D_LEN * 2);
            const int vd = wd_off(wid - 4, 0, lane);
#pragma unroll
            for (int i = 0; i < 16; ++i)
                dma_nt(rD, (unsigned)(size_t)lds + L_YB + (wid & 1) * 16384 + i * 1024, vd ^ ((2 * (i & 1) | (i & 4)) * 32), i * (4 * D_LEN));
        }
        const __amdgpu_buffer_rsrc_t rK = mk_rsrc(w2, wid == 0 ? (unsigned)(WS_CAND + 32 * 4096) : 0u);
        const int ok_ = lane < 40 ? (int)WS_KEYS + (xcd >> 2) * 8192 + (xcd & 3) * (KEY_S * 16) + (lane - 32) * KEY_S : (int)(WS_CAND + 32 * 4096);
        const int oc = (int)WS_CAND + 2048 + kq * 256 + n * 16;
        int best = 0;
        u32x4 v = {0u, 0u, 0u, 0u};
        auto request = [&]() {
            asm volatile("" ::: "memory");
            v = LD16_SC1(rK, lane < 32 ? oc + best * 4096 : ok_);
            SB();
        };
        auto settled = [&]() {
            const unsigned mx = umax4(v), mn = min(min(v[0], v[1]), min(v[2], v[3]));
            unsigned fv = lane < 32 ? ~mx : mn | (unsigned)(lane >> 3 ^ 4);
            asm volatile("" : "+v"(fv));
            const bool fine = fv != 0u;
            unsigned km = max(mx, dppu<0xB1>(mx));
            km = max(km, dppu<0x4E>(km));
            km = max(km, dppu<0x141>(km));
            const int nb = (int)(((0xffffu - ((unsigned)__builtin_amdgcn_readlane((int)km, 32) & 0xffffu)) >> 6) & 31u);
            const bool same = nb == best;
            best = nb;
            return __all(fine) && same;
        };
        if (wid == 0) {
            request();
            WAIT_VM(0);
            settled();
            request();
        } else if (wid >= 8 - BQ) WAIT_VM(16);
        else WAIT_VM(0);
        BARRIER();
        if (wid == 0) STAMP(8);
        bf16x8 wfr[4][4];
        {
            const int q = (lane >> 2) & 3, ky = q | ((g & 1) << 2);
#pragma unroll
            for (int u = 0; u < 4; ++u) {
                const int tro = (u >> 1) * 65536 + (8 * g + q) * 512 + (((wid * 2 + (u & 1)) ^ ky) * 32) + (lane & 3) * 8;
#pragma unroll
                for (int ks = 0; ks < 4; ++ks) {
                    const bf16x4 w0 = __builtin_amdgcn_ds_read_tr16_b64_v4i16((LAS bf16x4*)(lds + tro + ks * 16384));
                    const bf16x4 w1 = __builtin_amdgcn_ds_read_tr16_b64_v4i16((LAS bf16x4*)(lds + tro + ks * 16384 + 2048));
                    wfr[u][ks] = __builtin_shufflevector(w0, w1, 0, 1, 2, 3, 4, 5, 6, 7);
                }
            }
            SB();
#pragma unroll
            for (int i = 0; i < 16; ++i) asm volatile("" : "+v"(wfr[i >> 2][i & 3]));
        }
        if (wid >= 8 - BQ && lane < 16) *(LAS unsigned*)(lds + 8192 + wid * 64 + lane * 4) = 0u;
        __amdgpu_buffer_rsrc_t rPo = mk_rsrc(w2 + WS_PRE, 112 * 2048);
        int po = (n & 3) == 0 ? pre_off((grp ^ (g >> 1)) * 8 + wid) + kq * 256 + g * 64 + n * 4 : 112 * 2048;
        unsigned xw = (unsigned)(size_t)lds + (n >> 3) * 16384 + (g & 1) * 8192 + (n & 7) * 8, xr = (unsigned)(size_t)lds + (n & 1) * 8192 + g * 16;
        unsigned ix = (n & 1) ? 0xEEEEEEEEu : 0x44444444u;
        asm volatile("" : "+v"(po), "+v"(xw), "+v"(xr), "+v"(ix));
        u64 m1 = 0xFFFF0000FFFF0000ull, m2 = 0xFFFFFFFF00000000ull;
        f32x4 ac[4];
#pragma unroll
        for (int u = 0; u < 4; ++u) { ac[u] = (f32x4){0.f, 0.f, 0.f, 0.f}; asm volatile("" : "+v"(ac[u])); }
        asm volatile("" : "+s"(rPo), "+s"(m1), "+s"(m2));
        if (wid == 0) {
#pragma unroll 1
            for (int spin = 0; spin <= SPIN_CAP && !settled(); ++spin) {
                __builtin_amdgcn_s_sleep(1);
                request();
            }
            __builtin_amdgcn_s_waitcnt(0x0F70);
            if (lane < 32) *(LAS u32x2*)(size_t)xw = (u32x2){lane < 16 ? v[0] : v[1], lane < 16 ? v[2] : v[3]};
            WAIT_LGKM0();
            STAMP(9);
        }
        BARRIER();
        {
            bf16x8 xf[2];
#pragma unroll
            for (int j = 0; j < 2; ++j) xf[j] = *(const LAS bf16x8*)((const LAS unsigned char*)(size_t)xr + j * 16384);
            SB();
            float rs[3];
#pragma unroll
            for (int i = 0; i < 8; ++i) {
                const int u = i >> 1, j = i & 1;
                ac[u] = smf(xf[j], wfr[u][2 * j], wfr[u][2 * j + 1], ac[u], ix);
                SB();
                if (i == 4) { asm volatile("" : "+v"(ac[0])); rs[0] = ac[0][0] + ac[0][1]; asm volatile("" : "+v"(rs[0])); }
                if (i == 6) { asm volatile("" : "+v"(ac[1])); rs[0] = pick(m1, rs[0], ac[1][0] + ac[1][1]); asm volatile("" : "+v"(rs[0])); }
                if (i == 7) { asm volatile("" : "+v"(ac[2])); rs[2] = ac[2][0] + ac[2][1]; asm volatile("" : "+v"(rs[2])); }
                SB();
            }
            const unsigned e = min(__float_as_uint(pick(m2, rs[0], pick(m1, rs[2], ac[3][0] + ac[3][1]))), SENT - 1u);
            const u32x4 sv = {e, dppu<0x55>(e), dppu<0xAA>(e), dppu<0xFF>(e)};
            ST16_SC1(sv, rPo, po);
            SB();
            asm volatile("" :: "v"(v));
        }
        if (wid == 0) STAMP(14);
        if (wid >= 8 - BQ) {
            WAIT_VM(1);
            if (wid == 7) STAMP(15);
            stage2(w2, slot, wid - 4, lds + L_YB + (wid & 1) * 16384, lds + L_YB + (wid & 1) * 16384 + 8192, lds + wid * 64, 8192, lane);
            if (wid == 7) STAMP(10);
        }
    } else {
        const int rj = sidx - 112;
        if (blockIdx.x == 255 && tid == 0) {
            const u64 want = ((u64)ctl1[3] << 32) | ctl1[2];
            __hip_atomic_store((GAS u64*)(ws + WS_TAG), want, __ATOMIC_RELAXED, __HIP_MEMORY_SCOPE_AGENT);
        }
        if (wid == 4) {
            unsigned kk;
            const unsigned k = poll_winner(w2, xcd, lane, kk);
            if (lane == 0) ctl[4] = k;
            if (lane < 32) ctl[8 + lane] = kk;
            WAIT_LGKM0();
        }
        BARRIER();
        const unsigned kw = CTL(4);
        if (wid == 0) STAMP(16);
        {
            const unsigned kc = ctl[8 + wid * 4 + g];
            const __amdgpu_buffer_rsrc_t rL = mk_rsrc(w2 + WS_CAND + (size_t)wid * 16384, 16384);
            u32x2 hv2, nv2;
            for (int spin = 0;; ++spin) {
                asm volatile("" ::: "memory");
                hv2 = __builtin_amdgcn_raw_buffer_load_b64(rL, g * 4096 + (rj * 64 + n * 4) * 2, 0, 16);
                nv2 = __builtin_amdgcn_raw_buffer_load_b64(rL, g * 4096 + 2048 + (rj * 64 + n * 4) * 2, 0, 16);
                if (__all(max(max(hv2[0], hv2[1]), max(nv2[0], nv2[1])) != SENT) || spin > SPIN_CAP) break;
                __builtin_amdgcn_s_sleep(1);
            }
            u32x2 ov;
#pragma unroll
            for (int i = 0; i < 2; ++i) {
                const unsigned u = hv2[i], q = nv2[i];
                ov[i] = pk_bf16(bflo(u) + bflo(q), bfhi(u) + bfhi(q));
            }
            const int crow = (int)(0xffffu - (kc & 0xffffu)) & (S_LEN - 1);
            if (kc != kw) *(u32x2*)(a.out + (size_t)crow * D_LEN + rj * 64 + n * 4) = ov;
        }
        const int wrow = (int)(0xffffu - (kw & 0xffffu)) & (S_LEN - 1);
        const __amdgpu_buffer_rsrc_t rC = mk_rsrc(w2 + WS_CAND + (size_t)(wrow >> 6) * 4096, 4096);
        const __amdgpu_buffer_rsrc_t rPt = mk_rsrc(w2 + WS_PART, 112 * 4096);
        const int ocol = (rj >> 2) * 256 + ((lane >> 1) & 1) * 128 + (rj & 3) * 32 + (lane & 1) * 16 + (lane >> 2);
        unsigned hv, hnv;
        for (int spin = 0;; ++spin) {
            asm volatile("" ::: "memory");
            hv = __builtin_amdgcn_raw_buffer_load_b32(rC, (ocol & ~1) * 2, 0, 16);
            hnv = __builtin_amdgcn_raw_buffer_load_b32(rC, 2048 + (ocol & ~1) * 2, 0, 16);
            if (__all(max(hv, hnv) != SENT) || spin > SPIN_CAP) break;
        }
        float sgm = rbf(1.0f / (1.0f + __expf(-key2r(kw))));
        float hf = (ocol & 1) ? bfhi(hv) : bflo(hv), hnf = (ocol & 1) ? bfhi(hnv) : bflo(hnv);
        GAS unsigned short* optr = (GAS unsigned short*)(a.out + (size_t)wrow * D_LEN + ocol);
        unsigned rw = (unsigned)(size_t)red + (wid >> 2) * 1024 + (n * 4 + g) * 16 + (wid & 3) * 4, rr = (unsigned)(size_t)red + lane * 16;
        int po = wid * 16384 + (rj >> 3) * 8192 + g * 2048 + ((rj & 7) * 64 + n * 4) * 4;
        int po3 = po + 3 * 131072;
        asm volatile("" : "+v"(sgm), "+v"(optr), "+v"(hf), "+v"(hnf), "+v"(rw), "+v"(rr), "+v"(po), "+v"(po3));
        float sm;
#pragma unroll 1
        for (int spin = 0;; ++spin) {
            asm volatile("" ::: "memory");
            u32x4 pv[4];
#pragma unroll
            for (int i = 0; i < 4; ++i) pv[i] = __builtin_amdgcn_raw_buffer_load_b128(rPt, i < 3 ? po : po3, i < 3 ? i * 131072 : 0, 16);
            SB();
            unsigned t[4];
#pragma unroll
            for (int j = 0; j < 4; ++j) {
                const unsigned u0 = pv[0][j], u1 = pv[1][j], u2 = pv[2][j], u3 = pv[3][j];
                t[j] = __float_as_uint(((__uint_as_float(u0) + __uint_as_float(u1)) + __uint_as_float(u2)) + __uint_as_float(u3));
            }
            const auto r0 = __builtin_amdgcn_permlane32_swap(t[0], t[2], false, false), r1 = __builtin_amdgcn_permlane32_swap(t[1], t[3], false, false);
            const unsigned a0 = r0[0], a1 = r0[1], b0 = r1[0], b1 = r1[1];
            const auto r2 = __builtin_amdgcn_permlane16_swap(__float_as_uint(__uint_as_float(a0) + __uint_as_float(a1)),
                                                             __float_as_uint(__uint_as_float(b0) + __uint_as_float(b1)), false, false);
            const unsigned c0 = r2[0], c1 = r2[1];
            sm = __uint_as_float(c0) + __uint_as_float(c1);
            if (__builtin_expect(__builtin_amdgcn_ballot_w64(sm != sm) == 0, 1)) break;
            if (__all(max(max(umax4(pv[0]), umax4(pv[1])), max(umax4(pv[2]), umax4(pv[3]))) != SENT) || spin > SPIN_CAP) break;
            if (FIN_SLEEP) __builtin_amdgcn_s_sleep(FIN_SLEEP);
        }
        if (wid == 0) STAMP(17);
        *(LAS float*)(size_t)rw = sm;
        WAIT_LGKM0();
        BARRIER();
        if (wid == 0) {
            const f32x4 u = *(const LAS f32x4*)(size_t)rr, v = *(const LAS f32x4*)((const LAS unsigned char*)(size_t)rr + 1024);
            const f32x4 w = u + v;
            const float t = (w[0] + w[2]) + (w[1] + w[3]);
            *optr = (unsigned short)pk_bf16(hf + rbf(hnf + rbf(rbf(t) * sgm)), 0.f);
            STAMP(18);
        }
        __builtin_amdgcn_s_waitcnt(0x0F70);
    }
    FLUSH();
}

extern "C" void kernel_launch(void* const* d_in, const int* in_sizes, int n_in, void* d_out, int out_size, void* d_ws,
                              size_t ws_size, hipStream_t stream) {
    Args a;
    a.x = (const bf16_t*)d_in[0];
    a.cr = (const bf16_t*)d_in[1];
    a.ci = (const bf16_t*)d_in[2];
    a.wre = (const bf16_t*)d_in[3];
    a.wim = (const bf16_t*)d_in[4];
    a.w_r = (const bf16_t*)d_in[6];
    a.wg = (const bf16_t*)d_in[7];
    a.wu = (const bf16_t*)d_in[8];
    a.wd = (const bf16_t*)d_in[9];
    a.out = (bf16_t*)d_out;
    a.ws = (char*)d_ws + ((ws_size - ((size_t)24 << 20)) & ~(size_t)255);
    (void)hipFuncSetAttribute((const void*)fused_kernel, hipFuncAttributeMaxDynamicSharedMemorySize, LDS_BYTES);
    fused_kernel<<<256, 768, LDS_BYTES, stream>>>(a);
}
```

```cpp
#include <hip/hip_runtime.h>
#include <hip/hip_bf16.h>
#include <stdint.h>

typedef unsigned short bf16_t;
typedef short bf16x8 __attribute__((ext_vector_type(8)));
typedef short bf16x4 __attribute__((ext_vector_type(4)));
typedef float f32x4 __attribute__((ext_vector_type(4)));
typedef unsigned u32x4 __attribute__((ext_vector_type(4)));
typedef unsigned u32x2 __attribute__((ext_vector_type(2)));
typedef unsigned u32x3 __attribute__((ext_vector_type(3)));
typedef unsigned long long u64;
#define LAS __attribute__((address_space(3)))
#define GAS __attribute__((address_space(1)))

constexpr int S_LEN = 2048, D_LEN = 1024, F_LEN = 3584;
constexpr unsigned SENT = 0xFFFFFFFFu;
constexpr int SPIN_CAP = 1 << 17;

constexpr size_t WS_KEYS = 0;
constexpr size_t WS_FLAGS = 16384;
constexpr size_t WS_XRD = 65536;
constexpr size_t WS_FLAG1 = 196608;
constexpr size_t WS_TAG = 131072;
constexpr size_t WS_VOTE = WS_TAG + 4096;
constexpr size_t WS_PROBE = (size_t)20 << 20;
constexpr size_t WS_AT = (size_t)4 << 20;
constexpr size_t WS_Y = (size_t)12 << 20;
constexpr size_t WS_STATS = (size_t)16 << 20;
constexpr size_t WS_PART = WS_STATS + (512u << 10);
constexpr size_t WS_CAND = WS_STATS + (1024u << 10);
constexpr size_t WS_PRE = WS_STATS + (1152u << 10);
constexpr size_t FILL_BYTES = (size_t)1408 << 10;

__device__ __forceinline__ float bflo(unsigned v) { return __uint_as_float(v << 16); }
__device__ __forceinline__ float bfhi(unsigned v) { return __uint_as_float(v & 0xffff0000u); }
__device__ __forceinline__ unsigned pk_bf16(float lo, float hi) {
    typedef __bf16 bf2 __attribute__((ext_vector_type(2)));
    typedef float f2 __attribute__((ext_vector_type(2)));
    f2 v = {lo, hi};
    bf2 r = __builtin_convertvector(v, bf2);
    return __builtin_bit_cast(unsigned, r);
}
__device__ __forceinline__ float rbf(float x) { return __uint_as_float(pk_bf16(0.f, x)); }

template <int CTRL> __device__ __forceinline__ unsigned dppu(unsigned v) {
    return (unsigned)__builtin_amdgcn_update_dpp(0, (int)v, CTRL, 0xf, 0xf, true);
}
template <int CTRL> __device__ __forceinline__ float dppf(float v) { return __uint_as_float(dppu<CTRL>(__float_as_uint(v))); }
__device__ __forceinline__ void swap16(unsigned v, unsigned& a, unsigned& b) {
    auto r = __builtin_amdgcn_permlane16_swap(v, v, false, false);
    a = r[0]; b = r[1];
}
__device__ __forceinline__ void swap32(unsigned v, unsigned& a, unsigned& b) {
    auto r = __builtin_amdgcn_permlane32_swap(v, v, false, false);
    a = r[0]; b = r[1];
}
__device__ __forceinline__ float sum_rows(float v) {
    unsigned a, b;
    swap16(__float_as_uint(v), a, b); v = __uint_as_float(a) + __uint_as_float(b);
    swap32(__float_as_uint(v), a, b); v = __uint_as_float(a) + __uint_as_float(b);
    return v;
}
__device__ __forceinline__ float wave_sum(float v) {
    v += dppf<0xB1>(v);
    v += dppf<0x4E>(v);
    v += dppf<0x141>(v);
    v += dppf<0x140>(v);
    return sum_rows(v);
}
__device__ __forceinline__ unsigned wave_max_u32(unsigned v) {
    v = max(v, dppu<0xB1>(v));
    v = max(v, dppu<0x4E>(v));
    v = max(v, dppu<0x141>(v));
    v = max(v, dppu<0x140>(v));
    unsigned a, b;
    swap16(v, a, b); v = max(a, b);
    swap32(v, a, b); v = max(a, b);
    return v;
}
__device__ __forceinline__ unsigned wave_max_s(unsigned v) {
    v = max(v, dppu<0xB1>(v));
    v = max(v, dppu<0x4E>(v));
    v = max(v, dppu<0x141>(v));
    v = max(v, dppu<0x140>(v));
    v = max(v, dppu<0x142>(v));
    v = max(v, dppu<0x143>(v));
    return (unsigned)__builtin_amdgcn_readlane((int)v, 63);
}
__device__ __forceinline__ float sel4(int g, float a, float b, float c, float d) {
    const float t0 = (g & 1) ? b : a, t1 = (g & 1) ? d : c;
    return (g & 2) ? t1 : t0;
}
__device__ __forceinline__ float pick(u64 m, float a, float b) {
    float r;
    asm("v_cndmask_b32 %0, %1, %2, %3" : "=v"(r) : "v"(a), "v"(b), "s"(m));
    return r;
}
__device__ __forceinline__ unsigned umax4(u32x4 v) { return max(max(v[0], v[1]), max(v[2], v[3])); }

__device__ __forceinline__ void touch_line(const void* p, unsigned lds_dst) {
    unsigned keep;
    asm volatile("s_mov_b32 %0, m0\n\ts_mov_b32 m0, %2\n\ts_nop 0\n\tglobal_load_lds_dword %1, off\n\ts_mov_b32 m0, %0"
                 : "=&s"(keep) : "v"(p), "s"(lds_dst) : "memory");
}
__device__ __forceinline__ __amdgpu_buffer_rsrc_t mk_rsrc(const void* p, unsigned bytes) {
    return __builtin_amdgcn_make_buffer_rsrc((void*)p, 0, (int)bytes, 0x00020000);
}
#define LDS_LOAD(w, v, o, pol) "buffer_load_" w " " v ", %0, %2 offen" o pol " lds"
#define LDS_M0 "s_mov_b32 m0, %1\n\ts_nop 0\n\t"
__device__ __forceinline__ void dma_nt(__amdgpu_buffer_rsrc_t r, unsigned dst, int voff, int soff) {
    asm volatile(LDS_M0 LDS_LOAD("dwordx4", "%3", "", " nt") :: "s"(r), "s"(dst), "s"(soff), "v"(voff) : "memory", "m0");
}
template <bool SC1> __device__ __forceinline__ void dma_word(__amdgpu_buffer_rsrc_t r, unsigned dst, int voff) {
    if constexpr (SC1) asm volatile(LDS_M0 LDS_LOAD("dword", "%3", "", " sc1") :: "s"(r), "s"(dst), "n"(0), "v"(voff) : "memory", "m0");
    else asm volatile(LDS_M0 LDS_LOAD("dword", "%3", "", "") :: "s"(r), "s"(dst), "n"(0), "v"(voff) : "memory", "m0");
}
template <bool SC1> __device__ __forceinline__ void dma_2k(__amdgpu_buffer_rsrc_t r, unsigned dst, int v0, int v1, int soff) {
    if constexpr (SC1) asm volatile(LDS_M0 LDS_LOAD("dwordx4", "%3", "", " sc1") "\n\t" LDS_LOAD("dwordx4", "%4", " offset:1024", " sc1")
                 :: "s"(r), "s"(dst), "s"(soff), "v"(v0), "v"(v1) : "memory", "m0");
    else asm volatile(LDS_M0 LDS_LOAD("dwordx4", "%3", "", "") "\n\t" LDS_LOAD("dwordx4", "%4", " offset:1024", "")
                 :: "s"(r), "s"(dst), "s"(soff), "v"(v0), "v"(v1) : "memory", "m0");
}
template <bool SC1> __device__ __forceinline__ void dma_4k(__amdgpu_buffer_rsrc_t r, unsigned dst, const int (&v)[4], int soff) {
#define FOUR(pol) asm volatile(LDS_M0 LDS_LOAD("dwordx4", "%3", "", pol) "\n\t" LDS_LOAD("dwordx4", "%4", " offset:1024", pol) "\n\t" \
        LDS_LOAD("dwordx4", "%5", " offset:2048", pol) "\n\t" LDS_LOAD("dwordx4", "%6", " offset:3072", pol) \
        :: "s"(r), "s"(dst), "s"(soff), "v"(v[0]), "v"(v[1]), "v"(v[2]), "v"(v[3]) : "memory", "m0")
    if constexpr (SC1) FOUR(" sc1"); else FOUR("");
#undef FOUR
}
#define CODE_BYTES 25984
__device__ __forceinline__ __amdgpu_buffer_rsrc_t code_rsrc() {
    unsigned long long p; unsigned d;
    asm volatile("s_getpc_b64 %0\n1:\n\ts_mov_b32 %1, 1b-_Z12fused_kernel4Args" : "=s"(p), "=s"(d));
    return __builtin_amdgcn_make_buffer_rsrc((void*)(p - d), 0, CODE_BYTES, 0x00020000);
}
#define LD16_SC1(rs, off) __builtin_amdgcn_raw_buffer_load_b128(rs, off, 0, 16)
#define ST16_SC1(v, rs, off) __builtin_amdgcn_raw_buffer_store_b128(v, rs, off, 0, 16)

constexpr int BT = 128, BK = 64, NSTAGE = 4;
constexpr int TILE_BYTES = BT * BK * 2;
constexpr int STAGE_BYTES = 2 * TILE_BYTES;
constexpr int RING_BYTES = NSTAGE * STAGE_BYTES;
constexpr int L_DUMMY = RING_BYTES;
constexpr int L_CTL = RING_BYTES + 256;
constexpr int L_RED = RING_BYTES + 512;

#define WAIT_VM(n) asm volatile("s_waitcnt vmcnt(" #n ")" ::: "memory")
#define WAIT_VM0() do { asm volatile("" ::: "memory"); __builtin_amdgcn_s_waitcnt(0x0F70); asm volatile("s_waitcnt vmcnt(0)" ::: "memory"); } while (0)
#define WAIT_LGKM0() asm volatile("s_waitcnt lgkmcnt(0)" ::: "memory")
#define BARRIER() do { asm volatile("" ::: "memory"); __builtin_amdgcn_s_barrier(); asm volatile("" ::: "memory"); } while (0)

__device__ __forceinline__ int prow(int rho) {
    const int w = rho >> 6, m = (rho >> 4) & 3, q = (rho >> 2) & 3, r = rho & 3;
    return w * 64 + (m >> 1) * 32 + q * 8 + (m & 1) * 4 + r;
}
__device__ __forceinline__ unsigned r2key(float r, unsigned code) {
    const unsigned u = pk_bf16(0.f, r);
    return ((u ^ ((unsigned)((int)u >> 31) | 0x80000000u)) & 0xffff0000u) | code;
}
__device__ __forceinline__ float key2r(unsigned k) {
    const unsigned o = k >> 16;
    return bflo((o & 0x8000u) ? (o & 0x7fffu) : (~o & 0xffffu));
}
#define STAT_M 5
#define W_PRE 4
#define W_PRE_SL 12
#define BQ 2
#define POLL_T (STAT_M + 5)
#define AUX_SLEEP 12
#define WD_EARLY 2
#define C2_START 2
#define TAIL_PRIO 3
#define STAT_DELAY 12
#define AUX_STAY 1
#define CATCH1 4
#define CPF_MASK1 0x1080u
#define CPF_MASK2 0x1080u
#define CPF_STAG 5
#define CPF_FIX 0x1000u
#define KEY_S 128
#define G2_N 2
#define G3_N 2
#define FIN_SLEEP 1
#define G_N 8
#define POLL_SLEEP 4
__device__ __forceinline__ f32x4 smf(bf16x8 a, bf16x8 b0, bf16x8 b1, f32x4 d, unsigned idx) {
    typedef __bf16 bf8 __attribute__((ext_vector_type(8)));
    typedef __bf16 bf16v __attribute__((ext_vector_type(16)));
    const auto b = __builtin_shufflevector(b0, b1, 0, 1, 2, 3, 4, 5, 6, 7, 8, 9, 10, 11, 12, 13, 14, 15);
    return __builtin_amdgcn_smfmac_f32_16x16x64_bf16(__builtin_bit_cast(bf8, a), __builtin_bit_cast(bf16v, b), d, (int)idx, 0, 0);
}
__device__ __forceinline__ float dot2bf(unsigned a, unsigned b, float c) {
    typedef __bf16 bf2 __attribute__((ext_vector_type(2)));
    return __builtin_amdgcn_fdot2_f32_bf16(__builtin_bit_cast(bf2, a), __builtin_bit_cast(bf2, b), c, false);
}

#define STAMP(i) do { } while (0)
#define CSTAMP(i) do { } while (0)
#define FLUSH() do { } while (0)
#define TSV do { } while (0)

#define KSHIFT 4

constexpr int L_HT = ((32 + KSHIFT) % NSTAGE) * STAGE_BYTES;
constexpr int L_WD0 = ((33 + KSHIFT) % NSTAGE) * STAGE_BYTES;
constexpr int L_WD1 = ((34 + KSHIFT) % NSTAGE) * STAGE_BYTES;
constexpr int L_YB = RING_BYTES;
constexpr int L_ATAB = L_YB;
constexpr int LDS_BYTES = RING_BYTES + 32768;

#define SB() __builtin_amdgcn_sched_barrier(0)
template <int N> __device__ __forceinline__ void wait_vm() { asm volatile("s_waitcnt vmcnt(%0)" :: "n"(N) : "memory"); }
__device__ __forceinline__ int at_tile(int c, int par) { return ((c << 8) & 0x78000) | (((c << 2) & 0x2000) ^ par); }
__device__ __forceinline__ int at_panel(int ep) { return ep << 19; }
__device__ __forceinline__ int pre_off(int slot) { const int s_ = slot >= 56, l = slot - 56 * s_; return (l >> 2) * 16384 + s_ * 8192 + (l & 3) * 2048; }
static_assert(KSHIFT == 4, "flag samples must have replaced older contents of their slot; even tile counts");
struct Ring {
    __amdgpu_buffer_rsrc_t rP, rQ, rF;
    int voP[4], voQ[4], voF;
    LAS unsigned char* fl;
    unsigned lb;
    int sp, sq;
    int poff, par, xm;
    bool img;
    template <int LD, bool PIMG> __device__ __forceinline__ void setup(int lw, int lane) {
        const int r = lane >> 3, v0 = (PIMG ? r : (r & 3) + (r & 4) * 2) * (LD * 2) + (((lane & 7) ^ (lane >> 4)) << 4);
#pragma unroll
        for (int j = 0; j < 4; ++j) {
            voQ[j] = (v0 ^ ((j & 1) * 64)) + (PIMG ? j * 8 : (j & 1) * 16 + (j >> 1) * 4) * (LD * 2) - j * 1024;
            voP[j] = PIMG ? (lw >> 1) * 16384 + (lw & 1) * 4096 + lane * 16 : voQ[j];
        }
        img = PIMG;
    }
    __device__ __forceinline__ void seek(int col, int pof) {
        sq = col; poff = pof;
        sp = (img ? at_tile(sq, par) : sq) + poff;
    }
    __device__ __forceinline__ void step() {
        sq = (sq + BK * 2) & 4095;
        sp = (img ? at_tile(sq ^ xm, par) : sq) + poff;
        asm volatile("" : "+s"(sq), "+s"(sp));
    }
    __device__ __forceinline__ void sample() { dma_word<true>(rF, (unsigned)(size_t)fl, voF); }
    __device__ __forceinline__ unsigned peek(unsigned addr) {
        unsigned v;
        asm volatile("ds_read_b32 %0, %1\n\ts_waitcnt lgkmcnt(0)" : "=v"(v) : "v"(addr) : "memory");
        return v;
    }
    __device__ __forceinline__ void gate(unsigned want, unsigned sa, unsigned need, unsigned& have) {
#pragma unroll 1
        for (int spin = 0; spin <= SPIN_CAP && (need & ~have) != 0u; ++spin) {
            if (spin) { sample(); WAIT_VM(0); __builtin_amdgcn_s_sleep(1); }
            const unsigned long long m = __ballot(peek(sa) == want);
            have = (unsigned)m & (unsigned)(m >> 32);
        }
    }
    template <int AUX> __device__ __forceinline__ void issueP(LAS unsigned char* base) { dma_4k<AUX != 0>(rP, (unsigned)(size_t)base, voP, sp); }
    __device__ __forceinline__ void issueQ(LAS unsigned char* base) { dma_4k<false>(rQ, (unsigned)(size_t)base + TILE_BYTES, voQ, sq ^ xm); }
    template <bool P2> __device__ __forceinline__ void issue(LAS unsigned char* lds) {
        issueP<P2 ? 16 : 0>(lds + lb);
        issueQ(lds + lb);
        SB();
        if (P2) sample();
        lb = (lb + STAGE_BYTES) & (RING_BYTES - 1);
        asm volatile("" : "+s"(lb));
        step();
        SB();
    }
    template <int CAT> __device__ __forceinline__ void ramp(LAS unsigned char* lds) {
#pragma unroll 1
        for (int i = 1; i <= CAT + 1; ++i) {
            issue<false>(lds);
            if (i == CAT + 1) continue;
            WAIT_VM(8);
            BARRIER();
        }
    }
    template <bool P2, bool SAMP, int W0 = 16> __device__ __forceinline__ void steady(int cnt, int trig = 0, int v2 = 0) {
        int t, u;
#define LD(v, r, s, o) "buffer_load_dwordx4 " v ", " r ", " s " offen" o " lds\n\t"
        if constexpr (P2) {
#define BODY(SMP, TST, ENT) asm volatile("s_mov_b32 m0, %0\n" ENT "1:\n\ts_waitcnt vmcnt(16)\n2:\n\ts_barrier\n\t" \
            LD("%8", "%6", "%2", " sc1") LD("%8", "%6", "%2", " offset:1024 sc1") LD("%8", "%6", "%2", " offset:2048 sc1") LD("%8", "%6", "%2", " offset:3072 sc1") \
            SMP "s_xor_b32 %4, %1, %18\n\ts_add_i32 %1, %1, 0x80\n\ts_add_i32 m0, %0, 0x4000\n\ts_and_b32 %1, %1, 0xfff\n\t" \
            LD("%9", "%7", "%4", "") "s_xor_b32 %2, %1, %18\n\ts_lshl_b32 %5, %1, 2\n\ts_lshl_b32 %2, %2, 8\n\ts_and_b32 %2, %2, 0x78000\n\t" \
            LD("%10", "%7", "%4", " offset:1024") "s_and_b32 %5, %5, 0x2000\n\ts_xor_b32 %5, %5, %13\n\ts_or_b32 %2, %2, %5\n\ts_add_i32 %3, %3, -1\n\t" \
            LD("%11", "%7", "%4", " offset:2048") "s_add_i32 %0, %0, 0x8000\n\ts_and_b32 %0, %0, 0x1ffff\n\t" TST "s_cmp_lg_u32 %3, 0\n\t" \
            LD("%12", "%7", "%4", " offset:3072") "\n4:\n\ts_mov_b32 m0, %0\n\ts_cbranch_scc1 1b\n\ts_waitcnt vmcnt(16)\n\ts_barrier" \
            : "+s"(lb), "+s"(sq), "+s"(sp), "+s"(cnt), "=&s"(t), "=&s"(u) \
            : "s"(rP), "s"(rQ), "v"(voP[0]), "v"(voQ[0]), "v"(voQ[1]), "v"(voQ[2]), "v"(voQ[3]), "s"(par | poff), "s"(rF), "v"(voF), "s"((unsigned)(size_t)fl), "n"(W0), "s"(xm), \
              "s"(trig), "v"(v2) \
            : "memory", "scc", "m0")
            if constexpr (SAMP) BODY("s_mov_b32 m0, %16\n\ts_nop 0\n\tbuffer_load_dword %15, %14, 0 offen sc1 lds\n\t", "", "");
            else BODY("", "s_cmp_eq_u32 %3, %19\n\ts_cbranch_scc1 3b\n\t",
                      "\ts_waitcnt vmcnt(%17)\n\ts_branch 2f\n3:\n\t" LD("%12", "%7", "%4", " offset:3072") "s_mov_b32 m0, %16\n\ts_nop 0\n\tbuffer_load_dword %20, %7, 0 offen lds\n\t"
                      "s_add_i32 m0, %16, 0x200\n\ts_cmp_lg_u32 %3, 0\n\tbuffer_load_dword %15, %14, 0 offen sc1 lds\n\ts_branch 4f\n");
#undef BODY
        } else {
            asm volatile("s_mov_b32 m0, %0\n\ts_branch 2f\n1:\n\ts_waitcnt vmcnt(16)\n\ts_barrier\n2:\n\t"
                LD("%6", "%4", "%1", "") LD("%7", "%4", "%1", " offset:1024") LD("%8", "%4", "%1", " offset:2048") LD("%9", "%4", "%1", " offset:3072")
                "s_add_i32 m0, %0, 0x4000\n\ts_mov_b32 %3, %1\n\t"
                LD("%6", "%5", "%3", "") "s_add_i32 %1, %1, 0x80\n\ts_add_i32 %0, %0, 0x8000\n\t"
                LD("%7", "%5", "%3", " offset:1024") "s_and_b32 %0, %0, 0x1ffff\n\ts_add_i32 %2, %2, -1\n\t"
                LD("%8", "%5", "%3", " offset:2048") "s_cmp_lg_u32 %2, 0\n\t"
                LD("%9", "%5", "%3", " offset:3072") "s_mov_b32 m0, %0\n\ts_cbranch_scc1 1b\n\ts_waitcnt vmcnt(16)\n\ts_barrier"
                : "+s"(lb), "+s"(sq), "+s"(cnt), "=&s"(t)
                : "s"(rP), "s"(rQ), "v"(voQ[0]), "v"(voQ[1]), "v"(voQ[2]), "v"(voQ[3])
                : "memory", "scc", "m0");
            (void)u;
        }
#undef LD
    }
};

template <bool PLIN, bool FIRST, class Pre>
__device__ __forceinline__ void compute_loop(const LAS unsigned char* lds, f32x4 (&acc)[4][4], int NT, int wp, int wq, int lane, Pre pre,
                                             bf16x8 (&p0)[4], bf16x8 (&p1)[4], unsigned last) {
    const int fr = lane & 15, fq = lane >> 4, key = (fr >> 1) & 7;
    const unsigned l0 = (unsigned)(size_t)lds;
    const unsigned oP0 = l0 + (PLIN ? wp * 8192 + lane * 16 : (wp * 64 + fr) * 128 + ((fq ^ key) * 16));
    const unsigned oP1 = l0 + (PLIN ? wp * 8192 + 1024 + lane * 16 : (wp * 64 + fr) * 128 + (((4 + fq) ^ key) * 16));
    const unsigned oQ0 = l0 + TILE_BYTES + (wq * 64 + fr) * 128 + ((fq ^ key) * 16);
    const unsigned oQ1 = l0 + TILE_BYTES + (wq * 64 + fr) * 128 + (((4 + fq) ^ key) * 16);
    unsigned aP0 = oP0, aQ0 = oQ0, aP1 = oP1, aQ1 = oQ1;
    bf16x8 r0[4], r1[4], q0[4], q1[4];
    unsigned xp = 2 * STAGE_BYTES;
#pragma unroll
    for (int i = 0; i < 16; ++i) asm volatile("" : "+v"(acc[i >> 2][i & 3]));
    asm volatile("" : "+v"(aP0), "+v"(aQ0), "+v"(aP1), "+v"(aQ1));
#define MM(pp, qq, m, n) do { acc[m][n] = __builtin_amdgcn_mfma_f32_16x16x32_bf16(pp[m], qq[n], acc[m][n], 0, 0, 0); SB(); } while (0)
#define RD(dst, i, a, st) do { dst[i] = *(const LAS bf16x8*)((const LAS unsigned char*)(size_t)(a) + (st) * STAGE_BYTES + (i) * 2048); SB(); } while (0)
#define ADP(a) do { a ^= xp; asm volatile("" : "+v"(a)); SB(); } while (0)
#define ADR(a) do { a ^= 2 * STAGE_BYTES; asm volatile("" : "+v"(a)); SB(); } while (0)
    __builtin_amdgcn_s_waitcnt(0xC07F);
    SB();
    if (FIRST) BARRIER();
    SB();
    RD(p0, 0, aP0, 0); RD(q0, 0, aQ0, 0); RD(p1, 0, aP1, 0); RD(q1, 0, aQ1, 0); RD(p1, 1, aP1, 0); RD(p0, 1, aP0, 0);
    RD(p0, 2, aP0, 0); RD(p1, 2, aP1, 0); RD(p1, 3, aP1, 0); RD(p0, 3, aP0, 0);
    pre();
    SB();
    int t = NT;
#pragma unroll 1
    do {
        MM(p0, q0, 0, 0); MM(p1, q1, 0, 0); RD(q0, 1, aQ0, 0);
        MM(p1, q1, 1, 0); MM(p0, q0, 1, 0); RD(q1, 1, aQ1, 0);
        MM(p0, q0, 2, 0); MM(p1, q1, 2, 0); RD(q0, 2, aQ0, 0);
        MM(p1, q1, 3, 0); MM(p0, q0, 3, 0); RD(q1, 2, aQ1, 0);
        MM(p0, q0, 3, 1); MM(p1, q1, 3, 1); RD(q0, 3, aQ0, 0);
        MM(p1, q1, 2, 1); MM(p0, q0, 2, 1); RD(q1, 3, aQ1, 0);
        MM(p0, q0, 1, 1); MM(p1, q1, 1, 1);
        MM(p1, q1, 0, 1); MM(p0, q0, 0, 1);
        MM(p0, q0, 0, 2); MM(p1, q1, 0, 2); __builtin_amdgcn_s_waitcnt(0xC07F); SB(); BARRIER(); SB();
        MM(p1, q1, 1, 2); RD(r0, 0, aP0, 1); MM(p0, q0, 1, 2); RD(q0, 0, aQ0, 1);
        MM(p0, q0, 2, 2); RD(r1, 0, aP1, 1); MM(p1, q1, 2, 2); RD(q1, 0, aQ1, 1);
        MM(p1, q1, 3, 2); MM(p0, q0, 3, 2); RD(r1, 1, aP1, 1);
        MM(p0, q0, 3, 3); RD(r0, 1, aP0, 1); MM(p1, q1, 3, 3);
        MM(p1, q1, 2, 3); RD(r0, 2, aP0, 1); MM(p0, q0, 2, 3); RD(r1, 2, aP1, 1);
        MM(p0, q0, 1, 3); MM(p1, q1, 1, 3); RD(r1, 3, aP1, 1);
        MM(p1, q1, 0, 3); RD(r0, 3, aP0, 1); MM(p0, q0, 0, 3);

        MM(r0, q0, 0, 0); MM(r1, q1, 0, 0); RD(q0, 1, aQ0, 1);
        MM(r1, q1, 1, 0); MM(r0, q0, 1, 0); RD(q1, 1, aQ1, 1);
        MM(r0, q0, 2, 0); MM(r1, q1, 2, 0); RD(q0, 2, aQ0, 1);
        MM(r1, q1, 3, 0); MM(r0, q0, 3, 0); RD(q1, 2, aQ1, 1);
        MM(r0, q0, 3, 1); MM(r1, q1, 3, 1); RD(q0, 3, aQ0, 1);
        MM(r1, q1, 2, 1); MM(r0, q0, 2, 1); RD(q1, 3, aQ1, 1);
        MM(r0, q0, 1, 1); t -= 2; if (PLIN) xp = t == 0 ? last : 2 * STAGE_BYTES; asm volatile("" : "+s"(t), "+s"(xp)); SB(); MM(r1, q1, 1, 1); ADP(aP0);
        MM(r1, q1, 0, 1); ADP(aP1); MM(r0, q0, 0, 1); ADR(aQ0);
        MM(r0, q0, 0, 2); ADR(aQ1); MM(r1, q1, 0, 2); __builtin_amdgcn_s_waitcnt(0xC07F); SB(); BARRIER(); SB();
        MM(r1, q1, 1, 2); RD(p0, 0, aP0, 0); MM(r0, q0, 1, 2); RD(q0, 0, aQ0, 0);
        MM(r0, q0, 2, 2); RD(p1, 0, aP1, 0); MM(r1, q1, 2, 2); RD(q1, 0, aQ1, 0);
        MM(r1, q1, 3, 2); MM(r0, q0, 3, 2); RD(p1, 1, aP1, 0);
        MM(r0, q0, 3, 3); RD(p0, 1, aP0, 0); MM(r1, q1, 3, 3);
        MM(r1, q1, 2, 3); RD(p0, 2, aP0, 0); MM(r0, q0, 2, 3); RD(p1, 2, aP1, 0);
        MM(r0, q0, 1, 3); MM(r1, q1, 1, 3); RD(p1, 3, aP1, 0);
        MM(r1, q1, 0, 3); RD(p0, 3, aP0, 0); MM(r0, q0, 0, 3);
    } while (t != 0);
    SB();
#undef MM
#undef RD
#undef ADR
#undef ADP
}

struct Args {
    const bf16_t* x; const bf16_t* wre; const bf16_t* wim; char* ws; const bf16_t* cr; const bf16_t* ci;
    const bf16_t* w_r; const bf16_t* wg; const bf16_t* wu; const bf16_t* wd; bf16_t* out;
};

__device__ __forceinline__ unsigned poll_winner(const char* ws, int rep, int lane, unsigned& mine) {
    GAS unsigned* kp = (GAS unsigned*)(ws + WS_KEYS + (size_t)(rep >> 2) * 8192 + (rep & 3) * (KEY_S * 16) + ((lane >> 2) & 7) * KEY_S) + (lane & 3);
    unsigned kw;
    for (int spin = 0;; ++spin) {
            asm volatile("" ::: "memory");
        kw = __hip_atomic_load(kp, __ATOMIC_RELAXED, __HIP_MEMORY_SCOPE_AGENT);
        if (__all(kw != 0u) || spin > SPIN_CAP) break;
        __builtin_amdgcn_s_sleep(POLL_SLEEP);
    }
    mine = kw;
    return __builtin_amdgcn_readfirstlane(wave_max_u32(kw));
}

__device__ __forceinline__ void stage2(char* w2, int slot, int lw, LAS unsigned char* img0, LAS unsigned char* img1, LAS unsigned char* abuf, int zoff, int lane) {
    const int g = lane >> 4, n = lane & 15;
    const __amdgpu_buffer_rsrc_t rPi = mk_rsrc(w2 + WS_PRE + pre_off(slot), 2048);
    unsigned pp[8];
    auto request = [&]() {
        asm volatile("" ::: "memory");
#pragma unroll
        for (int k = 0; k < 8; ++k) pp[k] = __builtin_amdgcn_raw_buffer_load_b32(rPi, k * 256 + lane * 4, 0, 16);
        SB();
    };
    request();
    bf16x8 wf[16];
    {
        const int q = (lane >> 2) & 3, ky = q | ((g & 1) << 2);
#pragma unroll
        for (int j = 0; j < 16; ++j) {
            LAS unsigned char* t = (g < 2 ? img0 : img1) + (8 * (g & 1) + q) * 512 + (((j & 7) ^ ky) + (j & 8)) * 32 + (lane & 3) * 8;
            const bf16x4 w0 = __builtin_amdgcn_ds_read_tr16_b64_v4i16((LAS bf16x4*)t);
            const bf16x4 w1 = __builtin_amdgcn_ds_read_tr16_b64_v4i16((LAS bf16x4*)(t + 2048));
            wf[j] = __builtin_shufflevector(w0, w1, 0, 1, 2, 3, 4, 5, 6, 7);
        }
        SB();
#pragma unroll
        for (int j = 0; j < 16; ++j) asm volatile("" : "+v"(wf[j]));
    }
    unsigned aa[5], pto = lane * 16;
#pragma unroll
    for (int m = 0; m < 4; ++m) aa[m] = (unsigned)(size_t)abuf + ((n >> 2) == m && ((n >> 1) & 1) == (g >> 1) ? (n & 1) * 32 + (g & 1) * 16 : zoff + g * 16);
    aa[4] = (unsigned)(size_t)abuf + ((lane >> 1) & 1) * 32 + ((lane >> 4) & 1) * 16 + ((lane >> 2) & 3) * 4 + (lane & 1) * 2;
    unsigned ix = (n & 1) ? 0xEEEEEEEEu : 0x44444444u;
    __amdgpu_buffer_rsrc_t rPt = mk_rsrc(w2 + WS_PART + __builtin_amdgcn_readfirstlane((slot >> 2) * 16384 + (lw >> 1) * 8192 + (slot & 3) * 2048 + (lw & 1) * 1024), 1024);
    asm volatile("" : "+v"(aa[0]), "+v"(aa[1]), "+v"(aa[2]), "+v"(aa[3]), "+v"(aa[4]), "+v"(pto), "+v"(ix));
    asm volatile("" : "+s"(rPt));
    f32x4 d[2] = {{0.f, 0.f, 0.f, 0.f}, {0.f, 0.f, 0.f, 0.f}};
    asm volatile("" : "+v"(d[0]), "+v"(d[1]));
    SB();
    float sv;
#pragma unroll 1
    for (int spin = 0;; ++spin) {
        sv = ((__uint_as_float(pp[0]) + __uint_as_float(pp[1])) + (__uint_as_float(pp[2]) + __uint_as_float(pp[3]))) +
             ((__uint_as_float(pp[4]) + __uint_as_float(pp[5])) + (__uint_as_float(pp[6]) + __uint_as_float(pp[7])));
        if (__builtin_expect(__builtin_amdgcn_ballot_w64(sv != sv) == 0, 1)) break;
        if (__all(max(max(max(pp[0], pp[1]), max(pp[2], pp[3])), max(max(pp[4], pp[5]), max(pp[6], pp[7]))) != SENT) || spin > SPIN_CAP) break;
        request();
    }
    unsigned ug, uu;
    swap32(__float_as_uint(sv), ug, uu);
    const float gg = rbf(__uint_as_float(ug)), up = rbf(__uint_as_float(uu));
    const unsigned act = pk_bf16(rbf(gg * __builtin_amdgcn_rcpf(1.0f + __expf(-gg))) * up, 0.f);
    *(volatile LAS unsigned short*)(size_t)aa[4] = (unsigned short)act;
    bf16x8 af[4];
#pragma unroll
    for (int m = 0; m < 4; ++m) af[m] = *(volatile LAS bf16x8*)(size_t)aa[m];
    SB();
#pragma unroll
    for (int i = 0; i < 8; ++i) {
        const int j = 8 * (i & 1) + 2 * (i >> 1);
        d[i & 1] = smf(af[i >> 1], wf[j], wf[j + 1], d[i & 1], ix);
        SB();
    }
    u32x4 pv;
#pragma unroll
    for (int q = 0; q < 4; ++q) {
        pv[q] = min(__float_as_uint(d[q >> 1][(q & 1) * 2] + d[q >> 1][(q & 1) * 2 + 1]), SENT - 1u);
        asm volatile("" : "+v"(pv[q]));
    }
    ST16_SC1(pv, rPt, pto);
}
__device__ __forceinline__ int wd_off(int lw, int i, int lane) {
    const int r = 2 * (i & 5) + (lane >> 5), ky = (r & 3) | ((r >> 1) & 4), c = lane & 31;
    return (lane >> 5) * (D_LEN * 2) + lw * 512 + (((c >> 1) ^ ky) * 2 + (c & 1)) * 16;
}

__global__ __launch_bounds__(768, 3) void fused_kernel(const Args a) {
    extern __shared__ __attribute__((aligned(16))) unsigned char smem[];
    constexpr int K = S_LEN, NT1 = D_LEN / BK;
    const int tid = threadIdx.x, wid = __builtin_amdgcn_readfirstlane(tid >> 6), lane = tid & 63;
    const int xcd = blockIdx.x & 7, idx = blockIdx.x >> 3;
    TSV;
    LAS unsigned char* lds = (LAS unsigned char*)smem;
    if (idx < 1 && wid < 4) dma_word<false>(code_rsrc(), (unsigned)(size_t)lds + L_DUMMY, tid * 128);
    SB();
    const int h = xcd & 1, xq = xcd >> 1, rt = xq * 4 + (idx & 3), ct = idx >> 2;
    volatile LAS unsigned* ctl1 = (volatile LAS unsigned*)(lds + L_CTL);
#define NBR(k) ((rt + (k)) & 3)
    unsigned kx;
    Ring ring;
    if (__builtin_expect((wid & 12) == 4, 1)) {
        __builtin_amdgcn_s_setprio(3);
        ring.template setup<D_LEN, false>(wid - 4, lane);
        ring.rP = mk_rsrc(a.x + (size_t)(rt * 4 + wid - 4) * 32 * D_LEN, 32 * D_LEN * 2);
        ring.rQ = mk_rsrc((h == 0 ? a.wre : a.wim) + (size_t)(ct * 4 + wid - 4) * 32 * D_LEN, 32 * D_LEN * 2);
        ring.lb = (wid - 4) * 4096;
        ring.sp = ring.sq = ring.poff = ring.xm = 0;
        ring.template issue<false>(lds);
        __builtin_amdgcn_s_setprio(0);
        ring.par = (ct & 4) << 11;
        ring.template ramp<CATCH1>(lds);
        ring.template steady<false, false>(NT1 - 2 - CATCH1);
        ring.template setup<K, true>(wid - 4, lane);
        ring.rQ = mk_rsrc((h == 0 ? a.cr : a.ci) + (size_t)(rt * 4 + wid - 4) * 32 * K, 32 * K * 2);
        ring.seek(rt * 256, 0);
        LAS unsigned char* l0 = lds + (wid - 4) * 4096;
        ring.issueQ(l0); ring.step();
        WAIT_VM(12); BARRIER();
        ring.issueQ(l0 + STAGE_BYTES);
        ring.seek((xq * 4 + NBR(1)) * 256, 0);
        const int xr = (wid & 1) * 8 + (lane >> 4);
        int vx0 = ((((wid >> 1) & 1) * 64 + xr) * D_LEN + (((lane & 15) ^ xr) * 8)) * 2;
        int vx1 = ((((wid >> 1) & 1) * 64 + xr + 4) * D_LEN + (((lane & 15) ^ (xr + 4)) * 8)) * 2 - 1024;
        __amdgpu_buffer_rsrc_t rX = mk_rsrc(a.x + (size_t)rt * BT * D_LEN + ct * BT, BT * D_LEN * 2 - ct * BT * 2);
        unsigned xd = (unsigned)(size_t)lds + L_YB + ((wid & 2) ? 16384 : 4096) + (wid & 1) * 2048;
        unsigned xe = (wid & 2) ? xd + 12288 : (unsigned)(size_t)lds + 3 * STAGE_BYTES + (wid & 1) * 2048;
        asm volatile("" : "+v"(vx0), "+v"(vx1));
        asm volatile("" : "+s"(rX), "+s"(xd), "+s"(xe));
        WAIT_VM(8); BARRIER();
        ring.issueQ(l0 + 2 * STAGE_BYTES);
        ring.seek((xq * 4 + NBR(2)) * 256, 0);
        if (h != 0) {
#pragma unroll
            for (int k = 0; k < 3; ++k) dma_2k<false>(rX, xd + k * 4096, vx0, vx1, k * (16 * D_LEN * 2));
        }
        BARRIER();
        if (h != 0) dma_2k<false>(rX, xe, vx0, vx1, 3 * (16 * D_LEN * 2));
        ring.issueQ(l0 + 3 * STAGE_BYTES);
        kx = (unsigned)__builtin_amdgcn_readfirstlane((int)ctl1[4]);
        SB();
    }
    char* ws = a.ws + ((0ull - (unsigned long long)a.ws) & 0x1fffffull);
    const int g = lane >> 4, n = lane & 15;
    LAS float* tab = (LAS float*)(lds + L_RED);
    if (wid >= 8) {
        const int aw = wid - 8;
#define T1(t) do { if (aw == 1 && (t) <= 16) STAMP(40 + (t)); } while (0)
        GAS u64* tagp = (GAS u64*)(ws + WS_TAG);
        const int r0 = xq * 512 + h * 256 + idx * 8 + aw * 2;
        const int w0 = xq * 256 + idx * 8 + aw * 2;
        const u32x4* src = (const u32x4*)((n < 8 ? a.x + (size_t)(r0 + (n >> 2)) * D_LEN
                                                 : (h == 0 ? a.wre : a.wim) + (size_t)(w0 + (n >> 2) - 2) * D_LEN) + g * 32 + (n & 3) * 8);
        if (AUX_SLEEP) __builtin_amdgcn_s_sleep(AUX_SLEEP);
        u64 tag0 = __hip_atomic_load(tagp, __ATOMIC_RELAXED, __HIP_MEMORY_SCOPE_AGENT);
        u32x4 v[8];
#pragma unroll
        for (int j = 0; j < 4; ++j) v[j] = src[j * 16];
        __builtin_amdgcn_s_setprio(3);
        SB();
        const char* cb = (const char*)(h ? a.ci : a.cr) + (size_t)rt * (512u << 10) + ct * 65536 + (lane >> 2) * 4096 + (lane & 3) * 128;
        const GAS u32x4* wrp = (const GAS u32x4*)a.w_r + lane;
        asm volatile("" : "+v"(cb), "+v"(wrp));
        unsigned mask = aw == 1 ? CPF_MASK1 : aw == 2 ? CPF_MASK2 : 0u;
        mask = (mask & CPF_FIX) | ((mask & ~CPF_FIX) << (idx % CPF_STAG));
        int ti = 2 * xq + aw - 1;
        auto warm = [&](int t) { if (t >= 4 && ((mask >> t) & 1)) { touch_line(cb + (ti & 7) * 512, L_DUMMY); ti += 2; } };
        f32x4 d1 = {0.f, 0.f, 0.f, 0.f}, d2 = {0.f, 0.f, 0.f, 0.f};
        u32x4 ones = {0x3F803F80u, 0x3F803F80u, 0x3F803F80u, 0x3F803F80u};
        auto sums = [&](int j0) __attribute__((always_inline)) {
#pragma unroll
            for (int j = j0; j < j0 + 4; ++j) {
                const bf16x8 f = __builtin_bit_cast(bf16x8, v[j]);
                d1 = __builtin_amdgcn_mfma_f32_16x16x32_bf16(f, __builtin_bit_cast(bf16x8, ones), d1, 0, 0, 0);
                d2 = __builtin_amdgcn_mfma_f32_16x16x32_bf16(f, f, d2, 0, 0, 0);
            }
            asm volatile("" : "+v"(d1), "+v"(d2));
        };
        unsigned wl, wh;
        int ro;
        __amdgpu_buffer_rsrc_t rS;
        u64 m1 = 0xAAAAAAAAAAAAAAAAull, m2 = 0xCCCCCCCCCCCCCCCCull, mh = 0xFFFFFFFF00000000ull;
#pragma unroll
        for (int t = 0; t <= 5; ++t) {
            SB(); BARRIER(); SB();
            T1(t);
            if (t == 2) {
#pragma unroll
                for (int j = 4; j < 8; ++j) v[j] = src[j * 16];
            }
            if (t == 3) {
                asm volatile("" : "+v"(ones));
                sums(0);
                asm volatile("" : "+v"(tag0));
                const u64 want = tag0 * 6364136223846793005ull + 1442695040888963407ull;
                wl = (unsigned)want; wh = (unsigned)(want >> 32);
                asm volatile("" : "+v"(wl), "+v"(wh));
            }
            if (t == 4) { sums(4); asm volatile("s_nop 7\n\ts_nop 3" : "+v"(d2)); }
            if (t == 1) {
                rS = mk_rsrc(ws + WS_XRD, 65536);
                ro = n == g * 4 ? (g < 2 ? r0 + g : 2048 - 2 + h * 1024 + w0 + g) * 16 : 65536;
                asm volatile("" : "+v"(ro));
                asm volatile("" : "+s"(rS), "+s"(m1), "+s"(m2), "+s"(mh), "+s"(mask), "+s"(ti));
            }
        }
        float sm = (d1[0] + d1[1]) + (d1[2] + d1[3]);
        float sq = pick(m2, pick(m1, d2[0], d2[1]), pick(m1, d2[2], d2[3]));
        asm volatile("s_nop 1" : "+v"(sq));
        sq += dppf<0xB1>(sq);
        sq += dppf<0x4E>(sq);
        float mean = sm * (1.0f / D_LEN), var = rbf(rbf(fmaxf(sq * (1.0f / D_LEN) - mean * mean, 0.f)) + 1e-5f);
        asm volatile("" : "+v"(sm), "+v"(mean), "+v"(var));
        SB(); BARRIER(); SB();
        T1(6);
        {
            float rden = __builtin_amdgcn_rcpf(rbf(__builtin_amdgcn_sqrtf(var)));
            asm volatile("s_nop 1" : "+v"(rden));
            const u32x4 rec = {__float_as_uint(pick(mh, rden, sm)), wl, __float_as_uint(rbf(mean) * rden), wh};
            ST16_SC1(rec, rS, ro);
        }
        const __amdgpu_buffer_rsrc_t rV = mk_rsrc(ws + WS_VOTE, 512);
        if (aw == 3 && idx < 8) __builtin_amdgcn_raw_buffer_store_b64((u32x2){ctl1[5], wl}, rV, blockIdx.x * 8 + lane * 512, 0, 16);
        SB(); BARRIER(); SB();
        T1(STAT_M + 2);
        warm(STAT_M + 2);
        if (aw == 1 || aw == 2) {
#pragma unroll 1
            for (int t = STAT_M + 3; t <= NT1 + AUX_STAY; ++t) {
                BARRIER();
                T1(t);
                warm(t);
            }
            FLUSH();
            return;
        }
        if (aw == 3) {
            if (h == 0) {
                const u32x4 w0v = wrp[0], w1v = wrp[64];
                BARRIER(); BARRIER(); BARRIER(); BARRIER();
                SB();
                float t = 0.f;
#pragma unroll
                for (int i = 0; i < 4; ++i) { t = dot2bf(w0v[i], 0x3F803F80u, t); t = dot2bf(w1v[i], 0x3F803F80u, t); }
                SB(); BARRIER(); SB();
                t = wave_sum(t);
                ctl1[1] = __float_as_uint(t);
                WAIT_LGKM0();
            }
            return;
        }
#pragma unroll
        for (int t = STAT_M + 3; t <= POLL_T; ++t) BARRIER();
        SB();
        const int o0 = (rt * BT + lane) * 16, o1 = (2048 + (h * 8 + ct) * BT + lane) * 16, os = 1024;
        u32x4 q0, q1, q2, q3;
        u32x2 qv;
        auto request = [&]() {
            asm volatile("" ::: "memory");
            q0 = LD16_SC1(rS, o0); q1 = LD16_SC1(rS, o0 + os); q2 = LD16_SC1(rS, o1); q3 = LD16_SC1(rS, o1 + os);
            qv = __builtin_amdgcn_raw_buffer_load_b64(rV, lane * 8, 0, 16);
            SB();
        };
        request();
        int b = POLL_T + 1;
        for (int spin = 0; spin <= SPIN_CAP; ++spin) {
            if (b < NT1) { BARRIER(); ++b; }
            if (b < NT1) { BARRIER(); ++b; }
            SB();
            unsigned d = ((q0[1] ^ wl) | (q1[1] ^ wl)) | ((q2[1] ^ wl) | (q3[1] ^ wl)) | ((q0[3] ^ wh) | (q1[3] ^ wh)) | ((q2[3] ^ wh) | (q3[3] ^ wh)) | (qv[1] ^ wl);
            asm volatile("" : "+v"(d));
            if (__all(d == 0u)) break;
            request();
        }
        if (b < NT1) BARRIER();
        SB();
        LAS unsigned* tb = (LAS unsigned*)tab;
        const unsigned sg = h == 0 ? 0u : 0x80000000u;
        tb[lane] = q0[0] ^ sg; tb[64 + lane] = q1[0] ^ sg; tb[128 + lane] = q0[2] ^ sg; tb[192 + lane] = q1[2] ^ sg;
        tb[256 + lane] = q2[0]; tb[320 + lane] = q3[0];
        const unsigned vt = qv[0];
        const unsigned kx = __popcll(__ballot(vt == 2u)) > __popcll(__ballot(vt == 1u)) ? 8192u : 0u;
        if (lane == 0) { ctl1[0] = wl | 2u; ctl1[2] = wl; ctl1[3] = wh; ctl1[4] = kx; }
        *(LAS u32x2*)(lds + L_YB + 2560 + 256 + lane * 8) = (u32x2){0u, 0u};
        WAIT_LGKM0();
        return;
    }
#define CTL(i) ((unsigned)__builtin_amdgcn_readfirstlane((int)ctl[i]))
    const int wp = wid & 1, wq = (wid >> 1) & 1;
#define Y_OFF ((int)WS_Y + ((rt & 7) * 8 + ct) * 65536 + (wid & 3) * 16384 + (((rt >> 3) ^ (rt >> 2)) & 1) * 8192)
#define P2_DEFS \
    char* w2 = ws + kx; \
    const int sidx = idx < 28 ? xq * 28 + idx : 112 + xq * 4 + idx - 28;     \
    volatile LAS unsigned* ctl = (volatile LAS unsigned*)(lds + (h == 0 ? L_ATAB : L_CTL)); \
    const int NT = h == 0 ? 32 + KSHIFT : 32 - KSHIFT; \
    LAS float* rstat = (LAS float*)(lds + L_ATAB + 256);     \
    const int hp_ = rt * 2 + wq;                             \
    (void)sidx; (void)ctl; (void)NT; (void)rstat; (void)hp_; (void)w2
    if (__builtin_expect(wid < 4, 1)) {
        {
            const u32x4 sent4 = {SENT, SENT, SENT, SENT}, zero4 = {0u, 0u, 0u, 0u};
            const __amdgpu_buffer_rsrc_t rI = mk_rsrc(ws + WS_STATS + (size_t)blockIdx.x * (FILL_BYTES / 256), FILL_BYTES / 256);
            const __amdgpu_buffer_rsrc_t rZ = mk_rsrc(ws + (size_t)blockIdx.x * 160, 160);
            ST16_SC1(sent4, rI, tid * 16);
            if (wid < 2) ST16_SC1(sent4, rI, 4096 + tid * 16);
            if (wid == 2) ST16_SC1(zero4, rZ, lane * 16);
        }
        if (wid == 3 && idx < 8) {
            const unsigned r = (((unsigned)__builtin_amdgcn_s_memrealtime() * 2654435761u + blockIdx.x * 40503u) >> 9) & 0x1fdf80u;
            const unsigned first = (idx & 1) << 13;
            const char* pb = ws + WS_PROBE;
            unsigned va, vb, left;
            asm volatile("s_waitcnt lgkmcnt(0)\n\ts_load_dword %0, %3, %4\n\ts_load_dword %1, %3, %5\n\ts_waitcnt lgkmcnt(1)\n\t"
                         "s_nop 15\n\ts_getreg_b32 %2, hwreg(HW_REG_IB_STS, 8, 4)\n\ts_waitcnt lgkmcnt(0)"
                         : "=&s"(va), "=&s"(vb), "=&s"(left) : "s"(pb), "s"(r | first), "s"(r | (first ^ 8192u)) : "memory");
            const unsigned nearbit = (left != 0u ? first : first ^ 8192u) >> 13;
            if (lane == 0) ctl1[5] = nearbit == (unsigned)(xcd >> 2) ? 1u : 2u;
        }
        f32x4 acc[4][4];
#pragma unroll
        for (int i = 0; i < 16; ++i) acc[i >> 2][i & 3] = (f32x4){0.f, 0.f, 0.f, 0.f};
        unsigned tc = (unsigned)(size_t)(tab + wp * 64 + g * 8), tw = (unsigned)(size_t)(tab + 256 + prow(wq * 64 + n));
        unsigned ta = (unsigned)(size_t)lds + wp * STAGE_BYTES + wq * 8192 + lane * 16;
        constexpr int NIL = 1 << 30;
        __amdgpu_buffer_rsrc_t rW = mk_rsrc(ws, 18u << 20);
        const int hpan = rt * 2 + wq;
        int sk = (int)WS_AT + at_panel(h * 8 + ct) + at_tile((rt * 2 + wp) * 128, (ct & 4) << 11) + wq * 16384 + lane * 16;
        int st = lane != 0 ? NIL : (int)WS_FLAG1 + (1 - wp) * 32768 + h * 16384 + (rt >> 3) * 8192 + ct * 1024 + (rt & 7) * 128 + wq * 4;
        int so = h != 0 ? Y_OFF + lane * 16 : g != (n >> 2) ? NIL
               : (int)WS_STATS + (hpan & 15) * 32768 + (ct >> 2) * 16384 + (hpan >> 4) * 8192 + ((ct & 3) * 2 + wp) * 1024 + n * 16;
        int sf = lane != 0 ? NIL : (int)WS_FLAGS + (rt * 8 + ct) * 128 + wid * 4;
        u32x4 sel[2], aw[2];
        unsigned one[2];
#pragma unroll
        for (int i = 0; i < 4; ++i) {
            one[i & 1] = g == (n >> 2) && (i & 1) == ((n >> 1) & 1) ? ((n & 1) ? 0x3F800000u : 0x00003F80u) : 0u;
            aw[0][i] = aw[1][i] = (n & 3) == 0 ? 0x3F803F80u : 0u;
        }
        if ((n & 3) == 1) {
            const bf16_t* wr = a.w_r + ct * BT + wp * 64 + g * 8;
            aw[0] = *(const u32x4*)wr; aw[1] = *(const u32x4*)(wr + 32);
        }
        unsigned ea[2];
#pragma unroll
        for (int mp = 0; mp < 2; ++mp)
            ea[mp] = (unsigned)(size_t)lds + L_YB + (wq ? 16384 : 4096) + n * 256 + (((wp * 8 + mp * 4 + g) ^ n) * 16);
        asm volatile("" : "+v"(tc), "+v"(tw), "+v"(ta), "+v"(sk), "+v"(st), "+v"(so), "+v"(sf), "+v"(one[0]), "+v"(one[1]), "+v"(ea[0]), "+v"(ea[1]));
        asm volatile("" : "+s"(rW));
        unsigned tg;
        bf16x8 e0[4], e1[4];
        auto extra = [&]() __attribute__((always_inline)) {
            bf16x8 ef[4][2];
#pragma unroll
            for (int i = 0; i < 8; ++i)
                ef[i >> 1][i & 1] = *(const LAS bf16x8*)((const LAS unsigned char*)(size_t)(ea[i & 1] + (i < 6 || wq ? 0 : 3 * STAGE_BYTES - L_YB - 16384)) + (i >> 1) * 4096);
            SB();
#pragma unroll
            for (int i = 0; i < 16; ++i)
                acc[i & 3][i >> 2] = __builtin_amdgcn_mfma_f32_16x16x32_bf16(__builtin_bit_cast(bf16x8, sel[i & 1]), ef[i >> 2][(i >> 1) & 1], acc[i & 3][i >> 2], 0, 0, 0);
        };
        if (wid == 0) STAMP(0);
        compute_loop<false, true>(lds, acc, NT1, wp, wq, lane, [&]() { if (wid == 0) STAMP(24); WAIT_VM0(); if (wid == 0) STAMP(31); }, e0, e1, 0u);
        if (wid == 0) STAMP(25);
        {
            f32x4 rd[4], mr[4];
            float wsm[4];
#pragma unroll
            for (int m = 0; m < 4; ++m) {
                rd[m] = *(const LAS f32x4*)((const LAS float*)(size_t)tc + (m >> 1) * 32 + (m & 1) * 4);
                mr[m] = *(const LAS f32x4*)((const LAS float*)(size_t)tc + 128 + (m >> 1) * 32 + (m & 1) * 4);
                wsm[m] = *((const LAS float*)(size_t)tw + (m >> 1) * 32 + (m & 1) * 4);
            }
            const unsigned kv = ctl1[4];
            tg = ctl1[0];
            SB();
            sk += (int)kv; st += (int)kv; so += (int)kv; sf += (int)kv;
            kx = (unsigned)__builtin_amdgcn_readfirstlane((int)kv);
#pragma unroll
            for (int nn = 0; nn < 4; ++nn)
#pragma unroll
                for (int mp = 0; mp < 2; ++mp) {
                    const f32x4 u = acc[2 * mp][nn] * rd[2 * mp] - mr[2 * mp] * wsm[nn];
                    const f32x4 v = acc[2 * mp + 1][nn] * rd[2 * mp + 1] - mr[2 * mp + 1] * wsm[nn];
                    u32x4 w;
                    w[0] = pk_bf16(u[0], u[1]); w[1] = pk_bf16(u[2], u[3]);
                    w[2] = pk_bf16(v[0], v[1]); w[3] = pk_bf16(v[2], v[3]);
                    ST16_SC1(w, rW, sk + (nn * 2 + mp) * 1024);
                    *(LAS u32x4*)((LAS unsigned char*)(size_t)ta + (nn * 2 + mp) * 1024) = w;
                }
        }
        if (wid == 0) STAMP(26);
        WAIT_LGKM0();
        BARRIER();
        SB();
        {
#pragma unroll
            for (int i = 0; i < 16; ++i) { acc[i >> 2][i & 3] = (f32x4){0.f, 0.f, 0.f, 0.f}; asm volatile("" : "+v"(acc[i >> 2][i & 3])); }
            sel[0] = (u32x4){one[0], one[1], 0u, 0u}; sel[1] = (u32x4){0u, 0u, one[0], one[1]};
            asm volatile("" : "+v"(st), "+v"(so), "+v"(sf), "+v"(tg), "+v"(sel[0]), "+v"(sel[1]));
            SB();
            if (h != 0) extra();
            SB();
            WAIT_VM(0);
            __builtin_amdgcn_raw_buffer_store_b32(tg, rW, st, 0, 16);
            SB();
            if (wid == 0) STAMP(27);
        }
        compute_loop<true, false>(lds, acc, h == 0 ? 32 + KSHIFT : 32 - KSHIFT, wp, wq, lane, [&]() {
            if (wid == 0) { STAMP(h == 0 ? 1 : 19); CSTAMP(22); }
        }, e0, e1, (unsigned)((wp * 8192 + 2 * STAGE_BYTES) ^ (L_YB + wid * 8192)));
        P2_DEFS;
        if (wid == 0) { STAMP(h == 0 ? 2 : 12); CSTAMP(23); }
        u32x4 hq[4][2];
        auto cvt = [&](int nn, int j) __attribute__((always_inline)) {
            const f32x4 c = acc[j][nn];
            hq[nn][j >> 1][(j & 1) * 2] = pk_bf16(c[0], c[1]);
            hq[nn][j >> 1][(j & 1) * 2 + 1] = pk_bf16(c[2], c[3]);
        };
        int hs = h;
        asm volatile("" : "+s"(hs));
        if (__builtin_expect(hs == 0, 1)) {
            f32x4 d1[4], d2[4];
            u32x3 sv;
            auto add = [&](int nn, int j) __attribute__((always_inline)) {
                acc[j][nn] = __builtin_amdgcn_mfma_f32_16x16x32_bf16(__builtin_bit_cast(bf16x8, sel[j & 1]), j < 2 ? e0[nn] : e1[nn], acc[j][nn], 0, 0, 0);
            };
            auto sum = [&](int nn, int j) __attribute__((always_inline)) {
                const bf16x8 hf = __builtin_bit_cast(bf16x8, hq[nn][j >> 1]);
                const f32x4 z = {0.f, 0.f, 0.f, 0.f};
                if (j & 1) d1[nn] = __builtin_amdgcn_mfma_f32_16x16x32_bf16(__builtin_bit_cast(bf16x8, aw[j >> 1]), hf, j < 2 ? z : d1[nn], 0, 0, 0);
                else d2[nn] = __builtin_amdgcn_mfma_f32_16x16x32_bf16(hf, hf, j < 2 ? z : d2[nn], 0, 0, 0);
            };
            auto out = [&](int nn, int j) __attribute__((always_inline)) {
                if (j == 1) asm volatile("" : "+v"(d2[nn]));
                if (j == 2) asm volatile("" : "+v"(d1[nn]));
                if (j == 1) sv[1] = min(__float_as_uint(sel4(n, d2[nn][0], d2[nn][1], d2[nn][2], d2[nn][3])), SENT - 1u);
                if (j == 2) { sv[0] = min(__float_as_uint(d1[nn][0]), SENT - 1u); sv[2] = min(__float_as_uint(d1[nn][1]), SENT - 1u); }
                if (j == 3) __builtin_amdgcn_raw_buffer_store_b96(sv, rW, so + nn * 256, 0, 16);
            };
#define FOUR(m, v) _Pragma("unroll") for (int j = 0; j < 4; ++j) { m; SB(); v; SB(); }
            FOUR(add(0, j), (void)0) FOUR(add(1, j), cvt(0, j)) FOUR(sum(0, j), cvt(1, j)) FOUR(add(2, j), out(0, j))
            FOUR(sum(1, j), cvt(2, j)) FOUR(add(3, j), out(1, j)) FOUR(sum(2, j), cvt(3, j)) FOUR(sum(3, j), out(2, j))
            FOUR((void)0, out(3, j))
#undef FOUR
            if (wid == 0) STAMP(3);
#pragma unroll
            for (int nn = 0; nn < 4; ++nn)
#pragma unroll
                for (int mp = 0; mp < 2; ++mp)
                    *(LAS u32x4*)(lds + L_HT + (wq * 64 + nn * 16 + n) * 256 + (((wp * 8 + mp * 4 + g) ^ n) * 16)) = hq[nn][mp];
            WAIT_LGKM0();
            BARRIER();
            BARRIER();
            if (wid == 0) STAMP(6);
            const int crow = __builtin_amdgcn_readfirstlane((int)((volatile LAS unsigned*)(lds + L_ATAB))[2 + wq]) + wq * 64;
            const __amdgpu_buffer_rsrc_t rOut = mk_rsrc(a.out + (size_t)rt * BT * D_LEN, BT * D_LEN * 2);
#pragma unroll 2
            for (int j = 0; j < 8; ++j) {
                const int r = wq * 64 + wp * 32 + j * 4 + g;
                const float m_ = rstat[r * 2], d_ = rstat[r * 2 + 1];
                const u32x4 hh = *(const LAS u32x4*)(lds + L_HT + r * 256 + ((n ^ (r & 15)) * 16));
                u32x4 ov;
#pragma unroll
                for (int i = 0; i < 4; ++i) {
                    const unsigned u = hh[i];
                    const float h0 = bflo(u), h1 = bfhi(u);
                    const unsigned dd = pk_bf16(h0 - m_, h1 - m_);
                    const unsigned q = pk_bf16(bflo(dd) * d_, bfhi(dd) * d_);
                    ov[i] = pk_bf16(h0 + bflo(q), h1 + bfhi(q));
                }
                __builtin_amdgcn_raw_buffer_store_b128(ov, rOut, r == crow ? BT * D_LEN * 2 : (r * D_LEN + ct * BT + n * 8) * 2, 0, 0);
            }
            FLUSH();
            return;
        }
#pragma unroll
        for (int i = 0; i < 8; ++i) {
            cvt(i >> 1, (i & 1) * 2); cvt(i >> 1, (i & 1) * 2 + 1);
            __builtin_amdgcn_raw_buffer_store_b128(hq[i >> 1][i & 1], rW, so, i * 1024, 16);
            SB();
        }
        WAIT_VM0();
        __builtin_amdgcn_raw_buffer_store_b32(1u, rW, sf, 0, 16);
        if (wid == 0) STAMP(13);
    } else {
        LAS unsigned char* l0 = lds + (wid - 4) * 4096;
        P2_DEFS;
        __amdgpu_buffer_rsrc_t rD = mk_rsrc(h == 0 ? a.wd + (size_t)sidx * 32 * D_LEN : a.wg,
                                                  sidx >= 112 ? 0 : h != 0 ? D_LEN * F_LEN * 2 : wid < 8 - BQ ? 32 * D_LEN * 2 : 0);
        int vGe = h == 0 ? wd_off(wid & 3, 0, lane)
                : (((sidx & 7) * 128 + (wid & 3) * 16 + (lane >> 5)) * F_LEN + (sidx >> 3) * 256 + ((((lane & 31) >> 1) ^ (lane >> 5)) * 16) + (lane & 1) * 8) * 2;
        auto load_wd = [&](int i) __attribute__((always_inline)) {
            dma_nt(rD, (unsigned)(size_t)lds + (i < 8 ? L_WD0 : L_WD1) + (wid & 3) * 8192 + (i & 7) * 1024, vGe ^ ((2 * (i & 1) | (i & 4)) * 32), i * (4 * D_LEN));
        };
        const int lw = wid - 4;
        constexpr int NIL = 1 << 30;
        ring.rP = ring.rF = mk_rsrc(w2, (unsigned)WS_STATS);
        ring.voP[0] += (int)WS_AT + at_panel(h * 8 + ct);
        ring.voP[1] = ring.voP[2] = ring.voP[3] = ring.voP[0];
#define FLINE(k) (((k) >> 3) * 8192 + ((k) & 7) * 128)
        const int ft = (g & 1) * 32768 + (g >> 1) * 4 + (int)WS_FLAG1 + (lw == 0 ? ct * 1024 : NIL);
        int vall = FLINE(n) + ft + h * 16384;
        ring.voF = (n >> 2) == xq ? vall : NIL;
        ring.fl = lds + L_YB + (h == 0 ? lw * 8192 : lw ? 768 : 0) + 2560 + 256;
        unsigned sa = (unsigned)(size_t)lds + L_YB + 2560 + 256 + lane * 4;
        const unsigned tg = (unsigned)__builtin_amdgcn_readfirstlane((int)ctl1[0]);
        float wsum = __uint_as_float(ctl1[1]);
        ring.lb = lw * 4096;
        int cq[3], cp[3];
        unsigned mk[3];
#pragma unroll
        for (int k = 0; k < 3; ++k) {
            const int nb = xq * 4 + NBR(k + 1);
            cq[k] = nb * 256; cp[k] = at_tile(cq[k], ring.par); mk[k] = 0x10000u << nb;
            asm volatile("" : "+s"(cq[k]), "+s"(cp[k]), "+s"(mk[k]));
        }
        unsigned m2 = (mk[0] | mk[1] | mk[2]) >> 16;
        asm volatile("" : "+s"(m2), "+s"(ring.rP));
        asm volatile("" : "+v"(vall), "+v"(ring.voF), "+v"(ring.voP[0]), "+v"(sa), "+v"(wsum));
        ring.rF = ring.rP;
        ring.voP[1] = ring.voP[2] = ring.voP[3] = ring.voP[0];
        WAIT_VM(4);
        BARRIER();
        SB();
        int ve = Y_OFF + lane * 16;
        unsigned yd = (unsigned)(size_t)lds + L_YB + lw * 8192, fa = 0u;
        auto fetch = [&]() __attribute__((always_inline)) {
            if (wid == 4) STAMP(21);
            const int v4[4] = {ve, ve, ve, ve};
#pragma unroll
            for (int k = 0; k < 2; ++k) dma_4k<true>(ring.rP, yd + k * 4096, v4, k * 4096);
        };
        __amdgpu_buffer_rsrc_t rQ2 = mk_rsrc(a.ci + (size_t)(rt * 4 + lw) * 32 * K, 32 * K * 2);
        int vC2 = ((ct & 1) * 16 + (lane >> 2)) * (K * 2) + ((xq * 1024 + 2048 + (ct >> 1) * 512 + (lane & 3) * 128) & 4095);
        int vT2 = h == 0 && n < (KSHIFT + 1) / 2 ? FLINE((xq * 4 - 1 - n) & 15) + ft + 16384 : NIL;
        int vY = lane == 0 ? (int)WS_FLAGS + (rt * 8 + ct) * 128 + lw * 4 : NIL;
        int n1 = (h == 0 ? 32 : NT) - 9, n0 = n1 - 1 - (C2_START + 2 * ct + (idx & 1));
        int xm = (idx & 1) * 128;
        int c8 = (xq * 1024 + 1024) & 4095, p8 = at_tile(c8 ^ xm, ring.par);
        int cK = (xq * 1024 + (32 - KSHIFT) * 128) & 4095, pK = at_tile(cK ^ xm, ring.par) + at_panel(8);
        auto early = [&](int st, int i0, int i1) __attribute__((always_inline)) {
#pragma unroll
            for (int i = i0; i < i1; ++i)
                dma_nt(rD, (unsigned)(size_t)lds + (st * 4 + lw) * 8192 + i * 1024, vGe ^ ((2 * (i & 1) | (i & 4)) * 32), i * (4 * F_LEN) + st * (128 * F_LEN));
        };
        asm volatile("" : "+v"(vGe));
        asm volatile("" : "+s"(rQ2), "+s"(n0), "+s"(n1), "+s"(c8), "+s"(p8), "+s"(cK), "+s"(pK), "+s"(xm), "+s"(rD));
        asm volatile("" : "+v"(ve), "+v"(vC2), "+v"(vT2), "+v"(vY));
        unsigned have = 0u;
        SB();
#define GATE(m) do { if (__builtin_expect(((m) & ~have) != 0u, 0)) ring.gate(tg, sa, m, have); SB(); } while (0)
#define TILE(k, o, st) do { ring.sp = cp[k] + (o) * 256; ring.sq = cq[k] + (o); ring.template issueP<16>(l0 + (st) * STAGE_BYTES); \
                            ring.issueQ(l0 + (st) * STAGE_BYTES); ring.sample(); SB(); } while (0)
        ring.gate(tg, sa, mk[0], have);
        if (wid == 4) STAMP(28);
        ring.sp = cp[0]; ring.template issueP<16>(l0 + 2 * STAGE_BYTES); ring.sample();
        BARRIER();
        GATE(mk[1]);
        ring.sp = cp[1]; ring.template issueP<16>(l0 + 3 * STAGE_BYTES); ring.sample();
        GATE(mk[2]);
        TILE(2, 0, 0);
        WAIT_VM(15); BARRIER();
        if (wid == 4) STAMP(30);
        GATE(m2);
        ring.voF = vall;
        TILE(0, 128, 1);
        WAIT_VM(19); BARRIER();
        TILE(1, 128, 2);
        WAIT_VM(19); BARRIER();
        ring.sp = cp[2] + 128 * 256; ring.template issueP<16>(l0 + 3 * STAGE_BYTES);
        ring.sq = cq[2] + 128;
        SB();
        {
            const unsigned long long m = __ballot(ring.peek(sa) == tg);
            have = (unsigned)m & (unsigned)(m >> 32);
            if (__builtin_expect(have != 0xffffffffu, 0)) ring.gate(tg, sa, 0xffffffffu, have);
        }
        ring.fl -= 256;
        ring.voF = vT2;
        SB();
        ring.issueQ(l0 + 3 * STAGE_BYTES);
#undef GATE
#undef TILE
        ring.sq = c8; ring.sp = p8; ring.poff = 0; ring.xm = xm;
        if (wid == 4) { STAMP(29); CSTAMP(34); STAMP(36); }
        ring.template steady<true, false, 18>(n1, n0, vC2);
        ring.fl += 512;
        if (wid == 4) { CSTAMP(35); STAMP(37); }
        ring.template issueP<16>(lds + ring.lb);
        SB();
        {
            const unsigned long long m = __ballot(ring.peek(sa + 256) == tg);
            have = (unsigned)m & (unsigned)(m >> 32);
            asm volatile("" : "+s"(have));
        }
        int hs = h;
        asm volatile("" : "+s"(hs));
        if (__builtin_expect(hs == 0, 1)) {
            constexpr unsigned MK = ((1u << ((KSHIFT + 1) / 2)) - 1u) * 0x10001u;
            if (__builtin_expect((MK & ~have) != 0u, 0)) ring.gate(tg, sa + 256, MK, have);
            unsigned lq = ring.lb + TILE_BYTES, pp = ring.par | at_panel(8);
            int sq = ring.sq ^ ring.xm, cnt = KSHIFT;
            ring.lb = (ring.lb + STAGE_BYTES) & (RING_BYTES - 1);
            ring.fl -= 512;
            fa = (unsigned)(size_t)ring.fl;
            asm volatile("" : "+s"(lq), "+s"(sq), "+s"(cnt), "+s"(ring.lb), "+s"(pp), "+s"(yd));
            asm volatile("" : "+v"(fa));
            SB();
            dma_4k<false>(ring.rQ, lq, ring.voQ, sq);
            ring.voF = vY;
            ring.rQ = rQ2;
            ring.sq = cK; ring.sp = pK; ring.poff = 0; ring.par = pp;
            ring.template steady<true, true>(cnt);
            auto seen = [&]() __attribute__((always_inline)) { return __builtin_amdgcn_readfirstlane(ring.peek(fa)) == 1u; };
            if (__builtin_expect(seen(), 1)) {
                fetch();
                WAIT_VM(16); BARRIER();
                WAIT_VM(8); BARRIER();
            } else {
                ring.sample();
                WAIT_VM(10); BARRIER();
                const bool have = seen();
                if (have) { fetch(); WAIT_VM(9); } else { ring.sample(); WAIT_VM(2); }
                BARRIER();
                if (!have) {
#pragma unroll 1
                    for (int spin = 0; spin <= SPIN_CAP && !seen(); ++spin) { ring.sample(); WAIT_VM(0); }
                    fetch();
                }
            }
            if (wid == 4) STAMP(20);
            WAIT_VM0();
        } else {
            ring.issueQ(lds + ring.lb);
            WAIT_VM(16); BARRIER();
            early(0, 0, G_N);
            wait_vm<8 + G_N>(); BARRIER();
            early(1, 0, G2_N);
            wait_vm<G_N + G2_N>(); BARRIER();
            early(1, G2_N, G2_N + G3_N);
        }
        BARRIER();
        if (h == 0) {
            const int slot = sidx;
            LAS unsigned char* abuf = lds + L_ATAB + 2048 + lw * 128;
#pragma unroll
            for (int i = 0; i < WD_EARLY; ++i) load_wd(i);
            SB();
            __amdgpu_buffer_rsrc_t rS = mk_rsrc(w2 + WS_STATS + (hp_ & 15) * 32768 + (hp_ >> 4) * 8192, 24576);
            __amdgpu_buffer_rsrc_t rC = mk_rsrc(w2 + WS_CAND + (size_t)hp_ * 4096, 4096);
            __amdgpu_buffer_rsrc_t rKy = mk_rsrc(w2 + WS_KEYS + (hp_ >> 2) * KEY_S + (hp_ & 3) * 4, ct == 0 ? 8192 + 3 * KEY_S * 16 + 4 : 0);
            int vs = lane * 16, vk = !(lane & 1) ? 1 << 20 : (lane >> 2) * 8192 + (lane & 3) * (KEY_S * 16);
            int vc = (lane < 32 ? 2048 : 0) + ct * 256 + (lane & 31) * 8;
            unsigned code = 0xffffu - (unsigned)(hp_ * 64 + lane), hb = (lane & 31) * 8;
            unsigned ra = (unsigned)(size_t)(rstat + (wq * 64 + lane) * 2), ht = (unsigned)(size_t)lds + L_HT + wq * 16384;
            u64 lo = 0xFFFFFFFFull;
            asm volatile("" : "+s"(rS), "+s"(rC), "+s"(rKy), "+s"(ht), "+s"(lo));
            asm volatile("" : "+v"(vs), "+v"(vk), "+v"(vc), "+v"(code), "+v"(hb), "+v"(ra));
            SB();
            BARRIER();
            __builtin_amdgcn_s_setprio(TAIL_PRIO);
            if (lane < 16) *(LAS unsigned*)(abuf + 64 + lane * 4) = 0u;
            __builtin_amdgcn_s_sleep(STAT_DELAY);
            if (wp == 0) {
                float s1, s2, s3;
#pragma unroll 1
                for (int spin = 0;; ++spin) {
                    asm volatile("" ::: "memory");
                    u32x3 sv[16];
#pragma unroll
                    for (int j = 0; j < 16; ++j) { sv[j] = __builtin_amdgcn_raw_buffer_load_b96(rS, vs, (j >> 3) * 16384 + (j & 7) * 1024, 16); SB(); }
#pragma unroll
                    for (int j = 0; j < 16; ++j) {
                        const unsigned u0 = sv[j][0], u1 = sv[j][1], u2 = sv[j][2];
                        s1 = j ? s1 + __uint_as_float(u0) : __uint_as_float(u0);
                        s2 = j ? s2 + __uint_as_float(u1) : __uint_as_float(u1);
                        s3 = j ? s3 + __uint_as_float(u2) : __uint_as_float(u2);
                    }
                    float z = s1 + s3;
                    asm volatile("" : "+v"(z));
                    if (__builtin_expect(__builtin_amdgcn_ballot_w64(__builtin_isunordered(z, s2)) == 0, 1)) break;
                    unsigned mx = 0u;
#pragma unroll
                    for (int j = 0; j < 16; ++j) mx = max(max(mx, sv[j][0]), max(sv[j][1], sv[j][2]));
                    if (__all(mx != SENT) || spin > SPIN_CAP) break;
                }
                if (wid == 4) STAMP(4);
                const float mean = s1 * (1.0f / D_LEN);
                const float var = fmaxf(s2 * (1.0f / D_LEN) - mean * mean, 0.f);
                const float mb = rbf(mean), rden = __builtin_amdgcn_rcpf(rbf(__builtin_amdgcn_sqrtf(rbf(rbf(var) + 1e-5f))));
                const unsigned kb = wave_max_s(r2key((s3 - mb * wsum) * rden, code));
                __builtin_amdgcn_raw_buffer_store_b32(kb, rKy, vk, 0, 16);
                SB();
                {
                    const unsigned cr_ = ~kb & 63u;
                    u32x2 c = *(const LAS u32x2*)(size_t)(ht + cr_ * 256 + (hb ^ ((cr_ & 15) * 16)));
                    const float m_ = __int_as_float(__builtin_amdgcn_readlane(__float_as_int(mb), cr_));
                    const float d_ = __int_as_float(__builtin_amdgcn_readlane(__float_as_int(rden), cr_));
#pragma unroll
                    for (int i = 0; i < 2; ++i) {
                        const unsigned u = c[i];
                        const unsigned dd = pk_bf16(bflo(u) - m_, bfhi(u) - m_);
                        const unsigned q = pk_bf16(bflo(dd) * d_, bfhi(dd) * d_);
                        c[i] = min(__float_as_uint(pick(lo, __uint_as_float(u), __uint_as_float(q))), SENT - 1u);
                    }
                    __builtin_amdgcn_raw_buffer_store_b64(c, rC, vc, 0, 16);
                    SB();
                    *(LAS u32x2*)(size_t)ra = (u32x2){__float_as_uint(mb), __float_as_uint(rden)};
                    if (lane == 0) ctl[2 + wq] = cr_;
                }
                WAIT_LGKM0();
                if (wid == 4) STAMP(5);
            }
            BARRIER();
#pragma unroll
            for (int i = WD_EARLY; i < 16; ++i) load_wd(i);
            if (slot < 112 && wid < 8 - BQ) {
                WAIT_VM0();
                if (wid == 4) STAMP(7);
                stage2(w2, slot, lw, lds + L_WD0 + lw * 8192, lds + L_WD1 + lw * 8192, abuf, 64, lane);
                if (wid == 4) STAMP(11);
            }
            FLUSH();
            return;
        }
    }

    P2_DEFS;
    LAS float* red = (LAS float*)(lds + L_RED);
    if (sidx < 112) {
        const int slot = sidx, grp = slot >> 3, kq = slot & 7;
        const __amdgpu_buffer_rsrc_t rG = mk_rsrc(a.wg, D_LEN * F_LEN * 2), rU = mk_rsrc(a.wu, D_LEN * F_LEN * 2);
        auto pair = [&](int i) {
            const int d = wid * 16 + i * 2 + (lane >> 5), ky = (d & 3) | ((d >> 1) & 4), c = lane & 31;
            const int vgu = ((kq * 128 + d) * F_LEN + grp * 256 + (((c >> 1) ^ ky) * 16) + (c & 1) * 8) * 2;
            if (i >= (wid < 4 ? G_N : G2_N + G3_N)) dma_nt(rG, (unsigned)(size_t)lds + wid * 8192 + i * 1024, vgu, 0);
            dma_nt(rU, (unsigned)(size_t)lds + 65536 + wid * 8192 + i * 1024, vgu ^ 512, 0);
        };
        if (wid >= 4) {
            if (W_PRE) __builtin_amdgcn_s_sleep(W_PRE_SL);
#pragma unroll
            for (int i = 0; i < W_PRE; ++i) pair(i);
        }
        BARRIER();
        if (wid < 4) {
#pragma unroll
            for (int i = 0; i < W_PRE; ++i) pair(i);
        }
#pragma unroll
        for (int i = W_PRE; i < 8; ++i) pair(i);
        if (wid >= 8 - BQ) {
            const __amdgpu_buffer_rsrc_t rD = mk_rsrc(a.wd + (size_t)slot * 32 * D_LEN, 32 * D_LEN * 2);
            const int vd = wd_off(wid - 4, 0, lane);
#pragma unroll
            for (int i = 0; i < 16; ++i)
                dma_nt(rD, (unsigned)(size_t)lds + L_YB + (wid & 1) * 16384 + i * 1024, vd ^ ((2 * (i & 1) | (i & 4)) * 32), i * (4 * D_LEN));
        }
        const __amdgpu_buffer_rsrc_t rK = mk_rsrc(w2, wid == 0 ? (unsigned)(WS_CAND + 32 * 4096) : 0u);
        const int ok_ = lane < 40 ? (int)WS_KEYS + (xcd >> 2) * 8192 + (xcd & 3) * (KEY_S * 16) + (lane - 32) * KEY_S : (int)(WS_CAND + 32 * 4096);
        const int oc = (int)WS_CAND + 2048 + kq * 256 + n * 16;
        int best = 0;
        u32x4 v = {0u, 0u, 0u, 0u};
        auto request = [&]() {
            asm volatile("" ::: "memory");
            v = LD16_SC1(rK, lane < 32 ? oc + best * 4096 : ok_);
            SB();
        };
        auto settled = [&]() {
            const unsigned mx = umax4(v), mn = min(min(v[0], v[1]), min(v[2], v[3]));
            unsigned fv = lane < 32 ? ~mx : mn | (unsigned)(lane >> 3 ^ 4);
            asm volatile("" : "+v"(fv));
            const bool fine = fv != 0u;
            unsigned km = max(mx, dppu<0xB1>(mx));
            km = max(km, dppu<0x4E>(km));
            km = max(km, dppu<0x141>(km));
            const int nb = (int)(((0xffffu - ((unsigned)__builtin_amdgcn_readlane((int)km, 32) & 0xffffu)) >> 6) & 31u);
            const bool same = nb == best;
            best = nb;
            return __all(fine) && same;
        };
        if (wid == 0) {
            request();
            WAIT_VM(0);
            settled();
            request();
        } else if (wid >= 8 - BQ) WAIT_VM(16);
        else WAIT_VM(0);
        BARRIER();
        if (wid == 0) STAMP(8);
        bf16x8 wfr[4][4];
        {
            const int q = (lane >> 2) & 3, ky = q | ((g & 1) << 2);
#pragma unroll
            for (int u = 0; u < 4; ++u) {
                const int tro = (u >> 1) * 65536 + (8 * g + q) * 512 + (((wid * 2 + (u & 1)) ^ ky) * 32) + (lane & 3) * 8;
#pragma unroll
                for (int ks = 0; ks < 4; ++ks) {
                    const bf16x4 w0 = __builtin_amdgcn_ds_read_tr16_b64_v4i16((LAS bf16x4*)(lds + tro + ks * 16384));
                    const bf16x4 w1 = __builtin_amdgcn_ds_read_tr16_b64_v4i16((LAS bf16x4*)(lds + tro + ks * 16384 + 2048));
                    wfr[u][ks] = __builtin_shufflevector(w0, w1, 0, 1, 2, 3, 4, 5, 6, 7);
                }
            }
            SB();
#pragma unroll
            for (int i = 0; i < 16; ++i) asm volatile("" : "+v"(wfr[i >> 2][i & 3]));
        }
        if (wid >= 8 - BQ && lane < 16) *(LAS unsigned*)(lds + 8192 + wid * 64 + lane * 4) = 0u;
        __amdgpu_buffer_rsrc_t rPo = mk_rsrc(w2 + WS_PRE, 112 * 2048);
        int po = (n & 3) == 0 ? pre_off((grp ^ (g >> 1)) * 8 + wid) + kq * 256 + g * 64 + n * 4 : 112 * 2048;
        unsigned xw = (unsigned)(size_t)lds + (n >> 3) * 16384 + (g & 1) * 8192 + (n & 7) * 8, xr = (unsigned)(size_t)lds + (n & 1) * 8192 + g * 16;
        unsigned ix = (n & 1) ? 0xEEEEEEEEu : 0x44444444u;
        asm volatile("" : "+v"(po), "+v"(xw), "+v"(xr), "+v"(ix));
        u64 m1 = 0xFFFF0000FFFF0000ull, m2 = 0xFFFFFFFF00000000ull;
        f32x4 ac[4];
#pragma unroll
        for (int u = 0; u < 4; ++u) { ac[u] = (f32x4){0.f, 0.f, 0.f, 0.f}; asm volatile("" : "+v"(ac[u])); }
        asm volatile("" : "+s"(rPo), "+s"(m1), "+s"(m2));
        if (wid == 0) {
#pragma unroll 1
            for (int spin = 0; spin <= SPIN_CAP && !settled(); ++spin) {
                __builtin_amdgcn_s_sleep(1);
                request();
            }
            __builtin_amdgcn_s_waitcnt(0x0F70);
            if (lane < 32) *(LAS u32x2*)(size_t)xw = (u32x2){lane < 16 ? v[0] : v[1], lane < 16 ? v[2] : v[3]};
            WAIT_LGKM0();
            STAMP(9);
        }
        BARRIER();
        {
            bf16x8 xf[2];
#pragma unroll
            for (int j = 0; j < 2; ++j) xf[j] = *(const LAS bf16x8*)((const LAS unsigned char*)(size_t)xr + j * 16384);
            SB();
            float rs[3];
#pragma unroll
            for (int i = 0; i < 8; ++i) {
                const int u = i >> 1, j = i & 1;
                ac[u] = smf(xf[j], wfr[u][2 * j], wfr[u][2 * j + 1], ac[u], ix);
                SB();
                if (i == 4) { asm volatile("" : "+v"(ac[0])); rs[0] = ac[0][0] + ac[0][1]; asm volatile("" : "+v"(rs[0])); }
                if (i == 6) { asm volatile("" : "+v"(ac[1])); rs[0] = pick(m1, rs[0], ac[1][0] + ac[1][1]); asm volatile("" : "+v"(rs[0])); }
                if (i == 7) { asm volatile("" : "+v"(ac[2])); rs[2] = ac[2][0] + ac[2][1]; asm volatile("" : "+v"(rs[2])); }
                SB();
            }
            const unsigned e = min(__float_as_uint(pick(m2, rs[0], pick(m1, rs[2], ac[3][0] + ac[3][1]))), SENT - 1u);
            const u32x4 sv = {e, dppu<0x55>(e), dppu<0xAA>(e), dppu<0xFF>(e)};
            ST16_SC1(sv, rPo, po);
            SB();
            asm volatile("" :: "v"(v));
        }
        if (wid == 0) STAMP(14);
        if (wid >= 8 - BQ) {
            WAIT_VM(1);
            if (wid == 7) STAMP(15);
            stage2(w2, slot, wid - 4, lds + L_YB + (wid & 1) * 16384, lds + L_YB + (wid & 1) * 16384 + 8192, lds + wid * 64, 8192, lane);
            if (wid == 7) STAMP(10);
        }
    } else {
        const int rj = sidx - 112;
        if (blockIdx.x == 255 && tid == 0) {
            const u64 want = ((u64)ctl1[3] << 32) | ctl1[2];
            __hip_atomic_store((GAS u64*)(ws + WS_TAG), want, __ATOMIC_RELAXED, __HIP_MEMORY_SCOPE_AGENT);
        }
        if (wid == 4) {
            unsigned kk;
            const unsigned k = poll_winner(w2, xcd, lane, kk);
            if (lane == 0) ctl[4] = k;
            if (lane < 32) ctl[8 + lane] = kk;
            WAIT_LGKM0();
        }
        BARRIER();
        const unsigned kw = CTL(4);
        if (wid == 0) STAMP(16);
        {
            const unsigned kc = ctl[8 + wid * 4 + g];
            const __amdgpu_buffer_rsrc_t rL = mk_rsrc(w2 + WS_CAND + (size_t)wid * 16384, 16384);
            u32x2 hv2, nv2;
            for (int spin = 0;; ++spin) {
                asm volatile("" ::: "memory");
                hv2 = __builtin_amdgcn_raw_buffer_load_b64(rL, g * 4096 + (rj * 64 + n * 4) * 2, 0, 16);
                nv2 = __builtin_amdgcn_raw_buffer_load_b64(rL, g * 4096 + 2048 + (rj * 64 + n * 4) * 2, 0, 16);
                if (__all(max(max(hv2[0], hv2[1]), max(nv2[0], nv2[1])) != SENT) || spin > SPIN_CAP) break;
                __builtin_amdgcn_s_sleep(1);
            }
            u32x2 ov;
#pragma unroll
            for (int i = 0; i < 2; ++i) {
                const unsigned u = hv2[i], q = nv2[i];
                ov[i] = pk_bf16(bflo(u) + bflo(q), bfhi(u) + bfhi(q));
            }
            const int crow = (int)(0xffffu - (kc & 0xffffu)) & (S_LEN - 1);
            if (kc != kw) *(u32x2*)(a.out + (size_t)crow * D_LEN + rj * 64 + n * 4) = ov;
        }
        const int wrow = (int)(0xffffu - (kw & 0xffffu)) & (S_LEN - 1);
        const __amdgpu_buffer_rsrc_t rC = mk_rsrc(w2 + WS_CAND + (size_t)(wrow >> 6) * 4096, 4096);
        const __amdgpu_buffer_rsrc_t rPt = mk_rsrc(w2 + WS_PART, 112 * 4096);
        const int ocol = (rj >> 2) * 256 + ((lane >> 1) & 1) * 128 + (rj & 3) * 32 + (lane & 1) * 16 + (lane >> 2);
        unsigned hv, hnv;
        for (int spin = 0;; ++spin) {
            asm volatile("" ::: "memory");
            hv = __builtin_amdgcn_raw_buffer_load_b32(rC, (ocol & ~1) * 2, 0, 16);
            hnv = __builtin_amdgcn_raw_buffer_load_b32(rC, 2048 + (ocol & ~1) * 2, 0, 16);
            if (__all(max(hv, hnv) != SENT) || spin > SPIN_CAP) break;
        }
        float sgm = rbf(1.0f / (1.0f + __expf(-key2r(kw))));
        float hf = (ocol & 1) ? bfhi(hv) : bflo(hv), hnf = (ocol & 1) ? bfhi(hnv) : bflo(hnv);
        GAS unsigned short* optr = (GAS unsigned short*)(a.out + (size_t)wrow * D_LEN + ocol);
        unsigned rw = (unsigned)(size_t)red + (wid >> 2) * 1024 + (n * 4 + g) * 16 + (wid & 3) * 4, rr = (unsigned)(size_t)red + lane * 16;
        int po = wid * 16384 + (rj >> 3) * 8192 + g * 2048 + ((rj & 7) * 64 + n * 4) * 4;
        int po3 = po + 3 * 131072;
        asm volatile("" : "+v"(sgm), "+v"(optr), "+v"(hf), "+v"(hnf), "+v"(rw), "+v"(rr), "+v"(po), "+v"(po3));
        float sm;
#pragma unroll 1
        for (int spin = 0;; ++spin) {
            asm volatile("" ::: "memory");
            u32x4 pv[4];
#pragma unroll
            for (int i = 0; i < 4; ++i) pv[i] = __builtin_amdgcn_raw_buffer_load_b128(rPt, i < 3 ? po : po3, i < 3 ? i * 131072 : 0, 16);
            SB();
            unsigned t[4];
#pragma unroll
            for (int j = 0; j < 4; ++j) {
                const unsigned u0 = pv[0][j], u1 = pv[1][j], u2 = pv[2][j], u3 = pv[3][j];
                t[j] = __float_as_uint(((__uint_as_float(u0) + __uint_as_float(u1)) + __uint_as_float(u2)) + __uint_as_float(u3));
            }
            const auto r0 = __builtin_amdgcn_permlane32_swap(t[0], t[2], false, false), r1 = __builtin_amdgcn_permlane32_swap(t[1], t[3], false, false);
            const unsigned a0 = r0[0], a1 = r0[1], b0 = r1[0], b1 = r1[1];
            const auto r2 = __builtin_amdgcn_permlane16_swap(__float_as_uint(__uint_as_float(a0) + __uint_as_float(a1)),
                                                             __float_as_uint(__uint_as_float(b0) + __uint_as_float(b1)), false, false);
            const unsigned c0 = r2[0], c1 = r2[1];
            sm = __uint_as_float(c0) + __uint_as_float(c1);
            if (__builtin_expect(__builtin_amdgcn_ballot_w64(sm != sm) == 0, 1)) break;
            if (__all(max(max(umax4(pv[0]), umax4(pv[1])), max(umax4(pv[2]), umax4(pv[3]))) != SENT) || spin > SPIN_CAP) break;
            if (FIN_SLEEP) __builtin_amdgcn_s_sleep(FIN_SLEEP);
        }
        if (wid == 0) STAMP(17);
        *(LAS float*)(size_t)rw = sm;
        WAIT_LGKM0();
        BARRIER();
        if (wid == 0) {
            const f32x4 u = *(const LAS f32x4*)(size_t)rr, v = *(const LAS f32x4*)((const LAS unsigned char*)(size_t)rr + 1024);
            const f32x4 w = u + v;
            const float t = (w[0] + w[2]) + (w[1] + w[3]);
            *optr = (unsigned short)pk_bf16(hf + rbf(hnf + rbf(rbf(t) * sgm)), 0.f);
            STAMP(18);
            FLUSH();
            asm volatile("s_endpgm" ::: "memory");
        }
        __builtin_amdgcn_s_waitcnt(0x0F70);
    }
    FLUSH();
}

extern "C" void kernel_launch(void* const* d_in, const int* in_sizes, int n_in, void* d_out, int out_size, void* d_ws,
                              size_t ws_size, hipStream_t stream) {
    Args a;
    a.x = (const bf16_t*)d_in[0];
    a.cr = (const bf16_t*)d_in[1];
    a.ci = (const bf16_t*)d_in[2];
    a.wre = (const bf16_t*)d_in[3];
    a.wim = (const bf16_t*)d_in[4];
    a.w_r = (const bf16_t*)d_in[6];
    a.wg = (const bf16_t*)d_in[7];
    a.wu = (const bf16_t*)d_in[8];
    a.wd = (const bf16_t*)d_in[9];
    a.out = (bf16_t*)d_out;
    a.ws = (char*)d_ws + ((ws_size - ((size_t)24 << 20)) & ~(size_t)255);
    (void)hipFuncSetAttribute((const void*)fused_kernel, hipFuncAttributeMaxDynamicSharedMemorySize, LDS_BYTES);
    fused_kernel<<<256, 768, LDS_BYTES, stream>>>(a);
}
```

```cpp
#include <hip/hip_runtime.h>
#include <hip/hip_bf16.h>
#include <stdint.h>

typedef unsigned short bf16_t;
typedef short bf16x8 __attribute__((ext_vector_type(8)));
typedef short bf16x4 __attribute__((ext_vector_type(4)));
typedef float f32x4 __attribute__((ext_vector_type(4)));
typedef unsigned u32x4 __attribute__((ext_vector_type(4)));
typedef unsigned u32x2 __attribute__((ext_vector_type(2)));
typedef unsigned u32x3 __attribute__((ext_vector_type(3)));
typedef unsigned long long u64;
#define LAS __attribute__((address_space(3)))
#define GAS __attribute__((address_space(1)))

constexpr int S_LEN = 2048, D_LEN = 1024, F_LEN = 3584;
constexpr unsigned SENT = 0xFFFFFFFFu;
constexpr int SPIN_CAP = 1 << 17;

constexpr size_t WS_KEYS = 0;
constexpr size_t WS_FLAGS = 16384;
constexpr size_t WS_XRD = 65536;
constexpr size_t WS_FLAG1 = 196608;
constexpr size_t WS_TAG = 131072;
constexpr size_t WS_VOTE = WS_TAG + 4096;
constexpr size_t WS_PROBE = (size_t)20 << 20;
constexpr size_t WS_AT = (size_t)4 << 20;
constexpr size_t WS_Y = (size_t)12 << 20;
constexpr size_t WS_STATS = (size_t)16 << 20;
constexpr size_t WS_PART = WS_STATS + (256u << 10);
constexpr size_t WS_CAND = WS_STATS + (704u << 10);
constexpr size_t WS_PRE = WS_STATS + (832u << 10);
constexpr size_t FILL_BYTES = (size_t)1072 << 10;

__device__ __forceinline__ float bflo(unsigned v) { return __uint_as_float(v << 16); }
__device__ __forceinline__ float bfhi(unsigned v) { return __uint_as_float(v & 0xffff0000u); }
__device__ __forceinline__ unsigned pk_bf16(float lo, float hi) {
    typedef __bf16 bf2 __attribute__((ext_vector_type(2)));
    typedef float f2 __attribute__((ext_vector_type(2)));
    f2 v = {lo, hi};
    bf2 r = __builtin_convertvector(v, bf2);
    return __builtin_bit_cast(unsigned, r);
}
__device__ __forceinline__ float rbf(float x) { return __uint_as_float(pk_bf16(0.f, x)); }

template <int CTRL> __device__ __forceinline__ unsigned dppu(unsigned v) {
    return (unsigned)__builtin_amdgcn_update_dpp(0, (int)v, CTRL, 0xf, 0xf, true);
}
template <int CTRL> __device__ __forceinline__ float dppf(float v) { return __uint_as_float(dppu<CTRL>(__float_as_uint(v))); }
__device__ __forceinline__ void swap16(unsigned v, unsigned& a, unsigned& b) {
    auto r = __builtin_amdgcn_permlane16_swap(v, v, false, false);
    a = r[0]; b = r[1];
}
__device__ __forceinline__ void swap32(unsigned v, unsigned& a, unsigned& b) {
    auto r = __builtin_amdgcn_permlane32_swap(v, v, false, false);
    a = r[0]; b = r[1];
}
__device__ __forceinline__ float sum_rows(float v) {
    unsigned a, b;
    swap16(__float_as_uint(v), a, b); v = __uint_as_float(a) + __uint_as_float(b);
    swap32(__float_as_uint(v), a, b); v = __uint_as_float(a) + __uint_as_float(b);
    return v;
}
__device__ __forceinline__ float wave_sum(float v) {
    v += dppf<0xB1>(v);
    v += dppf<0x4E>(v);
    v += dppf<0x141>(v);
    v += dppf<0x140>(v);
    return sum_rows(v);
}
__device__ __forceinline__ unsigned wave_max_u32(unsigned v) {
    v = max(v, dppu<0xB1>(v));
    v = max(v, dppu<0x4E>(v));
    v = max(v, dppu<0x141>(v));
    v = max(v, dppu<0x140>(v));
    unsigned a, b;
    swap16(v, a, b); v = max(a, b);
    swap32(v, a, b); v = max(a, b);
    return v;
}
__device__ __forceinline__ unsigned wave_max_s(unsigned v) {
    v = max(v, dppu<0xB1>(v));
    v = max(v, dppu<0x4E>(v));
    v = max(v, dppu<0x141>(v));
    v = max(v, dppu<0x140>(v));
    v = max(v, dppu<0x142>(v));
    v = max(v, dppu<0x143>(v));
    return (unsigned)__builtin_amdgcn_readlane((int)v, 63);
}
__device__ __forceinline__ float sel4(int g, float a, float b, float c, float d) {
    const float t0 = (g & 1) ? b : a, t1 = (g & 1) ? d : c;
    return (g & 2) ? t1 : t0;
}
__device__ __forceinline__ float pick(u64 m, float a, float b) {
    float r;
    asm("v_cndmask_b32 %0, %1, %2, %3" : "=v"(r) : "v"(a), "v"(b), "s"(m));
    return r;
}
__device__ __forceinline__ unsigned umax4(u32x4 v) { return max(max(v[0], v[1]), max(v[2], v[3])); }

__device__ __forceinline__ void touch_line(const void* p, unsigned lds_dst) {
    unsigned keep;
    asm volatile("s_mov_b32 %0, m0\n\ts_mov_b32 m0, %2\n\ts_nop 0\n\tglobal_load_lds_dword %1, off\n\ts_mov_b32 m0, %0"
                 : "=&s"(keep) : "v"(p), "s"(lds_dst) : "memory");
}
__device__ __forceinline__ __amdgpu_buffer_rsrc_t mk_rsrc(const void* p, unsigned bytes) {
    return __builtin_amdgcn_make_buffer_rsrc((void*)p, 0, (int)bytes, 0x00020000);
}
#define LDS_LOAD(w, v, o, pol) "buffer_load_" w " " v ", %0, %2 offen" o pol " lds"
#define LDS_M0 "s_mov_b32 m0, %1\n\ts_nop 0\n\t"
__device__ __forceinline__ void dma_nt(__amdgpu_buffer_rsrc_t r, unsigned dst, int voff, int soff) {
    asm volatile(LDS_M0 LDS_LOAD("dwordx4", "%3", "", " nt") :: "s"(r), "s"(dst), "s"(soff), "v"(voff) : "memory", "m0");
}
template <bool SC1> __device__ __forceinline__ void dma_word(__amdgpu_buffer_rsrc_t r, unsigned dst, int voff) {
    if constexpr (SC1) asm volatile(LDS_M0 LDS_LOAD("dword", "%3", "", " sc1") :: "s"(r), "s"(dst), "n"(0), "v"(voff) : "memory", "m0");
    else asm volatile(LDS_M0 LDS_LOAD("dword", "%3", "", "") :: "s"(r), "s"(dst), "n"(0), "v"(voff) : "memory", "m0");
}
template <bool SC1> __device__ __forceinline__ void dma_2k(__amdgpu_buffer_rsrc_t r, unsigned dst, int v0, int v1, int soff) {
    if constexpr (SC1) asm volatile(LDS_M0 LDS_LOAD("dwordx4", "%3", "", " sc1") "\n\t" LDS_LOAD("dwordx4", "%4", " offset:1024", " sc1")
                 :: "s"(r), "s"(dst), "s"(soff), "v"(v0), "v"(v1) : "memory", "m0");
    else asm volatile(LDS_M0 LDS_LOAD("dwordx4", "%3", "", "") "\n\t" LDS_LOAD("dwordx4", "%4", " offset:1024", "")
                 :: "s"(r), "s"(dst), "s"(soff), "v"(v0), "v"(v1) : "memory", "m0");
}
template <bool SC1> __device__ __forceinline__ void dma_4k(__amdgpu_buffer_rsrc_t r, unsigned dst, const int (&v)[4], int soff) {
#define FOUR(pol) asm volatile(LDS_M0 LDS_LOAD("dwordx4", "%3", "", pol) "\n\t" LDS_LOAD("dwordx4", "%4", " offset:1024", pol) "\n\t" \
        LDS_LOAD("dwordx4", "%5", " offset:2048", pol) "\n\t" LDS_LOAD("dwordx4", "%6", " offset:3072", pol) \
        :: "s"(r), "s"(dst), "s"(soff), "v"(v[0]), "v"(v[1]), "v"(v[2]), "v"(v[3]) : "memory", "m0")
    if constexpr (SC1) FOUR(" sc1"); else FOUR("");
#undef FOUR
}
#define CODE_BYTES 25728
__device__ __forceinline__ __amdgpu_buffer_rsrc_t code_rsrc() {
    unsigned long long p; unsigned d;
    asm volatile("s_getpc_b64 %0\n1:\n\ts_mov_b32 %1, 1b-_Z12fused_kernel4Args" : "=s"(p), "=s"(d));
    return __builtin_amdgcn_make_buffer_rsrc((void*)(p - d), 0, CODE_BYTES, 0x00020000);
}
#define LD16_SC1(rs, off) __builtin_amdgcn_raw_buffer_load_b128(rs, off, 0, 16)
#define ST16_SC1(v, rs, off) __builtin_amdgcn_raw_buffer_store_b128(v, rs, off, 0, 16)

constexpr int BT = 128, BK = 64, NSTAGE = 4;
constexpr int TILE_BYTES = BT * BK * 2;
constexpr int STAGE_BYTES = 2 * TILE_BYTES;
constexpr int RING_BYTES = NSTAGE * STAGE_BYTES;
constexpr int L_DUMMY = RING_BYTES;
constexpr int L_CTL = RING_BYTES + 256;
constexpr int L_RED = RING_BYTES + 512;

#define WAIT_VM(n) asm volatile("s_waitcnt vmcnt(" #n ")" ::: "memory")
#define WAIT_VM0() do { asm volatile("" ::: "memory"); __builtin_amdgcn_s_waitcnt(0x0F70); asm volatile("s_waitcnt vmcnt(0)" ::: "memory"); } while (0)
#define WAIT_LGKM0() asm volatile("s_waitcnt lgkmcnt(0)" ::: "memory")
#define BARRIER() do { asm volatile("" ::: "memory"); __builtin_amdgcn_s_barrier(); asm volatile("" ::: "memory"); } while (0)

__device__ __forceinline__ int prow(int rho) {
    const int w = rho >> 6, m = (rho >> 4) & 3, q = (rho >> 2) & 3, r = rho & 3;
    return w * 64 + (m >> 1) * 32 + q * 8 + (m & 1) * 4 + r;
}
__device__ __forceinline__ unsigned r2key(float r, unsigned code) {
    const unsigned u = pk_bf16(0.f, r);
    return ((u ^ ((unsigned)((int)u >> 31) | 0x80000000u)) & 0xffff0000u) | code;
}
__device__ __forceinline__ float key2r(unsigned k) {
    const unsigned o = k >> 16;
    return bflo((o & 0x8000u) ? (o & 0x7fffu) : (~o & 0xffffu));
}
#define STAT_M 5
#define W_PRE 4
#define W_PRE_SL 12
#define BQ 2
#define POLL_T (STAT_M + 5)
#define AUX_SLEEP 12
#define WD_EARLY 4
#define C2_START 2
#define TAIL_PRIO 3
#define STAT_DELAY 8
#define AUX_STAY 1
#define CATCH1 4
#define CPF_MASK1 0x1080u
#define CPF_MASK2 0x1080u
#define CPF_STAG 5
#define CPF_FIX 0x1000u
#define KEY_S 128
#define G2_N 2
#define G3_N 2
#define FIN_SLEEP 1
#define G_N 8
#define POLL_SLEEP 4
__device__ __forceinline__ f32x4 smf(bf16x8 a, bf16x8 b0, bf16x8 b1, f32x4 d, unsigned idx) {
    typedef __bf16 bf8 __attribute__((ext_vector_type(8)));
    typedef __bf16 bf16v __attribute__((ext_vector_type(16)));
    const auto b = __builtin_shufflevector(b0, b1, 0, 1, 2, 3, 4, 5, 6, 7, 8, 9, 10, 11, 12, 13, 14, 15);
    return __builtin_amdgcn_smfmac_f32_16x16x64_bf16(__builtin_bit_cast(bf8, a), __builtin_bit_cast(bf16v, b), d, (int)idx, 0, 0);
}
__device__ __forceinline__ float dot2bf(unsigned a, unsigned b, float c) {
    typedef __bf16 bf2 __attribute__((ext_vector_type(2)));
    return __builtin_amdgcn_fdot2_f32_bf16(__builtin_bit_cast(bf2, a), __builtin_bit_cast(bf2, b), c, false);
}

#define STAMP(i) do { } while (0)
#define CSTAMP(i) do { } while (0)
#define FLUSH() do { } while (0)
#define TSV do { } while (0)

#define KSHIFT 4

constexpr int L_HT = ((32 + KSHIFT) % NSTAGE) * STAGE_BYTES;
constexpr int L_WD0 = ((33 + KSHIFT) % NSTAGE) * STAGE_BYTES;
constexpr int L_WD1 = ((34 + KSHIFT) % NSTAGE) * STAGE_BYTES;
constexpr int L_ST = ((35 + KSHIFT) % NSTAGE) * STAGE_BYTES;
constexpr int L_YB = RING_BYTES;
constexpr int L_ATAB = L_YB;
constexpr int LDS_BYTES = RING_BYTES + 32768;

#define SB() __builtin_amdgcn_sched_barrier(0)
template <int N> __device__ __forceinline__ void wait_vm() { asm volatile("s_waitcnt vmcnt(%0)" :: "n"(N) : "memory"); }
__device__ __forceinline__ int at_tile(int c, int par) { return ((c << 8) & 0x78000) | (((c << 2) & 0x2000) ^ par); }
__device__ __forceinline__ int at_panel(int ep) { return ep << 19; }
__device__ __forceinline__ int pre_off(int slot) { const int s_ = slot >= 56, l = slot - 56 * s_; return (l >> 2) * 16384 + s_ * 8192 + (l & 3) * 2048; }
static_assert(KSHIFT == 4, "flag samples must have replaced older contents of their slot; even tile counts");
struct Ring {
    __amdgpu_buffer_rsrc_t rP, rQ, rF;
    int voP[4], voQ[4], voF;
    LAS unsigned char* fl;
    unsigned lb;
    int sp, sq;
    int poff, par, xm;
    bool img;
    template <int LD, bool PIMG> __device__ __forceinline__ void setup(int lw, int lane) {
        const int r = lane >> 3, v0 = (PIMG ? r : (r & 3) + (r & 4) * 2) * (LD * 2) + (((lane & 7) ^ (lane >> 4)) << 4);
#pragma unroll
        for (int j = 0; j < 4; ++j) {
            voQ[j] = (v0 ^ ((j & 1) * 64)) + (PIMG ? j * 8 : (j & 1) * 16 + (j >> 1) * 4) * (LD * 2) - j * 1024;
            voP[j] = PIMG ? (lw >> 1) * 16384 + (lw & 1) * 4096 + lane * 16 : voQ[j];
        }
        img = PIMG;
    }
    __device__ __forceinline__ void seek(int col, int pof) {
        sq = col; poff = pof;
        sp = (img ? at_tile(sq, par) : sq) + poff;
    }
    __device__ __forceinline__ void step() {
        sq = (sq + BK * 2) & 4095;
        sp = (img ? at_tile(sq ^ xm, par) : sq) + poff;
        asm volatile("" : "+s"(sq), "+s"(sp));
    }
    __device__ __forceinline__ void sample() { dma_word<true>(rF, (unsigned)(size_t)fl, voF); }
    __device__ __forceinline__ unsigned peek(unsigned addr) {
        unsigned v;
        asm volatile("ds_read_b32 %0, %1\n\ts_waitcnt lgkmcnt(0)" : "=v"(v) : "v"(addr) : "memory");
        return v;
    }
    __device__ __forceinline__ void gate(unsigned want, unsigned sa, unsigned need, unsigned& have) {
#pragma unroll 1
        for (int spin = 0; spin <= SPIN_CAP && (need & ~have) != 0u; ++spin) {
            if (spin) { sample(); WAIT_VM(0); __builtin_amdgcn_s_sleep(1); }
            const unsigned long long m = __ballot(peek(sa) == want);
            have = (unsigned)m & (unsigned)(m >> 32);
        }
    }
    template <int AUX> __device__ __forceinline__ void issueP(LAS unsigned char* base) { dma_4k<AUX != 0>(rP, (unsigned)(size_t)base, voP, sp); }
    __device__ __forceinline__ void issueQ(LAS unsigned char* base) { dma_4k<false>(rQ, (unsigned)(size_t)base + TILE_BYTES, voQ, sq ^ xm); }
    template <bool P2> __device__ __forceinline__ void issue(LAS unsigned char* lds) {
        issueP<P2 ? 16 : 0>(lds + lb);
        issueQ(lds + lb);
        SB();
        if (P2) sample();
        lb = (lb + STAGE_BYTES) & (RING_BYTES - 1);
        asm volatile("" : "+s"(lb));
        step();
        SB();
    }
    template <int CAT> __device__ __forceinline__ void ramp(LAS unsigned char* lds) {
#pragma unroll 1
        for (int i = 1; i <= CAT + 1; ++i) {
            issue<false>(lds);
            if (i == CAT + 1) continue;
            WAIT_VM(8);
            BARRIER();
        }
    }
    template <bool P2, bool SAMP, int W0 = 16> __device__ __forceinline__ void steady(int cnt, int trig = 0, int v2 = 0) {
        int t, u;
#define LD(v, r, s, o) "buffer_load_dwordx4 " v ", " r ", " s " offen" o " lds\n\t"
        if constexpr (P2) {
#define BODY(SMP, TST, ENT) asm volatile("s_mov_b32 m0, %0\n" ENT "1:\n\ts_waitcnt vmcnt(16)\n2:\n\ts_barrier\n\t" \
            LD("%8", "%6", "%2", " sc1") LD("%8", "%6", "%2", " offset:1024 sc1") LD("%8", "%6", "%2", " offset:2048 sc1") LD("%8", "%6", "%2", " offset:3072 sc1") \
            SMP "s_xor_b32 %4, %1, %18\n\ts_add_i32 %1, %1, 0x80\n\ts_add_i32 m0, %0, 0x4000\n\ts_and_b32 %1, %1, 0xfff\n\t" \
            LD("%9", "%7", "%4", "") "s_xor_b32 %2, %1, %18\n\ts_lshl_b32 %5, %1, 2\n\ts_lshl_b32 %2, %2, 8\n\ts_and_b32 %2, %2, 0x78000\n\t" \
            LD("%10", "%7", "%4", " offset:1024") "s_and_b32 %5, %5, 0x2000\n\ts_xor_b32 %5, %5, %13\n\ts_or_b32 %2, %2, %5\n\ts_add_i32 %3, %3, -1\n\t" \
            LD("%11", "%7", "%4", " offset:2048") "s_add_i32 %0, %0, 0x8000\n\ts_and_b32 %0, %0, 0x1ffff\n\t" TST "s_cmp_lg_u32 %3, 0\n\t" \
            LD("%12", "%7", "%4", " offset:3072") "\n4:\n\ts_mov_b32 m0, %0\n\ts_cbranch_scc1 1b\n\ts_waitcnt vmcnt(16)\n\ts_barrier" \
            : "+s"(lb), "+s"(sq), "+s"(sp), "+s"(cnt), "=&s"(t), "=&s"(u) \
            : "s"(rP), "s"(rQ), "v"(voP[0]), "v"(voQ[0]), "v"(voQ[1]), "v"(voQ[2]), "v"(voQ[3]), "s"(par | poff), "s"(rF), "v"(voF), "s"((unsigned)(size_t)fl), "n"(W0), "s"(xm), \
              "s"(trig), "v"(v2) \
            : "memory", "scc", "m0")
            if constexpr (SAMP) BODY("s_mov_b32 m0, %16\n\ts_nop 0\n\tbuffer_load_dword %15, %14, 0 offen sc1 lds\n\t", "", "");
            else BODY("", "s_cmp_eq_u32 %3, %19\n\ts_cbranch_scc1 3b\n\t",
                      "\ts_waitcnt vmcnt(%17)\n\ts_branch 2f\n3:\n\t" LD("%12", "%7", "%4", " offset:3072") "s_mov_b32 m0, %16\n\ts_nop 0\n\tbuffer_load_dword %20, %7, 0 offen lds\n\t"
                      "s_add_i32 m0, %16, 0x200\n\ts_cmp_lg_u32 %3, 0\n\tbuffer_load_dword %15, %14, 0 offen sc1 lds\n\ts_branch 4f\n");
#undef BODY
        } else {
            asm volatile("s_mov_b32 m0, %0\n\ts_branch 2f\n1:\n\ts_waitcnt vmcnt(16)\n\ts_barrier\n2:\n\t"
                LD("%6", "%4", "%1", "") LD("%7", "%4", "%1", " offset:1024") LD("%8", "%4", "%1", " offset:2048") LD("%9", "%4", "%1", " offset:3072")
                "s_add_i32 m0, %0, 0x4000\n\ts_mov_b32 %3, %1\n\t"
                LD("%6", "%5", "%3", "") "s_add_i32 %1, %1, 0x80\n\ts_add_i32 %0, %0, 0x8000\n\t"
                LD("%7", "%5", "%3", " offset:1024") "s_and_b32 %0, %0, 0x1ffff\n\ts_add_i32 %2, %2, -1\n\t"
                LD("%8", "%5", "%3", " offset:2048") "s_cmp_lg_u32 %2, 0\n\t"
                LD("%9", "%5", "%3", " offset:3072") "s_mov_b32 m0, %0\n\ts_cbranch_scc1 1b\n\ts_waitcnt vmcnt(16)\n\ts_barrier"
                : "+s"(lb), "+s"(sq), "+s"(cnt), "=&s"(t)
                : "s"(rP), "s"(rQ), "v"(voQ[0]), "v"(voQ[1]), "v"(voQ[2]), "v"(voQ[3])
                : "memory", "scc", "m0");
            (void)u;
        }
#undef LD
    }
};

template <bool PLIN, bool FIRST, class Pre>
__device__ __forceinline__ void compute_loop(const LAS unsigned char* lds, f32x4 (&acc)[4][4], int NT, int wp, int wq, int lane, Pre pre,
                                             bf16x8 (&p0)[4], bf16x8 (&p1)[4], unsigned last) {
    const int fr = lane & 15, fq = lane >> 4, key = (fr >> 1) & 7;
    const unsigned l0 = (unsigned)(size_t)lds;
    const unsigned oP0 = l0 + (PLIN ? wp * 8192 + lane * 16 : (wp * 64 + fr) * 128 + ((fq ^ key) * 16));
    const unsigned oP1 = l0 + (PLIN ? wp * 8192 + 1024 + lane * 16 : (wp * 64 + fr) * 128 + (((4 + fq) ^ key) * 16));
    const unsigned oQ0 = l0 + TILE_BYTES + (wq * 64 + fr) * 128 + ((fq ^ key) * 16);
    const unsigned oQ1 = l0 + TILE_BYTES + (wq * 64 + fr) * 128 + (((4 + fq) ^ key) * 16);
    unsigned aP0 = oP0, aQ0 = oQ0, aP1 = oP1, aQ1 = oQ1;
    bf16x8 r0[4], r1[4], q0[4], q1[4];
    unsigned xp = 2 * STAGE_BYTES;
#pragma unroll
    for (int i = 0; i < 16; ++i) asm volatile("" : "+v"(acc[i >> 2][i & 3]));
    asm volatile("" : "+v"(aP0), "+v"(aQ0), "+v"(aP1), "+v"(aQ1));
#define MM(pp, qq, m, n) do { acc[m][n] = __builtin_amdgcn_mfma_f32_16x16x32_bf16(pp[m], qq[n], acc[m][n], 0, 0, 0); SB(); } while (0)
#define RD(dst, i, a, st) do { dst[i] = *(const LAS bf16x8*)((const LAS unsigned char*)(size_t)(a) + (st) * STAGE_BYTES + (i) * 2048); SB(); } while (0)
#define ADP(a) do { a ^= xp; asm volatile("" : "+v"(a)); SB(); } while (0)
#define ADR(a) do { a ^= 2 * STAGE_BYTES; asm volatile("" : "+v"(a)); SB(); } while (0)
    __builtin_amdgcn_s_waitcnt(0xC07F);
    SB();
    if (FIRST) BARRIER();
    SB();
    RD(p0, 0, aP0, 0); RD(q0, 0, aQ0, 0); RD(p1, 0, aP1, 0); RD(q1, 0, aQ1, 0); RD(p1, 1, aP1, 0); RD(p0, 1, aP0, 0);
    RD(p0, 2, aP0, 0); RD(p1, 2, aP1, 0); RD(p1, 3, aP1, 0); RD(p0, 3, aP0, 0);
    pre();
    SB();
    int t = NT;
#pragma unroll 1
    do {
        MM(p0, q0, 0, 0); MM(p1, q1, 0, 0); RD(q0, 1, aQ0, 0);
        MM(p1, q1, 1, 0); MM(p0, q0, 1, 0); RD(q1, 1, aQ1, 0);
        MM(p0, q0, 2, 0); MM(p1, q1, 2, 0); RD(q0, 2, aQ0, 0);
        MM(p1, q1, 3, 0); MM(p0, q0, 3, 0); RD(q1, 2, aQ1, 0);
        MM(p0, q0, 3, 1); MM(p1, q1, 3, 1); RD(q0, 3, aQ0, 0);
        MM(p1, q1, 2, 1); MM(p0, q0, 2, 1); RD(q1, 3, aQ1, 0);
        MM(p0, q0, 1, 1); MM(p1, q1, 1, 1);
        MM(p1, q1, 0, 1); MM(p0, q0, 0, 1);
        MM(p0, q0, 0, 2); MM(p1, q1, 0, 2); __builtin_amdgcn_s_waitcnt(0xC07F); SB(); BARRIER(); SB();
        MM(p1, q1, 1, 2); RD(r0, 0, aP0, 1); MM(p0, q0, 1, 2); RD(q0, 0, aQ0, 1);
        MM(p0, q0, 2, 2); RD(r1, 0, aP1, 1); MM(p1, q1, 2, 2); RD(q1, 0, aQ1, 1);
        MM(p1, q1, 3, 2); MM(p0, q0, 3, 2); RD(r1, 1, aP1, 1);
        MM(p0, q0, 3, 3); RD(r0, 1, aP0, 1); MM(p1, q1, 3, 3);
        MM(p1, q1, 2, 3); RD(r0, 2, aP0, 1); MM(p0, q0, 2, 3); RD(r1, 2, aP1, 1);
        MM(p0, q0, 1, 3); MM(p1, q1, 1, 3); RD(r1, 3, aP1, 1);
        MM(p1, q1, 0, 3); RD(r0, 3, aP0, 1); MM(p0, q0, 0, 3);

        MM(r0, q0, 0, 0); MM(r1, q1, 0, 0); RD(q0, 1, aQ0, 1);
        MM(r1, q1, 1, 0); MM(r0, q0, 1, 0); RD(q1, 1, aQ1, 1);
        MM(r0, q0, 2, 0); MM(r1, q1, 2, 0); RD(q0, 2, aQ0, 1);
        MM(r1, q1, 3, 0); MM(r0, q0, 3, 0); RD(q1, 2, aQ1, 1);
        MM(r0, q0, 3, 1); MM(r1, q1, 3, 1); RD(q0, 3, aQ0, 1);
        MM(r1, q1, 2, 1); MM(r0, q0, 2, 1); RD(q1, 3, aQ1, 1);
        MM(r0, q0, 1, 1); t -= 2; if (PLIN) xp = t == 0 ? last : 2 * STAGE_BYTES; asm volatile("" : "+s"(t), "+s"(xp)); SB(); MM(r1, q1, 1, 1); ADP(aP0);
        MM(r1, q1, 0, 1); ADP(aP1); MM(r0, q0, 0, 1); ADR(aQ0);
        MM(r0, q0, 0, 2); ADR(aQ1); MM(r1, q1, 0, 2); __builtin_amdgcn_s_waitcnt(0xC07F); SB(); BARRIER(); SB();
        MM(r1, q1, 1, 2); RD(p0, 0, aP0, 0); MM(r0, q0, 1, 2); RD(q0, 0, aQ0, 0);
        MM(r0, q0, 2, 2); RD(p1, 0, aP1, 0); MM(r1, q1, 2, 2); RD(q1, 0, aQ1, 0);
        MM(r1, q1, 3, 2); MM(r0, q0, 3, 2); RD(p1, 1, aP1, 0);
        MM(r0, q0, 3, 3); RD(p0, 1, aP0, 0); MM(r1, q1, 3, 3);
        MM(r1, q1, 2, 3); RD(p0, 2, aP0, 0); MM(r0, q0, 2, 3); RD(p1, 2, aP1, 0);
        MM(r0, q0, 1, 3); MM(r1, q1, 1, 3); RD(p1, 3, aP1, 0);
        MM(r1, q1, 0, 3); RD(p0, 3, aP0, 0); MM(r0, q0, 0, 3);
    } while (t != 0);
    SB();
#undef MM
#undef RD
#undef ADR
#undef ADP
}

struct Args {
    const bf16_t* x; const bf16_t* wre; const bf16_t* wim; char* ws; const bf16_t* cr; const bf16_t* ci;
    const bf16_t* w_r; const bf16_t* wg; const bf16_t* wu; const bf16_t* wd; bf16_t* out;
};

__device__ __forceinline__ unsigned poll_winner(const char* ws, int rep, int lane, unsigned& mine) {
    GAS unsigned* kp = (GAS unsigned*)(ws + WS_KEYS + (size_t)(rep >> 2) * 8192 + (rep & 3) * (KEY_S * 16) + ((lane >> 2) & 7) * KEY_S) + (lane & 3);
    unsigned kw;
    for (int spin = 0;; ++spin) {
            asm volatile("" ::: "memory");
        kw = __hip_atomic_load(kp, __ATOMIC_RELAXED, __HIP_MEMORY_SCOPE_AGENT);
        if (__all(kw != 0u) || spin > SPIN_CAP) break;
        __builtin_amdgcn_s_sleep(POLL_SLEEP);
    }
    mine = kw;
    return __builtin_amdgcn_readfirstlane(wave_max_u32(kw));
}

__device__ __forceinline__ void stage2(char* w2, int slot, int lw, LAS unsigned char* img0, LAS unsigned char* img1, LAS unsigned char* abuf, int zoff, int lane) {
    const int g = lane >> 4, n = lane & 15;
    const __amdgpu_buffer_rsrc_t rPi = mk_rsrc(w2 + WS_PRE + pre_off(slot), 2048);
    unsigned pp[8];
    auto request = [&]() {
        asm volatile("" ::: "memory");
#pragma unroll
        for (int k = 0; k < 8; ++k) pp[k] = __builtin_amdgcn_raw_buffer_load_b32(rPi, k * 256 + lane * 4, 0, 16);
        SB();
    };
    request();
    bf16x8 wf[16];
    {
        const int q = (lane >> 2) & 3, ky = q | ((g & 1) << 2);
#pragma unroll
        for (int j = 0; j < 16; ++j) {
            LAS unsigned char* t = (g < 2 ? img0 : img1) + (8 * (g & 1) + q) * 512 + (((j & 7) ^ ky) + (j & 8)) * 32 + (lane & 3) * 8;
            const bf16x4 w0 = __builtin_amdgcn_ds_read_tr16_b64_v4i16((LAS bf16x4*)t);
            const bf16x4 w1 = __builtin_amdgcn_ds_read_tr16_b64_v4i16((LAS bf16x4*)(t + 2048));
            wf[j] = __builtin_shufflevector(w0, w1, 0, 1, 2, 3, 4, 5, 6, 7);
        }
        SB();
#pragma unroll
        for (int j = 0; j < 16; ++j) asm volatile("" : "+v"(wf[j]));
    }
    unsigned aa[5], pto = lane * 16;
#pragma unroll
    for (int m = 0; m < 4; ++m) aa[m] = (unsigned)(size_t)abuf + ((n >> 2) == m && ((n >> 1) & 1) == (g >> 1) ? (n & 1) * 32 + (g & 1) * 16 : zoff + g * 16);
    aa[4] = (unsigned)(size_t)abuf + ((lane >> 1) & 1) * 32 + ((lane >> 4) & 1) * 16 + ((lane >> 2) & 3) * 4 + (lane & 1) * 2;
    unsigned ix = (n & 1) ? 0xEEEEEEEEu : 0x44444444u;
    __amdgpu_buffer_rsrc_t rPt = mk_rsrc(w2 + WS_PART + __builtin_amdgcn_readfirstlane((slot >> 2) * 16384 + (lw >> 1) * 8192 + (slot & 3) * 2048 + (lw & 1) * 1024), 1024);
    asm volatile("" : "+v"(aa[0]), "+v"(aa[1]), "+v"(aa[2]), "+v"(aa[3]), "+v"(aa[4]), "+v"(pto), "+v"(ix));
    asm volatile("" : "+s"(rPt));
    f32x4 d[2] = {{0.f, 0.f, 0.f, 0.f}, {0.f, 0.f, 0.f, 0.f}};
    asm volatile("" : "+v"(d[0]), "+v"(d[1]));
    SB();
    float sv;
#pragma unroll 1
    for (int spin = 0;; ++spin) {
        sv = ((__uint_as_float(pp[0]) + __uint_as_float(pp[1])) + (__uint_as_float(pp[2]) + __uint_as_float(pp[3]))) +
             ((__uint_as_float(pp[4]) + __uint_as_float(pp[5])) + (__uint_as_float(pp[6]) + __uint_as_float(pp[7])));
        if (__builtin_expect(__builtin_amdgcn_ballot_w64(sv != sv) == 0, 1)) break;
        if (__all(max(max(max(pp[0], pp[1]), max(pp[2], pp[3])), max(max(pp[4], pp[5]), max(pp[6], pp[7]))) != SENT) || spin > SPIN_CAP) break;
        request();
    }
    unsigned ug, uu;
    swap32(__float_as_uint(sv), ug, uu);
    const float gg = rbf(__uint_as_float(ug)), up = rbf(__uint_as_float(uu));
    const unsigned act = pk_bf16(rbf(gg * __builtin_amdgcn_rcpf(1.0f + __expf(-gg))) * up, 0.f);
    *(volatile LAS unsigned short*)(size_t)aa[4] = (unsigned short)act;
    bf16x8 af[4];
#pragma unroll
    for (int m = 0; m < 4; ++m) af[m] = *(volatile LAS bf16x8*)(size_t)aa[m];
    SB();
#pragma unroll
    for (int i = 0; i < 8; ++i) {
        const int j = 8 * (i & 1) + 2 * (i >> 1);
        d[i & 1] = smf(af[i >> 1], wf[j], wf[j + 1], d[i & 1], ix);
        SB();
    }
    u32x4 pv;
#pragma unroll
    for (int q = 0; q < 4; ++q) {
        pv[q] = min(__float_as_uint(d[q >> 1][(q & 1) * 2] + d[q >> 1][(q & 1) * 2 + 1]), SENT - 1u);
        asm volatile("" : "+v"(pv[q]));
    }
    ST16_SC1(pv, rPt, pto);
}
__device__ __forceinline__ int wd_off(int lw, int i, int lane) {
    const int r = 2 * (i & 5) + (lane >> 5), ky = (r & 3) | ((r >> 1) & 4), c = lane & 31;
    return (lane >> 5) * (D_LEN * 2) + lw * 512 + (((c >> 1) ^ ky) * 2 + (c & 1)) * 16;
}

__global__ __launch_bounds__(768, 3) void fused_kernel(const Args a) {
    extern __shared__ __attribute__((aligned(16))) unsigned char smem[];
    constexpr int K = S_LEN, NT1 = D_LEN / BK;
    const int tid = threadIdx.x, wid = __builtin_amdgcn_readfirstlane(tid >> 6), lane = tid & 63;
    const int xcd = blockIdx.x & 7, idx = blockIdx.x >> 3;
    TSV;
    LAS unsigned char* lds = (LAS unsigned char*)smem;
    if (idx < 1 && wid < 4) dma_word<false>(code_rsrc(), (unsigned)(size_t)lds + L_DUMMY, tid * 128);
    SB();
    const int h = xcd & 1, xq = xcd >> 1, rt = xq * 4 + (idx & 3), ct = idx >> 2;
    volatile LAS unsigned* ctl1 = (volatile LAS unsigned*)(lds + L_CTL);
#define NBR(k) ((rt + (k)) & 3)
    unsigned kx;
    Ring ring;
    if (__builtin_expect((wid & 12) == 4, 1)) {
        __builtin_amdgcn_s_setprio(3);
        ring.template setup<D_LEN, false>(wid - 4, lane);
        ring.rP = mk_rsrc(a.x + (size_t)(rt * 4 + wid - 4) * 32 * D_LEN, 32 * D_LEN * 2);
        ring.rQ = mk_rsrc((h == 0 ? a.wre : a.wim) + (size_t)(ct * 4 + wid - 4) * 32 * D_LEN, 32 * D_LEN * 2);
        ring.lb = (wid - 4) * 4096;
        ring.sp = ring.sq = ring.poff = ring.xm = 0;
        ring.template issue<false>(lds);
        __builtin_amdgcn_s_setprio(0);
        ring.par = (ct & 4) << 11;
        ring.template ramp<CATCH1>(lds);
        ring.template steady<false, false>(NT1 - 2 - CATCH1);
        ring.template setup<K, true>(wid - 4, lane);
        ring.rQ = mk_rsrc((h == 0 ? a.cr : a.ci) + (size_t)(rt * 4 + wid - 4) * 32 * K, 32 * K * 2);
        ring.seek(rt * 256, 0);
        LAS unsigned char* l0 = lds + (wid - 4) * 4096;
        ring.issueQ(l0); ring.step();
        WAIT_VM(12); BARRIER();
        ring.issueQ(l0 + STAGE_BYTES);
        ring.seek((xq * 4 + NBR(1)) * 256, 0);
        const int xr = (wid & 1) * 8 + (lane >> 4);
        int vx0 = ((((wid >> 1) & 1) * 64 + xr) * D_LEN + (((lane & 15) ^ xr) * 8)) * 2;
        int vx1 = ((((wid >> 1) & 1) * 64 + xr + 4) * D_LEN + (((lane & 15) ^ (xr + 4)) * 8)) * 2 - 1024;
        __amdgpu_buffer_rsrc_t rX = mk_rsrc(a.x + (size_t)rt * BT * D_LEN + ct * BT, BT * D_LEN * 2 - ct * BT * 2);
        unsigned xd = (unsigned)(size_t)lds + L_YB + ((wid & 2) ? 16384 : 4096) + (wid & 1) * 2048;
        unsigned xe = (wid & 2) ? xd + 12288 : (unsigned)(size_t)lds + 3 * STAGE_BYTES + (wid & 1) * 2048;
        asm volatile("" : "+v"(vx0), "+v"(vx1));
        asm volatile("" : "+s"(rX), "+s"(xd), "+s"(xe));
        WAIT_VM(8); BARRIER();
        ring.issueQ(l0 + 2 * STAGE_BYTES);
        ring.seek((xq * 4 + NBR(2)) * 256, 0);
        if (h != 0) {
#pragma unroll
            for (int k = 0; k < 3; ++k) dma_2k<false>(rX, xd + k * 4096, vx0, vx1, k * (16 * D_LEN * 2));
        }
        BARRIER();
        if (h != 0) dma_2k<false>(rX, xe, vx0, vx1, 3 * (16 * D_LEN * 2));
        ring.issueQ(l0 + 3 * STAGE_BYTES);
        kx = (unsigned)__builtin_amdgcn_readfirstlane((int)ctl1[4]);
        SB();
    }
    char* ws = a.ws + ((0ull - (unsigned long long)a.ws) & 0x1fffffull);
    const int g = lane >> 4, n = lane & 15;
    LAS float* tab = (LAS float*)(lds + L_RED);
    if (wid >= 8) {
        const int aw = wid - 8;
#define T1(t) do { if (aw == 1 && (t) <= 16) STAMP(40 + (t)); } while (0)
        GAS u64* tagp = (GAS u64*)(ws + WS_TAG);
        const int r0 = xq * 512 + h * 256 + idx * 8 + aw * 2;
        const int w0 = xq * 256 + idx * 8 + aw * 2;
        const u32x4* src = (const u32x4*)((n < 8 ? a.x + (size_t)(r0 + (n >> 2)) * D_LEN
                                                 : (h == 0 ? a.wre : a.wim) + (size_t)(w0 + (n >> 2) - 2) * D_LEN) + g * 32 + (n & 3) * 8);
        if (AUX_SLEEP) __builtin_amdgcn_s_sleep(AUX_SLEEP);
        u64 tag0 = __hip_atomic_load(tagp, __ATOMIC_RELAXED, __HIP_MEMORY_SCOPE_AGENT);
        u32x4 v[8];
#pragma unroll
        for (int j = 0; j < 4; ++j) v[j] = src[j * 16];
        __builtin_amdgcn_s_setprio(3);
        SB();
        const char* cb = (const char*)(h ? a.ci : a.cr) + (size_t)rt * (512u << 10) + ct * 65536 + (lane >> 2) * 4096 + (lane & 3) * 128;
        const GAS u32x4* wrp = (const GAS u32x4*)a.w_r + lane;
        asm volatile("" : "+v"(cb), "+v"(wrp));
        unsigned mask = aw == 1 ? CPF_MASK1 : aw == 2 ? CPF_MASK2 : 0u;
        mask = (mask & CPF_FIX) | ((mask & ~CPF_FIX) << (idx % CPF_STAG));
        int ti = 2 * xq + aw - 1;
        auto warm = [&](int t) { if (t >= 4 && ((mask >> t) & 1)) { touch_line(cb + (ti & 7) * 512, L_DUMMY); ti += 2; } };
        f32x4 d1 = {0.f, 0.f, 0.f, 0.f}, d2 = {0.f, 0.f, 0.f, 0.f};
        u32x4 ones = {0x3F803F80u, 0x3F803F80u, 0x3F803F80u, 0x3F803F80u};
        auto sums = [&](int j0) __attribute__((always_inline)) {
#pragma unroll
            for (int j = j0; j < j0 + 4; ++j) {
                const bf16x8 f = __builtin_bit_cast(bf16x8, v[j]);
                d1 = __builtin_amdgcn_mfma_f32_16x16x32_bf16(f, __builtin_bit_cast(bf16x8, ones), d1, 0, 0, 0);
                d2 = __builtin_amdgcn_mfma_f32_16x16x32_bf16(f, f, d2, 0, 0, 0);
            }
            asm volatile("" : "+v"(d1), "+v"(d2));
        };
        unsigned wl, wh;
        int ro;
        __amdgpu_buffer_rsrc_t rS;
        u64 m1 = 0xAAAAAAAAAAAAAAAAull, m2 = 0xCCCCCCCCCCCCCCCCull, mh = 0xFFFFFFFF00000000ull;
#pragma unroll
        for (int t = 0; t <= 5; ++t) {
            SB(); BARRIER(); SB();
            T1(t);
            if (t == 2) {
#pragma unroll
                for (int j = 4; j < 8; ++j) v[j] = src[j * 16];
            }
            if (t == 3) {
                asm volatile("" : "+v"(ones));
                sums(0);
                asm volatile("" : "+v"(tag0));
                const u64 want = tag0 * 6364136223846793005ull + 1442695040888963407ull;
                wl = (unsigned)want; wh = (unsigned)(want >> 32);
                asm volatile("" : "+v"(wl), "+v"(wh));
            }
            if (t == 4) { sums(4); asm volatile("s_nop 7\n\ts_nop 3" : "+v"(d2)); }
            if (t == 1) {
                rS = mk_rsrc(ws + WS_XRD, 65536);
                ro = n == g * 4 ? (g < 2 ? r0 + g : 2048 - 2 + h * 1024 + w0 + g) * 16 : 65536;
                asm volatile("" : "+v"(ro));
                asm volatile("" : "+s"(rS), "+s"(m1), "+s"(m2), "+s"(mh), "+s"(mask), "+s"(ti));
            }
        }
        float sm = (d1[0] + d1[1]) + (d1[2] + d1[3]);
        float sq = pick(m2, pick(m1, d2[0], d2[1]), pick(m1, d2[2], d2[3]));
        asm volatile("s_nop 1" : "+v"(sq));
        sq += dppf<0xB1>(sq);
        sq += dppf<0x4E>(sq);
        float mean = sm * (1.0f / D_LEN), var = rbf(rbf(fmaxf(sq * (1.0f / D_LEN) - mean * mean, 0.f)) + 1e-5f);
        asm volatile("" : "+v"(sm), "+v"(mean), "+v"(var));
        SB(); BARRIER(); SB();
        T1(6);
        {
            float rden = __builtin_amdgcn_rcpf(rbf(__builtin_amdgcn_sqrtf(var)));
            asm volatile("s_nop 1" : "+v"(rden));
            const u32x4 rec = {__float_as_uint(pick(mh, rden, sm)), wl, __float_as_uint(rbf(mean) * rden), wh};
            ST16_SC1(rec, rS, ro);
        }
        const __amdgpu_buffer_rsrc_t rV = mk_rsrc(ws + WS_VOTE, 512);
        if (aw == 3 && idx < 8) __builtin_amdgcn_raw_buffer_store_b64((u32x2){ctl1[5], wl}, rV, blockIdx.x * 8 + lane * 512, 0, 16);
        SB(); BARRIER(); SB();
        T1(STAT_M + 2);
        warm(STAT_M + 2);
        if (aw == 1 || aw == 2) {
#pragma unroll 1
            for (int t = STAT_M + 3; t <= NT1 + AUX_STAY; ++t) {
                BARRIER();
                T1(t);
                warm(t);
            }
            FLUSH();
            return;
        }
        if (aw == 3) {
            if (h == 0) {
                const u32x4 w0v = wrp[0], w1v = wrp[64];
                BARRIER(); BARRIER(); BARRIER(); BARRIER();
                SB();
                float t = 0.f;
#pragma unroll
                for (int i = 0; i < 4; ++i) { t = dot2bf(w0v[i], 0x3F803F80u, t); t = dot2bf(w1v[i], 0x3F803F80u, t); }
                SB(); BARRIER(); SB();
                t = wave_sum(t);
                ctl1[1] = __float_as_uint(t);
                WAIT_LGKM0();
            }
            return;
        }
#pragma unroll
        for (int t = STAT_M + 3; t <= POLL_T; ++t) BARRIER();
        SB();
        const int o0 = (rt * BT + lane) * 16, o1 = (2048 + (h * 8 + ct) * BT + lane) * 16, os = 1024;
        u32x4 q0, q1, q2, q3;
        u32x2 qv;
        auto request = [&]() {
            asm volatile("" ::: "memory");
            q0 = LD16_SC1(rS, o0); q1 = LD16_SC1(rS, o0 + os); q2 = LD16_SC1(rS, o1); q3 = LD16_SC1(rS, o1 + os);
            qv = __builtin_amdgcn_raw_buffer_load_b64(rV, lane * 8, 0, 16);
            SB();
        };
        request();
        int b = POLL_T + 1;
        for (int spin = 0; spin <= SPIN_CAP; ++spin) {
            if (b < NT1) { BARRIER(); ++b; }
            if (b < NT1) { BARRIER(); ++b; }
            SB();
            unsigned d = ((q0[1] ^ wl) | (q1[1] ^ wl)) | ((q2[1] ^ wl) | (q3[1] ^ wl)) | ((q0[3] ^ wh) | (q1[3] ^ wh)) | ((q2[3] ^ wh) | (q3[3] ^ wh)) | (qv[1] ^ wl);
            asm volatile("" : "+v"(d));
            if (__all(d == 0u)) break;
            request();
        }
        if (b < NT1) BARRIER();
        SB();
        LAS unsigned* tb = (LAS unsigned*)tab;
        const unsigned sg = h == 0 ? 0u : 0x80000000u;
        tb[lane] = q0[0] ^ sg; tb[64 + lane] = q1[0] ^ sg; tb[128 + lane] = q0[2] ^ sg; tb[192 + lane] = q1[2] ^ sg;
        tb[256 + lane] = q2[0]; tb[320 + lane] = q3[0];
        const unsigned vt = qv[0];
        const unsigned kx = __popcll(__ballot(vt == 2u)) > __popcll(__ballot(vt == 1u)) ? 8192u : 0u;
        if (lane == 0) { ctl1[0] = wl | 2u; ctl1[2] = wl; ctl1[3] = wh; ctl1[4] = kx; }
        *(LAS u32x2*)(lds + L_YB + 2560 + 256 + lane * 8) = (u32x2){0u, 0u};
        WAIT_LGKM0();
        return;
    }
#define CTL(i) ((unsigned)__builtin_amdgcn_readfirstlane((int)ctl[i]))
    const int wp = wid & 1, wq = (wid >> 1) & 1;
#define Y_OFF ((int)WS_Y + ((rt & 7) * 8 + ct) * 65536 + (wid & 3) * 16384 + (((rt >> 3) ^ (rt >> 2)) & 1) * 8192)
#define P2_DEFS \
    char* w2 = ws + kx; \
    const int sidx = idx < 28 ? xq * 28 + idx : 112 + xq * 4 + idx - 28;     \
    volatile LAS unsigned* ctl = (volatile LAS unsigned*)(lds + (h == 0 ? L_ATAB : L_CTL)); \
    const int NT = h == 0 ? 32 + KSHIFT : 32 - KSHIFT; \
    LAS float* rstat = (LAS float*)(lds + L_ATAB + 256);     \
    const int hp_ = rt * 2 + wq;                             \
    (void)sidx; (void)ctl; (void)NT; (void)rstat; (void)hp_; (void)w2
    if (__builtin_expect(wid < 4, 1)) {
        {
            const u32x4 sent4 = {SENT, SENT, SENT, SENT}, zero4 = {0u, 0u, 0u, 0u};
            const __amdgpu_buffer_rsrc_t rI = mk_rsrc(ws + WS_STATS + (size_t)blockIdx.x * (FILL_BYTES / 256), FILL_BYTES / 256);
            const __amdgpu_buffer_rsrc_t rZ = mk_rsrc(ws + (size_t)blockIdx.x * 160, 160);
            ST16_SC1(sent4, rI, tid * 16);
            if (wid < 1) ST16_SC1(sent4, rI, 4096 + tid * 16);
            if (wid == 2) ST16_SC1(zero4, rZ, lane * 16);
        }
        if (wid == 3 && idx < 8) {
            const unsigned r = (((unsigned)__builtin_amdgcn_s_memrealtime() * 2654435761u + blockIdx.x * 40503u) >> 9) & 0x1fdf80u;
            const unsigned first = (idx & 1) << 13;
            const char* pb = ws + WS_PROBE;
            unsigned va, vb, left;
            asm volatile("s_waitcnt lgkmcnt(0)\n\ts_load_dword %0, %3, %4\n\ts_load_dword %1, %3, %5\n\ts_waitcnt lgkmcnt(1)\n\t"
                         "s_nop 15\n\ts_getreg_b32 %2, hwreg(HW_REG_IB_STS, 8, 4)\n\ts_waitcnt lgkmcnt(0)"
                         : "=&s"(va), "=&s"(vb), "=&s"(left) : "s"(pb), "s"(r | first), "s"(r | (first ^ 8192u)) : "memory");
            const unsigned nearbit = (left != 0u ? first : first ^ 8192u) >> 13;
            if (lane == 0) ctl1[5] = nearbit == (unsigned)(xcd >> 2) ? 1u : 2u;
        }
        f32x4 acc[4][4];
#pragma unroll
        for (int i = 0; i < 16; ++i) acc[i >> 2][i & 3] = (f32x4){0.f, 0.f, 0.f, 0.f};
        unsigned tc = (unsigned)(size_t)(tab + wp * 64 + g * 8), tw = (unsigned)(size_t)(tab + 256 + prow(wq * 64 + n));
        unsigned ta = (unsigned)(size_t)lds + wp * STAGE_BYTES + wq * 8192 + lane * 16;
        constexpr int NIL = 1 << 30;
        __amdgpu_buffer_rsrc_t rW = mk_rsrc(ws, 18u << 20);
        int sk = (int)WS_AT + at_panel(h * 8 + ct) + at_tile((rt * 2 + wp) * 128, (ct & 4) << 11) + wq * 16384 + lane * 16;
        int st = lane != 0 ? NIL : (int)WS_FLAG1 + (1 - wp) * 32768 + h * 16384 + (rt >> 3) * 8192 + ct * 1024 + (rt & 7) * 128 + wq * 4;
        int so = Y_OFF + lane * 16;
        unsigned sl = (unsigned)(size_t)lds + L_ST + (g != (n >> 2) ? 4096 + wid * 2048 + lane * 16 : wp * 2048 + wq * 1024 + n * 16);
        int sf = lane != 0 ? NIL : (int)WS_FLAGS + (rt * 8 + ct) * 128 + wid * 4;
        u32x4 sel[2], aw[2];
        unsigned one[2];
#pragma unroll
        for (int i = 0; i < 4; ++i) {
            one[i & 1] = g == (n >> 2) && (i & 1) == ((n >> 1) & 1) ? ((n & 1) ? 0x3F800000u : 0x00003F80u) : 0u;
            aw[0][i] = aw[1][i] = (n & 3) == 0 ? 0x3F803F80u : 0u;
        }
        if ((n & 3) == 1) {
            const bf16_t* wr = a.w_r + ct * BT + wp * 64 + g * 8;
            aw[0] = *(const u32x4*)wr; aw[1] = *(const u32x4*)(wr + 32);
        }
        unsigned ea[2];
#pragma unroll
        for (int mp = 0; mp < 2; ++mp)
            ea[mp] = (unsigned)(size_t)lds + L_YB + (wq ? 16384 : 4096) + n * 256 + (((wp * 8 + mp * 4 + g) ^ n) * 16);
        asm volatile("" : "+v"(tc), "+v"(tw), "+v"(ta), "+v"(sk), "+v"(st), "+v"(so), "+v"(sl), "+v"(sf), "+v"(one[0]), "+v"(one[1]), "+v"(ea[0]), "+v"(ea[1]));
        asm volatile("" : "+s"(rW));
        unsigned tg;
        bf16x8 e0[4], e1[4];
        auto extra = [&]() __attribute__((always_inline)) {
            bf16x8 ef[4][2];
#pragma unroll
            for (int i = 0; i < 8; ++i)
                ef[i >> 1][i & 1] = *(const LAS bf16x8*)((const LAS unsigned char*)(size_t)(ea[i & 1] + (i < 6 || wq ? 0 : 3 * STAGE_BYTES - L_YB - 16384)) + (i >> 1) * 4096);
            SB();
#pragma unroll
            for (int i = 0; i < 16; ++i)
                acc[i & 3][i >> 2] = __builtin_amdgcn_mfma_f32_16x16x32_bf16(__builtin_bit_cast(bf16x8, sel[i & 1]), ef[i >> 2][(i >> 1) & 1], acc[i & 3][i >> 2], 0, 0, 0);
        };
        if (wid == 0) STAMP(0);
        compute_loop<false, true>(lds, acc, NT1, wp, wq, lane, [&]() { if (wid == 0) STAMP(24); WAIT_VM0(); if (wid == 0) STAMP(31); }, e0, e1, 0u);
        if (wid == 0) STAMP(25);
        {
            f32x4 rd[4], mr[4];
            float wsm[4];
#pragma unroll
            for (int m = 0; m < 4; ++m) {
                rd[m] = *(const LAS f32x4*)((const LAS float*)(size_t)tc + (m >> 1) * 32 + (m & 1) * 4);
                mr[m] = *(const LAS f32x4*)((const LAS float*)(size_t)tc + 128 + (m >> 1) * 32 + (m & 1) * 4);
                wsm[m] = *((const LAS float*)(size_t)tw + (m >> 1) * 32 + (m & 1) * 4);
            }
            const unsigned kv = ctl1[4];
            tg = ctl1[0];
            SB();
            sk += (int)kv; st += (int)kv; so += (int)kv; sf += (int)kv;
            kx = (unsigned)__builtin_amdgcn_readfirstlane((int)kv);
#pragma unroll
            for (int nn = 0; nn < 4; ++nn)
#pragma unroll
                for (int mp = 0; mp < 2; ++mp) {
                    const f32x4 u = acc[2 * mp][nn] * rd[2 * mp] - mr[2 * mp] * wsm[nn];
                    const f32x4 v = acc[2 * mp + 1][nn] * rd[2 * mp + 1] - mr[2 * mp + 1] * wsm[nn];
                    u32x4 w;
                    w[0] = pk_bf16(u[0], u[1]); w[1] = pk_bf16(u[2], u[3]);
                    w[2] = pk_bf16(v[0], v[1]); w[3] = pk_bf16(v[2], v[3]);
                    ST16_SC1(w, rW, sk + (nn * 2 + mp) * 1024);
                    *(LAS u32x4*)((LAS unsigned char*)(size_t)ta + (nn * 2 + mp) * 1024) = w;
                }
        }
        if (wid == 0) STAMP(26);
        WAIT_LGKM0();
        BARRIER();
        SB();
        {
#pragma unroll
            for (int i = 0; i < 16; ++i) { acc[i >> 2][i & 3] = (f32x4){0.f, 0.f, 0.f, 0.f}; asm volatile("" : "+v"(acc[i >> 2][i & 3])); }
            sel[0] = (u32x4){one[0], one[1], 0u, 0u}; sel[1] = (u32x4){0u, 0u, one[0], one[1]};
            asm volatile("" : "+v"(st), "+v"(so), "+v"(sf), "+v"(tg), "+v"(sel[0]), "+v"(sel[1]));
            SB();
            if (h != 0) extra();
            SB();
            WAIT_VM(0);
            __builtin_amdgcn_raw_buffer_store_b32(tg, rW, st, 0, 16);
            SB();
            if (wid == 0) STAMP(27);
        }
        compute_loop<true, false>(lds, acc, h == 0 ? 32 + KSHIFT : 32 - KSHIFT, wp, wq, lane, [&]() {
            if (wid == 0) { STAMP(h == 0 ? 1 : 19); CSTAMP(22); }
        }, e0, e1, (unsigned)((wp * 8192 + 2 * STAGE_BYTES) ^ (L_YB + wid * 8192)));
        P2_DEFS;
        if (wid == 0) { STAMP(h == 0 ? 2 : 12); CSTAMP(23); }
        u32x4 hq[4][2];
        auto cvt = [&](int nn, int j) __attribute__((always_inline)) {
            const f32x4 c = acc[j][nn];
            hq[nn][j >> 1][(j & 1) * 2] = pk_bf16(c[0], c[1]);
            hq[nn][j >> 1][(j & 1) * 2 + 1] = pk_bf16(c[2], c[3]);
        };
        int hs = h;
        asm volatile("" : "+s"(hs));
        if (__builtin_expect(hs == 0, 1)) {
            f32x4 d1[4], d2[4];
            u32x3 sv;
            auto add = [&](int nn, int j) __attribute__((always_inline)) {
                acc[j][nn] = __builtin_amdgcn_mfma_f32_16x16x32_bf16(__builtin_bit_cast(bf16x8, sel[j & 1]), j < 2 ? e0[nn] : e1[nn], acc[j][nn], 0, 0, 0);
            };
            auto sum = [&](int nn, int j) __attribute__((always_inline)) {
                const bf16x8 hf = __builtin_bit_cast(bf16x8, hq[nn][j >> 1]);
                const f32x4 z = {0.f, 0.f, 0.f, 0.f};
                if (j & 1) d1[nn] = __builtin_amdgcn_mfma_f32_16x16x32_bf16(__builtin_bit_cast(bf16x8, aw[j >> 1]), hf, j < 2 ? z : d1[nn], 0, 0, 0);
                else d2[nn] = __builtin_amdgcn_mfma_f32_16x16x32_bf16(hf, hf, j < 2 ? z : d2[nn], 0, 0, 0);
            };
            auto out = [&](int nn, int j) __attribute__((always_inline)) {
                if (j == 1) asm volatile("" : "+v"(d2[nn]));
                if (j == 2) asm volatile("" : "+v"(d1[nn]));
                if (j == 1) sv[1] = __float_as_uint(sel4(n, d2[nn][0], d2[nn][1], d2[nn][2], d2[nn][3]));
                if (j == 2) { const float u0 = d1[nn][0], u1 = d1[nn][1]; sv[0] = __float_as_uint(u0); sv[2] = __float_as_uint(u1); }
                if (j == 3) *(LAS u32x3*)((LAS unsigned char*)(size_t)sl + nn * 256) = sv;
            };
#define FOUR(m, v) _Pragma("unroll") for (int j = 0; j < 4; ++j) { m; SB(); v; SB(); }
            FOUR(add(0, j), (void)0) FOUR(add(1, j), cvt(0, j)) FOUR(sum(0, j), cvt(1, j)) FOUR(add(2, j), out(0, j))
            FOUR(sum(1, j), cvt(2, j)) FOUR(add(3, j), out(1, j)) FOUR(sum(2, j), cvt(3, j)) FOUR(sum(3, j), out(2, j))
            FOUR((void)0, out(3, j))
#undef FOUR
            WAIT_LGKM0();
            BARRIER();
            if (wid == 0) STAMP(3);
#pragma unroll
            for (int nn = 0; nn < 4; ++nn)
#pragma unroll
                for (int mp = 0; mp < 2; ++mp)
                    *(LAS u32x4*)(lds + L_HT + (wq * 64 + nn * 16 + n) * 256 + (((wp * 8 + mp * 4 + g) ^ n) * 16)) = hq[nn][mp];
            WAIT_LGKM0();
            BARRIER();
            BARRIER();
            if (wid == 0) STAMP(6);
            const int crow = __builtin_amdgcn_readfirstlane((int)((volatile LAS unsigned*)(lds + L_ATAB))[2 + wq]) + wq * 64;
            const __amdgpu_buffer_rsrc_t rOut = mk_rsrc(a.out + (size_t)rt * BT * D_LEN, BT * D_LEN * 2);
#pragma unroll 2
            for (int j = 0; j < 8; ++j) {
                const int r = wq * 64 + wp * 32 + j * 4 + g;
                const float m_ = rstat[r * 2], d_ = rstat[r * 2 + 1];
                const u32x4 hh = *(const LAS u32x4*)(lds + L_HT + r * 256 + ((n ^ (r & 15)) * 16));
                u32x4 ov;
#pragma unroll
                for (int i = 0; i < 4; ++i) {
                    const unsigned u = hh[i];
                    const float h0 = bflo(u), h1 = bfhi(u);
                    const unsigned dd = pk_bf16(h0 - m_, h1 - m_);
                    const unsigned q = pk_bf16(bflo(dd) * d_, bfhi(dd) * d_);
                    ov[i] = pk_bf16(h0 + bflo(q), h1 + bfhi(q));
                }
                __builtin_amdgcn_raw_buffer_store_b128(ov, rOut, r == crow ? BT * D_LEN * 2 : (r * D_LEN + ct * BT + n * 8) * 2, 0, 0);
            }
            FLUSH();
            return;
        }
#pragma unroll
        for (int i = 0; i < 8; ++i) {
            cvt(i >> 1, (i & 1) * 2); cvt(i >> 1, (i & 1) * 2 + 1);
            __builtin_amdgcn_raw_buffer_store_b128(hq[i >> 1][i & 1], rW, so, i * 1024, 16);
            SB();
        }
        WAIT_VM0();
        __builtin_amdgcn_raw_buffer_store_b32(1u, rW, sf, 0, 16);
        if (wid == 0) STAMP(13);
    } else {
        LAS unsigned char* l0 = lds + (wid - 4) * 4096;
        P2_DEFS;
        __amdgpu_buffer_rsrc_t rD = mk_rsrc(h == 0 ? a.wd + (size_t)sidx * 32 * D_LEN : a.wg,
                                                  sidx >= 112 ? 0 : h != 0 ? D_LEN * F_LEN * 2 : wid < 8 - BQ ? 32 * D_LEN * 2 : 0);
        int vGe = h == 0 ? wd_off(wid & 3, 0, lane)
                : (((sidx & 7) * 128 + (wid & 3) * 16 + (lane >> 5)) * F_LEN + (sidx >> 3) * 256 + ((((lane & 31) >> 1) ^ (lane >> 5)) * 16) + (lane & 1) * 8) * 2;
        auto load_wd = [&](int i) __attribute__((always_inline)) {
            dma_nt(rD, (unsigned)(size_t)lds + (i < 8 ? L_WD0 : L_WD1) + (wid & 3) * 8192 + (i & 7) * 1024, vGe ^ ((2 * (i & 1) | (i & 4)) * 32), i * (4 * D_LEN));
        };
        const int lw = wid - 4;
        constexpr int NIL = 1 << 30;
        ring.rP = ring.rF = mk_rsrc(w2, (unsigned)WS_STATS);
        ring.voP[0] += (int)WS_AT + at_panel(h * 8 + ct);
        ring.voP[1] = ring.voP[2] = ring.voP[3] = ring.voP[0];
#define FLINE(k) (((k) >> 3) * 8192 + ((k) & 7) * 128)
        const int ft = (g & 1) * 32768 + (g >> 1) * 4 + (int)WS_FLAG1 + (lw == 0 ? ct * 1024 : NIL);
        int vall = FLINE(n) + ft + h * 16384;
        ring.voF = (n >> 2) == xq ? vall : NIL;
        ring.fl = lds + L_YB + (h == 0 ? lw * 8192 : lw ? 768 : 0) + 2560 + 256;
        unsigned sa = (unsigned)(size_t)lds + L_YB + 2560 + 256 + lane * 4;
        const unsigned tg = (unsigned)__builtin_amdgcn_readfirstlane((int)ctl1[0]);
        float wsum = __uint_as_float(ctl1[1]);
        ring.lb = lw * 4096;
        int cq[3], cp[3];
        unsigned mk[3];
#pragma unroll
        for (int k = 0; k < 3; ++k) {
            const int nb = xq * 4 + NBR(k + 1);
            cq[k] = nb * 256; cp[k] = at_tile(cq[k], ring.par); mk[k] = 0x10000u << nb;
            asm volatile("" : "+s"(cq[k]), "+s"(cp[k]), "+s"(mk[k]));
        }
        unsigned m2 = (mk[0] | mk[1] | mk[2]) >> 16;
        asm volatile("" : "+s"(m2), "+s"(ring.rP));
        asm volatile("" : "+v"(vall), "+v"(ring.voF), "+v"(ring.voP[0]), "+v"(sa), "+v"(wsum));
        ring.rF = ring.rP;
        ring.voP[1] = ring.voP[2] = ring.voP[3] = ring.voP[0];
        WAIT_VM(4);
        BARRIER();
        SB();
        int ve = Y_OFF + lane * 16;
        unsigned yd = (unsigned)(size_t)lds + L_YB + lw * 8192, fa = 0u;
        auto fetch = [&]() __attribute__((always_inline)) {
            if (wid == 4) STAMP(21);
            const int v4[4] = {ve, ve, ve, ve};
#pragma unroll
            for (int k = 0; k < 2; ++k) dma_4k<true>(ring.rP, yd + k * 4096, v4, k * 4096);
        };
        __amdgpu_buffer_rsrc_t rQ2 = mk_rsrc(a.ci + (size_t)(rt * 4 + lw) * 32 * K, 32 * K * 2);
        int vC2 = ((ct & 1) * 16 + (lane >> 2)) * (K * 2) + ((xq * 1024 + 2048 + (ct >> 1) * 512 + (lane & 3) * 128) & 4095);
        int vT2 = h == 0 && n < (KSHIFT + 1) / 2 ? FLINE((xq * 4 - 1 - n) & 15) + ft + 16384 : NIL;
        int vY = lane == 0 ? (int)WS_FLAGS + (rt * 8 + ct) * 128 + lw * 4 : NIL;
        int n1 = (h == 0 ? 32 : NT) - 9, n0 = n1 - 1 - (C2_START + 2 * ct + (idx & 1));
        int xm = (idx & 1) * 128;
        int c8 = (xq * 1024 + 1024) & 4095, p8 = at_tile(c8 ^ xm, ring.par);
        int cK = (xq * 1024 + (32 - KSHIFT) * 128) & 4095, pK = at_tile(cK ^ xm, ring.par) + at_panel(8);
        auto early = [&](int st, int i0, int i1) __attribute__((always_inline)) {
#pragma unroll
            for (int i = i0; i < i1; ++i)
                dma_nt(rD, (unsigned)(size_t)lds + (st * 4 + lw) * 8192 + i * 1024, vGe ^ ((2 * (i & 1) | (i & 4)) * 32), i * (4 * F_LEN) + st * (128 * F_LEN));
        };
        asm volatile("" : "+v"(vGe));
        asm volatile("" : "+s"(rQ2), "+s"(n0), "+s"(n1), "+s"(c8), "+s"(p8), "+s"(cK), "+s"(pK), "+s"(xm), "+s"(rD));
        asm volatile("" : "+v"(ve), "+v"(vC2), "+v"(vT2), "+v"(vY));
        unsigned have = 0u;
        SB();
#define GATE(m) do { if (__builtin_expect(((m) & ~have) != 0u, 0)) ring.gate(tg, sa, m, have); SB(); } while (0)
#define TILE(k, o, st) do { ring.sp = cp[k] + (o) * 256; ring.sq = cq[k] + (o); ring.template issueP<16>(l0 + (st) * STAGE_BYTES); \
                            ring.issueQ(l0 + (st) * STAGE_BYTES); ring.sample(); SB(); } while (0)
        ring.gate(tg, sa, mk[0], have);
        if (wid == 4) STAMP(28);
        ring.sp = cp[0]; ring.template issueP<16>(l0 + 2 * STAGE_BYTES); ring.sample();
        BARRIER();
        GATE(mk[1]);
        ring.sp = cp[1]; ring.template issueP<16>(l0 + 3 * STAGE_BYTES); ring.sample();
        GATE(mk[2]);
        TILE(2, 0, 0);
        WAIT_VM(15); BARRIER();
        if (wid == 4) STAMP(30);
        GATE(m2);
        ring.voF = vall;
        TILE(0, 128, 1);
        WAIT_VM(19); BARRIER();
        TILE(1, 128, 2);
        WAIT_VM(19); BARRIER();
        ring.sp = cp[2] + 128 * 256; ring.template issueP<16>(l0 + 3 * STAGE_BYTES);
        ring.sq = cq[2] + 128;
        SB();
        {
            const unsigned long long m = __ballot(ring.peek(sa) == tg);
            have = (unsigned)m & (unsigned)(m >> 32);
            if (__builtin_expect(have != 0xffffffffu, 0)) ring.gate(tg, sa, 0xffffffffu, have);
        }
        ring.fl -= 256;
        ring.voF = vT2;
        SB();
        ring.issueQ(l0 + 3 * STAGE_BYTES);
#undef GATE
#undef TILE
        ring.sq = c8; ring.sp = p8; ring.poff = 0; ring.xm = xm;
        if (wid == 4) { STAMP(29); CSTAMP(34); STAMP(36); }
        ring.template steady<true, false, 18>(n1, n0, vC2);
        ring.fl += 512;
        if (wid == 4) { CSTAMP(35); STAMP(37); }
        ring.template issueP<16>(lds + ring.lb);
        SB();
        {
            const unsigned long long m = __ballot(ring.peek(sa + 256) == tg);
            have = (unsigned)m & (unsigned)(m >> 32);
            asm volatile("" : "+s"(have));
        }
        int hs = h;
        asm volatile("" : "+s"(hs));
        if (__builtin_expect(hs == 0, 1)) {
            constexpr unsigned MK = ((1u << ((KSHIFT + 1) / 2)) - 1u) * 0x10001u;
            if (__builtin_expect((MK & ~have) != 0u, 0)) ring.gate(tg, sa + 256, MK, have);
            unsigned lq = ring.lb + TILE_BYTES, pp = ring.par | at_panel(8);
            int sq = ring.sq ^ ring.xm, cnt = KSHIFT;
            ring.lb = (ring.lb + STAGE_BYTES) & (RING_BYTES - 1);
            ring.fl -= 512;
            fa = (unsigned)(size_t)ring.fl;
            asm volatile("" : "+s"(lq), "+s"(sq), "+s"(cnt), "+s"(ring.lb), "+s"(pp), "+s"(yd));
            asm volatile("" : "+v"(fa));
            SB();
            dma_4k<false>(ring.rQ, lq, ring.voQ, sq);
            ring.voF = vY;
            ring.rQ = rQ2;
            ring.sq = cK; ring.sp = pK; ring.poff = 0; ring.par = pp;
            ring.template steady<true, true>(cnt);
            auto seen = [&]() __attribute__((always_inline)) { return __builtin_amdgcn_readfirstlane(ring.peek(fa)) == 1u; };
            if (__builtin_expect(seen(), 1)) {
                fetch();
                WAIT_VM(16); BARRIER();
                WAIT_VM(8); BARRIER();
            } else {
                ring.sample();
                WAIT_VM(10); BARRIER();
                const bool have = seen();
                if (have) { fetch(); WAIT_VM(9); } else { ring.sample(); WAIT_VM(2); }
                BARRIER();
                if (!have) {
#pragma unroll 1
                    for (int spin = 0; spin <= SPIN_CAP && !seen(); ++spin) { ring.sample(); WAIT_VM(0); }
                    fetch();
                }
            }
            if (wid == 4) STAMP(20);
            WAIT_VM0();
        } else {
            ring.issueQ(lds + ring.lb);
            WAIT_VM(16); BARRIER();
            early(0, 0, G_N);
            wait_vm<8 + G_N>(); BARRIER();
            early(1, 0, G2_N);
            wait_vm<G_N + G2_N>(); BARRIER();
            early(1, G2_N, G2_N + G3_N);
        }
        BARRIER();
        if (h == 0) {
            const int slot = sidx;
            LAS unsigned char* abuf = lds + L_ATAB + 2048 + lw * 128;
#pragma unroll
            for (int i = 0; i < WD_EARLY; ++i) load_wd(i);
            SB();
            __amdgpu_buffer_rsrc_t rS = mk_rsrc(w2 + WS_STATS + (hp_ & 15) * 16384 + (hp_ >> 4) * 8192, 8192);
            __amdgpu_buffer_rsrc_t rC = mk_rsrc(w2 + WS_CAND + (size_t)hp_ * 4096, 4096);
            __amdgpu_buffer_rsrc_t rKy = mk_rsrc(w2 + WS_KEYS + (hp_ >> 2) * KEY_S + (hp_ & 3) * 4, ct == 0 ? 8192 + 3 * KEY_S * 16 + 4 : 0);
            int vs = lane * 16, vk = !(lane & 1) ? 1 << 20 : (lane >> 2) * 8192 + (lane & 3) * (KEY_S * 16);
            int vc = (lane < 32 ? 2048 : 0) + ct * 256 + (lane & 31) * 8;
            unsigned code = 0xffffu - (unsigned)(hp_ * 64 + lane), hb = (lane & 31) * 8;
            unsigned ra = (unsigned)(size_t)(rstat + (wq * 64 + lane) * 2), ht = (unsigned)(size_t)lds + L_HT + wq * 16384;
            u64 lo = 0xFFFFFFFFull;
            unsigned sr = (unsigned)(size_t)lds + L_ST + wq * 1024 + lane * 16;
            int vw = wp ? NIL : ct * 1024 + lane * 16;
            asm volatile("" : "+v"(sr), "+v"(vw));
            asm volatile("" : "+s"(rS), "+s"(rC), "+s"(rKy), "+s"(ht), "+s"(lo));
            asm volatile("" : "+v"(vs), "+v"(vk), "+v"(vc), "+v"(code), "+v"(hb), "+v"(ra));
            __builtin_amdgcn_s_setprio(TAIL_PRIO);
            SB();
            BARRIER();
            if (wp == 0) {
                const f32x4 u = *(const LAS f32x4*)(size_t)sr, v = *(const LAS f32x4*)((const LAS unsigned char*)(size_t)sr + 2048);
                const u32x4 w = {min(__float_as_uint(u[0] + v[0]), SENT - 1u), min(__float_as_uint(u[1] + v[1]), SENT - 1u), min(__float_as_uint(u[2] + v[2]), SENT - 1u), 0u};
                ST16_SC1(w, rS, vw);
            }
            SB();
            BARRIER();
            if (lane < 16) *(LAS unsigned*)(abuf + 64 + lane * 4) = 0u;
            __builtin_amdgcn_s_sleep(STAT_DELAY);
            if (wp == 0) {
                float s1, s2, s3;
#pragma unroll 1
                for (int spin = 0;; ++spin) {
                    asm volatile("" ::: "memory");
                    u32x3 sv[8];
#pragma unroll
                    for (int j = 0; j < 8; ++j) { sv[j] = __builtin_amdgcn_raw_buffer_load_b96(rS, vs, j * 1024, 16); SB(); }
#pragma unroll
                    for (int j = 0; j < 8; ++j) {
                        const unsigned u0 = sv[j][0], u1 = sv[j][1], u2 = sv[j][2];
                        s1 = j ? s1 + __uint_as_float(u0) : __uint_as_float(u0);
                        s2 = j ? s2 + __uint_as_float(u1) : __uint_as_float(u1);
                        s3 = j ? s3 + __uint_as_float(u2) : __uint_as_float(u2);
                    }
                    float z = s1 + s3;
                    asm volatile("" : "+v"(z));
                    if (__builtin_expect(__builtin_amdgcn_ballot_w64(__builtin_isunordered(z, s2)) == 0, 1)) break;
                    unsigned mx = 0u;
#pragma unroll
                    for (int j = 0; j < 8; ++j) mx = max(max(mx, sv[j][0]), max(sv[j][1], sv[j][2]));
                    if (__all(mx != SENT) || spin > SPIN_CAP) break;
                }
                if (wid == 4) STAMP(4);
                const float mean = s1 * (1.0f / D_LEN);
                const float var = fmaxf(s2 * (1.0f / D_LEN) - mean * mean, 0.f);
                const float mb = rbf(mean), rden = __builtin_amdgcn_rcpf(rbf(__builtin_amdgcn_sqrtf(rbf(rbf(var) + 1e-5f))));
                const unsigned kb = wave_max_s(r2key((s3 - mb * wsum) * rden, code));
                __builtin_amdgcn_raw_buffer_store_b32(kb, rKy, vk, 0, 16);
                SB();
                {
                    const unsigned cr_ = ~kb & 63u;
                    u32x2 c = *(const LAS u32x2*)(size_t)(ht + cr_ * 256 + (hb ^ ((cr_ & 15) * 16)));
                    const float m_ = __int_as_float(__builtin_amdgcn_readlane(__float_as_int(mb), cr_));
                    const float d_ = __int_as_float(__builtin_amdgcn_readlane(__float_as_int(rden), cr_));
#pragma unroll
                    for (int i = 0; i < 2; ++i) {
                        const unsigned u = c[i];
                        const unsigned dd = pk_bf16(bflo(u) - m_, bfhi(u) - m_);
                        const unsigned q = pk_bf16(bflo(dd) * d_, bfhi(dd) * d_);
                        c[i] = min(__float_as_uint(pick(lo, __uint_as_float(u), __uint_as_float(q))), SENT - 1u);
                    }
                    __builtin_amdgcn_raw_buffer_store_b64(c, rC, vc, 0, 16);
                    SB();
                    *(LAS u32x2*)(size_t)ra = (u32x2){__float_as_uint(mb), __float_as_uint(rden)};
                    if (lane == 0) ctl[2 + wq] = cr_;
                }
                WAIT_LGKM0();
                if (wid == 4) STAMP(5);
            }
            BARRIER();
#pragma unroll
            for (int i = WD_EARLY; i < 16; ++i) load_wd(i);
            if (slot < 112 && wid < 8 - BQ) {
                WAIT_VM0();
                if (wid == 4) STAMP(7);
                stage2(w2, slot, lw, lds + L_WD0 + lw * 8192, lds + L_WD1 + lw * 8192, abuf, 64, lane);
                if (wid == 4) STAMP(11);
            }
            FLUSH();
            return;
        }
    }

    P2_DEFS;
    LAS float* red = (LAS float*)(lds + L_RED);
    if (sidx < 112) {
        const int slot = sidx, grp = slot >> 3, kq = slot & 7;
        const __amdgpu_buffer_rsrc_t rG = mk_rsrc(a.wg, D_LEN * F_LEN * 2), rU = mk_rsrc(a.wu, D_LEN * F_LEN * 2);
        auto pair = [&](int i) {
            const int d = wid * 16 + i * 2 + (lane >> 5), ky = (d & 3) | ((d >> 1) & 4), c = lane & 31;
            const int vgu = ((kq * 128 + d) * F_LEN + grp * 256 + (((c >> 1) ^ ky) * 16) + (c & 1) * 8) * 2;
            if (i >= (wid < 4 ? G_N : G2_N + G3_N)) dma_nt(rG, (unsigned)(size_t)lds + wid * 8192 + i * 1024, vgu, 0);
            dma_nt(rU, (unsigned)(size_t)lds + 65536 + wid * 8192 + i * 1024, vgu ^ 512, 0);
        };
        if (wid >= 4) {
            if (W_PRE) __builtin_amdgcn_s_sleep(W_PRE_SL);
#pragma unroll
            for (int i = 0; i < W_PRE; ++i) pair(i);
        }
        BARRIER();
        if (wid < 4) {
#pragma unroll
            for (int i = 0; i < W_PRE; ++i) pair(i);
        }
#pragma unroll
        for (int i = W_PRE; i < 8; ++i) pair(i);
        if (wid >= 8 - BQ) {
            const __amdgpu_buffer_rsrc_t rD = mk_rsrc(a.wd + (size_t)slot * 32 * D_LEN, 32 * D_LEN * 2);
            const int vd = wd_off(wid - 4, 0, lane);
#pragma unroll
            for (int i = 0; i < 16; ++i)
                dma_nt(rD, (unsigned)(size_t)lds + L_YB + (wid & 1) * 16384 + i * 1024, vd ^ ((2 * (i & 1) | (i & 4)) * 32), i * (4 * D_LEN));
        }
        const __amdgpu_buffer_rsrc_t rK = mk_rsrc(w2, wid == 0 ? (unsigned)(WS_CAND + 32 * 4096) : 0u);
        const int ok_ = lane < 40 ? (int)WS_KEYS + (xcd >> 2) * 8192 + (xcd & 3) * (KEY_S * 16) + (lane - 32) * KEY_S : (int)(WS_CAND + 32 * 4096);
        const int oc = (int)WS_CAND + 2048 + kq * 256 + n * 16;
        int best = 0;
        u32x4 v = {0u, 0u, 0u, 0u};
        auto request = [&]() {
            asm volatile("" ::: "memory");
            v = LD16_SC1(rK, lane < 32 ? oc + best * 4096 : ok_);
            SB();
        };
        auto settled = [&]() {
            const unsigned mx = umax4(v), mn = min(min(v[0], v[1]), min(v[2], v[3]));
            unsigned fv = lane < 32 ? ~mx : mn | (unsigned)(lane >> 3 ^ 4);
            asm volatile("" : "+v"(fv));
            const bool fine = fv != 0u;
            unsigned km = max(mx, dppu<0xB1>(mx));
            km = max(km, dppu<0x4E>(km));
            km = max(km, dppu<0x141>(km));
            const int nb = (int)(((0xffffu - ((unsigned)__builtin_amdgcn_readlane((int)km, 32) & 0xffffu)) >> 6) & 31u);
            const bool same = nb == best;
            best = nb;
            return __all(fine) && same;
        };
        if (wid == 0) {
            request();
            WAIT_VM(0);
            settled();
            request();
        } else if (wid >= 8 - BQ) WAIT_VM(16);
        else WAIT_VM(0);
        BARRIER();
        if (wid == 0) STAMP(8);
        bf16x8 wfr[4][4];
        {
            const int q = (lane >> 2) & 3, ky = q | ((g & 1) << 2);
#pragma unroll
            for (int u = 0; u < 4; ++u) {
                const int tro = (u >> 1) * 65536 + (8 * g + q) * 512 + (((wid * 2 + (u & 1)) ^ ky) * 32) + (lane & 3) * 8;
#pragma unroll
                for (int ks = 0; ks < 4; ++ks) {
                    const bf16x4 w0 = __builtin_amdgcn_ds_read_tr16_b64_v4i16((LAS bf16x4*)(lds + tro + ks * 16384));
                    const bf16x4 w1 = __builtin_amdgcn_ds_read_tr16_b64_v4i16((LAS bf16x4*)(lds + tro + ks * 16384 + 2048));
                    wfr[u][ks] = __builtin_shufflevector(w0, w1, 0, 1, 2, 3, 4, 5, 6, 7);
                }
            }
            SB();
#pragma unroll
            for (int i = 0; i < 16; ++i) asm volatile("" : "+v"(wfr[i >> 2][i & 3]));
        }
        if (wid >= 8 - BQ && lane < 16) *(LAS unsigned*)(lds + 8192 + wid * 64 + lane * 4) = 0u;
        __amdgpu_buffer_rsrc_t rPo = mk_rsrc(w2 + WS_PRE, 112 * 2048);
        int po = (n & 3) == 0 ? pre_off((grp ^ (g >> 1)) * 8 + wid) + kq * 256 + g * 64 + n * 4 : 112 * 2048;
        unsigned xw = (unsigned)(size_t)lds + (n >> 3) * 16384 + (g & 1) * 8192 + (n & 7) * 8, xr = (unsigned)(size_t)lds + (n & 1) * 8192 + g * 16;
        unsigned ix = (n & 1) ? 0xEEEEEEEEu : 0x44444444u;
        asm volatile("" : "+v"(po), "+v"(xw), "+v"(xr), "+v"(ix));
        u64 m1 = 0xFFFF0000FFFF0000ull, m2 = 0xFFFFFFFF00000000ull;
        f32x4 ac[4];
#pragma unroll
        for (int u = 0; u < 4; ++u) { ac[u] = (f32x4){0.f, 0.f, 0.f, 0.f}; asm volatile("" : "+v"(ac[u])); }
        asm volatile("" : "+s"(rPo), "+s"(m1), "+s"(m2));
        if (wid == 0) {
#pragma unroll 1
            for (int spin = 0; spin <= SPIN_CAP && !settled(); ++spin) {
                __builtin_amdgcn_s_sleep(1);
                request();
            }
            __builtin_amdgcn_s_waitcnt(0x0F70);
            if (lane < 32) *(LAS u32x2*)(size_t)xw = (u32x2){lane < 16 ? v[0] : v[1], lane < 16 ? v[2] : v[3]};
            WAIT_LGKM0();
            STAMP(9);
        }
        BARRIER();
        {
            bf16x8 xf[2];
#pragma unroll
            for (int j = 0; j < 2; ++j) xf[j] = *(const LAS bf16x8*)((const LAS unsigned char*)(size_t)xr + j * 16384);
            SB();
            float rs[3];
#pragma unroll
            for (int i = 0; i < 8; ++i) {
                const int u = i >> 1, j = i & 1;
                ac[u] = smf(xf[j], wfr[u][2 * j], wfr[u][2 * j + 1], ac[u], ix);
                SB();
                if (i == 4) { asm volatile("" : "+v"(ac[0])); rs[0] = ac[0][0] + ac[0][1]; asm volatile("" : "+v"(rs[0])); }
                if (i == 6) { asm volatile("" : "+v"(ac[1])); rs[0] = pick(m1, rs[0], ac[1][0] + ac[1][1]); asm volatile("" : "+v"(rs[0])); }
                if (i == 7) { asm volatile("" : "+v"(ac[2])); rs[2] = ac[2][0] + ac[2][1]; asm volatile("" : "+v"(rs[2])); }
                SB();
            }
            const unsigned e = min(__float_as_uint(pick(m2, rs[0], pick(m1, rs[2], ac[3][0] + ac[3][1]))), SENT - 1u);
            const u32x4 sv = {e, dppu<0x55>(e), dppu<0xAA>(e), dppu<0xFF>(e)};
            ST16_SC1(sv, rPo, po);
            SB();
            asm volatile("" :: "v"(v));
        }
        if (wid == 0) STAMP(14);
        if (wid >= 8 - BQ) {
            WAIT_VM(1);
            if (wid == 7) STAMP(15);
            stage2(w2, slot, wid - 4, lds + L_YB + (wid & 1) * 16384, lds + L_YB + (wid & 1) * 16384 + 8192, lds + wid * 64, 8192, lane);
            if (wid == 7) STAMP(10);
        }
    } else {
        const int rj = sidx - 112;
        if (blockIdx.x == 255 && tid == 0) {
            const u64 want = ((u64)ctl1[3] << 32) | ctl1[2];
            __hip_atomic_store((GAS u64*)(ws + WS_TAG), want, __ATOMIC_RELAXED, __HIP_MEMORY_SCOPE_AGENT);
        }
        if (wid == 4) {
            unsigned kk;
            const unsigned k = poll_winner(w2, xcd, lane, kk);
            if (lane == 0) ctl[4] = k;
            if (lane < 32) ctl[8 + lane] = kk;
            WAIT_LGKM0();
        }
        BARRIER();
        const unsigned kw = CTL(4);
        if (wid == 0) STAMP(16);
        {
            const unsigned kc = ctl[8 + wid * 4 + g];
            const __amdgpu_buffer_rsrc_t rL = mk_rsrc(w2 + WS_CAND + (size_t)wid * 16384, 16384);
            u32x2 hv2, nv2;
            for (int spin = 0;; ++spin) {
                asm volatile("" ::: "memory");
                hv2 = __builtin_amdgcn_raw_buffer_load_b64(rL, g * 4096 + (rj * 64 + n * 4) * 2, 0, 16);
                nv2 = __builtin_amdgcn_raw_buffer_load_b64(rL, g * 4096 + 2048 + (rj * 64 + n * 4) * 2, 0, 16);
                if (__all(max(max(hv2[0], hv2[1]), max(nv2[0], nv2[1])) != SENT) || spin > SPIN_CAP) break;
                __builtin_amdgcn_s_sleep(1);
            }
            u32x2 ov;
#pragma unroll
            for (int i = 0; i < 2; ++i) {
                const unsigned u = hv2[i], q = nv2[i];
                ov[i] = pk_bf16(bflo(u) + bflo(q), bfhi(u) + bfhi(q));
            }
            const int crow = (int)(0xffffu - (kc & 0xffffu)) & (S_LEN - 1);
            if (kc != kw) *(u32x2*)(a.out + (size_t)crow * D_LEN + rj * 64 + n * 4) = ov;
        }
        const int wrow = (int)(0xffffu - (kw & 0xffffu)) & (S_LEN - 1);
        const __amdgpu_buffer_rsrc_t rC = mk_rsrc(w2 + WS_CAND + (size_t)(wrow >> 6) * 4096, 4096);
        const __amdgpu_buffer_rsrc_t rPt = mk_rsrc(w2 + WS_PART, 112 * 4096);
        const int ocol = (rj >> 2) * 256 + ((lane >> 1) & 1) * 128 + (rj & 3) * 32 + (lane & 1) * 16 + (lane >> 2);
        unsigned hv, hnv;
        for (int spin = 0;; ++spin) {
            asm volatile("" ::: "memory");
            hv = __builtin_amdgcn_raw_buffer_load_b32(rC, (ocol & ~1) * 2, 0, 16);
            hnv = __builtin_amdgcn_raw_buffer_load_b32(rC, 2048 + (ocol & ~1) * 2, 0, 16);
            if (__all(max(hv, hnv) != SENT) || spin > SPIN_CAP) break;
        }
        float sgm = rbf(1.0f / (1.0f + __expf(-key2r(kw))));
        float hf = (ocol & 1) ? bfhi(hv) : bflo(hv), hnf = (ocol & 1) ? bfhi(hnv) : bflo(hnv);
        GAS unsigned short* optr = (GAS unsigned short*)(a.out + (size_t)wrow * D_LEN + ocol);
        unsigned rw = (unsigned)(size_t)red + (wid >> 2) * 1024 + (n * 4 + g) * 16 + (wid & 3) * 4, rr = (unsigned)(size_t)red + lane * 16;
        int po = wid * 16384 + (rj >> 3) * 8192 + g * 2048 + ((rj & 7) * 64 + n * 4) * 4;
        int po3 = po + 3 * 131072;
        asm volatile("" : "+v"(sgm), "+v"(optr), "+v"(hf), "+v"(hnf), "+v"(rw), "+v"(rr), "+v"(po), "+v"(po3));
        float sm;
#pragma unroll 1
        for (int spin = 0;; ++spin) {
            asm volatile("" ::: "memory");
            u32x4 pv[4];
#pragma unroll
            for (int i = 0; i < 4; ++i) pv[i] = __builtin_amdgcn_raw_buffer_load_b128(rPt, i < 3 ? po : po3, i < 3 ? i * 131072 : 0, 16);
            SB();
            unsigned t[4];
#pragma unroll
            for (int j = 0; j < 4; ++j) {
                const unsigned u0 = pv[0][j], u1 = pv[1][j], u2 = pv[2][j], u3 = pv[3][j];
                t[j] = __float_as_uint(((__uint_as_float(u0) + __uint_as_float(u1)) + __uint_as_float(u2)) + __uint_as_float(u3));
            }
            const auto r0 = __builtin_amdgcn_permlane32_swap(t[0], t[2], false, false), r1 = __builtin_amdgcn_permlane32_swap(t[1], t[3], false, false);
            const unsigned a0 = r0[0], a1 = r0[1], b0 = r1[0], b1 = r1[1];
            const auto r2 = __builtin_amdgcn_permlane16_swap(__float_as_uint(__uint_as_float(a0) + __uint_as_float(a1)),
                                                             __float_as_uint(__uint_as_float(b0) + __uint_as_float(b1)), false, false);
            const unsigned c0 = r2[0], c1 = r2[1];
            sm = __uint_as_float(c0) + __uint_as_float(c1);
            if (__builtin_expect(__builtin_amdgcn_ballot_w64(sm != sm) == 0, 1)) break;
            if (__all(max(max(umax4(pv[0]), umax4(pv[1])), max(umax4(pv[2]), umax4(pv[3]))) != SENT) || spin > SPIN_CAP) break;
            if (FIN_SLEEP) __builtin_amdgcn_s_sleep(FIN_SLEEP);
        }
        if (wid == 0) STAMP(17);
        *(LAS float*)(size_t)rw = sm;
        WAIT_LGKM0();
        BARRIER();
        if (wid == 0) {
            const f32x4 u = *(const LAS f32x4*)(size_t)rr, v = *(const LAS f32x4*)((const LAS unsigned char*)(size_t)rr + 1024);
            const f32x4 w = u + v;
            const float t = (w[0] + w[2]) + (w[1] + w[3]);
            *optr = (unsigned short)pk_bf16(hf + rbf(hnf + rbf(rbf(t) * sgm)), 0.f);
            STAMP(18);
            FLUSH();
            asm volatile("s_endpgm" ::: "memory");
        }
        __builtin_amdgcn_s_waitcnt(0x0F70);
    }
    FLUSH();
}

extern "C" void kernel_launch(void* const* d_in, const int* in_sizes, int n_in, void* d_out, int out_size, void* d_ws,
                              size_t ws_size, hipStream_t stream) {
    Args a;
    a.x = (const bf16_t*)d_in[0];
    a.cr = (const bf16_t*)d_in[1];
    a.ci = (const bf16_t*)d_in[2];
    a.wre = (const bf16_t*)d_in[3];
    a.wim = (const bf16_t*)d_in[4];
    a.w_r = (const bf16_t*)d_in[6];
    a.wg = (const bf16_t*)d_in[7];
    a.wu = (const bf16_t*)d_in[8];
    a.wd = (const bf16_t*)d_in[9];
    a.out = (bf16_t*)d_out;
    a.ws = (char*)d_ws + ((ws_size - ((size_t)24 << 20)) & ~(size_t)255);
    (void)hipFuncSetAttribute((const void*)fused_kernel, hipFuncAttributeMaxDynamicSharedMemorySize, LDS_BYTES);
    fused_kernel<<<256, 768, LDS_BYTES, stream>>>(a);
}
```

```cpp
#include <hip/hip_runtime.h>
#include <hip/hip_bf16.h>
#include <stdint.h>

typedef unsigned short bf16_t;
typedef short bf16x8 __attribute__((ext_vector_type(8)));
typedef short bf16x4 __attribute__((ext_vector_type(4)));
typedef float f32x4 __attribute__((ext_vector_type(4)));
typedef unsigned u32x4 __attribute__((ext_vector_type(4)));
typedef unsigned u32x2 __attribute__((ext_vector_type(2)));
typedef unsigned u32x3 __attribute__((ext_vector_type(3)));
typedef unsigned long long u64;
#define LAS __attribute__((address_space(3)))
#define GAS __attribute__((address_space(1)))

constexpr int S_LEN = 2048, D_LEN = 1024, F_LEN = 3584;
constexpr unsigned SENT = 0xFFFFFFFFu;
constexpr int SPIN_CAP = 1 << 17;

constexpr size_t WS_KEYS = 0;
constexpr size_t WS_FLAGS = 16384;
constexpr size_t WS_XRD = 65536;
constexpr size_t WS_FLAG1 = 196608;
constexpr size_t WS_TAG = 131072;
constexpr size_t WS_VOTE = WS_TAG + 4096;
constexpr size_t WS_PROBE = (size_t)20 << 20;
constexpr size_t WS_AT = (size_t)4 << 20;
constexpr size_t WS_Y = (size_t)12 << 20;
constexpr size_t WS_STATS = (size_t)16 << 20;
constexpr size_t WS_PART = WS_STATS + (256u << 10);
constexpr size_t WS_CAND = WS_STATS + (704u << 10);
constexpr size_t WS_PRE = WS_STATS + (832u << 10);
constexpr size_t FILL_BYTES = (size_t)1072 << 10;

__device__ __forceinline__ float bflo(unsigned v) { return __uint_as_float(v << 16); }
__device__ __forceinline__ float bfhi(unsigned v) { return __uint_as_float(v & 0xffff0000u); }
__device__ __forceinline__ unsigned pk_bf16(float lo, float hi) {
    typedef __bf16 bf2 __attribute__((ext_vector_type(2)));
    typedef float f2 __attribute__((ext_vector_type(2)));
    f2 v = {lo, hi};
    bf2 r = __builtin_convertvector(v, bf2);
    return __builtin_bit_cast(unsigned, r);
}
__device__ __forceinline__ float rbf(float x) { return __uint_as_float(pk_bf16(0.f, x)); }

template <int CTRL> __device__ __forceinline__ unsigned dppu(unsigned v) {
    return (unsigned)__builtin_amdgcn_update_dpp(0, (int)v, CTRL, 0xf, 0xf, true);
}
template <int CTRL> __device__ __forceinline__ float dppf(float v) { return __uint_as_float(dppu<CTRL>(__float_as_uint(v))); }
__device__ __forceinline__ void swap16(unsigned v, unsigned& a, unsigned& b) {
    auto r = __builtin_amdgcn_permlane16_swap(v, v, false, false);
    a = r[0]; b = r[1];
}
__device__ __forceinline__ void swap32(unsigned v, unsigned& a, unsigned& b) {
    auto r = __builtin_amdgcn_permlane32_swap(v, v, false, false);
    a = r[0]; b = r[1];
}
__device__ __forceinline__ float sum_rows(float v) {
    unsigned a, b;
    swap16(__float_as_uint(v), a, b); v = __uint_as_float(a) + __uint_as_float(b);
    swap32(__float_as_uint(v), a, b); v = __uint_as_float(a) + __uint_as_float(b);
    return v;
}
__device__ __forceinline__ float wave_sum(float v) {
    v += dppf<0xB1>(v);
    v += dppf<0x4E>(v);
    v += dppf<0x141>(v);
    v += dppf<0x140>(v);
    return sum_rows(v);
}
__device__ __forceinline__ unsigned wave_max_u32(unsigned v) {
    v = max(v, dppu<0xB1>(v));
    v = max(v, dppu<0x4E>(v));
    v = max(v, dppu<0x141>(v));
    v = max(v, dppu<0x140>(v));
    unsigned a, b;
    swap16(v, a, b); v = max(a, b);
    swap32(v, a, b); v = max(a, b);
    return v;
}
__device__ __forceinline__ unsigned wave_max_s(unsigned v) {
    v = max(v, dppu<0xB1>(v));
    v = max(v, dppu<0x4E>(v));
    v = max(v, dppu<0x141>(v));
    v = max(v, dppu<0x140>(v));
    v = max(v, dppu<0x142>(v));
    v = max(v, dppu<0x143>(v));
    return (unsigned)__builtin_amdgcn_readlane((int)v, 63);
}
__device__ __forceinline__ float sel4(int g, float a, float b, float c, float d) {
    const float t0 = (g & 1) ? b : a, t1 = (g & 1) ? d : c;
    return (g & 2) ? t1 : t0;
}
__device__ __forceinline__ float pick(u64 m, float a, float b) {
    float r;
    asm("v_cndmask_b32 %0, %1, %2, %3" : "=v"(r) : "v"(a), "v"(b), "s"(m));
    return r;
}
__device__ __forceinline__ unsigned umax4(u32x4 v) { return max(max(v[0], v[1]), max(v[2], v[3])); }

__device__ __forceinline__ void touch_line(const void* p, unsigned lds_dst) {
    unsigned keep;
    asm volatile("s_mov_b32 %0, m0\n\ts_mov_b32 m0, %2\n\ts_nop 0\n\tglobal_load_lds_dword %1, off\n\ts_mov_b32 m0, %0"
                 : "=&s"(keep) : "v"(p), "s"(lds_dst) : "memory");
}
__device__ __forceinline__ __amdgpu_buffer_rsrc_t mk_rsrc(const void* p, unsigned bytes) {
    return __builtin_amdgcn_make_buffer_rsrc((void*)p, 0, (int)bytes, 0x00020000);
}
#define LDS_LOAD(w, v, o, pol) "buffer_load_" w " " v ", %0, %2 offen" o pol " lds"
#define LDS_M0 "s_mov_b32 m0, %1\n\ts_nop 0\n\t"
__device__ __forceinline__ void dma_nt(__amdgpu_buffer_rsrc_t r, unsigned dst, int voff, int soff) {
    asm volatile(LDS_M0 LDS_LOAD("dwordx4", "%3", "", " nt") :: "s"(r), "s"(dst), "s"(soff), "v"(voff) : "memory", "m0");
}
template <bool SC1> __device__ __forceinline__ void dma_word(__amdgpu_buffer_rsrc_t r, unsigned dst, int voff) {
    if constexpr (SC1) asm volatile(LDS_M0 LDS_LOAD("dword", "%3", "", " sc1") :: "s"(r), "s"(dst), "n"(0), "v"(voff) : "memory", "m0");
    else asm volatile(LDS_M0 LDS_LOAD("dword", "%3", "", "") :: "s"(r), "s"(dst), "n"(0), "v"(voff) : "memory", "m0");
}
template <bool SC1> __device__ __forceinline__ void dma_2k(__amdgpu_buffer_rsrc_t r, unsigned dst, int v0, int v1, int soff) {
    if constexpr (SC1) asm volatile(LDS_M0 LDS_LOAD("dwordx4", "%3", "", " sc1") "\n\t" LDS_LOAD("dwordx4", "%4", " offset:1024", " sc1")
                 :: "s"(r), "s"(dst), "s"(soff), "v"(v0), "v"(v1) : "memory", "m0");
    else asm volatile(LDS_M0 LDS_LOAD("dwordx4", "%3", "", "") "\n\t" LDS_LOAD("dwordx4", "%4", " offset:1024", "")
                 :: "s"(r), "s"(dst), "s"(soff), "v"(v0), "v"(v1) : "memory", "m0");
}
template <bool SC1> __device__ __forceinline__ void dma_4k(__amdgpu_buffer_rsrc_t r, unsigned dst, const int (&v)[4], int soff) {
#define FOUR(pol) asm volatile(LDS_M0 LDS_LOAD("dwordx4", "%3", "", pol) "\n\t" LDS_LOAD("dwordx4", "%4", " offset:1024", pol) "\n\t" \
        LDS_LOAD("dwordx4", "%5", " offset:2048", pol) "\n\t" LDS_LOAD("dwordx4", "%6", " offset:3072", pol) \
        :: "s"(r), "s"(dst), "s"(soff), "v"(v[0]), "v"(v[1]), "v"(v[2]), "v"(v[3]) : "memory", "m0")
    if constexpr (SC1) FOUR(" sc1"); else FOUR("");
#undef FOUR
}
#define CODE_BYTES 25472
__device__ __forceinline__ __amdgpu_buffer_rsrc_t code_rsrc() {
    unsigned long long p; unsigned d;
    asm volatile("s_getpc_b64 %0\n1:\n\ts_mov_b32 %1, 1b-_Z12fused_kernel4Args" : "=s"(p), "=s"(d));
    return __builtin_amdgcn_make_buffer_rsrc((void*)(p - d), 0, CODE_BYTES, 0x00020000);
}
#define LD16_SC1(rs, off) __builtin_amdgcn_raw_buffer_load_b128(rs, off, 0, 16)
#define ST16_SC1(v, rs, off) __builtin_amdgcn_raw_buffer_store_b128(v, rs, off, 0, 16)

constexpr int BT = 128, BK = 64, NSTAGE = 4;
constexpr int TILE_BYTES = BT * BK * 2;
constexpr int STAGE_BYTES = 2 * TILE_BYTES;
constexpr int RING_BYTES = NSTAGE * STAGE_BYTES;
constexpr int L_DUMMY = RING_BYTES;
constexpr int L_CTL = RING_BYTES + 256;
constexpr int L_RED = RING_BYTES + 512;

#define WAIT_VM(n) asm volatile("s_waitcnt vmcnt(" #n ")" ::: "memory")
#define WAIT_VM0() do { asm volatile("" ::: "memory"); __builtin_amdgcn_s_waitcnt(0x0F70); asm volatile("s_waitcnt vmcnt(0)" ::: "memory"); } while (0)
#define WAIT_LGKM0() asm volatile("s_waitcnt lgkmcnt(0)" ::: "memory")
#define BARRIER() do { asm volatile("" ::: "memory"); __builtin_amdgcn_s_barrier(); asm volatile("" ::: "memory"); } while (0)

__device__ __forceinline__ int prow(int rho) {
    const int w = rho >> 6, m = (rho >> 4) & 3, q = (rho >> 2) & 3, r = rho & 3;
    return w * 64 + (m >> 1) * 32 + q * 8 + (m & 1) * 4 + r;
}
__device__ __forceinline__ unsigned r2key(float r, unsigned code) {
    const unsigned u = pk_bf16(0.f, r);
    return ((u ^ ((unsigned)((int)u >> 31) | 0x80000000u)) & 0xffff0000u) | code;
}
__device__ __forceinline__ float key2r(unsigned k) {
    const unsigned o = k >> 16;
    return bflo((o & 0x8000u) ? (o & 0x7fffu) : (~o & 0xffffu));
}
#define STAT_M 5
#define BQ 2
#define POLL_T (STAT_M + 5)
#define AUX_SLEEP 12
#define WD_EARLY 4
#define C2_START 2
#define TAIL_PRIO 3
#define STAT_DELAY 8
#define AUX_STAY 1
#define CATCH1 4
#define CPF_MASK1 0x1080u
#define CPF_MASK2 0x1080u
#define CPF_STAG 5
#define CPF_FIX 0x1000u
#define KEY_S 128
#define TH_LIM 6
#define TH_N 6
#define FIN_SLEEP 1
#define POLL_SLEEP 4
__device__ __forceinline__ f32x4 smf(bf16x8 a, bf16x8 b0, bf16x8 b1, f32x4 d, unsigned idx) {
    typedef __bf16 bf8 __attribute__((ext_vector_type(8)));
    typedef __bf16 bf16v __attribute__((ext_vector_type(16)));
    const auto b = __builtin_shufflevector(b0, b1, 0, 1, 2, 3, 4, 5, 6, 7, 8, 9, 10, 11, 12, 13, 14, 15);
    return __builtin_amdgcn_smfmac_f32_16x16x64_bf16(__builtin_bit_cast(bf8, a), __builtin_bit_cast(bf16v, b), d, (int)idx, 0, 0);
}
__device__ __forceinline__ float dot2bf(unsigned a, unsigned b, float c) {
    typedef __bf16 bf2 __attribute__((ext_vector_type(2)));
    return __builtin_amdgcn_fdot2_f32_bf16(__builtin_bit_cast(bf2, a), __builtin_bit_cast(bf2, b), c, false);
}

#define STAMP(i) do { } while (0)
#define CSTAMP(i) do { } while (0)
#define FLUSH() do { } while (0)
#define TSV do { } while (0)

#define KSHIFT 4

constexpr int L_HT = ((32 + KSHIFT) % NSTAGE) * STAGE_BYTES;
constexpr int L_WD0 = ((33 + KSHIFT) % NSTAGE) * STAGE_BYTES;
constexpr int L_WD1 = ((34 + KSHIFT) % NSTAGE) * STAGE_BYTES;
constexpr int L_ST = ((35 + KSHIFT) % NSTAGE) * STAGE_BYTES;
constexpr int L_YB = RING_BYTES;
constexpr int L_ATAB = L_YB;
constexpr int LDS_BYTES = RING_BYTES + 32768;

#define SB() __builtin_amdgcn_sched_barrier(0)
template <int N> __device__ __forceinline__ void wait_vm() { asm volatile("s_waitcnt vmcnt(%0)" :: "n"(N) : "memory"); }
__device__ __forceinline__ int at_tile(int c, int par) { return ((c << 8) & 0x78000) | (((c << 2) & 0x2000) ^ par); }
__device__ __forceinline__ int at_panel(int ep) { return ep << 19; }
__device__ __forceinline__ int pre_off(int slot) { const int s_ = slot >= 56, l = slot - 56 * s_; return (l >> 2) * 16384 + s_ * 8192 + (l & 3) * 2048; }
static_assert(KSHIFT == 4, "flag samples must have replaced older contents of their slot; even tile counts");
struct Ring {
    __amdgpu_buffer_rsrc_t rP, rQ, rF;
    int voP[4], voQ[4], voF;
    LAS unsigned char* fl;
    unsigned lb;
    int sp, sq;
    int poff, par, xm;
    bool img;
    template <int LD, bool PIMG> __device__ __forceinline__ void setup(int lw, int lane) {
        const int r = lane >> 3, v0 = (PIMG ? r : (r & 3) + (r & 4) * 2) * (LD * 2) + (((lane & 7) ^ (lane >> 4)) << 4);
#pragma unroll
        for (int j = 0; j < 4; ++j) {
            voQ[j] = (v0 ^ ((j & 1) * 64)) + (PIMG ? j * 8 : (j & 1) * 16 + (j >> 1) * 4) * (LD * 2) - j * 1024;
            voP[j] = PIMG ? (lw >> 1) * 16384 + (lw & 1) * 4096 + lane * 16 : voQ[j];
        }
        img = PIMG;
    }
    __device__ __forceinline__ void seek(int col, int pof) {
        sq = col; poff = pof;
        sp = (img ? at_tile(sq, par) : sq) + poff;
    }
    __device__ __forceinline__ void step() {
        sq = (sq + BK * 2) & 4095;
        sp = (img ? at_tile(sq ^ xm, par) : sq) + poff;
        asm volatile("" : "+s"(sq), "+s"(sp));
    }
    __device__ __forceinline__ void sample() { dma_word<true>(rF, (unsigned)(size_t)fl, voF); }
    __device__ __forceinline__ unsigned peek(unsigned addr) {
        unsigned v;
        asm volatile("ds_read_b32 %0, %1\n\ts_waitcnt lgkmcnt(0)" : "=v"(v) : "v"(addr) : "memory");
        return v;
    }
    __device__ __forceinline__ void gate(unsigned want, unsigned sa, unsigned need, unsigned& have) {
#pragma unroll 1
        for (int spin = 0; spin <= SPIN_CAP && (need & ~have) != 0u; ++spin) {
            if (spin) { sample(); WAIT_VM(0); __builtin_amdgcn_s_sleep(1); }
            const unsigned long long m = __ballot(peek(sa) == want);
            have = (unsigned)m & (unsigned)(m >> 32);
        }
    }
    template <int AUX> __device__ __forceinline__ void issueP(LAS unsigned char* base) { dma_4k<AUX != 0>(rP, (unsigned)(size_t)base, voP, sp); }
    __device__ __forceinline__ void issueQ(LAS unsigned char* base) { dma_4k<false>(rQ, (unsigned)(size_t)base + TILE_BYTES, voQ, sq ^ xm); }
    template <bool P2> __device__ __forceinline__ void issue(LAS unsigned char* lds) {
        issueP<P2 ? 16 : 0>(lds + lb);
        issueQ(lds + lb);
        SB();
        if (P2) sample();
        lb = (lb + STAGE_BYTES) & (RING_BYTES - 1);
        asm volatile("" : "+s"(lb));
        step();
        SB();
    }
    template <int CAT> __device__ __forceinline__ void ramp(LAS unsigned char* lds) {
#pragma unroll 1
        for (int i = 1; i <= CAT + 1; ++i) {
            issue<false>(lds);
            if (i == CAT + 1) continue;
            WAIT_VM(8);
            BARRIER();
        }
    }
    template <bool P2, bool SAMP, int W0 = 16> __device__ __forceinline__ void steady(int cnt, int trig = 0, int v2 = 0) {
        int t, u;
#define LD(v, r, s, o) "buffer_load_dwordx4 " v ", " r ", " s " offen" o " lds\n\t"
        if constexpr (P2) {
#define BODY(SMP, TST, ENT) asm volatile("s_mov_b32 m0, %0\n" ENT "1:\n\ts_waitcnt vmcnt(16)\n2:\n\ts_barrier\n\t" \
            LD("%8", "%6", "%2", " sc1") LD("%8", "%6", "%2", " offset:1024 sc1") LD("%8", "%6", "%2", " offset:2048 sc1") LD("%8", "%6", "%2", " offset:3072 sc1") \
            SMP "s_xor_b32 %4, %1, %18\n\ts_add_i32 %1, %1, 0x80\n\ts_add_i32 m0, %0, 0x4000\n\ts_and_b32 %1, %1, 0xfff\n\t" \
            LD("%9", "%7", "%4", "") "s_xor_b32 %2, %1, %18\n\ts_lshl_b32 %5, %1, 2\n\ts_lshl_b32 %2, %2, 8\n\ts_and_b32 %2, %2, 0x78000\n\t" \
            LD("%10", "%7", "%4", " offset:1024") "s_and_b32 %5, %5, 0x2000\n\ts_xor_b32 %5, %5, %13\n\ts_or_b32 %2, %2, %5\n\ts_add_i32 %3, %3, -1\n\t" \
            LD("%11", "%7", "%4", " offset:2048") "s_add_i32 %0, %0, 0x8000\n\ts_and_b32 %0, %0, 0x1ffff\n\t" TST "s_cmp_lg_u32 %3, 0\n\t" \
            LD("%12", "%7", "%4", " offset:3072") "\n4:\n\ts_mov_b32 m0, %0\n\ts_cbranch_scc1 1b\n\ts_waitcnt vmcnt(16)\n\ts_barrier" \
            : "+s"(lb), "+s"(sq), "+s"(sp), "+s"(cnt), "=&s"(t), "=&s"(u) \
            : "s"(rP), "s"(rQ), "v"(voP[0]), "v"(voQ[0]), "v"(voQ[1]), "v"(voQ[2]), "v"(voQ[3]), "s"(par | poff), "s"(rF), "v"(voF), "s"((unsigned)(size_t)fl), "n"(W0), "s"(xm), \
              "s"(trig), "v"(v2) \
            : "memory", "scc", "m0")
            if constexpr (SAMP) BODY("s_mov_b32 m0, %16\n\ts_nop 0\n\tbuffer_load_dword %15, %14, 0 offen sc1 lds\n\t", "", "");
            else BODY("", "s_cmp_eq_u32 %3, %19\n\ts_cbranch_scc1 3b\n\t",
                      "\ts_waitcnt vmcnt(%17)\n\ts_branch 2f\n3:\n\t" LD("%12", "%7", "%4", " offset:3072") "s_mov_b32 m0, %16\n\ts_nop 0\n\tbuffer_load_dword %20, %7, 0 offen lds\n\t"
                      "s_add_i32 m0, %16, 0x200\n\ts_cmp_lg_u32 %3, 0\n\tbuffer_load_dword %15, %14, 0 offen sc1 lds\n\ts_branch 4f\n");
#undef BODY
        } else {
            asm volatile("s_mov_b32 m0, %0\n\ts_branch 2f\n1:\n\ts_waitcnt vmcnt(16)\n\ts_barrier\n2:\n\t"
                LD("%6", "%4", "%1", "") LD("%7", "%4", "%1", " offset:1024") LD("%8", "%4", "%1", " offset:2048") LD("%9", "%4", "%1", " offset:3072")
                "s_add_i32 m0, %0, 0x4000\n\ts_mov_b32 %3, %1\n\t"
                LD("%6", "%5", "%3", "") "s_add_i32 %1, %1, 0x80\n\ts_add_i32 %0, %0, 0x8000\n\t"
                LD("%7", "%5", "%3", " offset:1024") "s_and_b32 %0, %0, 0x1ffff\n\ts_add_i32 %2, %2, -1\n\t"
                LD("%8", "%5", "%3", " offset:2048") "s_cmp_lg_u32 %2, 0\n\t"
                LD("%9", "%5", "%3", " offset:3072") "s_mov_b32 m0, %0\n\ts_cbranch_scc1 1b\n\ts_waitcnt vmcnt(16)\n\ts_barrier"
                : "+s"(lb), "+s"(sq), "+s"(cnt), "=&s"(t)
                : "s"(rP), "s"(rQ), "v"(voQ[0]), "v"(voQ[1]), "v"(voQ[2]), "v"(voQ[3])
                : "memory", "scc", "m0");
            (void)u;
        }
#undef LD
    }
};

template <bool PLIN, bool FIRST, class Pre>
__device__ __forceinline__ void compute_loop(const LAS unsigned char* lds, f32x4 (&acc)[4][4], int NT, int wp, int wq, int lane, Pre pre,
                                             bf16x8 (&p0)[4], bf16x8 (&p1)[4], unsigned last) {
    const int fr = lane & 15, fq = lane >> 4, key = (fr >> 1) & 7;
    const unsigned l0 = (unsigned)(size_t)lds;
    const unsigned oP0 = l0 + (PLIN ? wp * 8192 + lane * 16 : (wp * 64 + fr) * 128 + ((fq ^ key) * 16));
    const unsigned oP1 = l0 + (PLIN ? wp * 8192 + 1024 + lane * 16 : (wp * 64 + fr) * 128 + (((4 + fq) ^ key) * 16));
    const unsigned oQ0 = l0 + TILE_BYTES + (wq * 64 + fr) * 128 + ((fq ^ key) * 16);
    const unsigned oQ1 = l0 + TILE_BYTES + (wq * 64 + fr) * 128 + (((4 + fq) ^ key) * 16);
    unsigned aP0 = oP0, aQ0 = oQ0, aP1 = oP1, aQ1 = oQ1;
    bf16x8 r0[4], r1[4], q0[4], q1[4];
    unsigned xp = 2 * STAGE_BYTES;
#pragma unroll
    for (int i = 0; i < 16; ++i) asm volatile("" : "+v"(acc[i >> 2][i & 3]));
    asm volatile("" : "+v"(aP0), "+v"(aQ0), "+v"(aP1), "+v"(aQ1));
#define MM(pp, qq, m, n) do { acc[m][n] = __builtin_amdgcn_mfma_f32_16x16x32_bf16(pp[m], qq[n], acc[m][n], 0, 0, 0); SB(); } while (0)
#define RD(dst, i, a, st) do { dst[i] = *(const LAS bf16x8*)((const LAS unsigned char*)(size_t)(a) + (st) * STAGE_BYTES + (i) * 2048); SB(); } while (0)
#define ADP(a) do { a ^= xp; asm volatile("" : "+v"(a)); SB(); } while (0)
#define ADR(a) do { a ^= 2 * STAGE_BYTES; asm volatile("" : "+v"(a)); SB(); } while (0)
    __builtin_amdgcn_s_waitcnt(0xC07F);
    SB();
    if (FIRST) BARRIER();
    SB();
    RD(p0, 0, aP0, 0); RD(q0, 0, aQ0, 0); RD(p1, 0, aP1, 0); RD(q1, 0, aQ1, 0); RD(p1, 1, aP1, 0); RD(p0, 1, aP0, 0);
    RD(p0, 2, aP0, 0); RD(p1, 2, aP1, 0); RD(p1, 3, aP1, 0); RD(p0, 3, aP0, 0);
    pre();
    SB();
    int t = NT;
#pragma unroll 1
    do {
        MM(p0, q0, 0, 0); MM(p1, q1, 0, 0); RD(q0, 1, aQ0, 0);
        MM(p1, q1, 1, 0); MM(p0, q0, 1, 0); RD(q1, 1, aQ1, 0);
        MM(p0, q0, 2, 0); MM(p1, q1, 2, 0); RD(q0, 2, aQ0, 0);
        MM(p1, q1, 3, 0); MM(p0, q0, 3, 0); RD(q1, 2, aQ1, 0);
        MM(p0, q0, 3, 1); MM(p1, q1, 3, 1); RD(q0, 3, aQ0, 0);
        MM(p1, q1, 2, 1); MM(p0, q0, 2, 1); RD(q1, 3, aQ1, 0);
        MM(p0, q0, 1, 1); MM(p1, q1, 1, 1);
        MM(p1, q1, 0, 1); MM(p0, q0, 0, 1);
        MM(p0, q0, 0, 2); MM(p1, q1, 0, 2); __builtin_amdgcn_s_waitcnt(0xC07F); SB(); BARRIER(); SB();
        MM(p1, q1, 1, 2); RD(r0, 0, aP0, 1); MM(p0, q0, 1, 2); RD(q0, 0, aQ0, 1);
        MM(p0, q0, 2, 2); RD(r1, 0, aP1, 1); MM(p1, q1, 2, 2); RD(q1, 0, aQ1, 1);
        MM(p1, q1, 3, 2); MM(p0, q0, 3, 2); RD(r1, 1, aP1, 1);
        MM(p0, q0, 3, 3); RD(r0, 1, aP0, 1); MM(p1, q1, 3, 3);
        MM(p1, q1, 2, 3); RD(r0, 2, aP0, 1); MM(p0, q0, 2, 3); RD(r1, 2, aP1, 1);
        MM(p0, q0, 1, 3); MM(p1, q1, 1, 3); RD(r1, 3, aP1, 1);
        MM(p1, q1, 0, 3); RD(r0, 3, aP0, 1); MM(p0, q0, 0, 3);

        MM(r0, q0, 0, 0); MM(r1, q1, 0, 0); RD(q0, 1, aQ0, 1);
        MM(r1, q1, 1, 0); MM(r0, q0, 1, 0); RD(q1, 1, aQ1, 1);
        MM(r0, q0, 2, 0); MM(r1, q1, 2, 0); RD(q0, 2, aQ0, 1);
        MM(r1, q1, 3, 0); MM(r0, q0, 3, 0); RD(q1, 2, aQ1, 1);
        MM(r0, q0, 3, 1); MM(r1, q1, 3, 1); RD(q0, 3, aQ0, 1);
        MM(r1, q1, 2, 1); MM(r0, q0, 2, 1); RD(q1, 3, aQ1, 1);
        MM(r0, q0, 1, 1); t -= 2; if (PLIN) xp = t == 0 ? last : 2 * STAGE_BYTES; asm volatile("" : "+s"(t), "+s"(xp)); SB(); MM(r1, q1, 1, 1); ADP(aP0);
        MM(r1, q1, 0, 1); ADP(aP1); MM(r0, q0, 0, 1); ADR(aQ0);
        MM(r0, q0, 0, 2); ADR(aQ1); MM(r1, q1, 0, 2); __builtin_amdgcn_s_waitcnt(0xC07F); SB(); BARRIER(); SB();
        MM(r1, q1, 1, 2); RD(p0, 0, aP0, 0); MM(r0, q0, 1, 2); RD(q0, 0, aQ0, 0);
        MM(r0, q0, 2, 2); RD(p1, 0, aP1, 0); MM(r1, q1, 2, 2); RD(q1, 0, aQ1, 0);
        MM(r1, q1, 3, 2); MM(r0, q0, 3, 2); RD(p1, 1, aP1, 0);
        MM(r0, q0, 3, 3); RD(p0, 1, aP0, 0); MM(r1, q1, 3, 3);
        MM(r1, q1, 2, 3); RD(p0, 2, aP0, 0); MM(r0, q0, 2, 3); RD(p1, 2, aP1, 0);
        MM(r0, q0, 1, 3); MM(r1, q1, 1, 3); RD(p1, 3, aP1, 0);
        MM(r1, q1, 0, 3); RD(p0, 3, aP0, 0); MM(r0, q0, 0, 3);
    } while (t != 0);
    SB();
#undef MM
#undef RD
#undef ADR
#undef ADP
}

struct Args {
    const bf16_t* x; const bf16_t* wre; const bf16_t* wim; char* ws; const bf16_t* cr; const bf16_t* ci;
    const bf16_t* w_r; const bf16_t* wg; const bf16_t* wu; const bf16_t* wd; bf16_t* out;
};

__device__ __forceinline__ unsigned poll_winner(const char* ws, int rep, int lane, unsigned& mine) {
    GAS unsigned* kp = (GAS unsigned*)(ws + WS_KEYS + (size_t)(rep >> 2) * 8192 + (rep & 3) * (KEY_S * 16) + ((lane >> 2) & 7) * KEY_S) + (lane & 3);
    unsigned kw;
    for (int spin = 0;; ++spin) {
            asm volatile("" ::: "memory");
        kw = __hip_atomic_load(kp, __ATOMIC_RELAXED, __HIP_MEMORY_SCOPE_AGENT);
        if (__all(kw != 0u) || spin > SPIN_CAP) break;
        __builtin_amdgcn_s_sleep(POLL_SLEEP);
    }
    mine = kw;
    return __builtin_amdgcn_readfirstlane(wave_max_u32(kw));
}

__device__ __forceinline__ void stage2(char* w2, int slot, int lw, LAS unsigned char* img0, LAS unsigned char* img1, LAS unsigned char* abuf, int zoff, int lane) {
    const int g = lane >> 4, n = lane & 15;
    const __amdgpu_buffer_rsrc_t rPi = mk_rsrc(w2 + WS_PRE + pre_off(slot), 2048);
    unsigned pp[8];
    auto request = [&]() {
        asm volatile("" ::: "memory");
#pragma unroll
        for (int k = 0; k < 8; ++k) pp[k] = __builtin_amdgcn_raw_buffer_load_b32(rPi, k * 256 + lane * 4, 0, 16);
        SB();
    };
    request();
    bf16x8 wf[16];
    {
        const int q = (lane >> 2) & 3, ky = q | ((g & 1) << 2);
#pragma unroll
        for (int j = 0; j < 16; ++j) {
            LAS unsigned char* t = (g < 2 ? img0 : img1) + (8 * (g & 1) + q) * 512 + (((j & 7) ^ ky) + (j & 8)) * 32 + (lane & 3) * 8;
            const bf16x4 w0 = __builtin_amdgcn_ds_read_tr16_b64_v4i16((LAS bf16x4*)t);
            const bf16x4 w1 = __builtin_amdgcn_ds_read_tr16_b64_v4i16((LAS bf16x4*)(t + 2048));
            wf[j] = __builtin_shufflevector(w0, w1, 0, 1, 2, 3, 4, 5, 6, 7);
        }
        SB();
#pragma unroll
        for (int j = 0; j < 16; ++j) asm volatile("" : "+v"(wf[j]));
    }
    unsigned aa[5], pto = lane * 16;
#pragma unroll
    for (int m = 0; m < 4; ++m) aa[m] = (unsigned)(size_t)abuf + ((n >> 2) == m && ((n >> 1) & 1) == (g >> 1) ? (n & 1) * 32 + (g & 1) * 16 : zoff + g * 16);
    aa[4] = (unsigned)(size_t)abuf + ((lane >> 1) & 1) * 32 + ((lane >> 4) & 1) * 16 + ((lane >> 2) & 3) * 4 + (lane & 1) * 2;
    unsigned ix = (n & 1) ? 0xEEEEEEEEu : 0x44444444u;
    __amdgpu_buffer_rsrc_t rPt = mk_rsrc(w2 + WS_PART + __builtin_amdgcn_readfirstlane((slot >> 2) * 16384 + (lw >> 1) * 8192 + (slot & 3) * 2048 + (lw & 1) * 1024), 1024);
    asm volatile("" : "+v"(aa[0]), "+v"(aa[1]), "+v"(aa[2]), "+v"(aa[3]), "+v"(aa[4]), "+v"(pto), "+v"(ix));
    asm volatile("" : "+s"(rPt));
    f32x4 d[2] = {{0.f, 0.f, 0.f, 0.f}, {0.f, 0.f, 0.f, 0.f}};
    asm volatile("" : "+v"(d[0]), "+v"(d[1]));
    SB();
    float sv;
#pragma unroll 1
    for (int spin = 0;; ++spin) {
        sv = ((__uint_as_float(pp[0]) + __uint_as_float(pp[1])) + (__uint_as_float(pp[2]) + __uint_as_float(pp[3]))) +
             ((__uint_as_float(pp[4]) + __uint_as_float(pp[5])) + (__uint_as_float(pp[6]) + __uint_as_float(pp[7])));
        if (__builtin_expect(__builtin_amdgcn_ballot_w64(sv != sv) == 0, 1)) break;
        if (__all(max(max(max(pp[0], pp[1]), max(pp[2], pp[3])), max(max(pp[4], pp[5]), max(pp[6], pp[7]))) != SENT) || spin > SPIN_CAP) break;
        request();
    }
    unsigned ug, uu;
    swap32(__float_as_uint(sv), ug, uu);
    const float gg = rbf(__uint_as_float(ug)), up = rbf(__uint_as_float(uu));
    const unsigned act = pk_bf16(rbf(gg * __builtin_amdgcn_rcpf(1.0f + __expf(-gg))) * up, 0.f);
    *(volatile LAS unsigned short*)(size_t)aa[4] = (unsigned short)act;
    bf16x8 af[4];
#pragma unroll
    for (int m = 0; m < 4; ++m) af[m] = *(volatile LAS bf16x8*)(size_t)aa[m];
    SB();
#pragma unroll
    for (int i = 0; i < 8; ++i) {
        const int j = 8 * (i & 1) + 2 * (i >> 1);
        d[i & 1] = smf(af[i >> 1], wf[j], wf[j + 1], d[i & 1], ix);
        SB();
    }
    u32x4 pv;
#pragma unroll
    for (int q = 0; q < 4; ++q) {
        pv[q] = min(__float_as_uint(d[q >> 1][(q & 1) * 2] + d[q >> 1][(q & 1) * 2 + 1]), SENT - 1u);
        asm volatile("" : "+v"(pv[q]));
    }
    ST16_SC1(pv, rPt, pto);
}
__device__ __forceinline__ int gu_off(int slot, int w, int lane) {
    const int hi = lane >> 5, c = lane & 31;
    return (((slot & 7) * 128 + 4 * w + hi) * F_LEN + (slot >> 3) * 256 + (((c >> 1) ^ hi ^ ((w & 2) << 1)) * 16) + (c & 1) * 8) * 2;
}
__device__ __forceinline__ int wd_off(int lw, int i, int lane) {
    const int r = 2 * (i & 5) + (lane >> 5), ky = (r & 3) | ((r >> 1) & 4), c = lane & 31;
    return (lane >> 5) * (D_LEN * 2) + lw * 512 + (((c >> 1) ^ ky) * 2 + (c & 1)) * 16;
}

__global__ __launch_bounds__(768, 3) void fused_kernel(const Args a) {
    extern __shared__ __attribute__((aligned(16))) unsigned char smem[];
    constexpr int K = S_LEN, NT1 = D_LEN / BK;
    const int tid = threadIdx.x, wid = __builtin_amdgcn_readfirstlane(tid >> 6), lane = tid & 63;
    const int xcd = blockIdx.x & 7, idx = blockIdx.x >> 3;
    TSV;
    LAS unsigned char* lds = (LAS unsigned char*)smem;
    if (idx < 1 && wid < 4) dma_word<false>(code_rsrc(), (unsigned)(size_t)lds + L_DUMMY, tid * 128);
    SB();
    const int h = xcd & 1, xq = xcd >> 1, rt = xq * 4 + (idx & 3), ct = idx >> 2;
    volatile LAS unsigned* ctl1 = (volatile LAS unsigned*)(lds + L_CTL);
#define NBR(k) ((rt + (k)) & 3)
    unsigned kx;
    Ring ring;
    if (__builtin_expect((wid & 12) == 4, 1)) {
        __builtin_amdgcn_s_setprio(3);
        ring.template setup<D_LEN, false>(wid - 4, lane);
        ring.rP = mk_rsrc(a.x + (size_t)(rt * 4 + wid - 4) * 32 * D_LEN, 32 * D_LEN * 2);
        ring.rQ = mk_rsrc((h == 0 ? a.wre : a.wim) + (size_t)(ct * 4 + wid - 4) * 32 * D_LEN, 32 * D_LEN * 2);
        ring.lb = (wid - 4) * 4096;
        ring.sp = ring.sq = ring.poff = ring.xm = 0;
        ring.template issue<false>(lds);
        __builtin_amdgcn_s_setprio(0);
        ring.par = (ct & 4) << 11;
        ring.template ramp<CATCH1>(lds);
        ring.template steady<false, false>(NT1 - 2 - CATCH1);
        ring.template setup<K, true>(wid - 4, lane);
        ring.rQ = mk_rsrc((h == 0 ? a.cr : a.ci) + (size_t)(rt * 4 + wid - 4) * 32 * K, 32 * K * 2);
        ring.seek(rt * 256, 0);
        LAS unsigned char* l0 = lds + (wid - 4) * 4096;
        ring.issueQ(l0); ring.step();
        WAIT_VM(12); BARRIER();
        ring.issueQ(l0 + STAGE_BYTES);
        ring.seek((xq * 4 + NBR(1)) * 256, 0);
        const int xr = (wid & 1) * 8 + (lane >> 4);
        int vx0 = ((((wid >> 1) & 1) * 64 + xr) * D_LEN + (((lane & 15) ^ xr) * 8)) * 2;
        int vx1 = ((((wid >> 1) & 1) * 64 + xr + 4) * D_LEN + (((lane & 15) ^ (xr + 4)) * 8)) * 2 - 1024;
        __amdgpu_buffer_rsrc_t rX = mk_rsrc(a.x + (size_t)rt * BT * D_LEN + ct * BT, BT * D_LEN * 2 - ct * BT * 2);
        unsigned xd = (unsigned)(size_t)lds + L_YB + ((wid & 2) ? 16384 : 4096) + (wid & 1) * 2048;
        unsigned xe = (wid & 2) ? xd + 12288 : (unsigned)(size_t)lds + 3 * STAGE_BYTES + (wid & 1) * 2048;
        asm volatile("" : "+v"(vx0), "+v"(vx1));
        asm volatile("" : "+s"(rX), "+s"(xd), "+s"(xe));
        WAIT_VM(8); BARRIER();
        ring.issueQ(l0 + 2 * STAGE_BYTES);
        ring.seek((xq * 4 + NBR(2)) * 256, 0);
        if (h != 0) {
#pragma unroll
            for (int k = 0; k < 3; ++k) dma_2k<false>(rX, xd + k * 4096, vx0, vx1, k * (16 * D_LEN * 2));
        }
        BARRIER();
        if (h != 0) dma_2k<false>(rX, xe, vx0, vx1, 3 * (16 * D_LEN * 2));
        ring.issueQ(l0 + 3 * STAGE_BYTES);
        kx = (unsigned)__builtin_amdgcn_readfirstlane((int)ctl1[4]);
        SB();
    }
    char* ws = a.ws + ((0ull - (unsigned long long)a.ws) & 0x1fffffull);
    const int g = lane >> 4, n = lane & 15;
    LAS float* tab = (LAS float*)(lds + L_RED);
    if (wid >= 8) {
        const int aw = wid - 8;
#define T1(t) do { if (aw == 1 && (t) <= 16) STAMP(40 + (t)); } while (0)
        GAS u64* tagp = (GAS u64*)(ws + WS_TAG);
        const int r0 = xq * 512 + h * 256 + idx * 8 + aw * 2;
        const int w0 = xq * 256 + idx * 8 + aw * 2;
        const u32x4* src = (const u32x4*)((n < 8 ? a.x + (size_t)(r0 + (n >> 2)) * D_LEN
                                                 : (h == 0 ? a.wre : a.wim) + (size_t)(w0 + (n >> 2) - 2) * D_LEN) + g * 32 + (n & 3) * 8);
        if (AUX_SLEEP) __builtin_amdgcn_s_sleep(AUX_SLEEP);
        u64 tag0 = __hip_atomic_load(tagp, __ATOMIC_RELAXED, __HIP_MEMORY_SCOPE_AGENT);
        u32x4 v[8];
#pragma unroll
        for (int j = 0; j < 4; ++j) v[j] = src[j * 16];
        __builtin_amdgcn_s_setprio(3);
        SB();
        const char* cb = (const char*)(h ? a.ci : a.cr) + (size_t)rt * (512u << 10) + ct * 65536 + (lane >> 2) * 4096 + (lane & 3) * 128;
        const GAS u32x4* wrp = (const GAS u32x4*)a.w_r + lane;
        asm volatile("" : "+v"(cb), "+v"(wrp));
        unsigned mask = aw == 1 ? CPF_MASK1 : aw == 2 ? CPF_MASK2 : 0u;
        mask = (mask & CPF_FIX) | ((mask & ~CPF_FIX) << (idx % CPF_STAG));
        int ti = 2 * xq + aw - 1;
        auto warm = [&](int t) { if (t >= 4 && ((mask >> t) & 1)) { touch_line(cb + (ti & 7) * 512, L_DUMMY); ti += 2; } };
        f32x4 d1 = {0.f, 0.f, 0.f, 0.f}, d2 = {0.f, 0.f, 0.f, 0.f};
        u32x4 ones = {0x3F803F80u, 0x3F803F80u, 0x3F803F80u, 0x3F803F80u};
        auto sums = [&](int j0) __attribute__((always_inline)) {
#pragma unroll
            for (int j = j0; j < j0 + 4; ++j) {
                const bf16x8 f = __builtin_bit_cast(bf16x8, v[j]);
                d1 = __builtin_amdgcn_mfma_f32_16x16x32_bf16(f, __builtin_bit_cast(bf16x8, ones), d1, 0, 0, 0);
                d2 = __builtin_amdgcn_mfma_f32_16x16x32_bf16(f, f, d2, 0, 0, 0);
            }
            asm volatile("" : "+v"(d1), "+v"(d2));
        };
        unsigned wl, wh;
        int ro;
        __amdgpu_buffer_rsrc_t rS;
        u64 m1 = 0xAAAAAAAAAAAAAAAAull, m2 = 0xCCCCCCCCCCCCCCCCull, mh = 0xFFFFFFFF00000000ull;
#pragma unroll
        for (int t = 0; t <= 5; ++t) {
            SB(); BARRIER(); SB();
            T1(t);
            if (t == 2) {
#pragma unroll
                for (int j = 4; j < 8; ++j) v[j] = src[j * 16];
            }
            if (t == 3) {
                asm volatile("" : "+v"(ones));
                sums(0);
                asm volatile("" : "+v"(tag0));
                const u64 want = tag0 * 6364136223846793005ull + 1442695040888963407ull;
                wl = (unsigned)want; wh = (unsigned)(want >> 32);
                asm volatile("" : "+v"(wl), "+v"(wh));
            }
            if (t == 4) { sums(4); asm volatile("s_nop 7\n\ts_nop 3" : "+v"(d2)); }
            if (t == 1) {
                rS = mk_rsrc(ws + WS_XRD, 65536);
                ro = n == g * 4 ? (g < 2 ? r0 + g : 2048 - 2 + h * 1024 + w0 + g) * 16 : 65536;
                asm volatile("" : "+v"(ro));
                asm volatile("" : "+s"(rS), "+s"(m1), "+s"(m2), "+s"(mh), "+s"(mask), "+s"(ti));
            }
        }
        float sm = (d1[0] + d1[1]) + (d1[2] + d1[3]);
        float sq = pick(m2, pick(m1, d2[0], d2[1]), pick(m1, d2[2], d2[3]));
        asm volatile("s_nop 1" : "+v"(sq));
        sq += dppf<0xB1>(sq);
        sq += dppf<0x4E>(sq);
        float mean = sm * (1.0f / D_LEN), var = rbf(rbf(fmaxf(sq * (1.0f / D_LEN) - mean * mean, 0.f)) + 1e-5f);
        asm volatile("" : "+v"(sm), "+v"(mean), "+v"(var));
        SB(); BARRIER(); SB();
        T1(6);
        {
            float rden = __builtin_amdgcn_rcpf(rbf(__builtin_amdgcn_sqrtf(var)));
            asm volatile("s_nop 1" : "+v"(rden));
            const u32x4 rec = {__float_as_uint(pick(mh, rden, sm)), wl, __float_as_uint(rbf(mean) * rden), wh};
            ST16_SC1(rec, rS, ro);
        }
        const __amdgpu_buffer_rsrc_t rV = mk_rsrc(ws + WS_VOTE, 512);
        if (aw == 3 && idx < 8) __builtin_amdgcn_raw_buffer_store_b64((u32x2){ctl1[5], wl}, rV, blockIdx.x * 8 + lane * 512, 0, 16);
        SB(); BARRIER(); SB();
        T1(STAT_M + 2);
        warm(STAT_M + 2);
        if (aw == 1 || aw == 2) {
#pragma unroll 1
            for (int t = STAT_M + 3; t <= NT1 + AUX_STAY; ++t) {
                BARRIER();
                T1(t);
                warm(t);
            }
            FLUSH();
            return;
        }
        if (aw == 3) {
            if (h == 0) {
                const u32x4 w0v = wrp[0], w1v = wrp[64];
                BARRIER(); BARRIER(); BARRIER(); BARRIER();
                SB();
                float t = 0.f;
#pragma unroll
                for (int i = 0; i < 4; ++i) { t = dot2bf(w0v[i], 0x3F803F80u, t); t = dot2bf(w1v[i], 0x3F803F80u, t); }
                SB(); BARRIER(); SB();
                t = wave_sum(t);
                ctl1[1] = __float_as_uint(t);
                WAIT_LGKM0();
            }
            return;
        }
#pragma unroll
        for (int t = STAT_M + 3; t <= POLL_T; ++t) BARRIER();
        SB();
        const int o0 = (rt * BT + lane) * 16, o1 = (2048 + (h * 8 + ct) * BT + lane) * 16, os = 1024;
        u32x4 q0, q1, q2, q3;
        u32x2 qv;
        auto request = [&]() {
            asm volatile("" ::: "memory");
            q0 = LD16_SC1(rS, o0); q1 = LD16_SC1(rS, o0 + os); q2 = LD16_SC1(rS, o1); q3 = LD16_SC1(rS, o1 + os);
            qv = __builtin_amdgcn_raw_buffer_load_b64(rV, lane * 8, 0, 16);
            SB();
        };
        request();
        int b = POLL_T + 1;
        for (int spin = 0; spin <= SPIN_CAP; ++spin) {
            if (b < NT1) { BARRIER(); ++b; }
            if (b < NT1) { BARRIER(); ++b; }
            SB();
            unsigned d = ((q0[1] ^ wl) | (q1[1] ^ wl)) | ((q2[1] ^ wl) | (q3[1] ^ wl)) | ((q0[3] ^ wh) | (q1[3] ^ wh)) | ((q2[3] ^ wh) | (q3[3] ^ wh)) | (qv[1] ^ wl);
            asm volatile("" : "+v"(d));
            if (__all(d == 0u)) break;
            request();
        }
        if (b < NT1) BARRIER();
        SB();
        LAS unsigned* tb = (LAS unsigned*)tab;
        const unsigned sg = h == 0 ? 0u : 0x80000000u;
        tb[lane] = q0[0] ^ sg; tb[64 + lane] = q1[0] ^ sg; tb[128 + lane] = q0[2] ^ sg; tb[192 + lane] = q1[2] ^ sg;
        tb[256 + lane] = q2[0]; tb[320 + lane] = q3[0];
        const unsigned vt = qv[0];
        const unsigned kx = __popcll(__ballot(vt == 2u)) > __popcll(__ballot(vt == 1u)) ? 8192u : 0u;
        if (lane == 0) { ctl1[0] = wl | 2u; ctl1[2] = wl; ctl1[3] = wh; ctl1[4] = kx; }
        *(LAS u32x2*)(lds + L_YB + 2560 + 256 + lane * 8) = (u32x2){0u, 0u};
        WAIT_LGKM0();
        return;
    }
#define CTL(i) ((unsigned)__builtin_amdgcn_readfirstlane((int)ctl[i]))
    const int wp = wid & 1, wq = (wid >> 1) & 1;
#define Y_OFF ((int)WS_Y + ((rt & 7) * 8 + ct) * 65536 + (wid & 3) * 16384 + (((rt >> 3) ^ (rt >> 2)) & 1) * 8192)
#define P2_DEFS \
    char* w2 = ws + kx; \
    const int sidx = idx < 28 ? xq * 28 + idx : 112 + xq * 4 + idx - 28;     \
    volatile LAS unsigned* ctl = (volatile LAS unsigned*)(lds + (h == 0 ? L_ATAB : L_CTL)); \
    const int NT = h == 0 ? 32 + KSHIFT : 32 - KSHIFT; \
    LAS float* rstat = (LAS float*)(lds + L_ATAB + 256);     \
    const int hp_ = rt * 2 + wq;                             \
    (void)sidx; (void)ctl; (void)NT; (void)rstat; (void)hp_; (void)w2
    if (__builtin_expect(wid < 4, 1)) {
        {
            const u32x4 sent4 = {SENT, SENT, SENT, SENT}, zero4 = {0u, 0u, 0u, 0u};
            const __amdgpu_buffer_rsrc_t rI = mk_rsrc(ws + WS_STATS + (size_t)blockIdx.x * (FILL_BYTES / 256), FILL_BYTES / 256);
            const __amdgpu_buffer_rsrc_t rZ = mk_rsrc(ws + (size_t)blockIdx.x * 160, 160);
            ST16_SC1(sent4, rI, tid * 16);
            if (wid < 1) ST16_SC1(sent4, rI, 4096 + tid * 16);
            if (wid == 2) ST16_SC1(zero4, rZ, lane * 16);
        }
        if (wid == 3 && idx < 8) {
            const unsigned r = (((unsigned)__builtin_amdgcn_s_memrealtime() * 2654435761u + blockIdx.x * 40503u) >> 9) & 0x1fdf80u;
            const unsigned first = (idx & 1) << 13;
            const char* pb = ws + WS_PROBE;
            unsigned va, vb, left;
            asm volatile("s_waitcnt lgkmcnt(0)\n\ts_load_dword %0, %3, %4\n\ts_load_dword %1, %3, %5\n\ts_waitcnt lgkmcnt(1)\n\t"
                         "s_nop 15\n\ts_getreg_b32 %2, hwreg(HW_REG_IB_STS, 8, 4)\n\ts_waitcnt lgkmcnt(0)"
                         : "=&s"(va), "=&s"(vb), "=&s"(left) : "s"(pb), "s"(r | first), "s"(r | (first ^ 8192u)) : "memory");
            const unsigned nearbit = (left != 0u ? first : first ^ 8192u) >> 13;
            if (lane == 0) ctl1[5] = nearbit == (unsigned)(xcd >> 2) ? 1u : 2u;
        }
        f32x4 acc[4][4];
#pragma unroll
        for (int i = 0; i < 16; ++i) acc[i >> 2][i & 3] = (f32x4){0.f, 0.f, 0.f, 0.f};
        unsigned tc = (unsigned)(size_t)(tab + wp * 64 + g * 8), tw = (unsigned)(size_t)(tab + 256 + prow(wq * 64 + n));
        unsigned ta = (unsigned)(size_t)lds + wp * STAGE_BYTES + wq * 8192 + lane * 16;
        constexpr int NIL = 1 << 30;
        __amdgpu_buffer_rsrc_t rW = mk_rsrc(ws, 18u << 20);
        int sk = (int)WS_AT + at_panel(h * 8 + ct) + at_tile((rt * 2 + wp) * 128, (ct & 4) << 11) + wq * 16384 + lane * 16;
        int st = lane != 0 ? NIL : (int)WS_FLAG1 + (1 - wp) * 32768 + h * 16384 + (rt >> 3) * 8192 + ct * 1024 + (rt & 7) * 128 + wq * 4;
        int so = Y_OFF + lane * 16;
        unsigned sl = (unsigned)(size_t)lds + L_ST + (g != (n >> 2) ? 4096 + wid * 2048 + lane * 16 : wp * 2048 + wq * 1024 + n * 16);
        int sf = lane != 0 ? NIL : (int)WS_FLAGS + (rt * 8 + ct) * 128 + wid * 4;
        u32x4 sel[2], aw[2];
        unsigned one[2];
#pragma unroll
        for (int i = 0; i < 4; ++i) {
            one[i & 1] = g == (n >> 2) && (i & 1) == ((n >> 1) & 1) ? ((n & 1) ? 0x3F800000u : 0x00003F80u) : 0u;
            aw[0][i] = aw[1][i] = (n & 3) == 0 ? 0x3F803F80u : 0u;
        }
        if ((n & 3) == 1) {
            const bf16_t* wr = a.w_r + ct * BT + wp * 64 + g * 8;
            aw[0] = *(const u32x4*)wr; aw[1] = *(const u32x4*)(wr + 32);
        }
        unsigned ea[2];
#pragma unroll
        for (int mp = 0; mp < 2; ++mp)
            ea[mp] = (unsigned)(size_t)lds + L_YB + (wq ? 16384 : 4096) + n * 256 + (((wp * 8 + mp * 4 + g) ^ n) * 16);
        asm volatile("" : "+v"(tc), "+v"(tw), "+v"(ta), "+v"(sk), "+v"(st), "+v"(so), "+v"(sl), "+v"(sf), "+v"(one[0]), "+v"(one[1]), "+v"(ea[0]), "+v"(ea[1]));
        asm volatile("" : "+s"(rW));
        unsigned tg;
        bf16x8 e0[4], e1[4];
        auto extra = [&]() __attribute__((always_inline)) {
            bf16x8 ef[4][2];
#pragma unroll
            for (int i = 0; i < 8; ++i)
                ef[i >> 1][i & 1] = *(const LAS bf16x8*)((const LAS unsigned char*)(size_t)(ea[i & 1] + (i < 6 || wq ? 0 : 3 * STAGE_BYTES - L_YB - 16384)) + (i >> 1) * 4096);
            SB();
#pragma unroll
            for (int i = 0; i < 16; ++i)
                acc[i & 3][i >> 2] = __builtin_amdgcn_mfma_f32_16x16x32_bf16(__builtin_bit_cast(bf16x8, sel[i & 1]), ef[i >> 2][(i >> 1) & 1], acc[i & 3][i >> 2], 0, 0, 0);
        };
        if (wid == 0) STAMP(0);
        compute_loop<false, true>(lds, acc, NT1, wp, wq, lane, [&]() { if (wid == 0) STAMP(24); WAIT_VM0(); if (wid == 0) STAMP(31); }, e0, e1, 0u);
        if (wid == 0) STAMP(25);
        {
            f32x4 rd[4], mr[4];
            float wsm[4];
#pragma unroll
            for (int m = 0; m < 4; ++m) {
                rd[m] = *(const LAS f32x4*)((const LAS float*)(size_t)tc + (m >> 1) * 32 + (m & 1) * 4);
                mr[m] = *(const LAS f32x4*)((const LAS float*)(size_t)tc + 128 + (m >> 1) * 32 + (m & 1) * 4);
                wsm[m] = *((const LAS float*)(size_t)tw + (m >> 1) * 32 + (m & 1) * 4);
            }
            const unsigned kv = ctl1[4];
            tg = ctl1[0];
            SB();
            sk += (int)kv; st += (int)kv; so += (int)kv; sf += (int)kv;
            kx = (unsigned)__builtin_amdgcn_readfirstlane((int)kv);
#pragma unroll
            for (int nn = 0; nn < 4; ++nn)
#pragma unroll
                for (int mp = 0; mp < 2; ++mp) {
                    const f32x4 u = acc[2 * mp][nn] * rd[2 * mp] - mr[2 * mp] * wsm[nn];
                    const f32x4 v = acc[2 * mp + 1][nn] * rd[2 * mp + 1] - mr[2 * mp + 1] * wsm[nn];
                    u32x4 w;
                    w[0] = pk_bf16(u[0], u[1]); w[1] = pk_bf16(u[2], u[3]);
                    w[2] = pk_bf16(v[0], v[1]); w[3] = pk_bf16(v[2], v[3]);
                    ST16_SC1(w, rW, sk + (nn * 2 + mp) * 1024);
                    *(LAS u32x4*)((LAS unsigned char*)(size_t)ta + (nn * 2 + mp) * 1024) = w;
                }
        }
        if (wid == 0) STAMP(26);
        WAIT_LGKM0();
        BARRIER();
        SB();
        {
#pragma unroll
            for (int i = 0; i < 16; ++i) { acc[i >> 2][i & 3] = (f32x4){0.f, 0.f, 0.f, 0.f}; asm volatile("" : "+v"(acc[i >> 2][i & 3])); }
            sel[0] = (u32x4){one[0], one[1], 0u, 0u}; sel[1] = (u32x4){0u, 0u, one[0], one[1]};
            asm volatile("" : "+v"(st), "+v"(so), "+v"(sf), "+v"(tg), "+v"(sel[0]), "+v"(sel[1]));
            SB();
            if (h != 0) extra();
            SB();
            WAIT_VM(0);
            __builtin_amdgcn_raw_buffer_store_b32(tg, rW, st, 0, 16);
            SB();
            if (wid == 0) STAMP(27);
        }
        compute_loop<true, false>(lds, acc, h == 0 ? 32 + KSHIFT : 32 - KSHIFT, wp, wq, lane, [&]() {
            if (wid == 0) { STAMP(h == 0 ? 1 : 19); CSTAMP(22); }
        }, e0, e1, (unsigned)((wp * 8192 + 2 * STAGE_BYTES) ^ (L_YB + wid * 8192)));
        P2_DEFS;
        if (wid == 0) { STAMP(h == 0 ? 2 : 12); CSTAMP(23); }
        u32x4 hq[4][2];
        auto cvt = [&](int nn, int j) __attribute__((always_inline)) {
            const f32x4 c = acc[j][nn];
            hq[nn][j >> 1][(j & 1) * 2] = pk_bf16(c[0], c[1]);
            hq[nn][j >> 1][(j & 1) * 2 + 1] = pk_bf16(c[2], c[3]);
        };
        int hs = h;
        asm volatile("" : "+s"(hs));
        if (__builtin_expect(hs == 0, 1)) {
            f32x4 d1[4], d2[4];
            u32x3 sv;
            auto add = [&](int nn, int j) __attribute__((always_inline)) {
                acc[j][nn] = __builtin_amdgcn_mfma_f32_16x16x32_bf16(__builtin_bit_cast(bf16x8, sel[j & 1]), j < 2 ? e0[nn] : e1[nn], acc[j][nn], 0, 0, 0);
            };
            auto sum = [&](int nn, int j) __attribute__((always_inline)) {
                const bf16x8 hf = __builtin_bit_cast(bf16x8, hq[nn][j >> 1]);
                const f32x4 z = {0.f, 0.f, 0.f, 0.f};
                if (j & 1) d1[nn] = __builtin_amdgcn_mfma_f32_16x16x32_bf16(__builtin_bit_cast(bf16x8, aw[j >> 1]), hf, j < 2 ? z : d1[nn], 0, 0, 0);
                else d2[nn] = __builtin_amdgcn_mfma_f32_16x16x32_bf16(hf, hf, j < 2 ? z : d2[nn], 0, 0, 0);
            };
            auto out = [&](int nn, int j) __attribute__((always_inline)) {
                if (j == 1) asm volatile("" : "+v"(d2[nn]));
                if (j == 2) asm volatile("" : "+v"(d1[nn]));
                if (j == 1) sv[1] = __float_as_uint(sel4(n, d2[nn][0], d2[nn][1], d2[nn][2], d2[nn][3]));
                if (j == 2) { const float u0 = d1[nn][0], u1 = d1[nn][1]; sv[0] = __float_as_uint(u0); sv[2] = __float_as_uint(u1); }
                if (j == 3) *(LAS u32x3*)((LAS unsigned char*)(size_t)sl + nn * 256) = sv;
            };
#define FOUR(m, v) _Pragma("unroll") for (int j = 0; j < 4; ++j) { m; SB(); v; SB(); }
            FOUR(add(0, j), (void)0) FOUR(add(1, j), cvt(0, j)) FOUR(sum(0, j), cvt(1, j)) FOUR(add(2, j), out(0, j))
            FOUR(sum(1, j), cvt(2, j)) FOUR(add(3, j), out(1, j)) FOUR(sum(2, j), cvt(3, j)) FOUR(sum(3, j), out(2, j))
            FOUR((void)0, out(3, j))
#undef FOUR
            WAIT_LGKM0();
            BARRIER();
            if (wid == 0) STAMP(3);
#pragma unroll
            for (int nn = 0; nn < 4; ++nn)
#pragma unroll
                for (int mp = 0; mp < 2; ++mp)
                    *(LAS u32x4*)(lds + L_HT + (wq * 64 + nn * 16 + n) * 256 + (((wp * 8 + mp * 4 + g) ^ n) * 16)) = hq[nn][mp];
            WAIT_LGKM0();
            BARRIER();
            BARRIER();
            if (wid == 0) STAMP(6);
            const int crow = __builtin_amdgcn_readfirstlane((int)((volatile LAS unsigned*)(lds + L_ATAB))[2 + wq]) + wq * 64;
            const __amdgpu_buffer_rsrc_t rOut = mk_rsrc(a.out + (size_t)rt * BT * D_LEN, BT * D_LEN * 2);
#pragma unroll 2
            for (int j = 0; j < 8; ++j) {
                const int r = wq * 64 + wp * 32 + j * 4 + g;
                const float m_ = rstat[r * 2], d_ = rstat[r * 2 + 1];
                const u32x4 hh = *(const LAS u32x4*)(lds + L_HT + r * 256 + ((n ^ (r & 15)) * 16));
                u32x4 ov;
#pragma unroll
                for (int i = 0; i < 4; ++i) {
                    const unsigned u = hh[i];
                    const float h0 = bflo(u), h1 = bfhi(u);
                    const unsigned dd = pk_bf16(h0 - m_, h1 - m_);
                    const unsigned q = pk_bf16(bflo(dd) * d_, bfhi(dd) * d_);
                    ov[i] = pk_bf16(h0 + bflo(q), h1 + bfhi(q));
                }
                __builtin_amdgcn_raw_buffer_store_b128(ov, rOut, r == crow ? BT * D_LEN * 2 : (r * D_LEN + ct * BT + n * 8) * 2, 0, 0);
            }
            FLUSH();
            return;
        }
#pragma unroll
        for (int i = 0; i < 8; ++i) {
            cvt(i >> 1, (i & 1) * 2); cvt(i >> 1, (i & 1) * 2 + 1);
            __builtin_amdgcn_raw_buffer_store_b128(hq[i >> 1][i & 1], rW, so, i * 1024, 16);
            SB();
        }
        WAIT_VM0();
        __builtin_amdgcn_raw_buffer_store_b32(1u, rW, sf, 0, 16);
        if (wid == 0) STAMP(13);
    } else {
        LAS unsigned char* l0 = lds + (wid - 4) * 4096;
        P2_DEFS;
        __amdgpu_buffer_rsrc_t rD = mk_rsrc(h == 0 ? a.wd + (size_t)sidx * 32 * D_LEN : a.wg,
                                                  sidx >= 112 ? 0 : h != 0 ? D_LEN * F_LEN * 2 : wid < 8 - BQ ? 32 * D_LEN * 2 : 0);
        int vGe = h == 0 ? wd_off(wid & 3, 0, lane)
                : gu_off(sidx, (wid & 3) * 2, lane);
        auto load_wd = [&](int i) __attribute__((always_inline)) {
            dma_nt(rD, (unsigned)(size_t)lds + (i < 8 ? L_WD0 : L_WD1) + (wid & 3) * 8192 + (i & 7) * 1024, vGe ^ ((2 * (i & 1) | (i & 4)) * 32), i * (4 * D_LEN));
        };
        const int lw = wid - 4;
        constexpr int NIL = 1 << 30;
        ring.rP = ring.rF = mk_rsrc(w2, (unsigned)WS_STATS);
        ring.voP[0] += (int)WS_AT + at_panel(h * 8 + ct);
        ring.voP[1] = ring.voP[2] = ring.voP[3] = ring.voP[0];
#define FLINE(k) (((k) >> 3) * 8192 + ((k) & 7) * 128)
        const int ft = (g & 1) * 32768 + (g >> 1) * 4 + (int)WS_FLAG1 + (lw == 0 ? ct * 1024 : NIL);
        int vall = FLINE(n) + ft + h * 16384;
        ring.voF = (n >> 2) == xq ? vall : NIL;
        ring.fl = lds + L_YB + (h == 0 ? lw * 8192 : lw ? 768 : 0) + 2560 + 256;
        unsigned sa = (unsigned)(size_t)lds + L_YB + 2560 + 256 + lane * 4;
        const unsigned tg = (unsigned)__builtin_amdgcn_readfirstlane((int)ctl1[0]);
        float wsum = __uint_as_float(ctl1[1]);
        ring.lb = lw * 4096;
        int cq[3], cp[3];
        unsigned mk[3];
#pragma unroll
        for (int k = 0; k < 3; ++k) {
            const int nb = xq * 4 + NBR(k + 1);
            cq[k] = nb * 256; cp[k] = at_tile(cq[k], ring.par); mk[k] = 0x10000u << nb;
            asm volatile("" : "+s"(cq[k]), "+s"(cp[k]), "+s"(mk[k]));
        }
        unsigned m2 = (mk[0] | mk[1] | mk[2]) >> 16;
        asm volatile("" : "+s"(m2), "+s"(ring.rP));
        asm volatile("" : "+v"(vall), "+v"(ring.voF), "+v"(ring.voP[0]), "+v"(sa), "+v"(wsum));
        ring.rF = ring.rP;
        ring.voP[1] = ring.voP[2] = ring.voP[3] = ring.voP[0];
        WAIT_VM(4);
        BARRIER();
        SB();
        int ve = Y_OFF + lane * 16;
        unsigned yd = (unsigned)(size_t)lds + L_YB + lw * 8192, fa = 0u;
        auto fetch = [&]() __attribute__((always_inline)) {
            if (wid == 4) STAMP(21);
            const int v4[4] = {ve, ve, ve, ve};
#pragma unroll
            for (int k = 0; k < 2; ++k) dma_4k<true>(ring.rP, yd + k * 4096, v4, k * 4096);
        };
        __amdgpu_buffer_rsrc_t rQ2 = mk_rsrc(h == 0 ? a.ci + (size_t)(rt * 4 + lw) * 32 * K : a.wu,
                                             h == 0 ? 32 * K * 2 : sidx < 112 ? D_LEN * F_LEN * 2 : 0);
        int vC2 = ((ct & 1) * 16 + (lane >> 2)) * (K * 2) + ((xq * 1024 + 2048 + (ct >> 1) * 512 + (lane & 3) * 128) & 4095);
        int vT2 = h == 0 && n < (KSHIFT + 1) / 2 ? FLINE((xq * 4 - 1 - n) & 15) + ft + 16384 : NIL;
        int vY = lane == 0 ? (int)WS_FLAGS + (rt * 8 + ct) * 128 + lw * 4 : NIL;
        int n1 = (h == 0 ? 32 : NT) - 9, n0 = n1 - 1 - (C2_START + 2 * ct + (idx & 1));
        int xm = (idx & 1) * 128;
        int c8 = (xq * 1024 + 1024) & 4095, p8 = at_tile(c8 ^ xm, ring.par);
        int cK = (xq * 1024 + (32 - KSHIFT) * 128) & 4095, pK = at_tile(cK ^ xm, ring.par) + at_panel(8);
        auto early = [&](int st, int i0, int i1) __attribute__((always_inline)) {
#pragma unroll
            for (int i = i0; i < i1; ++i) {
                const int m = i & 1, w = (i >> 1) & 1, j = i >> 2;
                dma_nt(m ? rQ2 : rD, (unsigned)(size_t)lds + st * STAGE_BYTES + m * 16384 + lw * 4096 + w * 2048 + j * 1024, vGe ^ (j * 64 + m * 512),
                       (32 * st + 4 * w + 2 * j) * (F_LEN * 2));
            }
        };
        asm volatile("" : "+v"(vGe));
        asm volatile("" : "+s"(rQ2), "+s"(n0), "+s"(n1), "+s"(c8), "+s"(p8), "+s"(cK), "+s"(pK), "+s"(xm), "+s"(rD));
        asm volatile("" : "+v"(ve), "+v"(vC2), "+v"(vT2), "+v"(vY));
        unsigned have = 0u;
        SB();
#define GATE(m) do { if (__builtin_expect(((m) & ~have) != 0u, 0)) ring.gate(tg, sa, m, have); SB(); } while (0)
#define TILE(k, o, st) do { ring.sp = cp[k] + (o) * 256; ring.sq = cq[k] + (o); ring.template issueP<16>(l0 + (st) * STAGE_BYTES); \
                            ring.issueQ(l0 + (st) * STAGE_BYTES); ring.sample(); SB(); } while (0)
        ring.gate(tg, sa, mk[0], have);
        if (wid == 4) STAMP(28);
        ring.sp = cp[0]; ring.template issueP<16>(l0 + 2 * STAGE_BYTES); ring.sample();
        BARRIER();
        GATE(mk[1]);
        ring.sp = cp[1]; ring.template issueP<16>(l0 + 3 * STAGE_BYTES); ring.sample();
        GATE(mk[2]);
        TILE(2, 0, 0);
        WAIT_VM(15); BARRIER();
        if (wid == 4) STAMP(30);
        GATE(m2);
        ring.voF = vall;
        TILE(0, 128, 1);
        WAIT_VM(19); BARRIER();
        TILE(1, 128, 2);
        WAIT_VM(19); BARRIER();
        ring.sp = cp[2] + 128 * 256; ring.template issueP<16>(l0 + 3 * STAGE_BYTES);
        ring.sq = cq[2] + 128;
        SB();
        {
            const unsigned long long m = __ballot(ring.peek(sa) == tg);
            have = (unsigned)m & (unsigned)(m >> 32);
            if (__builtin_expect(have != 0xffffffffu, 0)) ring.gate(tg, sa, 0xffffffffu, have);
        }
        ring.fl -= 256;
        ring.voF = vT2;
        SB();
        ring.issueQ(l0 + 3 * STAGE_BYTES);
#undef GATE
#undef TILE
        ring.sq = c8; ring.sp = p8; ring.poff = 0; ring.xm = xm;
        if (wid == 4) { STAMP(29); CSTAMP(34); STAMP(36); }
        ring.template steady<true, false, 18>(n1, n0, vC2);
        ring.fl += 512;
        if (wid == 4) { CSTAMP(35); STAMP(37); }
        ring.template issueP<16>(lds + ring.lb);
        SB();
        {
            const unsigned long long m = __ballot(ring.peek(sa + 256) == tg);
            have = (unsigned)m & (unsigned)(m >> 32);
            asm volatile("" : "+s"(have));
        }
        int hs = h;
        asm volatile("" : "+s"(hs));
        if (__builtin_expect(hs == 0, 1)) {
            constexpr unsigned MK = ((1u << ((KSHIFT + 1) / 2)) - 1u) * 0x10001u;
            if (__builtin_expect((MK & ~have) != 0u, 0)) ring.gate(tg, sa + 256, MK, have);
            unsigned lq = ring.lb + TILE_BYTES, pp = ring.par | at_panel(8);
            int sq = ring.sq ^ ring.xm, cnt = KSHIFT;
            ring.lb = (ring.lb + STAGE_BYTES) & (RING_BYTES - 1);
            ring.fl -= 512;
            fa = (unsigned)(size_t)ring.fl;
            asm volatile("" : "+s"(lq), "+s"(sq), "+s"(cnt), "+s"(ring.lb), "+s"(pp), "+s"(yd));
            asm volatile("" : "+v"(fa));
            SB();
            dma_4k<false>(ring.rQ, lq, ring.voQ, sq);
            ring.voF = vY;
            ring.rQ = rQ2;
            ring.sq = cK; ring.sp = pK; ring.poff = 0; ring.par = pp;
            ring.template steady<true, true>(cnt);
            auto seen = [&]() __attribute__((always_inline)) { return __builtin_amdgcn_readfirstlane(ring.peek(fa)) == 1u; };
            if (__builtin_expect(seen(), 1)) {
                fetch();
                WAIT_VM(16); BARRIER();
                WAIT_VM(8); BARRIER();
            } else {
                ring.sample();
                WAIT_VM(10); BARRIER();
                const bool have = seen();
                if (have) { fetch(); WAIT_VM(9); } else { ring.sample(); WAIT_VM(2); }
                BARRIER();
                if (!have) {
#pragma unroll 1
                    for (int spin = 0; spin <= SPIN_CAP && !seen(); ++spin) { ring.sample(); WAIT_VM(0); }
                    fetch();
                }
            }
            if (wid == 4) STAMP(20);
            WAIT_VM0();
        } else {
            ring.issueQ(lds + ring.lb);
            WAIT_VM(16); BARRIER();
            early(0, 0, 8);
            WAIT_VM(16); BARRIER();
            early(1, 0, 2);
            WAIT_VM(10); BARRIER();
            early(1, 2, 4);
        }
        BARRIER();
        if (h == 0) {
            const int slot = sidx;
            LAS unsigned char* abuf = lds + L_ATAB + 2048 + lw * 128;
#pragma unroll
            for (int i = 0; i < WD_EARLY; ++i) load_wd(i);
            SB();
            __amdgpu_buffer_rsrc_t rS = mk_rsrc(w2 + WS_STATS + (hp_ & 15) * 16384 + (hp_ >> 4) * 8192, 8192);
            __amdgpu_buffer_rsrc_t rC = mk_rsrc(w2 + WS_CAND + (size_t)hp_ * 4096, 4096);
            __amdgpu_buffer_rsrc_t rKy = mk_rsrc(w2 + WS_KEYS + (hp_ >> 2) * KEY_S + (hp_ & 3) * 4, ct == 0 ? 8192 + 3 * KEY_S * 16 + 4 : 0);
            int vs = lane * 16, vk = !(lane & 1) ? 1 << 20 : (lane >> 2) * 8192 + (lane & 3) * (KEY_S * 16);
            int vc = (lane < 32 ? 2048 : 0) + ct * 256 + (lane & 31) * 8;
            unsigned code = 0xffffu - (unsigned)(hp_ * 64 + lane), hb = (lane & 31) * 8;
            unsigned ra = (unsigned)(size_t)(rstat + (wq * 64 + lane) * 2), ht = (unsigned)(size_t)lds + L_HT + wq * 16384;
            u64 lo = 0xFFFFFFFFull;
            unsigned sr = (unsigned)(size_t)lds + L_ST + wq * 1024 + lane * 16;
            int vw = wp ? NIL : ct * 1024 + lane * 16;
            asm volatile("" : "+v"(sr), "+v"(vw));
            asm volatile("" : "+s"(rS), "+s"(rC), "+s"(rKy), "+s"(ht), "+s"(lo));
            asm volatile("" : "+v"(vs), "+v"(vk), "+v"(vc), "+v"(code), "+v"(hb), "+v"(ra));
            __builtin_amdgcn_s_setprio(TAIL_PRIO);
            SB();
            BARRIER();
            if (wp == 0) {
                const f32x4 u = *(const LAS f32x4*)(size_t)sr, v = *(const LAS f32x4*)((const LAS unsigned char*)(size_t)sr + 2048);
                const u32x4 w = {min(__float_as_uint(u[0] + v[0]), SENT - 1u), min(__float_as_uint(u[1] + v[1]), SENT - 1u), min(__float_as_uint(u[2] + v[2]), SENT - 1u), 0u};
                ST16_SC1(w, rS, vw);
            }
            SB();
            BARRIER();
            if (lane < 16) *(LAS unsigned*)(abuf + 64 + lane * 4) = 0u;
            __builtin_amdgcn_s_sleep(STAT_DELAY);
            if (wp == 0) {
                float s1, s2, s3;
#pragma unroll 1
                for (int spin = 0;; ++spin) {
                    asm volatile("" ::: "memory");
                    u32x3 sv[8];
#pragma unroll
                    for (int j = 0; j < 8; ++j) { sv[j] = __builtin_amdgcn_raw_buffer_load_b96(rS, vs, j * 1024, 16); SB(); }
#pragma unroll
                    for (int j = 0; j < 8; ++j) {
                        const unsigned u0 = sv[j][0], u1 = sv[j][1], u2 = sv[j][2];
                        s1 = j ? s1 + __uint_as_float(u0) : __uint_as_float(u0);
                        s2 = j ? s2 + __uint_as_float(u1) : __uint_as_float(u1);
                        s3 = j ? s3 + __uint_as_float(u2) : __uint_as_float(u2);
                    }
                    float z = s1 + s3;
                    asm volatile("" : "+v"(z));
                    if (__builtin_expect(__builtin_amdgcn_ballot_w64(__builtin_isunordered(z, s2)) == 0, 1)) break;
                    unsigned mx = 0u;
#pragma unroll
                    for (int j = 0; j < 8; ++j) mx = max(max(mx, sv[j][0]), max(sv[j][1], sv[j][2]));
                    if (__all(mx != SENT) || spin > SPIN_CAP) break;
                }
                if (wid == 4) STAMP(4);
                const float mean = s1 * (1.0f / D_LEN);
                const float var = fmaxf(s2 * (1.0f / D_LEN) - mean * mean, 0.f);
                const float mb = rbf(mean), rden = __builtin_amdgcn_rcpf(rbf(__builtin_amdgcn_sqrtf(rbf(rbf(var) + 1e-5f))));
                const unsigned kb = wave_max_s(r2key((s3 - mb * wsum) * rden, code));
                __builtin_amdgcn_raw_buffer_store_b32(kb, rKy, vk, 0, 16);
                SB();
                {
                    const unsigned cr_ = ~kb & 63u;
                    u32x2 c = *(const LAS u32x2*)(size_t)(ht + cr_ * 256 + (hb ^ ((cr_ & 15) * 16)));
                    const float m_ = __int_as_float(__builtin_amdgcn_readlane(__float_as_int(mb), cr_));
                    const float d_ = __int_as_float(__builtin_amdgcn_readlane(__float_as_int(rden), cr_));
#pragma unroll
                    for (int i = 0; i < 2; ++i) {
                        const unsigned u = c[i];
                        const unsigned dd = pk_bf16(bflo(u) - m_, bfhi(u) - m_);
                        const unsigned q = pk_bf16(bflo(dd) * d_, bfhi(dd) * d_);
                        c[i] = min(__float_as_uint(pick(lo, __uint_as_float(u), __uint_as_float(q))), SENT - 1u);
                    }
                    __builtin_amdgcn_raw_buffer_store_b64(c, rC, vc, 0, 16);
                    SB();
                    *(LAS u32x2*)(size_t)ra = (u32x2){__float_as_uint(mb), __float_as_uint(rden)};
                    if (lane == 0) ctl[2 + wq] = cr_;
                }
                WAIT_LGKM0();
                if (wid == 4) STAMP(5);
            }
            BARRIER();
#pragma unroll
            for (int i = WD_EARLY; i < 16; ++i) load_wd(i);
            if (slot < 112 && wid < 8 - BQ) {
                WAIT_VM0();
                if (wid == 4) STAMP(7);
                stage2(w2, slot, lw, lds + L_WD0 + lw * 8192, lds + L_WD1 + lw * 8192, abuf, 64, lane);
                if (wid == 4) STAMP(11);
            }
            FLUSH();
            return;
        }
    }

    P2_DEFS;
    LAS float* red = (LAS float*)(lds + L_RED);
    if (sidx < 112) {
        const int slot = sidx, grp = slot >> 3, kq = slot & 7;
        const __amdgpu_buffer_rsrc_t rG = mk_rsrc(a.wg, D_LEN * F_LEN * 2), rU = mk_rsrc(a.wu, D_LEN * F_LEN * 2);
        const int vgu = gu_off(slot, wid, lane);
        auto pair = [&](int st, int j) __attribute__((always_inline)) {
            dma_nt(rG, (unsigned)(size_t)lds + st * STAGE_BYTES + wid * 2048 + j * 1024, vgu ^ (j * 64), (32 * st + 2 * j) * (F_LEN * 2));
            dma_nt(rU, (unsigned)(size_t)lds + st * STAGE_BYTES + 16384 + wid * 2048 + j * 1024, vgu ^ (j * 64 + 512), (32 * st + 2 * j) * (F_LEN * 2));
        };
        if (wid >= 4) {
            auto one = [&](int st, int j, int m) __attribute__((always_inline)) {
                wait_vm<TH_LIM>();
                dma_nt(m ? rU : rG, (unsigned)(size_t)lds + st * STAGE_BYTES + m * 16384 + wid * 2048 + j * 1024, vgu ^ (j * 64 + m * 512), (32 * st + 2 * j) * (F_LEN * 2));
            };
#pragma unroll
            for (int i = 2; i < 2 + TH_N; ++i) one(1 + (i >> 2), (i >> 1) & 1, i & 1);
            BARRIER();
#pragma unroll
            for (int i = 2 + TH_N; i < 12; i += 2) pair(1 + (i >> 2), (i >> 1) & 1);
        } else {
            BARRIER();
            pair(1, 1); pair(2, 0); pair(2, 1); pair(3, 0); pair(3, 1);
        }
        if (wid >= 8 - BQ) {
            const __amdgpu_buffer_rsrc_t rD = mk_rsrc(a.wd + (size_t)slot * 32 * D_LEN, 32 * D_LEN * 2);
            const int vd = wd_off(wid - 4, 0, lane);
#pragma unroll
            for (int i = 0; i < 16; ++i)
                dma_nt(rD, (unsigned)(size_t)lds + L_YB + (wid & 1) * 16384 + i * 1024, vd ^ ((2 * (i & 1) | (i & 4)) * 32), i * (4 * D_LEN));
        }
        const __amdgpu_buffer_rsrc_t rK = mk_rsrc(w2, wid == 0 ? (unsigned)(WS_CAND + 32 * 4096) : 0u);
        const int ok_ = lane < 40 ? (int)WS_KEYS + (xcd >> 2) * 8192 + (xcd & 3) * (KEY_S * 16) + (lane - 32) * KEY_S : (int)(WS_CAND + 32 * 4096);
        const int oc = (int)WS_CAND + 2048 + kq * 256 + n * 16;
        int best = 0;
        u32x4 v = {0u, 0u, 0u, 0u};
        auto request = [&]() {
            asm volatile("" ::: "memory");
            v = LD16_SC1(rK, lane < 32 ? oc + best * 4096 : ok_);
            SB();
        };
        auto settled = [&]() {
            const unsigned mx = umax4(v), mn = min(min(v[0], v[1]), min(v[2], v[3]));
            unsigned fv = lane < 32 ? ~mx : mn | (unsigned)(lane >> 3 ^ 4);
            asm volatile("" : "+v"(fv));
            const bool fine = fv != 0u;
            unsigned km = max(mx, dppu<0xB1>(mx));
            km = max(km, dppu<0x4E>(km));
            km = max(km, dppu<0x141>(km));
            const int nb = (int)(((0xffffu - ((unsigned)__builtin_amdgcn_readlane((int)km, 32) & 0xffffu)) >> 6) & 31u);
            const bool same = nb == best;
            best = nb;
            return __all(fine) && same;
        };
        if (wid == 0) {
            request();
            WAIT_VM(0);
            settled();
            request();
        } else if (wid >= 8 - BQ) WAIT_VM(16);
        else WAIT_VM(0);
        BARRIER();
        if (wid == 0) STAMP(8);
        bf16x8 wfr[4][4];
        {
            const int q = (lane >> 2) & 3, ky = q | ((g & 1) << 2);
#pragma unroll
            for (int u = 0; u < 4; ++u) {
                const int tro = (u >> 1) * 16384 + (8 * g + q) * 512 + (((wid * 2 + (u & 1)) ^ ky) * 32) + (lane & 3) * 8;
#pragma unroll
                for (int ks = 0; ks < 4; ++ks) {
                    const bf16x4 w0 = __builtin_amdgcn_ds_read_tr16_b64_v4i16((LAS bf16x4*)(lds + tro + ks * STAGE_BYTES));
                    const bf16x4 w1 = __builtin_amdgcn_ds_read_tr16_b64_v4i16((LAS bf16x4*)(lds + tro + ks * STAGE_BYTES + 2048));
                    wfr[u][ks] = __builtin_shufflevector(w0, w1, 0, 1, 2, 3, 4, 5, 6, 7);
                }
            }
            SB();
#pragma unroll
            for (int i = 0; i < 16; ++i) asm volatile("" : "+v"(wfr[i >> 2][i & 3]));
        }
        if (wid >= 8 - BQ && lane < 16) *(LAS unsigned*)(lds + 8192 + wid * 64 + lane * 4) = 0u;
        __amdgpu_buffer_rsrc_t rPo = mk_rsrc(w2 + WS_PRE, 112 * 2048);
        int po = (n & 3) == 0 ? pre_off((grp ^ (g >> 1)) * 8 + wid) + kq * 256 + g * 64 + n * 4 : 112 * 2048;
        unsigned xw = (unsigned)(size_t)lds + (n >> 3) * 16384 + (g & 1) * 8192 + (n & 7) * 8, xr = (unsigned)(size_t)lds + (n & 1) * 8192 + g * 16;
        unsigned ix = (n & 1) ? 0xEEEEEEEEu : 0x44444444u;
        asm volatile("" : "+v"(po), "+v"(xw), "+v"(xr), "+v"(ix));
        u64 m1 = 0xFFFF0000FFFF0000ull, m2 = 0xFFFFFFFF00000000ull;
        f32x4 ac[4];
#pragma unroll
        for (int u = 0; u < 4; ++u) { ac[u] = (f32x4){0.f, 0.f, 0.f, 0.f}; asm volatile("" : "+v"(ac[u])); }
        asm volatile("" : "+s"(rPo), "+s"(m1), "+s"(m2));
        if (wid == 0) {
#pragma unroll 1
            for (int spin = 0; spin <= SPIN_CAP && !settled(); ++spin) {
                __builtin_amdgcn_s_sleep(1);
                request();
            }
            __builtin_amdgcn_s_waitcnt(0x0F70);
            if (lane < 32) *(LAS u32x2*)(size_t)xw = (u32x2){lane < 16 ? v[0] : v[1], lane < 16 ? v[2] : v[3]};
            WAIT_LGKM0();
            STAMP(9);
        }
        BARRIER();
        {
            bf16x8 xf[2];
#pragma unroll
            for (int j = 0; j < 2; ++j) xf[j] = *(const LAS bf16x8*)((const LAS unsigned char*)(size_t)xr + j * 16384);
            SB();
            float rs[3];
#pragma unroll
            for (int i = 0; i < 8; ++i) {
                const int u = i >> 1, j = i & 1;
                ac[u] = smf(xf[j], wfr[u][2 * j], wfr[u][2 * j + 1], ac[u], ix);
                SB();
                if (i == 4) { asm volatile("" : "+v"(ac[0])); rs[0] = ac[0][0] + ac[0][1]; asm volatile("" : "+v"(rs[0])); }
                if (i == 6) { asm volatile("" : "+v"(ac[1])); rs[0] = pick(m1, rs[0], ac[1][0] + ac[1][1]); asm volatile("" : "+v"(rs[0])); }
                if (i == 7) { asm volatile("" : "+v"(ac[2])); rs[2] = ac[2][0] + ac[2][1]; asm volatile("" : "+v"(rs[2])); }
                SB();
            }
            const unsigned e = min(__float_as_uint(pick(m2, rs[0], pick(m1, rs[2], ac[3][0] + ac[3][1]))), SENT - 1u);
            const u32x4 sv = {e, dppu<0x55>(e), dppu<0xAA>(e), dppu<0xFF>(e)};
            ST16_SC1(sv, rPo, po);
            SB();
            asm volatile("" :: "v"(v));
        }
        if (wid == 0) STAMP(14);
        if (wid >= 8 - BQ) {
            WAIT_VM(1);
            if (wid == 7) STAMP(15);
            stage2(w2, slot, wid - 4, lds + L_YB + (wid & 1) * 16384, lds + L_YB + (wid & 1) * 16384 + 8192, lds + wid * 64, 8192, lane);
            if (wid == 7) STAMP(10);
        }
    } else {
        const int rj = sidx - 112;
        if (blockIdx.x == 255 && tid == 0) {
            const u64 want = ((u64)ctl1[3] << 32) | ctl1[2];
            __hip_atomic_store((GAS u64*)(ws + WS_TAG), want, __ATOMIC_RELAXED, __HIP_MEMORY_SCOPE_AGENT);
        }
        if (wid == 4) {
            unsigned kk;
            const unsigned k = poll_winner(w2, xcd, lane, kk);
            if (lane == 0) ctl[4] = k;
            if (lane < 32) ctl[8 + lane] = kk;
            WAIT_LGKM0();
        }
        BARRIER();
        const unsigned kw = CTL(4);
        if (wid == 0) STAMP(16);
        {
            const unsigned kc = ctl[8 + wid * 4 + g];
            const __amdgpu_buffer_rsrc_t rL = mk_rsrc(w2 + WS_CAND + (size_t)wid * 16384, 16384);
            u32x2 hv2, nv2;
            for (int spin = 0;; ++spin) {
                asm volatile("" ::: "memory");
                hv2 = __builtin_amdgcn_raw_buffer_load_b64(rL, g * 4096 + (rj * 64 + n * 4) * 2, 0, 16);
                nv2 = __builtin_amdgcn_raw_buffer_load_b64(rL, g * 4096 + 2048 + (rj * 64 + n * 4) * 2, 0, 16);
                if (__all(max(max(hv2[0], hv2[1]), max(nv2[0], nv2[1])) != SENT) || spin > SPIN_CAP) break;
                __builtin_amdgcn_s_sleep(1);
            }
            u32x2 ov;
#pragma unroll
            for (int i = 0; i < 2; ++i) {
                const unsigned u = hv2[i], q = nv2[i];
                ov[i] = pk_bf16(bflo(u) + bflo(q), bfhi(u) + bfhi(q));
            }
            const int crow = (int)(0xffffu - (kc & 0xffffu)) & (S_LEN - 1);
            if (kc != kw) *(u32x2*)(a.out + (size_t)crow * D_LEN + rj * 64 + n * 4) = ov;
        }
        const int wrow = (int)(0xffffu - (kw & 0xffffu)) & (S_LEN - 1);
        const __amdgpu_buffer_rsrc_t rC = mk_rsrc(w2 + WS_CAND + (size_t)(wrow >> 6) * 4096, 4096);
        const __amdgpu_buffer_rsrc_t rPt = mk_rsrc(w2 + WS_PART, 112 * 4096);
        const int ocol = (rj >> 2) * 256 + ((lane >> 1) & 1) * 128 + (rj & 3) * 32 + (lane & 1) * 16 + (lane >> 2);
        unsigned hv, hnv;
        for (int spin = 0;; ++spin) {
            asm volatile("" ::: "memory");
            hv = __builtin_amdgcn_raw_buffer_load_b32(rC, (ocol & ~1) * 2, 0, 16);
            hnv = __builtin_amdgcn_raw_buffer_load_b32(rC, 2048 + (ocol & ~1) * 2, 0, 16);
            if (__all(max(hv, hnv) != SENT) || spin > SPIN_CAP) break;
        }
        float sgm = rbf(1.0f / (1.0f + __expf(-key2r(kw))));
        float hf = (ocol & 1) ? bfhi(hv) : bflo(hv), hnf = (ocol & 1) ? bfhi(hnv) : bflo(hnv);
        GAS unsigned short* optr = (GAS unsigned short*)(a.out + (size_t)wrow * D_LEN + ocol);
        unsigned rw = (unsigned)(size_t)red + (wid >> 2) * 1024 + (n * 4 + g) * 16 + (wid & 3) * 4, rr = (unsigned)(size_t)red + lane * 16;
        int po = wid * 16384 + (rj >> 3) * 8192 + g * 2048 + ((rj & 7) * 64 + n * 4) * 4;
        int po3 = po + 3 * 131072;
        asm volatile("" : "+v"(sgm), "+v"(optr), "+v"(hf), "+v"(hnf), "+v"(rw), "+v"(rr), "+v"(po), "+v"(po3));
        float sm;
#pragma unroll 1
        for (int spin = 0;; ++spin) {
            asm volatile("" ::: "memory");
            u32x4 pv[4];
#pragma unroll
            for (int i = 0; i < 4; ++i) pv[i] = __builtin_amdgcn_raw_buffer_load_b128(rPt, i < 3 ? po : po3, i < 3 ? i * 131072 : 0, 16);
            SB();
            unsigned t[4];
#pragma unroll
            for (int j = 0; j < 4; ++j) {
                const unsigned u0 = pv[0][j], u1 = pv[1][j], u2 = pv[2][j], u3 = pv[3][j];
                t[j] = __float_as_uint(((__uint_as_float(u0) + __uint_as_float(u1)) + __uint_as_float(u2)) + __uint_as_float(u3));
            }
            const auto r0 = __builtin_amdgcn_permlane32_swap(t[0], t[2], false, false), r1 = __builtin_amdgcn_permlane32_swap(t[1], t[3], false, false);
            const unsigned a0 = r0[0], a1 = r0[1], b0 = r1[0], b1 = r1[1];
            const auto r2 = __builtin_amdgcn_permlane16_swap(__float_as_uint(__uint_as_float(a0) + __uint_as_float(a1)),
                                                             __float_as_uint(__uint_as_float(b0) + __uint_as_float(b1)), false, false);
            const unsigned c0 = r2[0], c1 = r2[1];
            sm = __uint_as_float(c0) + __uint_as_float(c1);
            if (__builtin_expect(__builtin_amdgcn_ballot_w64(sm != sm) == 0, 1)) break;
            if (__all(max(max(umax4(pv[0]), umax4(pv[1])), max(umax4(pv[2]), umax4(pv[3]))) != SENT) || spin > SPIN_CAP) break;
            if (FIN_SLEEP) __builtin_amdgcn_s_sleep(FIN_SLEEP);
        }
        if (wid == 0) STAMP(17);
        *(LAS float*)(size_t)rw = sm;
        WAIT_LGKM0();
        BARRIER();
        if (wid == 0) {
            const f32x4 u = *(const LAS f32x4*)(size_t)rr, v = *(const LAS f32x4*)((const LAS unsigned char*)(size_t)rr + 1024);
            const f32x4 w = u + v;
            const float t = (w[0] + w[2]) + (w[1] + w[3]);
            *optr = (unsigned short)pk_bf16(hf + rbf(hnf + rbf(rbf(t) * sgm)), 0.f);
            STAMP(18);
            FLUSH();
            asm volatile("s_endpgm" ::: "memory");
        }
        __builtin_amdgcn_s_waitcnt(0x0F70);
    }
    FLUSH();
}

extern "C" void kernel_launch(void* const* d_in, const int* in_sizes, int n_in, void* d_out, int out_size, void* d_ws,
                              size_t ws_size, hipStream_t stream) {
    Args a;
    a.x = (const bf16_t*)d_in[0];
    a.cr = (const bf16_t*)d_in[1];
    a.ci = (const bf16_t*)d_in[2];
    a.wre = (const bf16_t*)d_in[3];
    a.wim = (const bf16_t*)d_in[4];
    a.w_r = (const bf16_t*)d_in[6];
    a.wg = (const bf16_t*)d_in[7];
    a.wu = (const bf16_t*)d_in[8];
    a.wd = (const bf16_t*)d_in[9];
    a.out = (bf16_t*)d_out;
    a.ws = (char*)d_ws + ((ws_size - ((size_t)24 << 20)) & ~(size_t)255);
    (void)hipFuncSetAttribute((const void*)fused_kernel, hipFuncAttributeMaxDynamicSharedMemorySize, LDS_BYTES);
    fused_kernel<<<256, 768, LDS_BYTES, stream>>>(a);
}
```

```cpp
#include <hip/hip_runtime.h>
#include <hip/hip_bf16.h>
#include <stdint.h>

typedef unsigned short bf16_t;
typedef short bf16x8 __attribute__((ext_vector_type(8)));
typedef short bf16x4 __attribute__((ext_vector_type(4)));
typedef float f32x4 __attribute__((ext_vector_type(4)));
typedef unsigned u32x4 __attribute__((ext_vector_type(4)));
typedef unsigned u32x2 __attribute__((ext_vector_type(2)));
typedef unsigned u32x3 __attribute__((ext_vector_type(3)));
typedef unsigned long long u64;
#define LAS __attribute__((address_space(3)))
#define GAS __attribute__((address_space(1)))

constexpr int S_LEN = 2048, D_LEN = 1024, F_LEN = 3584;
constexpr unsigned SENT = 0xFFFFFFFFu;
constexpr int SPIN_CAP = 1 << 17;

constexpr size_t WS_KEYS = 0;
constexpr size_t WS_XRD = 65536;
constexpr size_t WS_FLAG1 = 196608;
constexpr size_t WS_TAG = 131072;
constexpr size_t WS_VOTE = WS_TAG + 4096;
constexpr size_t WS_YSUM = (size_t)1 << 20;
constexpr size_t WS_PROBE = (size_t)20 << 20;
constexpr size_t WS_AT = (size_t)4 << 20;
constexpr size_t WS_Y = (size_t)12 << 20;
constexpr size_t WS_STATS = (size_t)16 << 20;
constexpr size_t WS_PART = WS_STATS + (256u << 10);
constexpr size_t WS_CAND = WS_STATS + (704u << 10);
constexpr size_t WS_PRE = WS_STATS + (832u << 10);
constexpr size_t FILL_BYTES = (size_t)1072 << 10;

__device__ __forceinline__ float bflo(unsigned v) { return __uint_as_float(v << 16); }
__device__ __forceinline__ float bfhi(unsigned v) { return __uint_as_float(v & 0xffff0000u); }
__device__ __forceinline__ unsigned pk_bf16(float lo, float hi) {
    typedef __bf16 bf2 __attribute__((ext_vector_type(2)));
    typedef float f2 __attribute__((ext_vector_type(2)));
    f2 v = {lo, hi};
    bf2 r = __builtin_convertvector(v, bf2);
    return __builtin_bit_cast(unsigned, r);
}
__device__ __forceinline__ float rbf(float x) { return __uint_as_float(pk_bf16(0.f, x)); }

template <int CTRL> __device__ __forceinline__ unsigned dppu(unsigned v) {
    return (unsigned)__builtin_amdgcn_update_dpp(0, (int)v, CTRL, 0xf, 0xf, true);
}
template <int CTRL> __device__ __forceinline__ float dppf(float v) { return __uint_as_float(dppu<CTRL>(__float_as_uint(v))); }
__device__ __forceinline__ void swap16(unsigned v, unsigned& a, unsigned& b) {
    auto r = __builtin_amdgcn_permlane16_swap(v, v, false, false);
    a = r[0]; b = r[1];
}
__device__ __forceinline__ void swap32(unsigned v, unsigned& a, unsigned& b) {
    auto r = __builtin_amdgcn_permlane32_swap(v, v, false, false);
    a = r[0]; b = r[1];
}
__device__ __forceinline__ float sum_rows(float v) {
    unsigned a, b;
    swap16(__float_as_uint(v), a, b); v = __uint_as_float(a) + __uint_as_float(b);
    swap32(__float_as_uint(v), a, b); v = __uint_as_float(a) + __uint_as_float(b);
    return v;
}
__device__ __forceinline__ float wave_sum(float v) {
    v += dppf<0xB1>(v);
    v += dppf<0x4E>(v);
    v += dppf<0x141>(v);
    v += dppf<0x140>(v);
    return sum_rows(v);
}
__device__ __forceinline__ unsigned wave_max_u32(unsigned v) {
    v = max(v, dppu<0xB1>(v));
    v = max(v, dppu<0x4E>(v));
    v = max(v, dppu<0x141>(v));
    v = max(v, dppu<0x140>(v));
    unsigned a, b;
    swap16(v, a, b); v = max(a, b);
    swap32(v, a, b); v = max(a, b);
    return v;
}
__device__ __forceinline__ unsigned wave_max_s(unsigned v) {
    v = max(v, dppu<0xB1>(v));
    v = max(v, dppu<0x4E>(v));
    v = max(v, dppu<0x141>(v));
    v = max(v, dppu<0x140>(v));
    v = max(v, dppu<0x142>(v));
    v = max(v, dppu<0x143>(v));
    return (unsigned)__builtin_amdgcn_readlane((int)v, 63);
}
__device__ __forceinline__ float sel4(int g, float a, float b, float c, float d) {
    const float t0 = (g & 1) ? b : a, t1 = (g & 1) ? d : c;
    return (g & 2) ? t1 : t0;
}
__device__ __forceinline__ float pick(u64 m, float a, float b) {
    float r;
    asm("v_cndmask_b32 %0, %1, %2, %3" : "=v"(r) : "v"(a), "v"(b), "s"(m));
    return r;
}
__device__ __forceinline__ unsigned umax4(u32x4 v) { return max(max(v[0], v[1]), max(v[2], v[3])); }

__device__ __forceinline__ void touch_line(const void* p, unsigned lds_dst) {
    unsigned keep;
    asm volatile("s_mov_b32 %0, m0\n\ts_mov_b32 m0, %2\n\ts_nop 0\n\tglobal_load_lds_dword %1, off\n\ts_mov_b32 m0, %0"
                 : "=&s"(keep) : "v"(p), "s"(lds_dst) : "memory");
}
__device__ __forceinline__ __amdgpu_buffer_rsrc_t mk_rsrc(const void* p, unsigned bytes) {
    return __builtin_amdgcn_make_buffer_rsrc((void*)p, 0, (int)bytes, 0x00020000);
}
#define LDS_LOAD(w, v, o, pol) "buffer_load_" w " " v ", %0, %2 offen" o pol " lds"
#define LDS_M0 "s_mov_b32 m0, %1\n\ts_nop 0\n\t"
__device__ __forceinline__ void dma_nt(__amdgpu_buffer_rsrc_t r, unsigned dst, int voff, int soff) {
    asm volatile(LDS_M0 LDS_LOAD("dwordx4", "%3", "", " nt") :: "s"(r), "s"(dst), "s"(soff), "v"(voff) : "memory", "m0");
}
template <bool SC1> __device__ __forceinline__ void dma_word(__amdgpu_buffer_rsrc_t r, unsigned dst, int voff) {
    if constexpr (SC1) asm volatile(LDS_M0 LDS_LOAD("dword", "%3", "", " sc1") :: "s"(r), "s"(dst), "n"(0), "v"(voff) : "memory", "m0");
    else asm volatile(LDS_M0 LDS_LOAD("dword", "%3", "", "") :: "s"(r), "s"(dst), "n"(0), "v"(voff) : "memory", "m0");
}
template <bool SC1> __device__ __forceinline__ void dma_2k(__amdgpu_buffer_rsrc_t r, unsigned dst, int v0, int v1, int soff) {
    if constexpr (SC1) asm volatile(LDS_M0 LDS_LOAD("dwordx4", "%3", "", " sc1") "\n\t" LDS_LOAD("dwordx4", "%4", " offset:1024", " sc1")
                 :: "s"(r), "s"(dst), "s"(soff), "v"(v0), "v"(v1) : "memory", "m0");
    else asm volatile(LDS_M0 LDS_LOAD("dwordx4", "%3", "", "") "\n\t" LDS_LOAD("dwordx4", "%4", " offset:1024", "")
                 :: "s"(r), "s"(dst), "s"(soff), "v"(v0), "v"(v1) : "memory", "m0");
}
template <bool SC1> __device__ __forceinline__ void dma_4k(__amdgpu_buffer_rsrc_t r, unsigned dst, const int (&v)[4], int soff) {
#define FOUR(pol) asm volatile(LDS_M0 LDS_LOAD("dwordx4", "%3", "", pol) "\n\t" LDS_LOAD("dwordx4", "%4", " offset:1024", pol) "\n\t" \
        LDS_LOAD("dwordx4", "%5", " offset:2048", pol) "\n\t" LDS_LOAD("dwordx4", "%6", " offset:3072", pol) \
        :: "s"(r), "s"(dst), "s"(soff), "v"(v[0]), "v"(v[1]), "v"(v[2]), "v"(v[3]) : "memory", "m0")
    if constexpr (SC1) FOUR(" sc1"); else FOUR("");
#undef FOUR
}
#define CODE_BYTES 25600
__device__ __forceinline__ __amdgpu_buffer_rsrc_t code_rsrc() {
    unsigned long long p; unsigned d;
    asm volatile("s_getpc_b64 %0\n1:\n\ts_mov_b32 %1, 1b-_Z12fused_kernel4Args" : "=s"(p), "=s"(d));
    return __builtin_amdgcn_make_buffer_rsrc((void*)(p - d), 0, CODE_BYTES, 0x00020000);
}
#define LD16_SC1(rs, off) __builtin_amdgcn_raw_buffer_load_b128(rs, off, 0, 16)
#define ST16_SC1(v, rs, off) __builtin_amdgcn_raw_buffer_store_b128(v, rs, off, 0, 16)

constexpr int BT = 128, BK = 64, NSTAGE = 4;
constexpr int TILE_BYTES = BT * BK * 2;
constexpr int STAGE_BYTES = 2 * TILE_BYTES;
constexpr int RING_BYTES = NSTAGE * STAGE_BYTES;
constexpr int L_DUMMY = RING_BYTES;
constexpr int L_CTL = RING_BYTES + 256;
constexpr int L_RED = RING_BYTES + 512;

#define WAIT_VM(n) asm volatile("s_waitcnt vmcnt(" #n ")" ::: "memory")
#define WAIT_VM0() do { asm volatile("" ::: "memory"); __builtin_amdgcn_s_waitcnt(0x0F70); asm volatile("s_waitcnt vmcnt(0)" ::: "memory"); } while (0)
#define WAIT_LGKM0() asm volatile("s_waitcnt lgkmcnt(0)" ::: "memory")
#define BARRIER() do { asm volatile("" ::: "memory"); __builtin_amdgcn_s_barrier(); asm volatile("" ::: "memory"); } while (0)

__device__ __forceinline__ int prow(int rho) {
    const int w = rho >> 6, m = (rho >> 4) & 3, q = (rho >> 2) & 3, r = rho & 3;
    return w * 64 + (m >> 1) * 32 + q * 8 + (m & 1) * 4 + r;
}
__device__ __forceinline__ unsigned r2key(float r, unsigned code) {
    const unsigned u = pk_bf16(0.f, r);
    return ((u ^ ((unsigned)((int)u >> 31) | 0x80000000u)) & 0xffff0000u) | code;
}
__device__ __forceinline__ float key2r(unsigned k) {
    const unsigned o = k >> 16;
    return bflo((o & 0x8000u) ? (o & 0x7fffu) : (~o & 0xffffu));
}
#define STAT_M 5
#define BQ 2
#define POLL_T (STAT_M + 5)
#define AUX_SLEEP 12
#define WD_EARLY 4
#define C2_START 2
#define TAIL_PRIO 3
#define STAT_DELAY 8
#define AUX_STAY 1
#define CATCH1 4
#define CPF_MASK1 0x1080u
#define CPF_MASK2 0x1080u
#define CPF_STAG 5
#define CPF_FIX 0x1000u
#define KEY_S 128
#define TH_LIM 6
#define FIN_SLEEP 1
#define POLL_SLEEP 4
__device__ __forceinline__ f32x4 smf(bf16x8 a, bf16x8 b0, bf16x8 b1, f32x4 d, unsigned idx) {
    typedef __bf16 bf8 __attribute__((ext_vector_type(8)));
    typedef __bf16 bf16v __attribute__((ext_vector_type(16)));
    const auto b = __builtin_shufflevector(b0, b1, 0, 1, 2, 3, 4, 5, 6, 7, 8, 9, 10, 11, 12, 13, 14, 15);
    return __builtin_amdgcn_smfmac_f32_16x16x64_bf16(__builtin_bit_cast(bf8, a), __builtin_bit_cast(bf16v, b), d, (int)idx, 0, 0);
}
__device__ __forceinline__ float dot2bf(unsigned a, unsigned b, float c) {
    typedef __bf16 bf2 __attribute__((ext_vector_type(2)));
    return __builtin_amdgcn_fdot2_f32_bf16(__builtin_bit_cast(bf2, a), __builtin_bit_cast(bf2, b), c, false);
}

#define STAMP(i) do { } while (0)
#define CSTAMP(i) do { } while (0)
#define FLUSH() do { } while (0)
#define TSV do { } while (0)

#define KSHIFT 4

constexpr int L_HT = ((32 + KSHIFT) % NSTAGE) * STAGE_BYTES;
constexpr int L_WD0 = ((33 + KSHIFT) % NSTAGE) * STAGE_BYTES;
constexpr int L_WD1 = ((34 + KSHIFT) % NSTAGE) * STAGE_BYTES;
constexpr int L_ST = ((35 + KSHIFT) % NSTAGE) * STAGE_BYTES;
constexpr int L_YB = RING_BYTES;
constexpr int L_ATAB = L_YB;
constexpr int LDS_BYTES = RING_BYTES + 32768;

#define SB() __builtin_amdgcn_sched_barrier(0)
template <int N> __device__ __forceinline__ void wait_vm() { asm volatile("s_waitcnt vmcnt(%0)" :: "n"(N) : "memory"); }
__device__ __forceinline__ int at_tile(int c, int par) { return ((c << 8) & 0x78000) | (((c << 2) & 0x2000) ^ par); }
__device__ __forceinline__ int at_panel(int ep) { return ep << 19; }
__device__ __forceinline__ int pre_off(int slot) { const int s_ = slot >= 56, l = slot - 56 * s_; return (l >> 2) * 16384 + s_ * 8192 + (l & 3) * 2048; }
static_assert(KSHIFT == 4, "ring stages of the last tiles");
struct Ring {
    __amdgpu_buffer_rsrc_t rP, rQ, rF;
    int voP[4], voQ[4], voF;
    LAS unsigned char* fl;
    unsigned lb;
    int sp, sq;
    int poff, par, xm;
    bool img;
    template <int LD, bool PIMG> __device__ __forceinline__ void setup(int lw, int lane) {
        const int r = lane >> 3, v0 = (PIMG ? r : (r & 3) + (r & 4) * 2) * (LD * 2) + (((lane & 7) ^ (lane >> 4)) << 4);
#pragma unroll
        for (int j = 0; j < 4; ++j) {
            voQ[j] = (v0 ^ ((j & 1) * 64)) + (PIMG ? j * 8 : (j & 1) * 16 + (j >> 1) * 4) * (LD * 2) - j * 1024;
            voP[j] = PIMG ? (lw >> 1) * 16384 + (lw & 1) * 4096 + lane * 16 : voQ[j];
        }
        img = PIMG;
    }
    __device__ __forceinline__ void seek(int col, int pof) {
        sq = col; poff = pof;
        sp = (img ? at_tile(sq, par) : sq) + poff;
    }
    __device__ __forceinline__ void step() {
        sq = (sq + BK * 2) & 4095;
        sp = (img ? at_tile(sq ^ xm, par) : sq) + poff;
        asm volatile("" : "+s"(sq), "+s"(sp));
    }
    __device__ __forceinline__ void sample() { dma_word<true>(rF, (unsigned)(size_t)fl, voF); }
    __device__ __forceinline__ unsigned peek(unsigned addr) {
        unsigned v;
        asm volatile("ds_read_b32 %0, %1\n\ts_waitcnt lgkmcnt(0)" : "=v"(v) : "v"(addr) : "memory");
        return v;
    }
    __device__ __forceinline__ void gate(unsigned want, unsigned sa, unsigned need, unsigned& have) {
#pragma unroll 1
        for (int spin = 0; spin <= SPIN_CAP && (need & ~have) != 0u; ++spin) {
            if (spin) { sample(); WAIT_VM(0); __builtin_amdgcn_s_sleep(1); }
            const unsigned long long m = __ballot(peek(sa) == want);
            have = (unsigned)m & (unsigned)(m >> 32);
        }
    }
    template <int AUX> __device__ __forceinline__ void issueP(LAS unsigned char* base) { dma_4k<AUX != 0>(rP, (unsigned)(size_t)base, voP, sp); }
    __device__ __forceinline__ void issueQ(LAS unsigned char* base) { dma_4k<false>(rQ, (unsigned)(size_t)base + TILE_BYTES, voQ, sq ^ xm); }
    template <bool P2> __device__ __forceinline__ void issue(LAS unsigned char* lds) {
        issueP<P2 ? 16 : 0>(lds + lb);
        issueQ(lds + lb);
        SB();
        if (P2) sample();
        lb = (lb + STAGE_BYTES) & (RING_BYTES - 1);
        asm volatile("" : "+s"(lb));
        step();
        SB();
    }
    template <int CAT> __device__ __forceinline__ void ramp(LAS unsigned char* lds) {
#pragma unroll 1
        for (int i = 1; i <= CAT + 1; ++i) {
            issue<false>(lds);
            if (i == CAT + 1) continue;
            WAIT_VM(8);
            BARRIER();
        }
    }
    template <bool P2, bool PLAIN, int W0 = 16> __device__ __forceinline__ void steady(int cnt, int trig = 0, int v2 = 0) {
        int t, u;
#define LD(v, r, s, o) "buffer_load_dwordx4 " v ", " r ", " s " offen" o " lds\n\t"
        if constexpr (P2) {
#define BODY(SMP, TST, ENT) asm volatile("s_mov_b32 m0, %0\n" ENT "1:\n\ts_waitcnt vmcnt(16)\n2:\n\ts_barrier\n\t" \
            LD("%8", "%6", "%2", " sc1") LD("%8", "%6", "%2", " offset:1024 sc1") LD("%8", "%6", "%2", " offset:2048 sc1") LD("%8", "%6", "%2", " offset:3072 sc1") \
            SMP "s_xor_b32 %4, %1, %18\n\ts_add_i32 %1, %1, 0x80\n\ts_add_i32 m0, %0, 0x4000\n\ts_and_b32 %1, %1, 0xfff\n\t" \
            LD("%9", "%7", "%4", "") "s_xor_b32 %2, %1, %18\n\ts_lshl_b32 %5, %1, 2\n\ts_lshl_b32 %2, %2, 8\n\ts_and_b32 %2, %2, 0x78000\n\t" \
            LD("%10", "%7", "%4", " offset:1024") "s_and_b32 %5, %5, 0x2000\n\ts_xor_b32 %5, %5, %13\n\ts_or_b32 %2, %2, %5\n\ts_add_i32 %3, %3, -1\n\t" \
            LD("%11", "%7", "%4", " offset:2048") "s_add_i32 %0, %0, 0x8000\n\ts_and_b32 %0, %0, 0x1ffff\n\t" TST "s_cmp_lg_u32 %3, 0\n\t" \
            LD("%12", "%7", "%4", " offset:3072") "\n4:\n\ts_mov_b32 m0, %0\n\ts_cbranch_scc1 1b\n\ts_waitcnt vmcnt(16)\n\ts_barrier" \
            : "+s"(lb), "+s"(sq), "+s"(sp), "+s"(cnt), "=&s"(t), "=&s"(u) \
            : "s"(rP), "s"(rQ), "v"(voP[0]), "v"(voQ[0]), "v"(voQ[1]), "v"(voQ[2]), "v"(voQ[3]), "s"(par | poff), "s"(rF), "v"(voF), "s"((unsigned)(size_t)fl), "n"(W0), "s"(xm), \
              "s"(trig), "v"(v2) \
            : "memory", "scc", "m0")
            if constexpr (PLAIN) BODY("", "", "");
            else BODY("", "s_cmp_eq_u32 %3, %19\n\ts_cbranch_scc1 3b\n\t",
                      "\ts_waitcnt vmcnt(%17)\n\ts_branch 2f\n3:\n\t" LD("%12", "%7", "%4", " offset:3072") "s_mov_b32 m0, %16\n\ts_nop 0\n\tbuffer_load_dword %20, %7, 0 offen lds\n\t"
                      "s_add_i32 m0, %16, 0x200\n\ts_cmp_lg_u32 %3, 0\n\tbuffer_load_dword %15, %14, 0 offen sc1 lds\n\ts_branch 4f\n");
#undef BODY
        } else {
            asm volatile("s_mov_b32 m0, %0\n\ts_branch 2f\n1:\n\ts_waitcnt vmcnt(16)\n\ts_barrier\n2:\n\t"
                LD("%6", "%4", "%1", "") LD("%7", "%4", "%1", " offset:1024") LD("%8", "%4", "%1", " offset:2048") LD("%9", "%4", "%1", " offset:3072")
                "s_add_i32 m0, %0, 0x4000\n\ts_mov_b32 %3, %1\n\t"
                LD("%6", "%5", "%3", "") "s_add_i32 %1, %1, 0x80\n\ts_add_i32 %0, %0, 0x8000\n\t"
                LD("%7", "%5", "%3", " offset:1024") "s_and_b32 %0, %0, 0x1ffff\n\ts_add_i32 %2, %2, -1\n\t"
                LD("%8", "%5", "%3", " offset:2048") "s_cmp_lg_u32 %2, 0\n\t"
                LD("%9", "%5", "%3", " offset:3072") "s_mov_b32 m0, %0\n\ts_cbranch_scc1 1b\n\ts_waitcnt vmcnt(16)\n\ts_barrier"
                : "+s"(lb), "+s"(sq), "+s"(cnt), "=&s"(t)
                : "s"(rP), "s"(rQ), "v"(voQ[0]), "v"(voQ[1]), "v"(voQ[2]), "v"(voQ[3])
                : "memory", "scc", "m0");
            (void)u;
        }
#undef LD
    }
};

template <bool PLIN, bool FIRST, class Pre>
__device__ __forceinline__ void compute_loop(const LAS unsigned char* lds, f32x4 (&acc)[4][4], int NT, int wp, int wq, int lane, Pre pre,
                                             bf16x8 (&p0)[4], bf16x8 (&p1)[4], unsigned last) {
    const int fr = lane & 15, fq = lane >> 4, key = (fr >> 1) & 7;
    const unsigned l0 = (unsigned)(size_t)lds;
    const unsigned oP0 = l0 + (PLIN ? wp * 8192 + lane * 16 : (wp * 64 + fr) * 128 + ((fq ^ key) * 16));
    const unsigned oP1 = l0 + (PLIN ? wp * 8192 + 1024 + lane * 16 : (wp * 64 + fr) * 128 + (((4 + fq) ^ key) * 16));
    const unsigned oQ0 = l0 + TILE_BYTES + (wq * 64 + fr) * 128 + ((fq ^ key) * 16);
    const unsigned oQ1 = l0 + TILE_BYTES + (wq * 64 + fr) * 128 + (((4 + fq) ^ key) * 16);
    unsigned aP0 = oP0, aQ0 = oQ0, aP1 = oP1, aQ1 = oQ1;
    bf16x8 r0[4], r1[4], q0[4], q1[4];
    unsigned xp = 2 * STAGE_BYTES;
#pragma unroll
    for (int i = 0; i < 16; ++i) asm volatile("" : "+v"(acc[i >> 2][i & 3]));
    asm volatile("" : "+v"(aP0), "+v"(aQ0), "+v"(aP1), "+v"(aQ1));
#define MM(pp, qq, m, n) do { acc[m][n] = __builtin_amdgcn_mfma_f32_16x16x32_bf16(pp[m], qq[n], acc[m][n], 0, 0, 0); SB(); } while (0)
#define RD(dst, i, a, st) do { dst[i] = *(const LAS bf16x8*)((const LAS unsigned char*)(size_t)(a) + (st) * STAGE_BYTES + (i) * 2048); SB(); } while (0)
#define ADP(a) do { a ^= xp; asm volatile("" : "+v"(a)); SB(); } while (0)
#define ADR(a) do { a ^= 2 * STAGE_BYTES; asm volatile("" : "+v"(a)); SB(); } while (0)
    __builtin_amdgcn_s_waitcnt(0xC07F);
    SB();
    if (FIRST) BARRIER();
    SB();
    RD(p0, 0, aP0, 0); RD(q0, 0, aQ0, 0); RD(p1, 0, aP1, 0); RD(q1, 0, aQ1, 0); RD(p1, 1, aP1, 0); RD(p0, 1, aP0, 0);
    RD(p0, 2, aP0, 0); RD(p1, 2, aP1, 0); RD(p1, 3, aP1, 0); RD(p0, 3, aP0, 0);
    pre();
    SB();
    int t = NT;
#pragma unroll 1
    do {
        MM(p0, q0, 0, 0); MM(p1, q1, 0, 0); RD(q0, 1, aQ0, 0);
        MM(p1, q1, 1, 0); MM(p0, q0, 1, 0); RD(q1, 1, aQ1, 0);
        MM(p0, q0, 2, 0); MM(p1, q1, 2, 0); RD(q0, 2, aQ0, 0);
        MM(p1, q1, 3, 0); MM(p0, q0, 3, 0); RD(q1, 2, aQ1, 0);
        MM(p0, q0, 3, 1); MM(p1, q1, 3, 1); RD(q0, 3, aQ0, 0);
        MM(p1, q1, 2, 1); MM(p0, q0, 2, 1); RD(q1, 3, aQ1, 0);
        MM(p0, q0, 1, 1); MM(p1, q1, 1, 1);
        MM(p1, q1, 0, 1); MM(p0, q0, 0, 1);
        MM(p0, q0, 0, 2); MM(p1, q1, 0, 2); __builtin_amdgcn_s_waitcnt(0xC07F); SB(); BARRIER(); SB();
        MM(p1, q1, 1, 2); RD(r0, 0, aP0, 1); MM(p0, q0, 1, 2); RD(q0, 0, aQ0, 1);
        MM(p0, q0, 2, 2); RD(r1, 0, aP1, 1); MM(p1, q1, 2, 2); RD(q1, 0, aQ1, 1);
        MM(p1, q1, 3, 2); MM(p0, q0, 3, 2); RD(r1, 1, aP1, 1);
        MM(p0, q0, 3, 3); RD(r0, 1, aP0, 1); MM(p1, q1, 3, 3);
        MM(p1, q1, 2, 3); RD(r0, 2, aP0, 1); MM(p0, q0, 2, 3); RD(r1, 2, aP1, 1);
        MM(p0, q0, 1, 3); MM(p1, q1, 1, 3); RD(r1, 3, aP1, 1);
        MM(p1, q1, 0, 3); RD(r0, 3, aP0, 1); MM(p0, q0, 0, 3);

        MM(r0, q0, 0, 0); MM(r1, q1, 0, 0); RD(q0, 1, aQ0, 1);
        MM(r1, q1, 1, 0); MM(r0, q0, 1, 0); RD(q1, 1, aQ1, 1);
        MM(r0, q0, 2, 0); MM(r1, q1, 2, 0); RD(q0, 2, aQ0, 1);
        MM(r1, q1, 3, 0); MM(r0, q0, 3, 0); RD(q1, 2, aQ1, 1);
        MM(r0, q0, 3, 1); MM(r1, q1, 3, 1); RD(q0, 3, aQ0, 1);
        MM(r1, q1, 2, 1); MM(r0, q0, 2, 1); RD(q1, 3, aQ1, 1);
        MM(r0, q0, 1, 1); t -= 2; if (PLIN) xp = t == 0 ? last : 2 * STAGE_BYTES; asm volatile("" : "+s"(t), "+s"(xp)); SB(); MM(r1, q1, 1, 1); ADP(aP0);
        MM(r1, q1, 0, 1); ADP(aP1); MM(r0, q0, 0, 1); ADR(aQ0);
        MM(r0, q0, 0, 2); ADR(aQ1); MM(r1, q1, 0, 2); __builtin_amdgcn_s_waitcnt(0xC07F); SB(); BARRIER(); SB();
        MM(r1, q1, 1, 2); RD(p0, 0, aP0, 0); MM(r0, q0, 1, 2); RD(q0, 0, aQ0, 0);
        MM(r0, q0, 2, 2); RD(p1, 0, aP1, 0); MM(r1, q1, 2, 2); RD(q1, 0, aQ1, 0);
        MM(r1, q1, 3, 2); MM(r0, q0, 3, 2); RD(p1, 1, aP1, 0);
        MM(r0, q0, 3, 3); RD(p0, 1, aP0, 0); MM(r1, q1, 3, 3);
        MM(r1, q1, 2, 3); RD(p0, 2, aP0, 0); MM(r0, q0, 2, 3); RD(p1, 2, aP1, 0);
        MM(r0, q0, 1, 3); MM(r1, q1, 1, 3); RD(p1, 3, aP1, 0);
        MM(r1, q1, 0, 3); RD(p0, 3, aP0, 0); MM(r0, q0, 0, 3);
    } while (t != 0);
    SB();
#undef MM
#undef RD
#undef ADR
#undef ADP
}

struct Args {
    const bf16_t* x; const bf16_t* wre; const bf16_t* wim; char* ws; const bf16_t* cr; const bf16_t* ci;
    const bf16_t* w_r; const bf16_t* wg; const bf16_t* wu; const bf16_t* wd; bf16_t* out;
};

__device__ __forceinline__ unsigned poll_winner(const char* ws, int rep, int lane, unsigned& mine) {
    GAS unsigned* kp = (GAS unsigned*)(ws + WS_KEYS + (size_t)(rep >> 2) * 8192 + (rep & 3) * (KEY_S * 16) + ((lane >> 2) & 7) * KEY_S) + (lane & 3);
    unsigned kw;
    for (int spin = 0;; ++spin) {
            asm volatile("" ::: "memory");
        kw = __hip_atomic_load(kp, __ATOMIC_RELAXED, __HIP_MEMORY_SCOPE_AGENT);
        if (__all(kw != 0u) || spin > SPIN_CAP) break;
        __builtin_amdgcn_s_sleep(POLL_SLEEP);
    }
    mine = kw;
    return __builtin_amdgcn_readfirstlane(wave_max_u32(kw));
}

__device__ __forceinline__ void stage2(char* w2, int slot, int lw, LAS unsigned char* img0, LAS unsigned char* img1, LAS unsigned char* abuf, int zoff, int lane) {
    const int g = lane >> 4, n = lane & 15;
    const __amdgpu_buffer_rsrc_t rPi = mk_rsrc(w2 + WS_PRE + pre_off(slot), 2048);
    unsigned pp[8];
    auto request = [&]() {
        asm volatile("" ::: "memory");
#pragma unroll
        for (int k = 0; k < 8; ++k) pp[k] = __builtin_amdgcn_raw_buffer_load_b32(rPi, k * 256 + lane * 4, 0, 16);
        SB();
    };
    request();
    bf16x8 wf[16];
    {
        const int q = (lane >> 2) & 3, ky = q | ((g & 1) << 2);
#pragma unroll
        for (int j = 0; j < 16; ++j) {
            LAS unsigned char* t = (g < 2 ? img0 : img1) + (8 * (g & 1) + q) * 512 + (((j & 7) ^ ky) + (j & 8)) * 32 + (lane & 3) * 8;
            const bf16x4 w0 = __builtin_amdgcn_ds_read_tr16_b64_v4i16((LAS bf16x4*)t);
            const bf16x4 w1 = __builtin_amdgcn_ds_read_tr16_b64_v4i16((LAS bf16x4*)(t + 2048));
            wf[j] = __builtin_shufflevector(w0, w1, 0, 1, 2, 3, 4, 5, 6, 7);
        }
        SB();
#pragma unroll
        for (int j = 0; j < 16; ++j) asm volatile("" : "+v"(wf[j]));
    }
    unsigned aa[5], pto = lane * 16;
#pragma unroll
    for (int m = 0; m < 4; ++m) aa[m] = (unsigned)(size_t)abuf + ((n >> 2) == m && ((n >> 1) & 1) == (g >> 1) ? (n & 1) * 32 + (g & 1) * 16 : zoff + g * 16);
    aa[4] = (unsigned)(size_t)abuf + ((lane >> 1) & 1) * 32 + ((lane >> 4) & 1) * 16 + ((lane >> 2) & 3) * 4 + (lane & 1) * 2;
    unsigned ix = (n & 1) ? 0xEEEEEEEEu : 0x44444444u;
    __amdgpu_buffer_rsrc_t rPt = mk_rsrc(w2 + WS_PART + __builtin_amdgcn_readfirstlane((slot >> 2) * 16384 + (lw >> 1) * 8192 + (slot & 3) * 2048 + (lw & 1) * 1024), 1024);
    asm volatile("" : "+v"(aa[0]), "+v"(aa[1]), "+v"(aa[2]), "+v"(aa[3]), "+v"(aa[4]), "+v"(pto), "+v"(ix));
    asm volatile("" : "+s"(rPt));
    f32x4 d[2] = {{0.f, 0.f, 0.f, 0.f}, {0.f, 0.f, 0.f, 0.f}};
    asm volatile("" : "+v"(d[0]), "+v"(d[1]));
    SB();
    float sv;
#pragma unroll 1
    for (int spin = 0;; ++spin) {
        sv = ((__uint_as_float(pp[0]) + __uint_as_float(pp[1])) + (__uint_as_float(pp[2]) + __uint_as_float(pp[3]))) +
             ((__uint_as_float(pp[4]) + __uint_as_float(pp[5])) + (__uint_as_float(pp[6]) + __uint_as_float(pp[7])));
        if (__builtin_expect(__builtin_amdgcn_ballot_w64(sv != sv) == 0, 1)) break;
        if (__all(max(max(max(pp[0], pp[1]), max(pp[2], pp[3])), max(max(pp[4], pp[5]), max(pp[6], pp[7]))) != SENT) || spin > SPIN_CAP) break;
        request();
    }
    unsigned ug, uu;
    swap32(__float_as_uint(sv), ug, uu);
    const float gg = rbf(__uint_as_float(ug)), up = rbf(__uint_as_float(uu));
    const unsigned act = pk_bf16(rbf(gg * __builtin_amdgcn_rcpf(1.0f + __expf(-gg))) * up, 0.f);
    *(volatile LAS unsigned short*)(size_t)aa[4] = (unsigned short)act;
    bf16x8 af[4];
#pragma unroll
    for (int m = 0; m < 4; ++m) af[m] = *(volatile LAS bf16x8*)(size_t)aa[m];
    SB();
#pragma unroll
    for (int i = 0; i < 8; ++i) {
        const int j = 8 * (i & 1) + 2 * (i >> 1);
        d[i & 1] = smf(af[i >> 1], wf[j], wf[j + 1], d[i & 1], ix);
        SB();
    }
    u32x4 pv;
#pragma unroll
    for (int q = 0; q < 4; ++q) {
        pv[q] = min(__float_as_uint(d[q >> 1][(q & 1) * 2] + d[q >> 1][(q & 1) * 2 + 1]), SENT - 1u);
        asm volatile("" : "+v"(pv[q]));
    }
    ST16_SC1(pv, rPt, pto);
}
__device__ __forceinline__ int gu_off(int slot, int w, int lane) {
    const int hi = lane >> 5, c = lane & 31;
    return (((slot & 7) * 128 + 4 * w + hi) * F_LEN + (slot >> 3) * 256 + (((c >> 1) ^ hi ^ ((w & 2) << 1)) * 16) + (c & 1) * 8) * 2;
}
__device__ __forceinline__ int wd_off(int lw, int i, int lane) {
    const int r = 2 * (i & 5) + (lane >> 5), ky = (r & 3) | ((r >> 1) & 4), c = lane & 31;
    return (lane >> 5) * (D_LEN * 2) + lw * 512 + (((c >> 1) ^ ky) * 2 + (c & 1)) * 16;
}

__global__ __launch_bounds__(768, 3) void fused_kernel(const Args a) {
    extern __shared__ __attribute__((aligned(16))) unsigned char smem[];
    constexpr int K = S_LEN, NT1 = D_LEN / BK;
    const int tid = threadIdx.x, wid = __builtin_amdgcn_readfirstlane(tid >> 6), lane = tid & 63;
    const int xcd = blockIdx.x & 7, idx = blockIdx.x >> 3;
    TSV;
    if (wid == 4) STAMP(57);
    LAS unsigned char* lds = (LAS unsigned char*)smem;
    if (idx < 1 && wid < 4) dma_word<false>(code_rsrc(), (unsigned)(size_t)lds + L_DUMMY, tid * 128);
    SB();
    const int h = xcd & 1, xq = xcd >> 1, rt = xq * 4 + (idx & 3), ct = idx >> 2;
    volatile LAS unsigned* ctl1 = (volatile LAS unsigned*)(lds + L_CTL);
#define NBR(k) ((rt + (k)) & 3)
    unsigned kx;
    Ring ring;
    if (__builtin_expect((wid & 12) == 4, 1)) {
        __builtin_amdgcn_s_setprio(3);
        ring.template setup<D_LEN, false>(wid - 4, lane);
        ring.rP = mk_rsrc(a.x + (size_t)(rt * 4 + wid - 4) * 32 * D_LEN, 32 * D_LEN * 2);
        ring.rQ = mk_rsrc((h == 0 ? a.wre : a.wim) + (size_t)(ct * 4 + wid - 4) * 32 * D_LEN, 32 * D_LEN * 2);
        ring.lb = (wid - 4) * 4096;
        ring.sp = ring.sq = ring.poff = ring.xm = 0;
        ring.template issue<false>(lds);
        __builtin_amdgcn_s_setprio(0);
        ring.par = (ct & 4) << 11;
        ring.template ramp<CATCH1>(lds);
        ring.template steady<false, false>(NT1 - 2 - CATCH1);
        ring.template setup<K, true>(wid - 4, lane);
        ring.rQ = mk_rsrc((h == 0 ? a.cr : a.ci) + (size_t)(rt * 4 + wid - 4) * 32 * K, 32 * K * 2);
        ring.seek(rt * 256, 0);
        LAS unsigned char* l0 = lds + (wid - 4) * 4096;
        ring.issueQ(l0); ring.step();
        WAIT_VM(12); BARRIER();
        ring.issueQ(l0 + STAGE_BYTES);
        ring.seek((xq * 4 + NBR(1)) * 256, 0);
        const int xr = (wid & 1) * 8 + (lane >> 4);
        int vx0 = ((((wid >> 1) & 1) * 64 + xr) * D_LEN + (((lane & 15) ^ xr) * 8)) * 2;
        int vx1 = ((((wid >> 1) & 1) * 64 + xr + 4) * D_LEN + (((lane & 15) ^ (xr + 4)) * 8)) * 2 - 1024;
        __amdgpu_buffer_rsrc_t rX = mk_rsrc(a.x + (size_t)rt * BT * D_LEN + ct * BT, BT * D_LEN * 2 - ct * BT * 2);
        unsigned xd = (unsigned)(size_t)lds + L_YB + ((wid & 2) ? 16384 : 4096) + (wid & 1) * 2048;
        unsigned xe = (wid & 2) ? xd + 12288 : (unsigned)(size_t)lds + 3 * STAGE_BYTES + (wid & 1) * 2048;
        asm volatile("" : "+v"(vx0), "+v"(vx1));
        asm volatile("" : "+s"(rX), "+s"(xd), "+s"(xe));
        WAIT_VM(8); BARRIER();
        ring.issueQ(l0 + 2 * STAGE_BYTES);
        ring.seek((xq * 4 + NBR(2)) * 256, 0);
        if (h != 0) {
#pragma unroll
            for (int k = 0; k < 3; ++k) dma_2k<false>(rX, xd + k * 4096, vx0, vx1, k * (16 * D_LEN * 2));
        }
        BARRIER();
        if (h != 0) dma_2k<false>(rX, xe, vx0, vx1, 3 * (16 * D_LEN * 2));
        ring.issueQ(l0 + 3 * STAGE_BYTES);
        kx = (unsigned)__builtin_amdgcn_readfirstlane((int)ctl1[4]);
        SB();
    }
    char* ws = a.ws + ((0ull - (unsigned long long)a.ws) & 0x1fffffull);
    const int g = lane >> 4, n = lane & 15;
    LAS float* tab = (LAS float*)(lds + L_RED);
    if (wid >= 8) {
        const int aw = wid - 8;
#define T1(t) do { if (aw == 1 && (t) <= 14) STAMP(40 + (t)); } while (0)
        GAS u64* tagp = (GAS u64*)(ws + WS_TAG);
        const int r0 = xq * 512 + h * 256 + idx * 8 + aw * 2;
        const int w0 = xq * 256 + idx * 8 + aw * 2;
        const u32x4* src = (const u32x4*)((n < 8 ? a.x + (size_t)(r0 + (n >> 2)) * D_LEN
                                                 : (h == 0 ? a.wre : a.wim) + (size_t)(w0 + (n >> 2) - 2) * D_LEN) + g * 32 + (n & 3) * 8);
        if (AUX_SLEEP) __builtin_amdgcn_s_sleep(AUX_SLEEP);
        u64 tag0 = __hip_atomic_load(tagp, __ATOMIC_RELAXED, __HIP_MEMORY_SCOPE_AGENT);
        u32x4 v[8];
#pragma unroll
        for (int j = 0; j < 4; ++j) v[j] = src[j * 16];
        __builtin_amdgcn_s_setprio(3);
        SB();
        const char* cb = (const char*)(h ? a.ci : a.cr) + (size_t)rt * (512u << 10) + ct * 65536 + (lane >> 2) * 4096 + (lane & 3) * 128;
        const GAS u32x4* wrp = (const GAS u32x4*)a.w_r + lane;
        asm volatile("" : "+v"(cb), "+v"(wrp));
        unsigned mask = aw == 1 ? CPF_MASK1 : aw == 2 ? CPF_MASK2 : 0u;
        mask = (mask & CPF_FIX) | ((mask & ~CPF_FIX) << (idx % CPF_STAG));
        int ti = 2 * xq + aw - 1;
        auto warm = [&](int t) { if (t >= 4 && ((mask >> t) & 1)) { touch_line(cb + (ti & 7) * 512, L_DUMMY); ti += 2; } };
        f32x4 d1 = {0.f, 0.f, 0.f, 0.f}, d2 = {0.f, 0.f, 0.f, 0.f};
        u32x4 ones = {0x3F803F80u, 0x3F803F80u, 0x3F803F80u, 0x3F803F80u};
        auto sums = [&](int j0) __attribute__((always_inline)) {
#pragma unroll
            for (int j = j0; j < j0 + 4; ++j) {
                const bf16x8 f = __builtin_bit_cast(bf16x8, v[j]);
                d1 = __builtin_amdgcn_mfma_f32_16x16x32_bf16(f, __builtin_bit_cast(bf16x8, ones), d1, 0, 0, 0);
                d2 = __builtin_amdgcn_mfma_f32_16x16x32_bf16(f, f, d2, 0, 0, 0);
            }
            asm volatile("" : "+v"(d1), "+v"(d2));
        };
        unsigned wl, wh;
        int ro;
        __amdgpu_buffer_rsrc_t rS;
        u64 m1 = 0xAAAAAAAAAAAAAAAAull, m2 = 0xCCCCCCCCCCCCCCCCull, mh = 0xFFFFFFFF00000000ull;
#pragma unroll
        for (int t = 0; t <= 5; ++t) {
            SB(); BARRIER(); SB();
            T1(t);
            if (t == 2) {
#pragma unroll
                for (int j = 4; j < 8; ++j) v[j] = src[j * 16];
            }
            if (t == 3) {
                asm volatile("" : "+v"(ones));
                sums(0);
                asm volatile("" : "+v"(tag0));
                const u64 want = tag0 * 6364136223846793005ull + 1442695040888963407ull;
                wl = (unsigned)want; wh = (unsigned)(want >> 32);
                asm volatile("" : "+v"(wl), "+v"(wh));
            }
            if (t == 4) { sums(4); asm volatile("s_nop 7\n\ts_nop 3" : "+v"(d2)); }
            if (t == 1) {
                rS = mk_rsrc(ws + WS_XRD, 65536);
                ro = n == g * 4 ? (g < 2 ? r0 + g : 2048 - 2 + h * 1024 + w0 + g) * 16 : 65536;
                asm volatile("" : "+v"(ro));
                asm volatile("" : "+s"(rS), "+s"(m1), "+s"(m2), "+s"(mh), "+s"(mask), "+s"(ti));
            }
        }
        float sm = (d1[0] + d1[1]) + (d1[2] + d1[3]);
        float sq = pick(m2, pick(m1, d2[0], d2[1]), pick(m1, d2[2], d2[3]));
        asm volatile("s_nop 1" : "+v"(sq));
        sq += dppf<0xB1>(sq);
        sq += dppf<0x4E>(sq);
        float mean = sm * (1.0f / D_LEN), var = rbf(rbf(fmaxf(sq * (1.0f / D_LEN) - mean * mean, 0.f)) + 1e-5f);
        asm volatile("" : "+v"(sm), "+v"(mean), "+v"(var));
        SB(); BARRIER(); SB();
        T1(6);
        {
            float rden = __builtin_amdgcn_rcpf(rbf(__builtin_amdgcn_sqrtf(var)));
            asm volatile("s_nop 1" : "+v"(rden));
            const u32x4 rec = {__float_as_uint(pick(mh, rden, sm)), wl, __float_as_uint(rbf(mean) * rden), wh};
            ST16_SC1(rec, rS, ro);
        }
        const __amdgpu_buffer_rsrc_t rV = mk_rsrc(ws + WS_VOTE, 512);
        if (aw == 3 && idx < 8) __builtin_amdgcn_raw_buffer_store_b64((u32x2){ctl1[5], wl}, rV, blockIdx.x * 8 + lane * 512, 0, 16);
        SB(); BARRIER(); SB();
        T1(STAT_M + 2);
        warm(STAT_M + 2);
        if (aw == 1 || aw == 2) {
#pragma unroll 1
            for (int t = STAT_M + 3; t <= NT1 + AUX_STAY; ++t) {
                BARRIER();
                T1(t);
                warm(t);
            }
            FLUSH();
            return;
        }
        if (aw == 3) {
            if (h == 0) {
                const u32x4 w0v = wrp[0], w1v = wrp[64];
                BARRIER(); BARRIER(); BARRIER(); BARRIER();
                SB();
                float t = 0.f;
#pragma unroll
                for (int i = 0; i < 4; ++i) { t = dot2bf(w0v[i], 0x3F803F80u, t); t = dot2bf(w1v[i], 0x3F803F80u, t); }
                SB(); BARRIER(); SB();
                t = wave_sum(t);
                ctl1[1] = __float_as_uint(t);
                WAIT_LGKM0();
            }
            return;
        }
#pragma unroll
        for (int t = STAT_M + 3; t <= POLL_T; ++t) BARRIER();
        SB();
        const int o0 = (rt * BT + lane) * 16, o1 = (2048 + (h * 8 + ct) * BT + lane) * 16, os = 1024;
        u32x4 q0, q1, q2, q3;
        u32x2 qv;
        auto request = [&]() {
            asm volatile("" ::: "memory");
            q0 = LD16_SC1(rS, o0); q1 = LD16_SC1(rS, o0 + os); q2 = LD16_SC1(rS, o1); q3 = LD16_SC1(rS, o1 + os);
            qv = __builtin_amdgcn_raw_buffer_load_b64(rV, lane * 8, 0, 16);
            SB();
        };
        request();
        int b = POLL_T + 1;
        for (int spin = 0; spin <= SPIN_CAP; ++spin) {
            if (b < NT1) { BARRIER(); ++b; }
            if (b < NT1) { BARRIER(); ++b; }
            SB();
            unsigned d = ((q0[1] ^ wl) | (q1[1] ^ wl)) | ((q2[1] ^ wl) | (q3[1] ^ wl)) | ((q0[3] ^ wh) | (q1[3] ^ wh)) | ((q2[3] ^ wh) | (q3[3] ^ wh)) | (qv[1] ^ wl);
            asm volatile("" : "+v"(d));
            if (__all(d == 0u)) break;
            request();
        }
        if (b < NT1) BARRIER();
        SB();
        LAS unsigned* tb = (LAS unsigned*)tab;
        const unsigned sg = h == 0 ? 0u : 0x80000000u;
        tb[lane] = q0[0] ^ sg; tb[64 + lane] = q1[0] ^ sg; tb[128 + lane] = q0[2] ^ sg; tb[192 + lane] = q1[2] ^ sg;
        tb[256 + lane] = q2[0]; tb[320 + lane] = q3[0];
        const unsigned vt = qv[0];
        const unsigned kx = __popcll(__ballot(vt == 2u)) > __popcll(__ballot(vt == 1u)) ? 8192u : 0u;
        if (lane == 0) { ctl1[0] = wl | 2u; ctl1[2] = wl; ctl1[3] = wh; ctl1[4] = kx; }
        *(LAS u32x2*)(lds + L_YB + 2560 + 256 + lane * 8) = (u32x2){0u, 0u};
        WAIT_LGKM0();
        return;
    }
#define CTL(i) ((unsigned)__builtin_amdgcn_readfirstlane((int)ctl[i]))
    const int wp = wid & 1, wq = (wid >> 1) & 1;
#define Y_OFF ((int)WS_Y + ((rt & 7) * 8 + ct) * 65536 + (wid & 3) * 16384 + (((rt >> 3) ^ (rt >> 2)) & 1) * 8192)
#define YS_OFF ((int)WS_YSUM + ((rt & 7) * 8 + ct) * 16384 + (rt >> 3) * 8192 + (wid & 3) * 256 + lane * 4)
#define P2_DEFS \
    char* w2 = ws + kx; \
    const int sidx = idx < 28 ? xq * 28 + idx : 112 + xq * 4 + idx - 28;     \
    volatile LAS unsigned* ctl = (volatile LAS unsigned*)(lds + (h == 0 ? L_ATAB : L_CTL)); \
    const int NT = h == 0 ? 32 + KSHIFT : 32 - KSHIFT; \
    LAS float* rstat = (LAS float*)(lds + L_ATAB + 256);     \
    const int hp_ = rt * 2 + wq;                             \
    (void)sidx; (void)ctl; (void)NT; (void)rstat; (void)hp_; (void)w2
    if (__builtin_expect(wid < 4, 1)) {
        {
            const u32x4 sent4 = {SENT, SENT, SENT, SENT}, zero4 = {0u, 0u, 0u, 0u};
            const __amdgpu_buffer_rsrc_t rI = mk_rsrc(ws + WS_STATS + (size_t)blockIdx.x * (FILL_BYTES / 256), FILL_BYTES / 256);
            const __amdgpu_buffer_rsrc_t rZ = mk_rsrc(ws + (size_t)blockIdx.x * 96, 96);
            ST16_SC1(sent4, rI, tid * 16);
            if (wid < 1) ST16_SC1(sent4, rI, 4096 + tid * 16);
            if (wid == 2) ST16_SC1(zero4, rZ, lane * 16);
        }
        if (wid == 3 && idx < 8) {
            const unsigned r = (((unsigned)__builtin_amdgcn_s_memrealtime() * 2654435761u + blockIdx.x * 40503u) >> 9) & 0x1fdf80u;
            const unsigned first = (idx & 1) << 13;
            const char* pb = ws + WS_PROBE;
            unsigned va, vb, left;
            asm volatile("s_waitcnt lgkmcnt(0)\n\ts_load_dword %0, %3, %4\n\ts_load_dword %1, %3, %5\n\ts_waitcnt lgkmcnt(1)\n\t"
                         "s_nop 15\n\ts_getreg_b32 %2, hwreg(HW_REG_IB_STS, 8, 4)\n\ts_waitcnt lgkmcnt(0)"
                         : "=&s"(va), "=&s"(vb), "=&s"(left) : "s"(pb), "s"(r | first), "s"(r | (first ^ 8192u)) : "memory");
            const unsigned nearbit = (left != 0u ? first : first ^ 8192u) >> 13;
            if (lane == 0) ctl1[5] = nearbit == (unsigned)(xcd >> 2) ? 1u : 2u;
        }
        f32x4 acc[4][4];
#pragma unroll
        for (int i = 0; i < 16; ++i) acc[i >> 2][i & 3] = (f32x4){0.f, 0.f, 0.f, 0.f};
        unsigned tc = (unsigned)(size_t)(tab + wp * 64 + g * 8), tw = (unsigned)(size_t)(tab + 256 + prow(wq * 64 + n));
        unsigned ta = (unsigned)(size_t)lds + wp * STAGE_BYTES + wq * 8192 + lane * 16;
        constexpr int NIL = 1 << 30;
        __amdgpu_buffer_rsrc_t rW = mk_rsrc(ws, 18u << 20);
        int sk = (int)WS_AT + at_panel(h * 8 + ct) + at_tile((rt * 2 + wp) * 128, (ct & 4) << 11) + wq * 16384 + lane * 16;
        int st = lane != 0 ? NIL : (int)WS_FLAG1 + (1 - wp) * 32768 + h * 16384 + (rt >> 3) * 8192 + ct * 1024 + (rt & 7) * 128 + wq * 4;
        int so = Y_OFF + lane * 16;
        unsigned sl = (unsigned)(size_t)lds + L_ST + (g != (n >> 2) ? 4096 + wid * 2048 + lane * 16 : wp * 2048 + wq * 1024 + n * 16);
        int sc = YS_OFF;
        u32x4 sel[2], aw[2];
        unsigned one[2];
#pragma unroll
        for (int i = 0; i < 4; ++i) {
            one[i & 1] = g == (n >> 2) && (i & 1) == ((n >> 1) & 1) ? ((n & 1) ? 0x3F800000u : 0x00003F80u) : 0u;
            aw[0][i] = aw[1][i] = (n & 3) == 0 ? 0x3F803F80u : 0u;
        }
        if ((n & 3) == 1) {
            const bf16_t* wr = a.w_r + ct * BT + wp * 64 + g * 8;
            aw[0] = *(const u32x4*)wr; aw[1] = *(const u32x4*)(wr + 32);
        }
        unsigned ea[2];
#pragma unroll
        for (int mp = 0; mp < 2; ++mp)
            ea[mp] = (unsigned)(size_t)lds + L_YB + (wq ? 16384 : 4096) + n * 256 + (((wp * 8 + mp * 4 + g) ^ n) * 16);
        asm volatile("" : "+v"(tc), "+v"(tw), "+v"(ta), "+v"(sk), "+v"(st), "+v"(so), "+v"(sl), "+v"(sc), "+v"(one[0]), "+v"(one[1]), "+v"(ea[0]), "+v"(ea[1]));
        asm volatile("" : "+s"(rW));
        unsigned tg;
        bf16x8 e0[4], e1[4];
        auto extra = [&]() __attribute__((always_inline)) {
            bf16x8 ef[4][2];
#pragma unroll
            for (int i = 0; i < 8; ++i)
                ef[i >> 1][i & 1] = *(const LAS bf16x8*)((const LAS unsigned char*)(size_t)(ea[i & 1] + (i < 6 || wq ? 0 : 3 * STAGE_BYTES - L_YB - 16384)) + (i >> 1) * 4096);
            SB();
#pragma unroll
            for (int i = 0; i < 16; ++i)
                acc[i & 3][i >> 2] = __builtin_amdgcn_mfma_f32_16x16x32_bf16(__builtin_bit_cast(bf16x8, sel[i & 1]), ef[i >> 2][(i >> 1) & 1], acc[i & 3][i >> 2], 0, 0, 0);
        };
        if (wid == 0) STAMP(0);
        compute_loop<false, true>(lds, acc, NT1, wp, wq, lane, [&]() { if (wid == 0) STAMP(24); WAIT_VM0(); if (wid == 0) STAMP(31); }, e0, e1, 0u);
        if (wid == 0) STAMP(25);
        {
            f32x4 rd[4], mr[4];
            float wsm[4];
#pragma unroll
            for (int m = 0; m < 4; ++m) {
                rd[m] = *(const LAS f32x4*)((const LAS float*)(size_t)tc + (m >> 1) * 32 + (m & 1) * 4);
                mr[m] = *(const LAS f32x4*)((const LAS float*)(size_t)tc + 128 + (m >> 1) * 32 + (m & 1) * 4);
                wsm[m] = *((const LAS float*)(size_t)tw + (m >> 1) * 32 + (m & 1) * 4);
            }
            const unsigned kv = ctl1[4];
            tg = ctl1[0];
            SB();
            sk += (int)kv; st += (int)kv; so += (int)kv; sc += (int)kv;
            kx = (unsigned)__builtin_amdgcn_readfirstlane((int)kv);
#pragma unroll
            for (int nn = 0; nn < 4; ++nn)
#pragma unroll
                for (int mp = 0; mp < 2; ++mp) {
                    const f32x4 u = acc[2 * mp][nn] * rd[2 * mp] - mr[2 * mp] * wsm[nn];
                    const f32x4 v = acc[2 * mp + 1][nn] * rd[2 * mp + 1] - mr[2 * mp + 1] * wsm[nn];
                    u32x4 w;
                    w[0] = pk_bf16(u[0], u[1]); w[1] = pk_bf16(u[2], u[3]);
                    w[2] = pk_bf16(v[0], v[1]); w[3] = pk_bf16(v[2], v[3]);
                    ST16_SC1(w, rW, sk + (nn * 2 + mp) * 1024);
                    *(LAS u32x4*)((LAS unsigned char*)(size_t)ta + (nn * 2 + mp) * 1024) = w;
                }
        }
        if (wid == 0) STAMP(26);
        WAIT_LGKM0();
        BARRIER();
        SB();
        {
#pragma unroll
            for (int i = 0; i < 16; ++i) { acc[i >> 2][i & 3] = (f32x4){0.f, 0.f, 0.f, 0.f}; asm volatile("" : "+v"(acc[i >> 2][i & 3])); }
            sel[0] = (u32x4){one[0], one[1], 0u, 0u}; sel[1] = (u32x4){0u, 0u, one[0], one[1]};
            asm volatile("" : "+v"(st), "+v"(so), "+v"(sc), "+v"(tg), "+v"(sel[0]), "+v"(sel[1]));
            SB();
            if (h != 0) extra();
            SB();
            WAIT_VM(0);
            __builtin_amdgcn_raw_buffer_store_b32(tg, rW, st, 0, 16);
            SB();
            if (wid == 0) STAMP(27);
        }
        compute_loop<true, false>(lds, acc, h == 0 ? 32 + KSHIFT : 32 - KSHIFT, wp, wq, lane, [&]() {
            if (wid == 0) { STAMP(h == 0 ? 1 : 19); CSTAMP(22); }
        }, e0, e1, (unsigned)((wp * 8192 + 2 * STAGE_BYTES) ^ (L_YB + wid * 8192)));
        P2_DEFS;
        if (wid == 0) { STAMP(h == 0 ? 2 : 12); CSTAMP(23); }
        u32x4 hq[4][2];
        auto cvt = [&](int nn, int j) __attribute__((always_inline)) {
            const f32x4 c = acc[j][nn];
            hq[nn][j >> 1][(j & 1) * 2] = pk_bf16(c[0], c[1]);
            hq[nn][j >> 1][(j & 1) * 2 + 1] = pk_bf16(c[2], c[3]);
        };
        int hs = h;
        asm volatile("" : "+s"(hs));
        if (__builtin_expect(hs == 0, 1)) {
            f32x4 d1[4], d2[4];
            u32x3 sv;
            auto add = [&](int nn, int j) __attribute__((always_inline)) {
                acc[j][nn] = __builtin_amdgcn_mfma_f32_16x16x32_bf16(__builtin_bit_cast(bf16x8, sel[j & 1]), j < 2 ? e0[nn] : e1[nn], acc[j][nn], 0, 0, 0);
            };
            auto sum = [&](int nn, int j) __attribute__((always_inline)) {
                const bf16x8 hf = __builtin_bit_cast(bf16x8, hq[nn][j >> 1]);
                const f32x4 z = {0.f, 0.f, 0.f, 0.f};
                if (j & 1) d1[nn] = __builtin_amdgcn_mfma_f32_16x16x32_bf16(__builtin_bit_cast(bf16x8, aw[j >> 1]), hf, j < 2 ? z : d1[nn], 0, 0, 0);
                else d2[nn] = __builtin_amdgcn_mfma_f32_16x16x32_bf16(hf, hf, j < 2 ? z : d2[nn], 0, 0, 0);
            };
            auto out = [&](int nn, int j) __attribute__((always_inline)) {
                if (j == 1) asm volatile("" : "+v"(d2[nn]));
                if (j == 2) asm volatile("" : "+v"(d1[nn]));
                if (j == 1) sv[1] = __float_as_uint(sel4(n, d2[nn][0], d2[nn][1], d2[nn][2], d2[nn][3]));
                if (j == 2) { const float u0 = d1[nn][0], u1 = d1[nn][1]; sv[0] = __float_as_uint(u0); sv[2] = __float_as_uint(u1); }
                if (j == 3) *(LAS u32x3*)((LAS unsigned char*)(size_t)sl + nn * 256) = sv;
            };
#define FOUR(m, v) _Pragma("unroll") for (int j = 0; j < 4; ++j) { m; SB(); v; SB(); }
            FOUR(add(0, j), (void)0) FOUR(add(1, j), cvt(0, j)) FOUR(sum(0, j), cvt(1, j)) FOUR(add(2, j), out(0, j))
            FOUR(sum(1, j), cvt(2, j)) FOUR(add(3, j), out(1, j)) FOUR(sum(2, j), cvt(3, j)) FOUR(sum(3, j), out(2, j))
            FOUR((void)0, out(3, j))
#undef FOUR
            WAIT_LGKM0();
            BARRIER();
            if (wid == 0) STAMP(3);
#pragma unroll
            for (int nn = 0; nn < 4; ++nn)
#pragma unroll
                for (int mp = 0; mp < 2; ++mp)
                    *(LAS u32x4*)(lds + L_HT + (wq * 64 + nn * 16 + n) * 256 + (((wp * 8 + mp * 4 + g) ^ n) * 16)) = hq[nn][mp];
            WAIT_LGKM0();
            BARRIER();
            BARRIER();
            if (wid == 0) STAMP(6);
            const int crow = __builtin_amdgcn_readfirstlane((int)((volatile LAS unsigned*)(lds + L_ATAB))[2 + wq]) + wq * 64;
            const __amdgpu_buffer_rsrc_t rOut = mk_rsrc(a.out + (size_t)rt * BT * D_LEN, BT * D_LEN * 2);
#pragma unroll 2
            for (int j = 0; j < 8; ++j) {
                const int r = wq * 64 + wp * 32 + j * 4 + g;
                const float m_ = rstat[r * 2], d_ = rstat[r * 2 + 1];
                const u32x4 hh = *(const LAS u32x4*)(lds + L_HT + r * 256 + ((n ^ (r & 15)) * 16));
                u32x4 ov;
#pragma unroll
                for (int i = 0; i < 4; ++i) {
                    const unsigned u = hh[i];
                    const float h0 = bflo(u), h1 = bfhi(u);
                    const unsigned dd = pk_bf16(h0 - m_, h1 - m_);
                    const unsigned q = pk_bf16(bflo(dd) * d_, bfhi(dd) * d_);
                    ov[i] = pk_bf16(h0 + bflo(q), h1 + bfhi(q));
                }
                __builtin_amdgcn_raw_buffer_store_b128(ov, rOut, r == crow ? BT * D_LEN * 2 : (r * D_LEN + ct * BT + n * 8) * 2, 0, 0);
            }
            FLUSH();
            return;
        }
        __builtin_amdgcn_s_setprio(3);
#pragma unroll
        for (int i = 0; i < 8; ++i) {
            cvt(i >> 1, (i & 1) * 2); cvt(i >> 1, (i & 1) * 2 + 1);
            const u32x4 w = hq[i >> 1][i & 1];
            __builtin_amdgcn_raw_buffer_store_b128(w, rW, so, i * 1024, 16);
            SB();
            tg = (tg + w[0] + w[1]) + w[2] + w[3];
            asm volatile("" : "+v"(tg));
            SB();
        }
        __builtin_amdgcn_raw_buffer_store_b32(tg, rW, sc, 0, 16);
        SB();
        __builtin_amdgcn_s_setprio(0);
        if (wid == 0) STAMP(13);
    } else {
        LAS unsigned char* l0 = lds + (wid - 4) * 4096;
        P2_DEFS;
        __amdgpu_buffer_rsrc_t rD = mk_rsrc(h == 0 ? a.wd + (size_t)sidx * 32 * D_LEN : a.wg,
                                                  sidx >= 112 ? 0 : h != 0 ? D_LEN * F_LEN * 2 : wid < 8 - BQ ? 32 * D_LEN * 2 : 0);
        int vGe = h == 0 ? wd_off(wid & 3, 0, lane)
                : gu_off(sidx, (wid & 3) * 2, lane);
        auto load_wd = [&](int i) __attribute__((always_inline)) {
            dma_nt(rD, (unsigned)(size_t)lds + (i < 8 ? L_WD0 : L_WD1) + (wid & 3) * 8192 + (i & 7) * 1024, vGe ^ ((2 * (i & 1) | (i & 4)) * 32), i * (4 * D_LEN));
        };
        const int lw = wid - 4;
        constexpr int NIL = 1 << 30;
        ring.rP = ring.rF = mk_rsrc(w2, (unsigned)WS_STATS);
        ring.voP[0] += (int)WS_AT + at_panel(h * 8 + ct);
        ring.voP[1] = ring.voP[2] = ring.voP[3] = ring.voP[0];
#define FLINE(k) (((k) >> 3) * 8192 + ((k) & 7) * 128)
        const int ft = (g & 1) * 32768 + (g >> 1) * 4 + (int)WS_FLAG1 + (lw == 0 ? ct * 1024 : NIL);
        int vall = FLINE(n) + ft + h * 16384;
        ring.voF = (n >> 2) == xq ? vall : NIL;
        ring.fl = lds + L_YB + (h == 0 ? lw * 8192 : lw ? 768 : 0) + 2560 + 256;
        unsigned sa = (unsigned)(size_t)lds + L_YB + 2560 + 256 + lane * 4;
        const unsigned tg = (unsigned)__builtin_amdgcn_readfirstlane((int)ctl1[0]);
        float wsum = __uint_as_float(ctl1[1]);
        ring.lb = lw * 4096;
        int cq[3], cp[3];
        unsigned mk[3];
#pragma unroll
        for (int k = 0; k < 3; ++k) {
            const int nb = xq * 4 + NBR(k + 1);
            cq[k] = nb * 256; cp[k] = at_tile(cq[k], ring.par); mk[k] = 0x10000u << nb;
            asm volatile("" : "+s"(cq[k]), "+s"(cp[k]), "+s"(mk[k]));
        }
        unsigned m2 = (mk[0] | mk[1] | mk[2]) >> 16;
        asm volatile("" : "+s"(m2), "+s"(ring.rP));
        asm volatile("" : "+v"(vall), "+v"(ring.voF), "+v"(ring.voP[0]), "+v"(sa), "+v"(wsum));
        ring.rF = ring.rP;
        ring.voP[1] = ring.voP[2] = ring.voP[3] = ring.voP[0];
        WAIT_VM(4);
        BARRIER();
        SB();
        int ve = Y_OFF + lane * 16, vs_ = YS_OFF;
        unsigned yd = (unsigned)(size_t)lds + L_YB + lw * 8192, yc = (unsigned)(size_t)lds + L_HT + lw * 256;
        auto fetch = [&]() __attribute__((always_inline)) {
            if (wid == 4) STAMP(21);
            const int v4[4] = {ve, ve, ve, ve};
#pragma unroll
            for (int k = 0; k < 2; ++k) dma_4k<true>(ring.rP, yd + k * 4096, v4, k * 4096);
            dma_word<true>(ring.rP, yc, vs_);
        };
        __amdgpu_buffer_rsrc_t rQ2 = mk_rsrc(h == 0 ? a.ci + (size_t)(rt * 4 + lw) * 32 * K : a.wu,
                                             h == 0 ? 32 * K * 2 : sidx < 112 ? D_LEN * F_LEN * 2 : 0);
        int vC2 = ((ct & 1) * 16 + (lane >> 2)) * (K * 2) + ((xq * 1024 + 2048 + (ct >> 1) * 512 + (lane & 3) * 128) & 4095);
        int vT2 = h == 0 && n < (KSHIFT + 1) / 2 ? FLINE((xq * 4 - 1 - n) & 15) + ft + 16384 : NIL;
        int n1 = (h == 0 ? 32 : NT) - 9, n0 = n1 - 1 - (C2_START + 2 * ct + (idx & 1));
        int xm = (idx & 1) * 128;
        int c8 = (xq * 1024 + 1024) & 4095, p8 = at_tile(c8 ^ xm, ring.par);
        int cK = (xq * 1024 + (32 - KSHIFT) * 128) & 4095, pK = at_tile(cK ^ xm, ring.par) + at_panel(8);
        auto early = [&](int st, int i0, int i1) __attribute__((always_inline)) {
#pragma unroll
            for (int i = i0; i < i1; ++i) {
                const int m = i & 1, w = (i >> 1) & 1, j = i >> 2;
                dma_nt(m ? rQ2 : rD, (unsigned)(size_t)lds + st * STAGE_BYTES + m * 16384 + lw * 4096 + w * 2048 + j * 1024, vGe ^ (j * 64 + m * 512),
                       (32 * st + 4 * w + 2 * j) * (F_LEN * 2));
            }
        };
        asm volatile("" : "+v"(vGe));
        asm volatile("" : "+s"(rQ2), "+s"(n0), "+s"(n1), "+s"(c8), "+s"(p8), "+s"(cK), "+s"(pK), "+s"(xm), "+s"(rD));
        asm volatile("" : "+v"(ve), "+v"(vs_), "+v"(vC2), "+v"(vT2));
        unsigned have = 0u;
        SB();
#define GATE(m) do { if (__builtin_expect(((m) & ~have) != 0u, 0)) ring.gate(tg, sa, m, have); SB(); } while (0)
#define TILE(k, o, st) do { ring.sp = cp[k] + (o) * 256; ring.sq = cq[k] + (o); ring.template issueP<16>(l0 + (st) * STAGE_BYTES); \
                            ring.issueQ(l0 + (st) * STAGE_BYTES); ring.sample(); SB(); } while (0)
        ring.gate(tg, sa, mk[0], have);
        if (wid == 4) STAMP(28);
        ring.sp = cp[0]; ring.template issueP<16>(l0 + 2 * STAGE_BYTES); ring.sample();
        BARRIER();
        GATE(mk[1]);
        ring.sp = cp[1]; ring.template issueP<16>(l0 + 3 * STAGE_BYTES); ring.sample();
        GATE(mk[2]);
        TILE(2, 0, 0);
        WAIT_VM(15); BARRIER();
        if (wid == 4) STAMP(30);
        GATE(m2);
        ring.voF = vall;
        TILE(0, 128, 1);
        WAIT_VM(19); BARRIER();
        TILE(1, 128, 2);
        WAIT_VM(19); BARRIER();
        ring.sp = cp[2] + 128 * 256; ring.template issueP<16>(l0 + 3 * STAGE_BYTES);
        ring.sq = cq[2] + 128;
        SB();
        {
            const unsigned long long m = __ballot(ring.peek(sa) == tg);
            have = (unsigned)m & (unsigned)(m >> 32);
            if (__builtin_expect(have != 0xffffffffu, 0)) ring.gate(tg, sa, 0xffffffffu, have);
        }
        ring.fl -= 256;
        ring.voF = vT2;
        SB();
        ring.issueQ(l0 + 3 * STAGE_BYTES);
#undef GATE
#undef TILE
        ring.sq = c8; ring.sp = p8; ring.poff = 0; ring.xm = xm;
        if (wid == 4) { STAMP(29); CSTAMP(34); STAMP(36); }
        ring.template steady<true, false, 18>(n1, n0, vC2);
        ring.fl += 512;
        if (wid == 4) { CSTAMP(35); STAMP(37); }
        ring.template issueP<16>(lds + ring.lb);
        SB();
        {
            const unsigned long long m = __ballot(ring.peek(sa + 256) == tg);
            have = (unsigned)m & (unsigned)(m >> 32);
            asm volatile("" : "+s"(have));
        }
        int hs = h;
        asm volatile("" : "+s"(hs));
        if (__builtin_expect(hs == 0, 1)) {
            constexpr unsigned MK = ((1u << ((KSHIFT + 1) / 2)) - 1u) * 0x10001u;
            if (__builtin_expect((MK & ~have) != 0u, 0)) ring.gate(tg, sa + 256, MK, have);
            unsigned lq = ring.lb + TILE_BYTES, pp = ring.par | at_panel(8);
            int sq = ring.sq ^ ring.xm, cnt = KSHIFT;
            ring.lb = (ring.lb + STAGE_BYTES) & (RING_BYTES - 1);
            asm volatile("" : "+s"(lq), "+s"(sq), "+s"(cnt), "+s"(ring.lb), "+s"(pp), "+s"(yd), "+s"(yc));
            unsigned ya = yd + lane * 16, ca = yc + lane * 4;
            asm volatile("" : "+v"(ya), "+v"(ca));
            SB();
            dma_4k<false>(ring.rQ, lq, ring.voQ, sq);
            ring.rQ = rQ2;
            ring.sq = cK; ring.sp = pK; ring.poff = 0; ring.par = pp;
            ring.template steady<true, true>(cnt);
            unsigned t0, t1;
            auto half = [&](int k) __attribute__((always_inline)) {
                u32x4 w[4];
#pragma unroll
                for (int i = 0; i < 4; ++i) w[i] = *(volatile LAS u32x4*)((LAS unsigned char*)(size_t)ya + k * 4096 + i * 1024);
                if (k) t0 -= *(volatile LAS unsigned*)(size_t)ca; else { t0 = tg; t1 = 0u; }
#pragma unroll
                for (int i = 0; i < 4; ++i) {
                    t0 = t0 + w[i][0] + w[i][1]; t1 = t1 + w[i][2] + w[i][3];
                    asm volatile("" : "+v"(t0), "+v"(t1));
                }
            };
            __builtin_amdgcn_s_setprio(3);
            fetch();
            WAIT_VM(17); BARRIER();
            WAIT_VM(5); half(0);
            BARRIER();
            WAIT_VM0(); half(1);
            if (__builtin_expect(__builtin_amdgcn_ballot_w64(t0 + t1 != 0u) != 0, 0)) {
#pragma unroll 1
                for (int spin = 0; spin <= SPIN_CAP; ++spin) {
                    if (wid == 4) STAMP(55);
                    fetch();
                    WAIT_VM0(); half(0); half(1);
                    if (__builtin_amdgcn_ballot_w64(t0 + t1 != 0u) == 0) break;
                }
            }
            if (wid == 4) STAMP(20);
        } else {
            ring.issueQ(lds + ring.lb);
            WAIT_VM(16); BARRIER();
            early(0, 0, 8);
            WAIT_VM(16); BARRIER();
            early(1, 0, 2);
            WAIT_VM(10); BARRIER();
        }
        BARRIER();
        if (h != 0) {
#pragma unroll
            for (int i = 2; i < 4; ++i) { wait_vm<TH_LIM>(); early(1, i, i + 1); }
        }
        if (h == 0) {
            const int slot = sidx;
            LAS unsigned char* abuf = lds + L_ATAB + 2048 + lw * 128;
#pragma unroll
            for (int i = 0; i < WD_EARLY; ++i) load_wd(i);
            SB();
            __amdgpu_buffer_rsrc_t rS = mk_rsrc(w2 + WS_STATS + (hp_ & 15) * 16384 + (hp_ >> 4) * 8192, 8192);
            __amdgpu_buffer_rsrc_t rC = mk_rsrc(w2 + WS_CAND + (size_t)hp_ * 4096, 4096);
            __amdgpu_buffer_rsrc_t rKy = mk_rsrc(w2 + WS_KEYS + (hp_ >> 2) * KEY_S + (hp_ & 3) * 4, ct == 0 ? 8192 + 3 * KEY_S * 16 + 4 : 0);
            int vs = lane * 16, vk = !(lane & 1) ? 1 << 20 : (lane >> 2) * 8192 + (lane & 3) * (KEY_S * 16);
            int vc = (lane < 32 ? 2048 : 0) + ct * 256 + (lane & 31) * 8;
            unsigned code = 0xffffu - (unsigned)(hp_ * 64 + lane), hb = (lane & 31) * 8;
            unsigned ra = (unsigned)(size_t)(rstat + (wq * 64 + lane) * 2), ht = (unsigned)(size_t)lds + L_HT + wq * 16384;
            u64 lo = 0xFFFFFFFFull;
            unsigned sr = (unsigned)(size_t)lds + L_ST + wq * 1024 + lane * 16;
            int vw = wp ? NIL : ct * 1024 + lane * 16;
            asm volatile("" : "+v"(sr), "+v"(vw));
            asm volatile("" : "+s"(rS), "+s"(rC), "+s"(rKy), "+s"(ht), "+s"(lo));
            asm volatile("" : "+v"(vs), "+v"(vk), "+v"(vc), "+v"(code), "+v"(hb), "+v"(ra));
            __builtin_amdgcn_s_setprio(TAIL_PRIO);
            SB();
            BARRIER();
            if (wp == 0) {
                const f32x4 u = *(const LAS f32x4*)(size_t)sr, v = *(const LAS f32x4*)((const LAS unsigned char*)(size_t)sr + 2048);
                const u32x4 w = {min(__float_as_uint(u[0] + v[0]), SENT - 1u), min(__float_as_uint(u[1] + v[1]), SENT - 1u), min(__float_as_uint(u[2] + v[2]), SENT - 1u), 0u};
                ST16_SC1(w, rS, vw);
            }
            SB();
            BARRIER();
            if (lane < 16) *(LAS unsigned*)(abuf + 64 + lane * 4) = 0u;
            __builtin_amdgcn_s_sleep(STAT_DELAY);
            if (wp == 0) {
                float s1, s2, s3;
#pragma unroll 1
                for (int spin = 0;; ++spin) {
                    asm volatile("" ::: "memory");
                    u32x3 sv[8];
#pragma unroll
                    for (int j = 0; j < 8; ++j) { sv[j] = __builtin_amdgcn_raw_buffer_load_b96(rS, vs, j * 1024, 16); SB(); }
#pragma unroll
                    for (int j = 0; j < 8; ++j) {
                        const unsigned u0 = sv[j][0], u1 = sv[j][1], u2 = sv[j][2];
                        s1 = j ? s1 + __uint_as_float(u0) : __uint_as_float(u0);
                        s2 = j ? s2 + __uint_as_float(u1) : __uint_as_float(u1);
                        s3 = j ? s3 + __uint_as_float(u2) : __uint_as_float(u2);
                    }
                    float z = s1 + s3;
                    asm volatile("" : "+v"(z));
                    if (__builtin_expect(__builtin_amdgcn_ballot_w64(__builtin_isunordered(z, s2)) == 0, 1)) break;
                    unsigned mx = 0u;
#pragma unroll
                    for (int j = 0; j < 8; ++j) mx = max(max(mx, sv[j][0]), max(sv[j][1], sv[j][2]));
                    if (__all(mx != SENT) || spin > SPIN_CAP) break;
                }
                if (wid == 4) STAMP(4);
                const float mean = s1 * (1.0f / D_LEN);
                const float var = fmaxf(s2 * (1.0f / D_LEN) - mean * mean, 0.f);
                const float mb = rbf(mean), rden = __builtin_amdgcn_rcpf(rbf(__builtin_amdgcn_sqrtf(rbf(rbf(var) + 1e-5f))));
                const unsigned kb = wave_max_s(r2key((s3 - mb * wsum) * rden, code));
                __builtin_amdgcn_raw_buffer_store_b32(kb, rKy, vk, 0, 16);
                SB();
                {
                    const unsigned cr_ = ~kb & 63u;
                    u32x2 c = *(const LAS u32x2*)(size_t)(ht + cr_ * 256 + (hb ^ ((cr_ & 15) * 16)));
                    const float m_ = __int_as_float(__builtin_amdgcn_readlane(__float_as_int(mb), cr_));
                    const float d_ = __int_as_float(__builtin_amdgcn_readlane(__float_as_int(rden), cr_));
#pragma unroll
                    for (int i = 0; i < 2; ++i) {
                        const unsigned u = c[i];
                        const unsigned dd = pk_bf16(bflo(u) - m_, bfhi(u) - m_);
                        const unsigned q = pk_bf16(bflo(dd) * d_, bfhi(dd) * d_);
                        c[i] = min(__float_as_uint(pick(lo, __uint_as_float(u), __uint_as_float(q))), SENT - 1u);
                    }
                    __builtin_amdgcn_raw_buffer_store_b64(c, rC, vc, 0, 16);
                    SB();
                    *(LAS u32x2*)(size_t)ra = (u32x2){__float_as_uint(mb), __float_as_uint(rden)};
                    if (lane == 0) ctl[2 + wq] = cr_;
                }
                WAIT_LGKM0();
                if (wid == 4) STAMP(5);
            }
            BARRIER();
#pragma unroll
            for (int i = WD_EARLY; i < 16; ++i) load_wd(i);
            if (slot < 112 && wid < 8 - BQ) {
                WAIT_VM0();
                if (wid == 4) STAMP(7);
                stage2(w2, slot, lw, lds + L_WD0 + lw * 8192, lds + L_WD1 + lw * 8192, abuf, 64, lane);
                if (wid == 4) STAMP(11);
            }
            FLUSH();
            return;
        }
    }

    P2_DEFS;
    LAS float* red = (LAS float*)(lds + L_RED);
    if (sidx < 112) {
        const int slot = sidx, grp = slot >> 3, kq = slot & 7;
        const __amdgpu_buffer_rsrc_t rG = mk_rsrc(a.wg, D_LEN * F_LEN * 2), rU = mk_rsrc(a.wu, D_LEN * F_LEN * 2);
        const int vgu = gu_off(slot, wid, lane);
        auto pair = [&](int st, int j) __attribute__((always_inline)) {
            dma_nt(rG, (unsigned)(size_t)lds + st * STAGE_BYTES + wid * 2048 + j * 1024, vgu ^ (j * 64), (32 * st + 2 * j) * (F_LEN * 2));
            dma_nt(rU, (unsigned)(size_t)lds + st * STAGE_BYTES + 16384 + wid * 2048 + j * 1024, vgu ^ (j * 64 + 512), (32 * st + 2 * j) * (F_LEN * 2));
        };
        BARRIER();
        pair(1, 1); pair(2, 0); pair(2, 1); pair(3, 0); pair(3, 1);
        if (wid >= 8 - BQ) {
            const __amdgpu_buffer_rsrc_t rD = mk_rsrc(a.wd + (size_t)slot * 32 * D_LEN, 32 * D_LEN * 2);
            const int vd = wd_off(wid - 4, 0, lane);
#pragma unroll
            for (int i = 0; i < 16; ++i)
                dma_nt(rD, (unsigned)(size_t)lds + L_YB + (wid & 1) * 16384 + i * 1024, vd ^ ((2 * (i & 1) | (i & 4)) * 32), i * (4 * D_LEN));
        }
        const __amdgpu_buffer_rsrc_t rK = mk_rsrc(w2, wid == 0 ? (unsigned)(WS_CAND + 32 * 4096) : 0u);
        const int ok_ = lane < 40 ? (int)WS_KEYS + (xcd >> 2) * 8192 + (xcd & 3) * (KEY_S * 16) + (lane - 32) * KEY_S : (int)(WS_CAND + 32 * 4096);
        const int oc = (int)WS_CAND + 2048 + kq * 256 + n * 16;
        int best = 0;
        u32x4 v = {0u, 0u, 0u, 0u};
        auto request = [&]() {
            asm volatile("" ::: "memory");
            v = LD16_SC1(rK, lane < 32 ? oc + best * 4096 : ok_);
            SB();
        };
        auto settled = [&]() {
            const unsigned mx = umax4(v), mn = min(min(v[0], v[1]), min(v[2], v[3]));
            unsigned fv = lane < 32 ? ~mx : mn | (unsigned)(lane >> 3 ^ 4);
            asm volatile("" : "+v"(fv));
            const bool fine = fv != 0u;
            unsigned km = max(mx, dppu<0xB1>(mx));
            km = max(km, dppu<0x4E>(km));
            km = max(km, dppu<0x141>(km));
            const int nb = (int)(((0xffffu - ((unsigned)__builtin_amdgcn_readlane((int)km, 32) & 0xffffu)) >> 6) & 31u);
            const bool same = nb == best;
            best = nb;
            return __all(fine) && same;
        };
        if (wid == 0) {
            request();
            WAIT_VM(0);
            settled();
            request();
        } else if (wid >= 8 - BQ) WAIT_VM(16);
        else WAIT_VM(0);
        BARRIER();
        if (wid == 0) STAMP(8);
        bf16x8 wfr[4][4];
        {
            const int q = (lane >> 2) & 3, ky = q | ((g & 1) << 2);
#pragma unroll
            for (int u = 0; u < 4; ++u) {
                const int tro = (u >> 1) * 16384 + (8 * g + q) * 512 + (((wid * 2 + (u & 1)) ^ ky) * 32) + (lane & 3) * 8;
#pragma unroll
                for (int ks = 0; ks < 4; ++ks) {
                    const bf16x4 w0 = __builtin_amdgcn_ds_read_tr16_b64_v4i16((LAS bf16x4*)(lds + tro + ks * STAGE_BYTES));
                    const bf16x4 w1 = __builtin_amdgcn_ds_read_tr16_b64_v4i16((LAS bf16x4*)(lds + tro + ks * STAGE_BYTES + 2048));
                    wfr[u][ks] = __builtin_shufflevector(w0, w1, 0, 1, 2, 3, 4, 5, 6, 7);
                }
            }
            SB();
#pragma unroll
            for (int i = 0; i < 16; ++i) asm volatile("" : "+v"(wfr[i >> 2][i & 3]));
        }
        if (wid >= 8 - BQ && lane < 16) *(LAS unsigned*)(lds + 8192 + wid * 64 + lane * 4) = 0u;
        __amdgpu_buffer_rsrc_t rPo = mk_rsrc(w2 + WS_PRE, 112 * 2048);
        int po = (n & 3) == 0 ? pre_off((grp ^ (g >> 1)) * 8 + wid) + kq * 256 + g * 64 + n * 4 : 112 * 2048;
        unsigned xw = (unsigned)(size_t)lds + (n >> 3) * 16384 + (g & 1) * 8192 + (n & 7) * 8, xr = (unsigned)(size_t)lds + (n & 1) * 8192 + g * 16;
        unsigned ix = (n & 1) ? 0xEEEEEEEEu : 0x44444444u;
        asm volatile("" : "+v"(po), "+v"(xw), "+v"(xr), "+v"(ix));
        u64 m1 = 0xFFFF0000FFFF0000ull, m2 = 0xFFFFFFFF00000000ull;
        f32x4 ac[4];
#pragma unroll
        for (int u = 0; u < 4; ++u) { ac[u] = (f32x4){0.f, 0.f, 0.f, 0.f}; asm volatile("" : "+v"(ac[u])); }
        asm volatile("" : "+s"(rPo), "+s"(m1), "+s"(m2));
        if (wid == 0) {
#pragma unroll 1
            for (int spin = 0; spin <= SPIN_CAP && !settled(); ++spin) {
                __builtin_amdgcn_s_sleep(1);
                request();
            }
            __builtin_amdgcn_s_waitcnt(0x0F70);
            if (lane < 32) *(LAS u32x2*)(size_t)xw = (u32x2){lane < 16 ? v[0] : v[1], lane < 16 ? v[2] : v[3]};
            WAIT_LGKM0();
            STAMP(9);
        }
        BARRIER();
        {
            bf16x8 xf[2];
#pragma unroll
            for (int j = 0; j < 2; ++j) xf[j] = *(const LAS bf16x8*)((const LAS unsigned char*)(size_t)xr + j * 16384);
            SB();
            float rs[3];
#pragma unroll
            for (int i = 0; i < 8; ++i) {
                const int u = i >> 1, j = i & 1;
                ac[u] = smf(xf[j], wfr[u][2 * j], wfr[u][2 * j + 1], ac[u], ix);
                SB();
                if (i == 4) { asm volatile("" : "+v"(ac[0])); rs[0] = ac[0][0] + ac[0][1]; asm volatile("" : "+v"(rs[0])); }
                if (i == 6) { asm volatile("" : "+v"(ac[1])); rs[0] = pick(m1, rs[0], ac[1][0] + ac[1][1]); asm volatile("" : "+v"(rs[0])); }
                if (i == 7) { asm volatile("" : "+v"(ac[2])); rs[2] = ac[2][0] + ac[2][1]; asm volatile("" : "+v"(rs[2])); }
                SB();
            }
            const unsigned e = min(__float_as_uint(pick(m2, rs[0], pick(m1, rs[2], ac[3][0] + ac[3][1]))), SENT - 1u);
            const u32x4 sv = {e, dppu<0x55>(e), dppu<0xAA>(e), dppu<0xFF>(e)};
            ST16_SC1(sv, rPo, po);
            SB();
            asm volatile("" :: "v"(v));
        }
        if (wid == 0) STAMP(14);
        if (wid >= 8 - BQ) {
            WAIT_VM(1);
            if (wid == 7) STAMP(15);
            stage2(w2, slot, wid - 4, lds + L_YB + (wid & 1) * 16384, lds + L_YB + (wid & 1) * 16384 + 8192, lds + wid * 64, 8192, lane);
            if (wid == 7) STAMP(10);
        }
    } else {
        const int rj = sidx - 112;
        if (blockIdx.x == 255 && tid == 0) {
            const u64 want = ((u64)ctl1[3] << 32) | ctl1[2];
            __hip_atomic_store((GAS u64*)(ws + WS_TAG), want, __ATOMIC_RELAXED, __HIP_MEMORY_SCOPE_AGENT);
        }
        if (wid == 4) {
            unsigned kk;
            const unsigned k = poll_winner(w2, xcd, lane, kk);
            if (lane == 0) ctl[4] = k;
            if (lane < 32) ctl[8 + lane] = kk;
            WAIT_LGKM0();
        }
        BARRIER();
        const unsigned kw = CTL(4);
        if (wid == 0) STAMP(16);
        {
            const unsigned kc = ctl[8 + wid * 4 + g];
            const __amdgpu_buffer_rsrc_t rL = mk_rsrc(w2 + WS_CAND + (size_t)wid * 16384, 16384);
            u32x2 hv2, nv2;
            for (int spin = 0;; ++spin) {
                asm volatile("" ::: "memory");
                hv2 = __builtin_amdgcn_raw_buffer_load_b64(rL, g * 4096 + (rj * 64 + n * 4) * 2, 0, 16);
                nv2 = __builtin_amdgcn_raw_buffer_load_b64(rL, g * 4096 + 2048 + (rj * 64 + n * 4) * 2, 0, 16);
                if (__all(max(max(hv2[0], hv2[1]), max(nv2[0], nv2[1])) != SENT) || spin > SPIN_CAP) break;
                __builtin_amdgcn_s_sleep(1);
            }
            u32x2 ov;
#pragma unroll
            for (int i = 0; i < 2; ++i) {
                const unsigned u = hv2[i], q = nv2[i];
                ov[i] = pk_bf16(bflo(u) + bflo(q), bfhi(u) + bfhi(q));
            }
            const int crow = (int)(0xffffu - (kc & 0xffffu)) & (S_LEN - 1);
            if (kc != kw) *(u32x2*)(a.out + (size_t)crow * D_LEN + rj * 64 + n * 4) = ov;
        }
        const int wrow = (int)(0xffffu - (kw & 0xffffu)) & (S_LEN - 1);
        const __amdgpu_buffer_rsrc_t rC = mk_rsrc(w2 + WS_CAND + (size_t)(wrow >> 6) * 4096, 4096);
        const __amdgpu_buffer_rsrc_t rPt = mk_rsrc(w2 + WS_PART, 112 * 4096);
        const int ocol = (rj >> 2) * 256 + ((lane >> 1) & 1) * 128 + (rj & 3) * 32 + (lane & 1) * 16 + (lane >> 2);
        unsigned hv, hnv;
        for (int spin = 0;; ++spin) {
            asm volatile("" ::: "memory");
            hv = __builtin_amdgcn_raw_buffer_load_b32(rC, (ocol & ~1) * 2, 0, 16);
            hnv = __builtin_amdgcn_raw_buffer_load_b32(rC, 2048 + (ocol & ~1) * 2, 0, 16);
            if (__all(max(hv, hnv) != SENT) || spin > SPIN_CAP) break;
        }
        float sgm = rbf(1.0f / (1.0f + __expf(-key2r(kw))));
        float hf = (ocol & 1) ? bfhi(hv) : bflo(hv), hnf = (ocol & 1) ? bfhi(hnv) : bflo(hnv);
        GAS unsigned short* optr = (GAS unsigned short*)(a.out + (size_t)wrow * D_LEN + ocol);
        unsigned rw = (unsigned)(size_t)red + (wid >> 2) * 1024 + (n * 4 + g) * 16 + (wid & 3) * 4, rr = (unsigned)(size_t)red + lane * 16;
        int po = wid * 16384 + (rj >> 3) * 8192 + g * 2048 + ((rj & 7) * 64 + n * 4) * 4;
        int po3 = po + 3 * 131072;
        asm volatile("" : "+v"(sgm), "+v"(optr), "+v"(hf), "+v"(hnf), "+v"(rw), "+v"(rr), "+v"(po), "+v"(po3));
        float sm;
#pragma unroll 1
        for (int spin = 0;; ++spin) {
            asm volatile("" ::: "memory");
            u32x4 pv[4];
#pragma unroll
            for (int i = 0; i < 4; ++i) pv[i] = __builtin_amdgcn_raw_buffer_load_b128(rPt, i < 3 ? po : po3, i < 3 ? i * 131072 : 0, 16);
            SB();
            unsigned t[4];
#pragma unroll
            for (int j = 0; j < 4; ++j) {
                const unsigned u0 = pv[0][j], u1 = pv[1][j], u2 = pv[2][j], u3 = pv[3][j];
                t[j] = __float_as_uint(((__uint_as_float(u0) + __uint_as_float(u1)) + __uint_as_float(u2)) + __uint_as_float(u3));
            }
            const auto r0 = __builtin_amdgcn_permlane32_swap(t[0], t[2], false, false), r1 = __builtin_amdgcn_permlane32_swap(t[1], t[3], false, false);
            const unsigned a0 = r0[0], a1 = r0[1], b0 = r1[0], b1 = r1[1];
            const auto r2 = __builtin_amdgcn_permlane16_swap(__float_as_uint(__uint_as_float(a0) + __uint_as_float(a1)),
                                                             __float_as_uint(__uint_as_float(b0) + __uint_as_float(b1)), false, false);
            const unsigned c0 = r2[0], c1 = r2[1];
            sm = __uint_as_float(c0) + __uint_as_float(c1);
            if (__builtin_expect(__builtin_amdgcn_ballot_w64(sm != sm) == 0, 1)) break;
            if (__all(max(max(umax4(pv[0]), umax4(pv[1])), max(umax4(pv[2]), umax4(pv[3]))) != SENT) || spin > SPIN_CAP) break;
            if (FIN_SLEEP) __builtin_amdgcn_s_sleep(FIN_SLEEP);
        }
        if (wid == 0) STAMP(17);
        *(LAS float*)(size_t)rw = sm;
        WAIT_LGKM0();
        BARRIER();
        if (wid == 0) {
            const f32x4 u = *(const LAS f32x4*)(size_t)rr, v = *(const LAS f32x4*)((const LAS unsigned char*)(size_t)rr + 1024);
            const f32x4 w = u + v;
            const float t = (w[0] + w[2]) + (w[1] + w[3]);
            *optr = (unsigned short)pk_bf16(hf + rbf(hnf + rbf(rbf(t) * sgm)), 0.f);
            STAMP(18);
            FLUSH();
            asm volatile("s_endpgm" ::: "memory");
        }
        __builtin_amdgcn_s_waitcnt(0x0F70);
    }
    FLUSH();
}

extern "C" void kernel_launch(void* const* d_in, const int* in_sizes, int n_in, void* d_out, int out_size, void* d_ws,
                              size_t ws_size, hipStream_t stream) {
    Args a;
    a.x = (const bf16_t*)d_in[0];
    a.cr = (const bf16_t*)d_in[1];
    a.ci = (const bf16_t*)d_in[2];
    a.wre = (const bf16_t*)d_in[3];
    a.wim = (const bf16_t*)d_in[4];
    a.w_r = (const bf16_t*)d_in[6];
    a.wg = (const bf16_t*)d_in[7];
    a.wu = (const bf16_t*)d_in[8];
    a.wd = (const bf16_t*)d_in[9];
    a.out = (bf16_t*)d_out;
    a.ws = (char*)d_ws + ((ws_size - ((size_t)24 << 20)) & ~(size_t)255);
    (void)hipFuncSetAttribute((const void*)fused_kernel, hipFuncAttributeMaxDynamicSharedMemorySize, LDS_BYTES);
    fused_kernel<<<256, 768, LDS_BYTES, stream>>>(a);
}
```

```cpp
#include <hip/hip_runtime.h>
#include <hip/hip_bf16.h>
#include <stdint.h>

typedef unsigned short bf16_t;
typedef short bf16x8 __attribute__((ext_vector_type(8)));
typedef short bf16x4 __attribute__((ext_vector_type(4)));
typedef float f32x4 __attribute__((ext_vector_type(4)));
typedef unsigned u32x4 __attribute__((ext_vector_type(4)));
typedef unsigned u32x2 __attribute__((ext_vector_type(2)));
typedef unsigned u32x3 __attribute__((ext_vector_type(3)));
typedef unsigned long long u64;
#define LAS __attribute__((address_space(3)))
#define GAS __attribute__((address_space(1)))

constexpr int S_LEN = 2048, D_LEN = 1024, F_LEN = 3584;
constexpr unsigned SENT = 0xFFFFFFFFu;
constexpr int SPIN_CAP = 1 << 17;

constexpr size_t WS_KEYS = 0;
constexpr size_t WS_FLAGS = 16384;
constexpr size_t WS_XRD = 65536;
constexpr size_t WS_FLAG1 = 196608;
constexpr size_t WS_TAG = 131072;
constexpr size_t WS_VOTE = WS_TAG + 4096;
constexpr size_t WS_PROBE = (size_t)20 << 20;
constexpr size_t WS_AT = (size_t)4 << 20;
constexpr size_t WS_Y = (size_t)12 << 20;
constexpr size_t WS_STATS = (size_t)16 << 20;
constexpr size_t WS_PART = WS_STATS + (256u << 10);
constexpr size_t WS_CAND = WS_STATS + (704u << 10);
constexpr size_t WS_PRE = WS_STATS + (832u << 10);
constexpr size_t FILL_BYTES = (size_t)1072 << 10;

__device__ __forceinline__ float bflo(unsigned v) { return __uint_as_float(v << 16); }
__device__ __forceinline__ float bfhi(unsigned v) { return __uint_as_float(v & 0xffff0000u); }
__device__ __forceinline__ unsigned pk_bf16(float lo, float hi) {
    typedef __bf16 bf2 __attribute__((ext_vector_type(2)));
    typedef float f2 __attribute__((ext_vector_type(2)));
    f2 v = {lo, hi};
    bf2 r = __builtin_convertvector(v, bf2);
    return __builtin_bit_cast(unsigned, r);
}
__device__ __forceinline__ float rbf(float x) { return __uint_as_float(pk_bf16(0.f, x)); }

template <int CTRL> __device__ __forceinline__ unsigned dppu(unsigned v) {
    return (unsigned)__builtin_amdgcn_update_dpp(0, (int)v, CTRL, 0xf, 0xf, true);
}
template <int CTRL> __device__ __forceinline__ float dppf(float v) { return __uint_as_float(dppu<CTRL>(__float_as_uint(v))); }
__device__ __forceinline__ void swap16(unsigned v, unsigned& a, unsigned& b) {
    auto r = __builtin_amdgcn_permlane16_swap(v, v, false, false);
    a = r[0]; b = r[1];
}
__device__ __forceinline__ void swap32(unsigned v, unsigned& a, unsigned& b) {
    auto r = __builtin_amdgcn_permlane32_swap(v, v, false, false);
    a = r[0]; b = r[1];
}
__device__ __forceinline__ float sum_rows(float v) {
    unsigned a, b;
    swap16(__float_as_uint(v), a, b); v = __uint_as_float(a) + __uint_as_float(b);
    swap32(__float_as_uint(v), a, b); v = __uint_as_float(a) + __uint_as_float(b);
    return v;
}
__device__ __forceinline__ float wave_sum(float v) {
    v += dppf<0xB1>(v);
    v += dppf<0x4E>(v);
    v += dppf<0x141>(v);
    v += dppf<0x140>(v);
    return sum_rows(v);
}
__device__ __forceinline__ unsigned wave_max_u32(unsigned v) {
    v = max(v, dppu<0xB1>(v));
    v = max(v, dppu<0x4E>(v));
    v = max(v, dppu<0x141>(v));
    v = max(v, dppu<0x140>(v));
    unsigned a, b;
    swap16(v, a, b); v = max(a, b);
    swap32(v, a, b); v = max(a, b);
    return v;
}
__device__ __forceinline__ unsigned wave_max_s(unsigned v) {
    v = max(v, dppu<0xB1>(v));
    v = max(v, dppu<0x4E>(v));
    v = max(v, dppu<0x141>(v));
    v = max(v, dppu<0x140>(v));
    v = max(v, dppu<0x142>(v));
    v = max(v, dppu<0x143>(v));
    return (unsigned)__builtin_amdgcn_readlane((int)v, 63);
}
__device__ __forceinline__ float sel4(int g, float a, float b, float c, float d) {
    const float t0 = (g & 1) ? b : a, t1 = (g & 1) ? d : c;
    return (g & 2) ? t1 : t0;
}
__device__ __forceinline__ float pick(u64 m, float a, float b) {
    float r;
    asm("v_cndmask_b32 %0, %1, %2, %3" : "=v"(r) : "v"(a), "v"(b), "s"(m));
    return r;
}
__device__ __forceinline__ unsigned umax4(u32x4 v) { return max(max(v[0], v[1]), max(v[2], v[3])); }

__device__ __forceinline__ void touch_line(const void* p, unsigned lds_dst) {
    unsigned keep;
    asm volatile("s_mov_b32 %0, m0\n\ts_mov_b32 m0, %2\n\ts_nop 0\n\tglobal_load_lds_dword %1, off\n\ts_mov_b32 m0, %0"
                 : "=&s"(keep) : "v"(p), "s"(lds_dst) : "memory");
}
__device__ __forceinline__ __amdgpu_buffer_rsrc_t mk_rsrc(const void* p, unsigned bytes) {
    return __builtin_amdgcn_make_buffer_rsrc((void*)p, 0, (int)bytes, 0x00020000);
}
#define LDS_LOAD(w, v, o, pol) "buffer_load_" w " " v ", %0, %2 offen" o pol " lds"
#define LDS_M0 "s_mov_b32 m0, %1\n\ts_nop 0\n\t"
__device__ __forceinline__ void dma_nt(__amdgpu_buffer_rsrc_t r, unsigned dst, int voff, int soff) {
    asm volatile(LDS_M0 LDS_LOAD("dwordx4", "%3", "", " nt") :: "s"(r), "s"(dst), "s"(soff), "v"(voff) : "memory", "m0");
}
template <bool SC1> __device__ __forceinline__ void dma_word(__amdgpu_buffer_rsrc_t r, unsigned dst, int voff) {
    if constexpr (SC1) asm volatile(LDS_M0 LDS_LOAD("dword", "%3", "", " sc1") :: "s"(r), "s"(dst), "n"(0), "v"(voff) : "memory", "m0");
    else asm volatile(LDS_M0 LDS_LOAD("dword", "%3", "", "") :: "s"(r), "s"(dst), "n"(0), "v"(voff) : "memory", "m0");
}
template <bool SC1> __device__ __forceinline__ void dma_2k(__amdgpu_buffer_rsrc_t r, unsigned dst, int v0, int v1, int soff) {
    if constexpr (SC1) asm volatile(LDS_M0 LDS_LOAD("dwordx4", "%3", "", " sc1") "\n\t" LDS_LOAD("dwordx4", "%4", " offset:1024", " sc1")
                 :: "s"(r), "s"(dst), "s"(soff), "v"(v0), "v"(v1) : "memory", "m0");
    else asm volatile(LDS_M0 LDS_LOAD("dwordx4", "%3", "", "") "\n\t" LDS_LOAD("dwordx4", "%4", " offset:1024", "")
                 :: "s"(r), "s"(dst), "s"(soff), "v"(v0), "v"(v1) : "memory", "m0");
}
template <bool SC1> __device__ __forceinline__ void dma_4k(__amdgpu_buffer_rsrc_t r, unsigned dst, const int (&v)[4], int soff) {
#define FOUR(pol) asm volatile(LDS_M0 LDS_LOAD("dwordx4", "%3", "", pol) "\n\t" LDS_LOAD("dwordx4", "%4", " offset:1024", pol) "\n\t" \
        LDS_LOAD("dwordx4", "%5", " offset:2048", pol) "\n\t" LDS_LOAD("dwordx4", "%6", " offset:3072", pol) \
        :: "s"(r), "s"(dst), "s"(soff), "v"(v[0]), "v"(v[1]), "v"(v[2]), "v"(v[3]) : "memory", "m0")
    if constexpr (SC1) FOUR(" sc1"); else FOUR("");
#undef FOUR
}
#define CODE_BYTES 25472
__device__ __forceinline__ __amdgpu_buffer_rsrc_t code_rsrc() {
    unsigned long long p; unsigned d;
    asm volatile("s_getpc_b64 %0\n1:\n\ts_mov_b32 %1, 1b-_Z12fused_kernel4Args" : "=s"(p), "=s"(d));
    return __builtin_amdgcn_make_buffer_rsrc((void*)(p - d), 0, CODE_BYTES, 0x00020000);
}
#define LD16_SC1(rs, off) __builtin_amdgcn_raw_buffer_load_b128(rs, off, 0, 16)
#define ST16_SC1(v, rs, off) __builtin_amdgcn_raw_buffer_store_b128(v, rs, off, 0, 16)

constexpr int BT = 128, BK = 64, NSTAGE = 4;
constexpr int TILE_BYTES = BT * BK * 2;
constexpr int STAGE_BYTES = 2 * TILE_BYTES;
constexpr int RING_BYTES = NSTAGE * STAGE_BYTES;
constexpr int L_DUMMY = RING_BYTES;
constexpr int L_CTL = RING_BYTES + 256;
constexpr int L_RED = RING_BYTES + 512;

#define WAIT_VM(n) asm volatile("s_waitcnt vmcnt(" #n ")" ::: "memory")
#define WAIT_VM0() do { asm volatile("" ::: "memory"); __builtin_amdgcn_s_waitcnt(0x0F70); asm volatile("s_waitcnt vmcnt(0)" ::: "memory"); } while (0)
#define WAIT_LGKM0() asm volatile("s_waitcnt lgkmcnt(0)" ::: "memory")
#define BARRIER() do { asm volatile("" ::: "memory"); __builtin_amdgcn_s_barrier(); asm volatile("" ::: "memory"); } while (0)

__device__ __forceinline__ int prow(int rho) {
    const int w = rho >> 6, m = (rho >> 4) & 3, q = (rho >> 2) & 3, r = rho & 3;
    return w * 64 + (m >> 1) * 32 + q * 8 + (m & 1) * 4 + r;
}
__device__ __forceinline__ unsigned r2key(float r, unsigned code) {
    const unsigned u = pk_bf16(0.f, r);
    return ((u ^ ((unsigned)((int)u >> 31) | 0x80000000u)) & 0xffff0000u) | code;
}
__device__ __forceinline__ float key2r(unsigned k) {
    const unsigned o = k >> 16;
    return bflo((o & 0x8000u) ? (o & 0x7fffu) : (~o & 0xffffu));
}
#define STAT_M 5
#define BQ 2
#define POLL_T (STAT_M + 5)
#define AUX_SLEEP 12
#define WD_EARLY 4
#define C2_START 2
#define TAIL_PRIO 3
#define STAT_DELAY 8
#define AUX_STAY 1
#define CATCH1 4
#define CPF_MASK1 0x1080u
#define CPF_MASK2 0x1080u
#define CPF_STAG 5
#define CPF_FIX 0x1000u
#define KEY_S 128
#define TH_LIM 6
#define TH_N 6
#define FIN_SLEEP 1
#define POLL_SLEEP 4
__device__ __forceinline__ f32x4 smf(bf16x8 a, bf16x8 b0, bf16x8 b1, f32x4 d, unsigned idx) {
    typedef __bf16 bf8 __attribute__((ext_vector_type(8)));
    typedef __bf16 bf16v __attribute__((ext_vector_type(16)));
    const auto b = __builtin_shufflevector(b0, b1, 0, 1, 2, 3, 4, 5, 6, 7, 8, 9, 10, 11, 12, 13, 14, 15);
    return __builtin_amdgcn_smfmac_f32_16x16x64_bf16(__builtin_bit_cast(bf8, a), __builtin_bit_cast(bf16v, b), d, (int)idx, 0, 0);
}
__device__ __forceinline__ float dot2bf(unsigned a, unsigned b, float c) {
    typedef __bf16 bf2 __attribute__((ext_vector_type(2)));
    return __builtin_amdgcn_fdot2_f32_bf16(__builtin_bit_cast(bf2, a), __builtin_bit_cast(bf2, b), c, false);
}

#define STAMP(i) do { } while (0)
#define CSTAMP(i) do { } while (0)
#define FLUSH() do { } while (0)
#define TSV do { } while (0)

#define KSHIFT 4

constexpr int L_HT = ((32 + KSHIFT) % NSTAGE) * STAGE_BYTES;
constexpr int L_WD0 = ((33 + KSHIFT) % NSTAGE) * STAGE_BYTES;
constexpr int L_WD1 = ((34 + KSHIFT) % NSTAGE) * STAGE_BYTES;
constexpr int L_ST = ((35 + KSHIFT) % NSTAGE) * STAGE_BYTES;
constexpr int L_YB = RING_BYTES;
constexpr int L_ATAB = L_YB;
constexpr int LDS_BYTES = RING_BYTES + 32768;

#define SB() __builtin_amdgcn_sched_barrier(0)
template <int N> __device__ __forceinline__ void wait_vm() { asm volatile("s_waitcnt vmcnt(%0)" :: "n"(N) : "memory"); }
__device__ __forceinline__ int at_tile(int c, int par) { return ((c << 8) & 0x78000) | (((c << 2) & 0x2000) ^ par); }
__device__ __forceinline__ int at_panel(int ep) { return ep << 19; }
__device__ __forceinline__ int pre_off(int slot) { const int s_ = slot >= 56, l = slot - 56 * s_; return (l >> 2) * 16384 + s_ * 8192 + (l & 3) * 2048; }
static_assert(KSHIFT == 4, "flag samples must have replaced older contents of their slot; even tile counts");
struct Ring {
    __amdgpu_buffer_rsrc_t rP, rQ, rF;
    int voP[4], voQ[4], voF;
    LAS unsigned char* fl;
    unsigned lb;
    int sp, sq;
    int poff, par, xm;
    bool img;
    template <int LD, bool PIMG> __device__ __forceinline__ void setup(int lw, int lane) {
        const int r = lane >> 3, v0 = (PIMG ? r : (r & 3) + (r & 4) * 2) * (LD * 2) + (((lane & 7) ^ (lane >> 4)) << 4);
#pragma unroll
        for (int j = 0; j < 4; ++j) {
            voQ[j] = (v0 ^ ((j & 1) * 64)) + (PIMG ? j * 8 : (j & 1) * 16 + (j >> 1) * 4) * (LD * 2) - j * 1024;
            voP[j] = PIMG ? (lw >> 1) * 16384 + (lw & 1) * 4096 + lane * 16 : voQ[j];
        }
        img = PIMG;
    }
    __device__ __forceinline__ void seek(int col, int pof) {
        sq = col; poff = pof;
        sp = (img ? at_tile(sq, par) : sq) + poff;
    }
    __device__ __forceinline__ void step() {
        sq = (sq + BK * 2) & 4095;
        sp = (img ? at_tile(sq ^ xm, par) : sq) + poff;
        asm volatile("" : "+s"(sq), "+s"(sp));
    }
    __device__ __forceinline__ void sample() { dma_word<true>(rF, (unsigned)(size_t)fl, voF); }
    __device__ __forceinline__ unsigned peek(unsigned addr) {
        unsigned v;
        asm volatile("ds_read_b32 %0, %1\n\ts_waitcnt lgkmcnt(0)" : "=v"(v) : "v"(addr) : "memory");
        return v;
    }
    __device__ __forceinline__ void gate(unsigned want, unsigned sa, unsigned need, unsigned& have) {
#pragma unroll 1
        for (int spin = 0; spin <= SPIN_CAP && (need & ~have) != 0u; ++spin) {
            if (spin) { sample(); WAIT_VM(0); __builtin_amdgcn_s_sleep(1); }
            const unsigned long long m = __ballot(peek(sa) == want);
            have = (unsigned)m & (unsigned)(m >> 32);
        }
    }
    template <int AUX> __device__ __forceinline__ void issueP(LAS unsigned char* base) { dma_4k<AUX != 0>(rP, (unsigned)(size_t)base, voP, sp); }
    __device__ __forceinline__ void issueQ(LAS unsigned char* base) { dma_4k<false>(rQ, (unsigned)(size_t)base + TILE_BYTES, voQ, sq ^ xm); }
    template <bool P2> __device__ __forceinline__ void issue(LAS unsigned char* lds) {
        issueP<P2 ? 16 : 0>(lds + lb);
        issueQ(lds + lb);
        SB();
        if (P2) sample();
        lb = (lb + STAGE_BYTES) & (RING_BYTES - 1);
        asm volatile("" : "+s"(lb));
        step();
        SB();
    }
    template <int CAT> __device__ __forceinline__ void ramp(LAS unsigned char* lds) {
#pragma unroll 1
        for (int i = 1; i <= CAT + 1; ++i) {
            issue<false>(lds);
            if (i == CAT + 1) continue;
            WAIT_VM(8);
            BARRIER();
        }
    }
    template <bool P2, bool SAMP, int W0 = 16> __device__ __forceinline__ void steady(int cnt, int trig = 0, int v2 = 0) {
        int t, u;
#define LD(v, r, s, o) "buffer_load_dwordx4 " v ", " r ", " s " offen" o " lds\n\t"
        if constexpr (P2) {
#define BODY(SMP, TST, ENT) asm volatile("s_mov_b32 m0, %0\n" ENT "1:\n\ts_waitcnt vmcnt(16)\n2:\n\ts_barrier\n\t" \
            LD("%8", "%6", "%2", " sc1") LD("%8", "%6", "%2", " offset:1024 sc1") LD("%8", "%6", "%2", " offset:2048 sc1") LD("%8", "%6", "%2", " offset:3072 sc1") \
            SMP "s_xor_b32 %4, %1, %18\n\ts_add_i32 %1, %1, 0x80\n\ts_add_i32 m0, %0, 0x4000\n\ts_and_b32 %1, %1, 0xfff\n\t" \
            LD("%9", "%7", "%4", "") "s_xor_b32 %2, %1, %18\n\ts_lshl_b32 %5, %1, 2\n\ts_lshl_b32 %2, %2, 8\n\ts_and_b32 %2, %2, 0x78000\n\t" \
            LD("%10", "%7", "%4", " offset:1024") "s_and_b32 %5, %5, 0x2000\n\ts_xor_b32 %5, %5, %13\n\ts_or_b32 %2, %2, %5\n\ts_add_i32 %3, %3, -1\n\t" \
            LD("%11", "%7", "%4", " offset:2048") "s_add_i32 %0, %0, 0x8000\n\ts_and_b32 %0, %0, 0x1ffff\n\t" TST "s_cmp_lg_u32 %3, 0\n\t" \
            LD("%12", "%7", "%4", " offset:3072") "\n4:\n\ts_mov_b32 m0, %0\n\ts_cbranch_scc1 1b\n\ts_waitcnt vmcnt(16)\n\ts_barrier" \
            : "+s"(lb), "+s"(sq), "+s"(sp), "+s"(cnt), "=&s"(t), "=&s"(u) \
            : "s"(rP), "s"(rQ), "v"(voP[0]), "v"(voQ[0]), "v"(voQ[1]), "v"(voQ[2]), "v"(voQ[3]), "s"(par | poff), "s"(rF), "v"(voF), "s"((unsigned)(size_t)fl), "n"(W0), "s"(xm), \
              "s"(trig), "v"(v2) \
            : "memory", "scc", "m0")
            if constexpr (SAMP) BODY("s_mov_b32 m0, %16\n\ts_nop 0\n\tbuffer_load_dword %15, %14, 0 offen sc1 lds\n\t", "", "");
            else BODY("", "s_cmp_eq_u32 %3, %19\n\ts_cbranch_scc1 3b\n\t",
                      "\ts_waitcnt vmcnt(%17)\n\ts_branch 2f\n3:\n\t" LD("%12", "%7", "%4", " offset:3072") "s_mov_b32 m0, %16\n\ts_nop 0\n\tbuffer_load_dword %20, %7, 0 offen lds\n\t"
                      "s_add_i32 m0, %16, 0x200\n\ts_cmp_lg_u32 %3, 0\n\tbuffer_load_dword %15, %14, 0 offen sc1 lds\n\ts_branch 4f\n");
#undef BODY
        } else {
            asm volatile("s_mov_b32 m0, %0\n\ts_branch 2f\n1:\n\ts_waitcnt vmcnt(16)\n\ts_barrier\n2:\n\t"
                LD("%6", "%4", "%1", "") LD("%7", "%4", "%1", " offset:1024") LD("%8", "%4", "%1", " offset:2048") LD("%9", "%4", "%1", " offset:3072")
                "s_add_i32 m0, %0, 0x4000\n\ts_mov_b32 %3, %1\n\t"
                LD("%6", "%5", "%3", "") "s_add_i32 %1, %1, 0x80\n\ts_add_i32 %0, %0, 0x8000\n\t"
                LD("%7", "%5", "%3", " offset:1024") "s_and_b32 %0, %0, 0x1ffff\n\ts_add_i32 %2, %2, -1\n\t"
                LD("%8", "%5", "%3", " offset:2048") "s_cmp_lg_u32 %2, 0\n\t"
                LD("%9", "%5", "%3", " offset:3072") "s_mov_b32 m0, %0\n\ts_cbranch_scc1 1b\n\ts_waitcnt vmcnt(16)\n\ts_barrier"
                : "+s"(lb), "+s"(sq), "+s"(cnt), "=&s"(t)
                : "s"(rP), "s"(rQ), "v"(voQ[0]), "v"(voQ[1]), "v"(voQ[2]), "v"(voQ[3])
                : "memory", "scc", "m0");
            (void)u;
        }
#undef LD
    }
};

template <bool PLIN, bool FIRST, class Pre>
__device__ __forceinline__ void compute_loop(const LAS unsigned char* lds, f32x4 (&acc)[4][4], int NT, int wp, int wq, int lane, Pre pre,
                                             bf16x8 (&p0)[4], bf16x8 (&p1)[4], unsigned last) {
    const int fr = lane & 15, fq = lane >> 4, key = (fr >> 1) & 7;
    const unsigned l0 = (unsigned)(size_t)lds;
    const unsigned oP0 = l0 + (PLIN ? wp * 8192 + lane * 16 : (wp * 64 + fr) * 128 + ((fq ^ key) * 16));
    const unsigned oP1 = l0 + (PLIN ? wp * 8192 + 1024 + lane * 16 : (wp * 64 + fr) * 128 + (((4 + fq) ^ key) * 16));
    const unsigned oQ0 = l0 + TILE_BYTES + (wq * 64 + fr) * 128 + ((fq ^ key) * 16);
    const unsigned oQ1 = l0 + TILE_BYTES + (wq * 64 + fr) * 128 + (((4 + fq) ^ key) * 16);
    unsigned aP0 = oP0, aQ0 = oQ0, aP1 = oP1, aQ1 = oQ1;
    bf16x8 r0[4], r1[4], q0[4], q1[4];
    unsigned xp = 2 * STAGE_BYTES;
#pragma unroll
    for (int i = 0; i < 16; ++i) asm volatile("" : "+v"(acc[i >> 2][i & 3]));
    asm volatile("" : "+v"(aP0), "+v"(aQ0), "+v"(aP1), "+v"(aQ1));
#define MM(pp, qq, m, n) do { acc[m][n] = __builtin_amdgcn_mfma_f32_16x16x32_bf16(pp[m], qq[n], acc[m][n], 0, 0, 0); SB(); } while (0)
#define RD(dst, i, a, st) do { dst[i] = *(const LAS bf16x8*)((const LAS unsigned char*)(size_t)(a) + (st) * STAGE_BYTES + (i) * 2048); SB(); } while (0)
#define ADP(a) do { a ^= xp; asm volatile("" : "+v"(a)); SB(); } while (0)
#define ADR(a) do { a ^= 2 * STAGE_BYTES; asm volatile("" : "+v"(a)); SB(); } while (0)
    __builtin_amdgcn_s_waitcnt(0xC07F);
    SB();
    if (FIRST) BARRIER();
    SB();
    RD(p0, 0, aP0, 0); RD(q0, 0, aQ0, 0); RD(p1, 0, aP1, 0); RD(q1, 0, aQ1, 0); RD(p1, 1, aP1, 0); RD(p0, 1, aP0, 0);
    RD(p0, 2, aP0, 0); RD(p1, 2, aP1, 0); RD(p1, 3, aP1, 0); RD(p0, 3, aP0, 0);
    pre();
    SB();
    int t = NT;
#pragma unroll 1
    do {
        MM(p0, q0, 0, 0); MM(p1, q1, 0, 0); RD(q0, 1, aQ0, 0);
        MM(p1, q1, 1, 0); MM(p0, q0, 1, 0); RD(q1, 1, aQ1, 0);
        MM(p0, q0, 2, 0); MM(p1, q1, 2, 0); RD(q0, 2, aQ0, 0);
        MM(p1, q1, 3, 0); MM(p0, q0, 3, 0); RD(q1, 2, aQ1, 0);
        MM(p0, q0, 3, 1); MM(p1, q1, 3, 1); RD(q0, 3, aQ0, 0);
        MM(p1, q1, 2, 1); MM(p0, q0, 2, 1); RD(q1, 3, aQ1, 0);
        MM(p0, q0, 1, 1); MM(p1, q1, 1, 1);
        MM(p1, q1, 0, 1); MM(p0, q0, 0, 1);
        MM(p0, q0, 0, 2); MM(p1, q1, 0, 2); __builtin_amdgcn_s_waitcnt(0xC07F); SB(); BARRIER(); SB();
        MM(p1, q1, 1, 2); RD(r0, 0, aP0, 1); MM(p0, q0, 1, 2); RD(q0, 0, aQ0, 1);
        MM(p0, q0, 2, 2); RD(r1, 0, aP1, 1); MM(p1, q1, 2, 2); RD(q1, 0, aQ1, 1);
        MM(p1, q1, 3, 2); MM(p0, q0, 3, 2); RD(r1, 1, aP1, 1);
        MM(p0, q0, 3, 3); RD(r0, 1, aP0, 1); MM(p1, q1, 3, 3);
        MM(p1, q1, 2, 3); RD(r0, 2, aP0, 1); MM(p0, q0, 2, 3); RD(r1, 2, aP1, 1);
        MM(p0, q0, 1, 3); MM(p1, q1, 1, 3); RD(r1, 3, aP1, 1);
        MM(p1, q1, 0, 3); RD(r0, 3, aP0, 1); MM(p0, q0, 0, 3);

        MM(r0, q0, 0, 0); MM(r1, q1, 0, 0); RD(q0, 1, aQ0, 1);
        MM(r1, q1, 1, 0); MM(r0, q0, 1, 0); RD(q1, 1, aQ1, 1);
        MM(r0, q0, 2, 0); MM(r1, q1, 2, 0); RD(q0, 2, aQ0, 1);
        MM(r1, q1, 3, 0); MM(r0, q0, 3, 0); RD(q1, 2, aQ1, 1);
        MM(r0, q0, 3, 1); MM(r1, q1, 3, 1); RD(q0, 3, aQ0, 1);
        MM(r1, q1, 2, 1); MM(r0, q0, 2, 1); RD(q1, 3, aQ1, 1);
        MM(r0, q0, 1, 1); t -= 2; if (PLIN) xp = t == 0 ? last : 2 * STAGE_BYTES; asm volatile("" : "+s"(t), "+s"(xp)); SB(); MM(r1, q1, 1, 1); ADP(aP0);
        MM(r1, q1, 0, 1); ADP(aP1); MM(r0, q0, 0, 1); ADR(aQ0);
        MM(r0, q0, 0, 2); ADR(aQ1); MM(r1, q1, 0, 2); __builtin_amdgcn_s_waitcnt(0xC07F); SB(); BARRIER(); SB();
        MM(r1, q1, 1, 2); RD(p0, 0, aP0, 0); MM(r0, q0, 1, 2); RD(q0, 0, aQ0, 0);
        MM(r0, q0, 2, 2); RD(p1, 0, aP1, 0); MM(r1, q1, 2, 2); RD(q1, 0, aQ1, 0);
        MM(r1, q1, 3, 2); MM(r0, q0, 3, 2); RD(p1, 1, aP1, 0);
        MM(r0, q0, 3, 3); RD(p0, 1, aP0, 0); MM(r1, q1, 3, 3);
        MM(r1, q1, 2, 3); RD(p0, 2, aP0, 0); MM(r0, q0, 2, 3); RD(p1, 2, aP1, 0);
        MM(r0, q0, 1, 3); MM(r1, q1, 1, 3); RD(p1, 3, aP1, 0);
        MM(r1, q1, 0, 3); RD(p0, 3, aP0, 0); MM(r0, q0, 0, 3);
    } while (t != 0);
    SB();
#undef MM
#undef RD
#undef ADR
#undef ADP
}

struct Args {
    const bf16_t* x; const bf16_t* wre; const bf16_t* wim; char* ws; const bf16_t* cr; const bf16_t* ci;
    const bf16_t* w_r; const bf16_t* wg; const bf16_t* wu; const bf16_t* wd; bf16_t* out;
};

__device__ __forceinline__ unsigned poll_winner(const char* ws, int rep, int lane, unsigned& mine) {
    GAS unsigned* kp = (GAS unsigned*)(ws + WS_KEYS + (size_t)(rep >> 2) * 8192 + (rep & 3) * (KEY_S * 16) + ((lane >> 2) & 7) * KEY_S) + (lane & 3);
    unsigned kw;
    for (int spin = 0;; ++spin) {
            asm volatile("" ::: "memory");
        kw = __hip_atomic_load(kp, __ATOMIC_RELAXED, __HIP_MEMORY_SCOPE_AGENT);
        if (__all(kw != 0u) || spin > SPIN_CAP) break;
        __builtin_amdgcn_s_sleep(POLL_SLEEP);
    }
    mine = kw;
    return __builtin_amdgcn_readfirstlane(wave_max_u32(kw));
}

__device__ __forceinline__ void stage2(char* w2, int slot, int lw, LAS unsigned char* img0, LAS unsigned char* img1, LAS unsigned char* abuf, int zoff, int lane) {
    const int g = lane >> 4, n = lane & 15;
    const __amdgpu_buffer_rsrc_t rPi = mk_rsrc(w2 + WS_PRE + pre_off(slot), 2048);
    unsigned pp[8];
    auto request = [&]() {
        asm volatile("" ::: "memory");
#pragma unroll
        for (int k = 0; k < 8; ++k) pp[k] = __builtin_amdgcn_raw_buffer_load_b32(rPi, k * 256 + lane * 4, 0, 16);
        SB();
    };
    request();
    bf16x8 wf[16];
    {
        const int q = (lane >> 2) & 3, ky = q | ((g & 1) << 2);
#pragma unroll
        for (int j = 0; j < 16; ++j) {
            LAS unsigned char* t = (g < 2 ? img0 : img1) + (8 * (g & 1) + q) * 512 + (((j & 7) ^ ky) + (j & 8)) * 32 + (lane & 3) * 8;
            const bf16x4 w0 = __builtin_amdgcn_ds_read_tr16_b64_v4i16((LAS bf16x4*)t);
            const bf16x4 w1 = __builtin_amdgcn_ds_read_tr16_b64_v4i16((LAS bf16x4*)(t + 2048));
            wf[j] = __builtin_shufflevector(w0, w1, 0, 1, 2, 3, 4, 5, 6, 7);
        }
        SB();
#pragma unroll
        for (int j = 0; j < 16; ++j) asm volatile("" : "+v"(wf[j]));
    }
    unsigned aa[5], pto = lane * 16;
#pragma unroll
    for (int m = 0; m < 4; ++m) aa[m] = (unsigned)(size_t)abuf + ((n >> 2) == m && ((n >> 1) & 1) == (g >> 1) ? (n & 1) * 32 + (g & 1) * 16 : zoff + g * 16);
    aa[4] = (unsigned)(size_t)abuf + ((lane >> 1) & 1) * 32 + ((lane >> 4) & 1) * 16 + ((lane >> 2) & 3) * 4 + (lane & 1) * 2;
    unsigned ix = (n & 1) ? 0xEEEEEEEEu : 0x44444444u;
    __amdgpu_buffer_rsrc_t rPt = mk_rsrc(w2 + WS_PART + __builtin_amdgcn_readfirstlane((slot >> 2) * 16384 + (lw >> 1) * 8192 + (slot & 3) * 2048 + (lw & 1) * 1024), 1024);
    asm volatile("" : "+v"(aa[0]), "+v"(aa[1]), "+v"(aa[2]), "+v"(aa[3]), "+v"(aa[4]), "+v"(pto), "+v"(ix));
    asm volatile("" : "+s"(rPt));
    f32x4 d[2] = {{0.f, 0.f, 0.f, 0.f}, {0.f, 0.f, 0.f, 0.f}};
    asm volatile("" : "+v"(d[0]), "+v"(d[1]));
    SB();
    float sv;
#pragma unroll 1
    for (int spin = 0;; ++spin) {
        sv = ((__uint_as_float(pp[0]) + __uint_as_float(pp[1])) + (__uint_as_float(pp[2]) + __uint_as_float(pp[3]))) +
             ((__uint_as_float(pp[4]) + __uint_as_float(pp[5])) + (__uint_as_float(pp[6]) + __uint_as_float(pp[7])));
        if (__builtin_expect(__builtin_amdgcn_ballot_w64(sv != sv) == 0, 1)) break;
        if (__all(max(max(max(pp[0], pp[1]), max(pp[2], pp[3])), max(max(pp[4], pp[5]), max(pp[6], pp[7]))) != SENT) || spin > SPIN_CAP) break;
        request();
    }
    unsigned ug, uu;
    swap32(__float_as_uint(sv), ug, uu);
    const float gg = rbf(__uint_as_float(ug)), up = rbf(__uint_as_float(uu));
    const unsigned act = pk_bf16(rbf(gg * __builtin_amdgcn_rcpf(1.0f + __expf(-gg))) * up, 0.f);
    *(volatile LAS unsigned short*)(size_t)aa[4] = (unsigned short)act;
    bf16x8 af[4];
#pragma unroll
    for (int m = 0; m < 4; ++m) af[m] = *(volatile LAS bf16x8*)(size_t)aa[m];
    SB();
#pragma unroll
    for (int i = 0; i < 8; ++i) {
        const int j = 8 * (i & 1) + 2 * (i >> 1);
        d[i & 1] = smf(af[i >> 1], wf[j], wf[j + 1], d[i & 1], ix);
        SB();
    }
    u32x4 pv;
#pragma unroll
    for (int q = 0; q < 4; ++q) {
        pv[q] = min(__float_as_uint(d[q >> 1][(q & 1) * 2] + d[q >> 1][(q & 1) * 2 + 1]), SENT - 1u);
        asm volatile("" : "+v"(pv[q]));
    }
    ST16_SC1(pv, rPt, pto);
}
__device__ __forceinline__ int gu_off(int slot, int w, int lane) {
    const int hi = lane >> 5, c = lane & 31;
    return (((slot & 7) * 128 + 4 * w + hi) * F_LEN + (slot >> 3) * 256 + (((c >> 1) ^ hi ^ ((w & 2) << 1)) * 16) + (c & 1) * 8) * 2;
}
__device__ __forceinline__ int wd_off(int lw, int i, int lane) {
    const int r = 2 * (i & 5) + (lane >> 5), ky = (r & 3) | ((r >> 1) & 4), c = lane & 31;
    return (lane >> 5) * (D_LEN * 2) + lw * 512 + (((c >> 1) ^ ky) * 2 + (c & 1)) * 16;
}

__global__ __launch_bounds__(768, 3) void fused_kernel(const Args a) {
    extern __shared__ __attribute__((aligned(16))) unsigned char smem[];
    constexpr int K = S_LEN, NT1 = D_LEN / BK;
    const int tid = threadIdx.x, wid = __builtin_amdgcn_readfirstlane(tid >> 6), lane = tid & 63;
    const int xcd = blockIdx.x & 7, idx = blockIdx.x >> 3;
    TSV;
    if (wid == 4) STAMP(57);
    LAS unsigned char* lds = (LAS unsigned char*)smem;
    if (idx < 1 && wid < 4) dma_word<false>(code_rsrc(), (unsigned)(size_t)lds + L_DUMMY, tid * 128);
    SB();
    const int h = xcd & 1, xq = xcd >> 1, rt = xq * 4 + (idx & 3), ct = idx >> 2;
    volatile LAS unsigned* ctl1 = (volatile LAS unsigned*)(lds + L_CTL);
#define NBR(k) ((rt + (k)) & 3)
    unsigned kx;
    Ring ring;
    if (__builtin_expect((wid & 12) == 4, 1)) {
        __builtin_amdgcn_s_setprio(3);
        ring.template setup<D_LEN, false>(wid - 4, lane);
        ring.rP = mk_rsrc(a.x + (size_t)(rt * 4 + wid - 4) * 32 * D_LEN, 32 * D_LEN * 2);
        ring.rQ = mk_rsrc((h == 0 ? a.wre : a.wim) + (size_t)(ct * 4 + wid - 4) * 32 * D_LEN, 32 * D_LEN * 2);
        ring.lb = (wid - 4) * 4096;
        ring.sp = ring.sq = ring.poff = ring.xm = 0;
        if (wid == 4) STAMP(58);
        ring.template issue<false>(lds);
        if (wid == 4) STAMP(59);
        __builtin_amdgcn_s_setprio(0);
        ring.par = (ct & 4) << 11;
        ring.template ramp<CATCH1>(lds);
        ring.template steady<false, false>(NT1 - 2 - CATCH1);
        ring.template setup<K, true>(wid - 4, lane);
        ring.rQ = mk_rsrc((h == 0 ? a.cr : a.ci) + (size_t)(rt * 4 + wid - 4) * 32 * K, 32 * K * 2);
        ring.seek(rt * 256, 0);
        LAS unsigned char* l0 = lds + (wid - 4) * 4096;
        ring.issueQ(l0); ring.step();
        WAIT_VM(12); BARRIER();
        ring.issueQ(l0 + STAGE_BYTES);
        ring.seek((xq * 4 + NBR(1)) * 256, 0);
        const int xr = (wid & 1) * 8 + (lane >> 4);
        int vx0 = ((((wid >> 1) & 1) * 64 + xr) * D_LEN + (((lane & 15) ^ xr) * 8)) * 2;
        int vx1 = ((((wid >> 1) & 1) * 64 + xr + 4) * D_LEN + (((lane & 15) ^ (xr + 4)) * 8)) * 2 - 1024;
        __amdgpu_buffer_rsrc_t rX = mk_rsrc(a.x + (size_t)rt * BT * D_LEN + ct * BT, BT * D_LEN * 2 - ct * BT * 2);
        unsigned xd = (unsigned)(size_t)lds + L_YB + ((wid & 2) ? 16384 : 4096) + (wid & 1) * 2048;
        unsigned xe = (wid & 2) ? xd + 12288 : (unsigned)(size_t)lds + 3 * STAGE_BYTES + (wid & 1) * 2048;
        asm volatile("" : "+v"(vx0), "+v"(vx1));
        asm volatile("" : "+s"(rX), "+s"(xd), "+s"(xe));
        WAIT_VM(8); BARRIER();
        ring.issueQ(l0 + 2 * STAGE_BYTES);
        ring.seek((xq * 4 + NBR(2)) * 256, 0);
        if (h != 0) {
#pragma unroll
            for (int k = 0; k < 3; ++k) dma_2k<false>(rX, xd + k * 4096, vx0, vx1, k * (16 * D_LEN * 2));
        }
        BARRIER();
        if (h != 0) dma_2k<false>(rX, xe, vx0, vx1, 3 * (16 * D_LEN * 2));
        ring.issueQ(l0 + 3 * STAGE_BYTES);
        kx = (unsigned)__builtin_amdgcn_readfirstlane((int)ctl1[4]);
        SB();
    }
    char* ws = a.ws + ((0ull - (unsigned long long)a.ws) & 0x1fffffull);
    const int g = lane >> 4, n = lane & 15;
    LAS float* tab = (LAS float*)(lds + L_RED);
    if (wid >= 8) {
        const int aw = wid - 8;
#define T1(t) do { if (aw == 1 && (t) <= 16) STAMP(40 + (t)); } while (0)
        GAS u64* tagp = (GAS u64*)(ws + WS_TAG);
        const int r0 = xq * 512 + h * 256 + idx * 8 + aw * 2;
        const int w0 = xq * 256 + idx * 8 + aw * 2;
        const u32x4* src = (const u32x4*)((n < 8 ? a.x + (size_t)(r0 + (n >> 2)) * D_LEN
                                                 : (h == 0 ? a.wre : a.wim) + (size_t)(w0 + (n >> 2) - 2) * D_LEN) + g * 32 + (n & 3) * 8);
        if (AUX_SLEEP) __builtin_amdgcn_s_sleep(AUX_SLEEP);
        u64 tag0 = __hip_atomic_load(tagp, __ATOMIC_RELAXED, __HIP_MEMORY_SCOPE_AGENT);
        u32x4 v[8];
#pragma unroll
        for (int j = 0; j < 4; ++j) v[j] = src[j * 16];
        __builtin_amdgcn_s_setprio(3);
        SB();
        const char* cb = (const char*)(h ? a.ci : a.cr) + (size_t)rt * (512u << 10) + ct * 65536 + (lane >> 2) * 4096 + (lane & 3) * 128;
        const GAS u32x4* wrp = (const GAS u32x4*)a.w_r + lane;
        asm volatile("" : "+v"(cb), "+v"(wrp));
        unsigned mask = aw == 1 ? CPF_MASK1 : aw == 2 ? CPF_MASK2 : 0u;
        mask = (mask & CPF_FIX) | ((mask & ~CPF_FIX) << (idx % CPF_STAG));
        int ti = 2 * xq + aw - 1;
        auto warm = [&](int t) { if (t >= 4 && ((mask >> t) & 1)) { touch_line(cb + (ti & 7) * 512, L_DUMMY); ti += 2; } };
        f32x4 d1 = {0.f, 0.f, 0.f, 0.f}, d2 = {0.f, 0.f, 0.f, 0.f};
        u32x4 ones = {0x3F803F80u, 0x3F803F80u, 0x3F803F80u, 0x3F803F80u};
        auto sums = [&](int j0) __attribute__((always_inline)) {
#pragma unroll
            for (int j = j0; j < j0 + 4; ++j) {
                const bf16x8 f = __builtin_bit_cast(bf16x8, v[j]);
                d1 = __builtin_amdgcn_mfma_f32_16x16x32_bf16(f, __builtin_bit_cast(bf16x8, ones), d1, 0, 0, 0);
                d2 = __builtin_amdgcn_mfma_f32_16x16x32_bf16(f, f, d2, 0, 0, 0);
            }
            asm volatile("" : "+v"(d1), "+v"(d2));
        };
        unsigned wl, wh;
        int ro;
        __amdgpu_buffer_rsrc_t rS;
        u64 m1 = 0xAAAAAAAAAAAAAAAAull, m2 = 0xCCCCCCCCCCCCCCCCull, mh = 0xFFFFFFFF00000000ull;
#pragma unroll
        for (int t = 0; t <= 5; ++t) {
            SB(); BARRIER(); SB();
            T1(t);
            if (t == 2) {
#pragma unroll
                for (int j = 4; j < 8; ++j) v[j] = src[j * 16];
            }
            if (t == 3) {
                asm volatile("" : "+v"(ones));
                sums(0);
                asm volatile("" : "+v"(tag0));
                const u64 want = tag0 * 6364136223846793005ull + 1442695040888963407ull;
                wl = (unsigned)want; wh = (unsigned)(want >> 32);
                asm volatile("" : "+v"(wl), "+v"(wh));
            }
            if (t == 4) { sums(4); asm volatile("s_nop 7\n\ts_nop 3" : "+v"(d2)); }
            if (t == 1) {
                rS = mk_rsrc(ws + WS_XRD, 65536);
                ro = n == g * 4 ? (g < 2 ? r0 + g : 2048 - 2 + h * 1024 + w0 + g) * 16 : 65536;
                asm volatile("" : "+v"(ro));
                asm volatile("" : "+s"(rS), "+s"(m1), "+s"(m2), "+s"(mh), "+s"(mask), "+s"(ti));
            }
        }
        float sm = (d1[0] + d1[1]) + (d1[2] + d1[3]);
        float sq = pick(m2, pick(m1, d2[0], d2[1]), pick(m1, d2[2], d2[3]));
        asm volatile("s_nop 1" : "+v"(sq));
        sq += dppf<0xB1>(sq);
        sq += dppf<0x4E>(sq);
        float mean = sm * (1.0f / D_LEN), var = rbf(rbf(fmaxf(sq * (1.0f / D_LEN) - mean * mean, 0.f)) + 1e-5f);
        asm volatile("" : "+v"(sm), "+v"(mean), "+v"(var));
        SB(); BARRIER(); SB();
        T1(6);
        {
            float rden = __builtin_amdgcn_rcpf(rbf(__builtin_amdgcn_sqrtf(var)));
            asm volatile("s_nop 1" : "+v"(rden));
            const u32x4 rec = {__float_as_uint(pick(mh, rden, sm)), wl, __float_as_uint(rbf(mean) * rden), wh};
            ST16_SC1(rec, rS, ro);
        }
        const __amdgpu_buffer_rsrc_t rV = mk_rsrc(ws + WS_VOTE, 512);
        if (aw == 3 && idx < 8) __builtin_amdgcn_raw_buffer_store_b64((u32x2){ctl1[5], wl}, rV, blockIdx.x * 8 + lane * 512, 0, 16);
        SB(); BARRIER(); SB();
        T1(STAT_M + 2);
        warm(STAT_M + 2);
        if (aw == 1 || aw == 2) {
#pragma unroll 1
            for (int t = STAT_M + 3; t <= NT1 + AUX_STAY; ++t) {
                BARRIER();
                T1(t);
                warm(t);
            }
            FLUSH();
            return;
        }
        if (aw == 3) {
            if (h == 0) {
                const u32x4 w0v = wrp[0], w1v = wrp[64];
                BARRIER(); BARRIER(); BARRIER(); BARRIER();
                SB();
                float t = 0.f;
#pragma unroll
                for (int i = 0; i < 4; ++i) { t = dot2bf(w0v[i], 0x3F803F80u, t); t = dot2bf(w1v[i], 0x3F803F80u, t); }
                SB(); BARRIER(); SB();
                t = wave_sum(t);
                ctl1[1] = __float_as_uint(t);
                WAIT_LGKM0();
            }
            return;
        }
#pragma unroll
        for (int t = STAT_M + 3; t <= POLL_T; ++t) BARRIER();
        SB();
        const int o0 = (rt * BT + lane) * 16, o1 = (2048 + (h * 8 + ct) * BT + lane) * 16, os = 1024;
        u32x4 q0, q1, q2, q3;
        u32x2 qv;
        auto request = [&]() {
            asm volatile("" ::: "memory");
            q0 = LD16_SC1(rS, o0); q1 = LD16_SC1(rS, o0 + os); q2 = LD16_SC1(rS, o1); q3 = LD16_SC1(rS, o1 + os);
            qv = __builtin_amdgcn_raw_buffer_load_b64(rV, lane * 8, 0, 16);
            SB();
        };
        request();
        int b = POLL_T + 1;
        for (int spin = 0; spin <= SPIN_CAP; ++spin) {
            if (b < NT1) { BARRIER(); ++b; }
            if (b < NT1) { BARRIER(); ++b; }
            SB();
            unsigned d = ((q0[1] ^ wl) | (q1[1] ^ wl)) | ((q2[1] ^ wl) | (q3[1] ^ wl)) | ((q0[3] ^ wh) | (q1[3] ^ wh)) | ((q2[3] ^ wh) | (q3[3] ^ wh)) | (qv[1] ^ wl);
            asm volatile("" : "+v"(d));
            if (__all(d == 0u)) break;
            request();
        }
        if (b < NT1) BARRIER();
        SB();
        LAS unsigned* tb = (LAS unsigned*)tab;
        const unsigned sg = h == 0 ? 0u : 0x80000000u;
        tb[lane] = q0[0] ^ sg; tb[64 + lane] = q1[0] ^ sg; tb[128 + lane] = q0[2] ^ sg; tb[192 + lane] = q1[2] ^ sg;
        tb[256 + lane] = q2[0]; tb[320 + lane] = q3[0];
        const unsigned vt = qv[0];
        const unsigned kx = __popcll(__ballot(vt == 2u)) > __popcll(__ballot(vt == 1u)) ? 8192u : 0u;
        if (lane == 0) { ctl1[0] = wl | 2u; ctl1[2] = wl; ctl1[3] = wh; ctl1[4] = kx; }
        *(LAS u32x2*)(lds + L_YB + 2560 + 256 + lane * 8) = (u32x2){0u, 0u};
        WAIT_LGKM0();
        return;
    }
#define CTL(i) ((unsigned)__builtin_amdgcn_readfirstlane((int)ctl[i]))
    const int wp = wid & 1, wq = (wid >> 1) & 1;
#define Y_OFF ((int)WS_Y + ((rt & 7) * 8 + ct) * 65536 + (wid & 3) * 16384 + (((rt >> 3) ^ (rt >> 2)) & 1) * 8192)
#define P2_DEFS \
    char* w2 = ws + kx; \
    const int sidx = idx < 28 ? xq * 28 + idx : 112 + xq * 4 + idx - 28;     \
    volatile LAS unsigned* ctl = (volatile LAS unsigned*)(lds + (h == 0 ? L_ATAB : L_CTL)); \
    const int NT = h == 0 ? 32 + KSHIFT : 32 - KSHIFT; \
    LAS float* rstat = (LAS float*)(lds + L_ATAB + 256);     \
    const int hp_ = rt * 2 + wq;                             \
    (void)sidx; (void)ctl; (void)NT; (void)rstat; (void)hp_; (void)w2
    if (__builtin_expect(wid < 4, 1)) {
        {
            const u32x4 sent4 = {SENT, SENT, SENT, SENT}, zero4 = {0u, 0u, 0u, 0u};
            const __amdgpu_buffer_rsrc_t rI = mk_rsrc(ws + WS_STATS + (size_t)blockIdx.x * (FILL_BYTES / 256), FILL_BYTES / 256);
            const __amdgpu_buffer_rsrc_t rZ = mk_rsrc(ws + (size_t)blockIdx.x * 160, 160);
            ST16_SC1(sent4, rI, tid * 16);
            if (wid < 1) ST16_SC1(sent4, rI, 4096 + tid * 16);
            if (wid == 2) ST16_SC1(zero4, rZ, lane * 16);
        }
        if (wid == 3 && idx < 8) {
            const unsigned r = (((unsigned)__builtin_amdgcn_s_memrealtime() * 2654435761u + blockIdx.x * 40503u) >> 9) & 0x1fdf80u;
            const unsigned first = (idx & 1) << 13;
            const char* pb = ws + WS_PROBE;
            unsigned va, vb, left;
            asm volatile("s_waitcnt lgkmcnt(0)\n\ts_load_dword %0, %3, %4\n\ts_load_dword %1, %3, %5\n\ts_waitcnt lgkmcnt(1)\n\t"
                         "s_nop 15\n\ts_getreg_b32 %2, hwreg(HW_REG_IB_STS, 8, 4)\n\ts_waitcnt lgkmcnt(0)"
                         : "=&s"(va), "=&s"(vb), "=&s"(left) : "s"(pb), "s"(r | first), "s"(r | (first ^ 8192u)) : "memory");
            const unsigned nearbit = (left != 0u ? first : first ^ 8192u) >> 13;
            if (lane == 0) ctl1[5] = nearbit == (unsigned)(xcd >> 2) ? 1u : 2u;
        }
        f32x4 acc[4][4];
#pragma unroll
        for (int i = 0; i < 16; ++i) acc[i >> 2][i & 3] = (f32x4){0.f, 0.f, 0.f, 0.f};
        unsigned tc = (unsigned)(size_t)(tab + wp * 64 + g * 8), tw = (unsigned)(size_t)(tab + 256 + prow(wq * 64 + n));
        unsigned ta = (unsigned)(size_t)lds + wp * STAGE_BYTES + wq * 8192 + lane * 16;
        constexpr int NIL = 1 << 30;
        __amdgpu_buffer_rsrc_t rW = mk_rsrc(ws, 18u << 20);
        int sk = (int)WS_AT + at_panel(h * 8 + ct) + at_tile((rt * 2 + wp) * 128, (ct & 4) << 11) + wq * 16384 + lane * 16;
        int st = lane != 0 ? NIL : (int)WS_FLAG1 + (1 - wp) * 32768 + h * 16384 + (rt >> 3) * 8192 + ct * 1024 + (rt & 7) * 128 + wq * 4;
        int so = Y_OFF + lane * 16;
        unsigned sl = (unsigned)(size_t)lds + L_ST + (g != (n >> 2) ? 4096 + wid * 2048 + lane * 16 : wp * 2048 + wq * 1024 + n * 16);
        int sf = lane != 0 ? NIL : (int)WS_FLAGS + (rt * 8 + ct) * 128 + wid * 4;
        u32x4 sel[2], aw[2];
        unsigned one[2];
#pragma unroll
        for (int i = 0; i < 4; ++i) {
            one[i & 1] = g == (n >> 2) && (i & 1) == ((n >> 1) & 1) ? ((n & 1) ? 0x3F800000u : 0x00003F80u) : 0u;
            aw[0][i] = aw[1][i] = (n & 3) == 0 ? 0x3F803F80u : 0u;
        }
        if ((n & 3) == 1) {
            const bf16_t* wr = a.w_r + ct * BT + wp * 64 + g * 8;
            aw[0] = *(const u32x4*)wr; aw[1] = *(const u32x4*)(wr + 32);
        }
        unsigned ea[2];
#pragma unroll
        for (int mp = 0; mp < 2; ++mp)
            ea[mp] = (unsigned)(size_t)lds + L_YB + (wq ? 16384 : 4096) + n * 256 + (((wp * 8 + mp * 4 + g) ^ n) * 16);
        asm volatile("" : "+v"(tc), "+v"(tw), "+v"(ta), "+v"(sk), "+v"(st), "+v"(so), "+v"(sl), "+v"(sf), "+v"(one[0]), "+v"(one[1]), "+v"(ea[0]), "+v"(ea[1]));
        asm volatile("" : "+s"(rW));
        unsigned tg;
        bf16x8 e0[4], e1[4];
        auto extra = [&]() __attribute__((always_inline)) {
            bf16x8 ef[4][2];
#pragma unroll
            for (int i = 0; i < 8; ++i)
                ef[i >> 1][i & 1] = *(const LAS bf16x8*)((const LAS unsigned char*)(size_t)(ea[i & 1] + (i < 6 || wq ? 0 : 3 * STAGE_BYTES - L_YB - 16384)) + (i >> 1) * 4096);
            SB();
#pragma unroll
            for (int i = 0; i < 16; ++i)
                acc[i & 3][i >> 2] = __builtin_amdgcn_mfma_f32_16x16x32_bf16(__builtin_bit_cast(bf16x8, sel[i & 1]), ef[i >> 2][(i >> 1) & 1], acc[i & 3][i >> 2], 0, 0, 0);
        };
        if (wid == 0) STAMP(0);
        compute_loop<false, true>(lds, acc, NT1, wp, wq, lane, [&]() { if (wid == 0) STAMP(24); WAIT_VM0(); if (wid == 0) STAMP(31); }, e0, e1, 0u);
        if (wid == 0) STAMP(25);
        {
            f32x4 rd[4], mr[4];
            float wsm[4];
#pragma unroll
            for (int m = 0; m < 4; ++m) {
                rd[m] = *(const LAS f32x4*)((const LAS float*)(size_t)tc + (m >> 1) * 32 + (m & 1) * 4);
                mr[m] = *(const LAS f32x4*)((const LAS float*)(size_t)tc + 128 + (m >> 1) * 32 + (m & 1) * 4);
                wsm[m] = *((const LAS float*)(size_t)tw + (m >> 1) * 32 + (m & 1) * 4);
            }
            const unsigned kv = ctl1[4];
            tg = ctl1[0];
            SB();
            sk += (int)kv; st += (int)kv; so += (int)kv; sf += (int)kv;
            kx = (unsigned)__builtin_amdgcn_readfirstlane((int)kv);
#pragma unroll
            for (int nn = 0; nn < 4; ++nn)
#pragma unroll
                for (int mp = 0; mp < 2; ++mp) {
                    const f32x4 u = acc[2 * mp][nn] * rd[2 * mp] - mr[2 * mp] * wsm[nn];
                    const f32x4 v = acc[2 * mp + 1][nn] * rd[2 * mp + 1] - mr[2 * mp + 1] * wsm[nn];
                    u32x4 w;
                    w[0] = pk_bf16(u[0], u[1]); w[1] = pk_bf16(u[2], u[3]);
                    w[2] = pk_bf16(v[0], v[1]); w[3] = pk_bf16(v[2], v[3]);
                    ST16_SC1(w, rW, sk + (nn * 2 + mp) * 1024);
                    *(LAS u32x4*)((LAS unsigned char*)(size_t)ta + (nn * 2 + mp) * 1024) = w;
                }
        }
        if (wid == 0) STAMP(26);
        WAIT_LGKM0();
        BARRIER();
        SB();
        {
#pragma unroll
            for (int i = 0; i < 16; ++i) { acc[i >> 2][i & 3] = (f32x4){0.f, 0.f, 0.f, 0.f}; asm volatile("" : "+v"(acc[i >> 2][i & 3])); }
            sel[0] = (u32x4){one[0], one[1], 0u, 0u}; sel[1] = (u32x4){0u, 0u, one[0], one[1]};
            asm volatile("" : "+v"(st), "+v"(so), "+v"(sf), "+v"(tg), "+v"(sel[0]), "+v"(sel[1]));
            SB();
            if (h != 0) extra();
            SB();
            WAIT_VM(0);
            __builtin_amdgcn_raw_buffer_store_b32(tg, rW, st, 0, 16);
            SB();
            if (wid == 0) STAMP(27);
        }
        compute_loop<true, false>(lds, acc, h == 0 ? 32 + KSHIFT : 32 - KSHIFT, wp, wq, lane, [&]() {
            if (wid == 0) { STAMP(h == 0 ? 1 : 19); CSTAMP(22); }
        }, e0, e1, (unsigned)((wp * 8192 + 2 * STAGE_BYTES) ^ (L_YB + wid * 8192)));
        P2_DEFS;
        if (wid == 0) { STAMP(h == 0 ? 2 : 12); CSTAMP(23); }
        u32x4 hq[4][2];
        auto cvt = [&](int nn, int j) __attribute__((always_inline)) {
            const f32x4 c = acc[j][nn];
            hq[nn][j >> 1][(j & 1) * 2] = pk_bf16(c[0], c[1]);
            hq[nn][j >> 1][(j & 1) * 2 + 1] = pk_bf16(c[2], c[3]);
        };
        int hs = h;
        asm volatile("" : "+s"(hs));
        if (__builtin_expect(hs == 0, 1)) {
            f32x4 d1[4], d2[4];
            u32x3 sv;
            auto add = [&](int nn, int j) __attribute__((always_inline)) {
                acc[j][nn] = __builtin_amdgcn_mfma_f32_16x16x32_bf16(__builtin_bit_cast(bf16x8, sel[j & 1]), j < 2 ? e0[nn] : e1[nn], acc[j][nn], 0, 0, 0);
            };
            auto sum = [&](int nn, int j) __attribute__((always_inline)) {
                const bf16x8 hf = __builtin_bit_cast(bf16x8, hq[nn][j >> 1]);
                const f32x4 z = {0.f, 0.f, 0.f, 0.f};
                if (j & 1) d1[nn] = __builtin_amdgcn_mfma_f32_16x16x32_bf16(__builtin_bit_cast(bf16x8, aw[j >> 1]), hf, j < 2 ? z : d1[nn], 0, 0, 0);
                else d2[nn] = __builtin_amdgcn_mfma_f32_16x16x32_bf16(hf, hf, j < 2 ? z : d2[nn], 0, 0, 0);
            };
            auto out = [&](int nn, int j) __attribute__((always_inline)) {
                if (j == 1) asm volatile("" : "+v"(d2[nn]));
                if (j == 2) asm volatile("" : "+v"(d1[nn]));
                if (j == 1) sv[1] = __float_as_uint(sel4(n, d2[nn][0], d2[nn][1], d2[nn][2], d2[nn][3]));
                if (j == 2) { const float u0 = d1[nn][0], u1 = d1[nn][1]; sv[0] = __float_as_uint(u0); sv[2] = __float_as_uint(u1); }
                if (j == 3) *(LAS u32x3*)((LAS unsigned char*)(size_t)sl + nn * 256) = sv;
            };
#define FOUR(m, v) _Pragma("unroll") for (int j = 0; j < 4; ++j) { m; SB(); v; SB(); }
            FOUR(add(0, j), (void)0) FOUR(add(1, j), cvt(0, j)) FOUR(sum(0, j), cvt(1, j)) FOUR(add(2, j), out(0, j))
            FOUR(sum(1, j), cvt(2, j)) FOUR(add(3, j), out(1, j)) FOUR(sum(2, j), cvt(3, j)) FOUR(sum(3, j), out(2, j))
            FOUR((void)0, out(3, j))
#undef FOUR
            WAIT_LGKM0();
            BARRIER();
            if (wid == 0) STAMP(3);
#pragma unroll
            for (int nn = 0; nn < 4; ++nn)
#pragma unroll
                for (int mp = 0; mp < 2; ++mp)
                    *(LAS u32x4*)(lds + L_HT + (wq * 64 + nn * 16 + n) * 256 + (((wp * 8 + mp * 4 + g) ^ n) * 16)) = hq[nn][mp];
            WAIT_LGKM0();
            BARRIER();
            BARRIER();
            if (wid == 0) STAMP(6);
            const int crow = __builtin_amdgcn_readfirstlane((int)((volatile LAS unsigned*)(lds + L_ATAB))[2 + wq]) + wq * 64;
            const __amdgpu_buffer_rsrc_t rOut = mk_rsrc(a.out + (size_t)rt * BT * D_LEN, BT * D_LEN * 2);
#pragma unroll 2
            for (int j = 0; j < 8; ++j) {
                const int r = wq * 64 + wp * 32 + j * 4 + g;
                const float m_ = rstat[r * 2], d_ = rstat[r * 2 + 1];
                const u32x4 hh = *(const LAS u32x4*)(lds + L_HT + r * 256 + ((n ^ (r & 15)) * 16));
                u32x4 ov;
#pragma unroll
                for (int i = 0; i < 4; ++i) {
                    const unsigned u = hh[i];
                    const float h0 = bflo(u), h1 = bfhi(u);
                    const unsigned dd = pk_bf16(h0 - m_, h1 - m_);
                    const unsigned q = pk_bf16(bflo(dd) * d_, bfhi(dd) * d_);
                    ov[i] = pk_bf16(h0 + bflo(q), h1 + bfhi(q));
                }
                __builtin_amdgcn_raw_buffer_store_b128(ov, rOut, r == crow ? BT * D_LEN * 2 : (r * D_LEN + ct * BT + n * 8) * 2, 0, 0);
            }
            FLUSH();
            return;
        }
#pragma unroll
        for (int i = 0; i < 8; ++i) {
            cvt(i >> 1, (i & 1) * 2); cvt(i >> 1, (i & 1) * 2 + 1);
            __builtin_amdgcn_raw_buffer_store_b128(hq[i >> 1][i & 1], rW, so, i * 1024, 16);
            SB();
        }
        WAIT_VM0();
        __builtin_amdgcn_raw_buffer_store_b32(1u, rW, sf, 0, 16);
        if (wid == 0) STAMP(13);
    } else {
        LAS unsigned char* l0 = lds + (wid - 4) * 4096;
        P2_DEFS;
        __amdgpu_buffer_rsrc_t rD = mk_rsrc(h == 0 ? a.wd + (size_t)sidx * 32 * D_LEN : a.wg,
                                                  sidx >= 112 ? 0 : h != 0 ? D_LEN * F_LEN * 2 : wid < 8 - BQ ? 32 * D_LEN * 2 : 0);
        int vGe = h == 0 ? wd_off(wid & 3, 0, lane)
                : gu_off(sidx, (wid & 3) * 2, lane);
        auto load_wd = [&](int i) __attribute__((always_inline)) {
            dma_nt(rD, (unsigned)(size_t)lds + (i < 8 ? L_WD0 : L_WD1) + (wid & 3) * 8192 + (i & 7) * 1024, vGe ^ ((2 * (i & 1) | (i & 4)) * 32), i * (4 * D_LEN));
        };
        const int lw = wid - 4;
        constexpr int NIL = 1 << 30;
        ring.rP = ring.rF = mk_rsrc(w2, (unsigned)WS_STATS);
        ring.voP[0] += (int)WS_AT + at_panel(h * 8 + ct);
        ring.voP[1] = ring.voP[2] = ring.voP[3] = ring.voP[0];
#define FLINE(k) (((k) >> 3) * 8192 + ((k) & 7) * 128)
        const int ft = (g & 1) * 32768 + (g >> 1) * 4 + (int)WS_FLAG1 + (lw == 0 ? ct * 1024 : NIL);
        int vall = FLINE(n) + ft + h * 16384;
        ring.voF = (n >> 2) == xq ? vall : NIL;
        ring.fl = lds + L_YB + (h == 0 ? lw * 8192 : lw ? 768 : 0) + 2560 + 256;
        unsigned sa = (unsigned)(size_t)lds + L_YB + 2560 + 256 + lane * 4;
        const unsigned tg = (unsigned)__builtin_amdgcn_readfirstlane((int)ctl1[0]);
        float wsum = __uint_as_float(ctl1[1]);
        ring.lb = lw * 4096;
        int cq[3], cp[3];
        unsigned mk[3];
#pragma unroll
        for (int k = 0; k < 3; ++k) {
            const int nb = xq * 4 + NBR(k + 1);
            cq[k] = nb * 256; cp[k] = at_tile(cq[k], ring.par); mk[k] = 0x10000u << nb;
            asm volatile("" : "+s"(cq[k]), "+s"(cp[k]), "+s"(mk[k]));
        }
        unsigned m2 = (mk[0] | mk[1] | mk[2]) >> 16;
        asm volatile("" : "+s"(m2), "+s"(ring.rP));
        asm volatile("" : "+v"(vall), "+v"(ring.voF), "+v"(ring.voP[0]), "+v"(sa), "+v"(wsum));
        ring.rF = ring.rP;
        ring.voP[1] = ring.voP[2] = ring.voP[3] = ring.voP[0];
        WAIT_VM(4);
        BARRIER();
        SB();
        int ve = Y_OFF + lane * 16;
        unsigned yd = (unsigned)(size_t)lds + L_YB + lw * 8192, fa = 0u;
        auto fetch = [&]() __attribute__((always_inline)) {
            if (wid == 4) STAMP(21);
            const int v4[4] = {ve, ve, ve, ve};
#pragma unroll
            for (int k = 0; k < 2; ++k) dma_4k<true>(ring.rP, yd + k * 4096, v4, k * 4096);
        };
        __amdgpu_buffer_rsrc_t rQ2 = mk_rsrc(h == 0 ? a.ci + (size_t)(rt * 4 + lw) * 32 * K : a.wu,
                                             h == 0 ? 32 * K * 2 : sidx < 112 ? D_LEN * F_LEN * 2 : 0);
        int vC2 = ((ct & 1) * 16 + (lane >> 2)) * (K * 2) + ((xq * 1024 + 2048 + (ct >> 1) * 512 + (lane & 3) * 128) & 4095);
        int vT2 = h == 0 && n < (KSHIFT + 1) / 2 ? FLINE((xq * 4 - 1 - n) & 15) + ft + 16384 : NIL;
        int vY = lane == 0 ? (int)WS_FLAGS + (rt * 8 + ct) * 128 + lw * 4 : NIL;
        int n1 = (h == 0 ? 32 : NT) - 9, n0 = n1 - 1 - (C2_START + 2 * ct + (idx & 1));
        int xm = (idx & 1) * 128;
        int c8 = (xq * 1024 + 1024) & 4095, p8 = at_tile(c8 ^ xm, ring.par);
        int cK = (xq * 1024 + (32 - KSHIFT) * 128) & 4095, pK = at_tile(cK ^ xm, ring.par) + at_panel(8);
        auto early = [&](int st, int i0, int i1) __attribute__((always_inline)) {
#pragma unroll
            for (int i = i0; i < i1; ++i) {
                const int m = i & 1, w = (i >> 1) & 1, j = i >> 2;
                dma_nt(m ? rQ2 : rD, (unsigned)(size_t)lds + st * STAGE_BYTES + m * 16384 + lw * 4096 + w * 2048 + j * 1024, vGe ^ (j * 64 + m * 512),
                       (32 * st + 4 * w + 2 * j) * (F_LEN * 2));
            }
        };
        asm volatile("" : "+v"(vGe));
        asm volatile("" : "+s"(rQ2), "+s"(n0), "+s"(n1), "+s"(c8), "+s"(p8), "+s"(cK), "+s"(pK), "+s"(xm), "+s"(rD));
        asm volatile("" : "+v"(ve), "+v"(vC2), "+v"(vT2), "+v"(vY));
        unsigned have = 0u;
        SB();
#define GATE(m) do { if (__builtin_expect(((m) & ~have) != 0u, 0)) ring.gate(tg, sa, m, have); SB(); } while (0)
#define TILE(k, o, st) do { ring.sp = cp[k] + (o) * 256; ring.sq = cq[k] + (o); ring.template issueP<16>(l0 + (st) * STAGE_BYTES); \
                            ring.issueQ(l0 + (st) * STAGE_BYTES); ring.sample(); SB(); } while (0)
        ring.gate(tg, sa, mk[0], have);
        if (wid == 4) STAMP(28);
        ring.sp = cp[0]; ring.template issueP<16>(l0 + 2 * STAGE_BYTES); ring.sample();
        BARRIER();
        GATE(mk[1]);
        ring.sp = cp[1]; ring.template issueP<16>(l0 + 3 * STAGE_BYTES); ring.sample();
        GATE(mk[2]);
        TILE(2, 0, 0);
        WAIT_VM(15); BARRIER();
        if (wid == 4) STAMP(30);
        GATE(m2);
        ring.voF = vall;
        TILE(0, 128, 1);
        WAIT_VM(19); BARRIER();
        TILE(1, 128, 2);
        WAIT_VM(19); BARRIER();
        ring.sp = cp[2] + 128 * 256; ring.template issueP<16>(l0 + 3 * STAGE_BYTES);
        ring.sq = cq[2] + 128;
        SB();
        {
            const unsigned long long m = __ballot(ring.peek(sa) == tg);
            have = (unsigned)m & (unsigned)(m >> 32);
            if (__builtin_expect(have != 0xffffffffu, 0)) ring.gate(tg, sa, 0xffffffffu, have);
        }
        ring.fl -= 256;
        ring.voF = vT2;
        SB();
        ring.issueQ(l0 + 3 * STAGE_BYTES);
#undef GATE
#undef TILE
        ring.sq = c8; ring.sp = p8; ring.poff = 0; ring.xm = xm;
        if (wid == 4) { STAMP(29); CSTAMP(34); STAMP(36); }
        ring.template steady<true, false, 18>(n1, n0, vC2);
        ring.fl += 512;
        if (wid == 4) { CSTAMP(35); STAMP(37); }
        ring.template issueP<16>(lds + ring.lb);
        SB();
        {
            const unsigned long long m = __ballot(ring.peek(sa + 256) == tg);
            have = (unsigned)m & (unsigned)(m >> 32);
            asm volatile("" : "+s"(have));
        }
        int hs = h;
        asm volatile("" : "+s"(hs));
        if (__builtin_expect(hs == 0, 1)) {
            constexpr unsigned MK = ((1u << ((KSHIFT + 1) / 2)) - 1u) * 0x10001u;
            if (__builtin_expect((MK & ~have) != 0u, 0)) ring.gate(tg, sa + 256, MK, have);
            unsigned lq = ring.lb + TILE_BYTES, pp = ring.par | at_panel(8);
            int sq = ring.sq ^ ring.xm, cnt = KSHIFT;
            ring.lb = (ring.lb + STAGE_BYTES) & (RING_BYTES - 1);
            ring.fl -= 512;
            fa = (unsigned)(size_t)ring.fl;
            asm volatile("" : "+s"(lq), "+s"(sq), "+s"(cnt), "+s"(ring.lb), "+s"(pp), "+s"(yd));
            asm volatile("" : "+v"(fa));
            SB();
            dma_4k<false>(ring.rQ, lq, ring.voQ, sq);
            ring.voF = vY;
            ring.rQ = rQ2;
            ring.sq = cK; ring.sp = pK; ring.poff = 0; ring.par = pp;
            ring.template steady<true, true>(cnt);
            auto seen = [&]() __attribute__((always_inline)) { return __builtin_amdgcn_readfirstlane(ring.peek(fa)) == 1u; };
            if (__builtin_expect(seen(), 1)) {
                fetch();
                WAIT_VM(16); BARRIER();
                WAIT_VM(8); BARRIER();
            } else {
                ring.sample();
                WAIT_VM(10); BARRIER();
                const bool have = seen();
                if (have) { fetch(); WAIT_VM(9); } else { ring.sample(); WAIT_VM(2); }
                BARRIER();
                if (!have) {
#pragma unroll 1
                    for (int spin = 0; spin <= SPIN_CAP && !seen(); ++spin) { ring.sample(); WAIT_VM(0); }
                    fetch();
                }
            }
            if (wid == 4) STAMP(20);
            WAIT_VM0();
        } else {
            ring.issueQ(lds + ring.lb);
            WAIT_VM(16); BARRIER();
            early(0, 0, 8);
            WAIT_VM(16); BARRIER();
            early(1, 0, 2);
            WAIT_VM(10); BARRIER();
            early(1, 2, 4);
        }
        BARRIER();
        if (h == 0) {
            const int slot = sidx;
            LAS unsigned char* abuf = lds + L_ATAB + 2048 + lw * 128;
#pragma unroll
            for (int i = 0; i < WD_EARLY; ++i) load_wd(i);
            SB();
            __amdgpu_buffer_rsrc_t rS = mk_rsrc(w2 + WS_STATS + (hp_ & 15) * 16384 + (hp_ >> 4) * 8192, 8192);
            __amdgpu_buffer_rsrc_t rC = mk_rsrc(w2 + WS_CAND + (size_t)hp_ * 4096, 4096);
            __amdgpu_buffer_rsrc_t rKy = mk_rsrc(w2 + WS_KEYS + (hp_ >> 2) * KEY_S + (hp_ & 3) * 4, ct == 0 ? 8192 + 3 * KEY_S * 16 + 4 : 0);
            int vs = lane * 16, vk = !(lane & 1) ? 1 << 20 : (lane >> 2) * 8192 + (lane & 3) * (KEY_S * 16);
            int vc = (lane < 32 ? 2048 : 0) + ct * 256 + (lane & 31) * 8;
            unsigned code = 0xffffu - (unsigned)(hp_ * 64 + lane), hb = (lane & 31) * 8;
            unsigned ra = (unsigned)(size_t)(rstat + (wq * 64 + lane) * 2), ht = (unsigned)(size_t)lds + L_HT + wq * 16384;
            u64 lo = 0xFFFFFFFFull;
            unsigned sr = (unsigned)(size_t)lds + L_ST + wq * 1024 + lane * 16;
            int vw = wp ? NIL : ct * 1024 + lane * 16;
            asm volatile("" : "+v"(sr), "+v"(vw));
            asm volatile("" : "+s"(rS), "+s"(rC), "+s"(rKy), "+s"(ht), "+s"(lo));
            asm volatile("" : "+v"(vs), "+v"(vk), "+v"(vc), "+v"(code), "+v"(hb), "+v"(ra));
            __builtin_amdgcn_s_setprio(TAIL_PRIO);
            SB();
            BARRIER();
            if (wp == 0) {
                const f32x4 u = *(const LAS f32x4*)(size_t)sr, v = *(const LAS f32x4*)((const LAS unsigned char*)(size_t)sr + 2048);
                const u32x4 w = {min(__float_as_uint(u[0] + v[0]), SENT - 1u), min(__float_as_uint(u[1] + v[1]), SENT - 1u), min(__float_as_uint(u[2] + v[2]), SENT - 1u), 0u};
                ST16_SC1(w, rS, vw);
            }
            SB();
            BARRIER();
            if (lane < 16) *(LAS unsigned*)(abuf + 64 + lane * 4) = 0u;
            __builtin_amdgcn_s_sleep(STAT_DELAY);
            if (wp == 0) {
                float s1, s2, s3;
#pragma unroll 1
                for (int spin = 0;; ++spin) {
                    asm volatile("" ::: "memory");
                    u32x3 sv[8];
#pragma unroll
                    for (int j = 0; j < 8; ++j) { sv[j] = __builtin_amdgcn_raw_buffer_load_b96(rS, vs, j * 1024, 16); SB(); }
#pragma unroll
                    for (int j = 0; j < 8; ++j) {
                        const unsigned u0 = sv[j][0], u1 = sv[j][1], u2 = sv[j][2];
                        s1 = j ? s1 + __uint_as_float(u0) : __uint_as_float(u0);
                        s2 = j ? s2 + __uint_as_float(u1) : __uint_as_float(u1);
                        s3 = j ? s3 + __uint_as_float(u2) : __uint_as_float(u2);
                    }
                    float z = s1 + s3;
                    asm volatile("" : "+v"(z));
                    if (__builtin_expect(__builtin_amdgcn_ballot_w64(__builtin_isunordered(z, s2)) == 0, 1)) break;
                    unsigned mx = 0u;
#pragma unroll
                    for (int j = 0; j < 8; ++j) mx = max(max(mx, sv[j][0]), max(sv[j][1], sv[j][2]));
                    if (__all(mx != SENT) || spin > SPIN_CAP) break;
                }
                if (wid == 4) STAMP(4);
                const float mean = s1 * (1.0f / D_LEN);
                const float var = fmaxf(s2 * (1.0f / D_LEN) - mean * mean, 0.f);
                const float mb = rbf(mean), rden = __builtin_amdgcn_rcpf(rbf(__builtin_amdgcn_sqrtf(rbf(rbf(var) + 1e-5f))));
                const unsigned kb = wave_max_s(r2key((s3 - mb * wsum) * rden, code));
                __builtin_amdgcn_raw_buffer_store_b32(kb, rKy, vk, 0, 16);
                SB();
                {
                    const unsigned cr_ = ~kb & 63u;
                    u32x2 c = *(const LAS u32x2*)(size_t)(ht + cr_ * 256 + (hb ^ ((cr_ & 15) * 16)));
                    const float m_ = __int_as_float(__builtin_amdgcn_readlane(__float_as_int(mb), cr_));
                    const float d_ = __int_as_float(__builtin_amdgcn_readlane(__float_as_int(rden), cr_));
#pragma unroll
                    for (int i = 0; i < 2; ++i) {
                        const unsigned u = c[i];
                        const unsigned dd = pk_bf16(bflo(u) - m_, bfhi(u) - m_);
                        const unsigned q = pk_bf16(bflo(dd) * d_, bfhi(dd) * d_);
                        c[i] = min(__float_as_uint(pick(lo, __uint_as_float(u), __uint_as_float(q))), SENT - 1u);
                    }
                    __builtin_amdgcn_raw_buffer_store_b64(c, rC, vc, 0, 16);
                    SB();
                    *(LAS u32x2*)(size_t)ra = (u32x2){__float_as_uint(mb), __float_as_uint(rden)};
                    if (lane == 0) ctl[2 + wq] = cr_;
                }
                WAIT_LGKM0();
                if (wid == 4) STAMP(5);
            }
            BARRIER();
#pragma unroll
            for (int i = WD_EARLY; i < 16; ++i) load_wd(i);
            if (slot < 112 && wid < 8 - BQ) {
                WAIT_VM0();
                if (wid == 4) STAMP(7);
                stage2(w2, slot, lw, lds + L_WD0 + lw * 8192, lds + L_WD1 + lw * 8192, abuf, 64, lane);
                if (wid == 4) STAMP(11);
            }
            FLUSH();
            return;
        }
    }

    P2_DEFS;
    LAS float* red = (LAS float*)(lds + L_RED);
    if (sidx < 112) {
        const int slot = sidx, grp = slot >> 3, kq = slot & 7;
        const __amdgpu_buffer_rsrc_t rG = mk_rsrc(a.wg, D_LEN * F_LEN * 2), rU = mk_rsrc(a.wu, D_LEN * F_LEN * 2);
        const int vgu = gu_off(slot, wid, lane);
        auto pair = [&](int st, int j) __attribute__((always_inline)) {
            dma_nt(rG, (unsigned)(size_t)lds + st * STAGE_BYTES + wid * 2048 + j * 1024, vgu ^ (j * 64), (32 * st + 2 * j) * (F_LEN * 2));
            dma_nt(rU, (unsigned)(size_t)lds + st * STAGE_BYTES + 16384 + wid * 2048 + j * 1024, vgu ^ (j * 64 + 512), (32 * st + 2 * j) * (F_LEN * 2));
        };
        if (wid >= 4) {
            auto one = [&](int st, int j, int m) __attribute__((always_inline)) {
                wait_vm<TH_LIM>();
                dma_nt(m ? rU : rG, (unsigned)(size_t)lds + st * STAGE_BYTES + m * 16384 + wid * 2048 + j * 1024, vgu ^ (j * 64 + m * 512), (32 * st + 2 * j) * (F_LEN * 2));
            };
#pragma unroll
            for (int i = 2; i < 2 + TH_N; ++i) one(1 + (i >> 2), (i >> 1) & 1, i & 1);
            BARRIER();
#pragma unroll
            for (int i = 2 + TH_N; i < 12; i += 2) pair(1 + (i >> 2), (i >> 1) & 1);
        } else {
            BARRIER();
            pair(1, 1); pair(2, 0); pair(2, 1); pair(3, 0); pair(3, 1);
        }
        if (wid >= 8 - BQ) {
            const __amdgpu_buffer_rsrc_t rD = mk_rsrc(a.wd + (size_t)slot * 32 * D_LEN, 32 * D_LEN * 2);
            const int vd = wd_off(wid - 4, 0, lane);
#pragma unroll
            for (int i = 0; i < 16; ++i)
                dma_nt(rD, (unsigned)(size_t)lds + L_YB + (wid & 1) * 16384 + i * 1024, vd ^ ((2 * (i & 1) | (i & 4)) * 32), i * (4 * D_LEN));
        }
        const __amdgpu_buffer_rsrc_t rK = mk_rsrc(w2, wid == 0 ? (unsigned)(WS_CAND + 32 * 4096) : 0u);
        const int ok_ = lane < 40 ? (int)WS_KEYS + (xcd >> 2) * 8192 + (xcd & 3) * (KEY_S * 16) + (lane - 32) * KEY_S : (int)(WS_CAND + 32 * 4096);
        const int oc = (int)WS_CAND + 2048 + kq * 256 + n * 16;
        int best = 0;
        u32x4 v = {0u, 0u, 0u, 0u};
        auto request = [&]() {
            asm volatile("" ::: "memory");
            v = LD16_SC1(rK, lane < 32 ? oc + best * 4096 : ok_);
            SB();
        };
        auto settled = [&]() {
            const unsigned mx = umax4(v), mn = min(min(v[0], v[1]), min(v[2], v[3]));
            unsigned fv = lane < 32 ? ~mx : mn | (unsigned)(lane >> 3 ^ 4);
            asm volatile("" : "+v"(fv));
            const bool fine = fv != 0u;
            unsigned km = max(mx, dppu<0xB1>(mx));
            km = max(km, dppu<0x4E>(km));
            km = max(km, dppu<0x141>(km));
            const int nb = (int)(((0xffffu - ((unsigned)__builtin_amdgcn_readlane((int)km, 32) & 0xffffu)) >> 6) & 31u);
            const bool same = nb == best;
            best = nb;
            return __all(fine) && same;
        };
        if (wid == 0) {
            request();
            WAIT_VM(0);
            settled();
            request();
        } else if (wid >= 8 - BQ) WAIT_VM(16);
        else WAIT_VM(0);
        BARRIER();
        if (wid == 0) STAMP(8);
        bf16x8 wfr[4][4];
        {
            const int q = (lane >> 2) & 3, ky = q | ((g & 1) << 2);
#pragma unroll
            for (int u = 0; u < 4; ++u) {
                const int tro = (u >> 1) * 16384 + (8 * g + q) * 512 + (((wid * 2 + (u & 1)) ^ ky) * 32) + (lane & 3) * 8;
#pragma unroll
                for (int ks = 0; ks < 4; ++ks) {
                    const bf16x4 w0 = __builtin_amdgcn_ds_read_tr16_b64_v4i16((LAS bf16x4*)(lds + tro + ks * STAGE_BYTES));
                    const bf16x4 w1 = __builtin_amdgcn_ds_read_tr16_b64_v4i16((LAS bf16x4*)(lds + tro + ks * STAGE_BYTES + 2048));
                    wfr[u][ks] = __builtin_shufflevector(w0, w1, 0, 1, 2, 3, 4, 5, 6, 7);
                }
            }
            SB();
#pragma unroll
            for (int i = 0; i < 16; ++i) asm volatile("" : "+v"(wfr[i >> 2][i & 3]));
        }
        if (wid >= 8 - BQ && lane < 16) *(LAS unsigned*)(lds + 8192 + wid * 64 + lane * 4) = 0u;
        __amdgpu_buffer_rsrc_t rPo = mk_rsrc(w2 + WS_PRE, 112 * 2048);
        int po = (n & 3) == 0 ? pre_off((grp ^ (g >> 1)) * 8 + wid) + kq * 256 + g * 64 + n * 4 : 112 * 2048;
        unsigned xw = (unsigned)(size_t)lds + (n >> 3) * 16384 + (g & 1) * 8192 + (n & 7) * 8, xr = (unsigned)(size_t)lds + (n & 1) * 8192 + g * 16;
        unsigned ix = (n & 1) ? 0xEEEEEEEEu : 0x44444444u;
        asm volatile("" : "+v"(po), "+v"(xw), "+v"(xr), "+v"(ix));
        u64 m1 = 0xFFFF0000FFFF0000ull, m2 = 0xFFFFFFFF00000000ull;
        f32x4 ac[4];
#pragma unroll
        for (int u = 0; u < 4; ++u) { ac[u] = (f32x4){0.f, 0.f, 0.f, 0.f}; asm volatile("" : "+v"(ac[u])); }
        asm volatile("" : "+s"(rPo), "+s"(m1), "+s"(m2));
        if (wid == 0) {
#pragma unroll 1
            for (int spin = 0; spin <= SPIN_CAP && !settled(); ++spin) {
                __builtin_amdgcn_s_sleep(1);
                request();
            }
            __builtin_amdgcn_s_waitcnt(0x0F70);
            if (lane < 32) *(LAS u32x2*)(size_t)xw = (u32x2){lane < 16 ? v[0] : v[1], lane < 16 ? v[2] : v[3]};
            WAIT_LGKM0();
            STAMP(9);
        }
        BARRIER();
        {
            bf16x8 xf[2];
#pragma unroll
            for (int j = 0; j < 2; ++j) xf[j] = *(const LAS bf16x8*)((const LAS unsigned char*)(size_t)xr + j * 16384);
            SB();
            float rs[3];
#pragma unroll
            for (int i = 0; i < 8; ++i) {
                const int u = i >> 1, j = i & 1;
                ac[u] = smf(xf[j], wfr[u][2 * j], wfr[u][2 * j + 1], ac[u], ix);
                SB();
                if (i == 4) { asm volatile("" : "+v"(ac[0])); rs[0] = ac[0][0] + ac[0][1]; asm volatile("" : "+v"(rs[0])); }
                if (i == 6) { asm volatile("" : "+v"(ac[1])); rs[0] = pick(m1, rs[0], ac[1][0] + ac[1][1]); asm volatile("" : "+v"(rs[0])); }
                if (i == 7) { asm volatile("" : "+v"(ac[2])); rs[2] = ac[2][0] + ac[2][1]; asm volatile("" : "+v"(rs[2])); }
                SB();
            }
            const unsigned e = min(__float_as_uint(pick(m2, rs[0], pick(m1, rs[2], ac[3][0] + ac[3][1]))), SENT - 1u);
            const u32x4 sv = {e, dppu<0x55>(e), dppu<0xAA>(e), dppu<0xFF>(e)};
            ST16_SC1(sv, rPo, po);
            SB();
            asm volatile("" :: "v"(v));
        }
        if (wid == 0) STAMP(14);
        if (wid >= 8 - BQ) {
            WAIT_VM(1);
            if (wid == 7) STAMP(15);
            stage2(w2, slot, wid - 4, lds + L_YB + (wid & 1) * 16384, lds + L_YB + (wid & 1) * 16384 + 8192, lds + wid * 64, 8192, lane);
            if (wid == 7) STAMP(10);
        }
    } else {
        const int rj = sidx - 112;
        if (blockIdx.x == 255 && tid == 0) {
            const u64 want = ((u64)ctl1[3] << 32) | ctl1[2];
            __hip_atomic_store((GAS u64*)(ws + WS_TAG), want, __ATOMIC_RELAXED, __HIP_MEMORY_SCOPE_AGENT);
        }
        if (wid == 4) {
            unsigned kk;
            const unsigned k = poll_winner(w2, xcd, lane, kk);
            if (lane == 0) ctl[4] = k;
            if (lane < 32) ctl[8 + lane] = kk;
            WAIT_LGKM0();
        }
        BARRIER();
        const unsigned kw = CTL(4);
        if (wid == 0) STAMP(16);
        {
            const unsigned kc = ctl[8 + wid * 4 + g];
            const __amdgpu_buffer_rsrc_t rL = mk_rsrc(w2 + WS_CAND + (size_t)wid * 16384, 16384);
            u32x2 hv2, nv2;
            for (int spin = 0;; ++spin) {
                asm volatile("" ::: "memory");
                hv2 = __builtin_amdgcn_raw_buffer_load_b64(rL, g * 4096 + (rj * 64 + n * 4) * 2, 0, 16);
                nv2 = __builtin_amdgcn_raw_buffer_load_b64(rL, g * 4096 + 2048 + (rj * 64 + n * 4) * 2, 0, 16);
                if (__all(max(max(hv2[0], hv2[1]), max(nv2[0], nv2[1])) != SENT) || spin > SPIN_CAP) break;
                __builtin_amdgcn_s_sleep(1);
            }
            u32x2 ov;
#pragma unroll
            for (int i = 0; i < 2; ++i) {
                const unsigned u = hv2[i], q = nv2[i];
                ov[i] = pk_bf16(bflo(u) + bflo(q), bfhi(u) + bfhi(q));
            }
            const int crow = (int)(0xffffu - (kc & 0xffffu)) & (S_LEN - 1);
            if (kc != kw) *(u32x2*)(a.out + (size_t)crow * D_LEN + rj * 64 + n * 4) = ov;
        }
        const int wrow = (int)(0xffffu - (kw & 0xffffu)) & (S_LEN - 1);
        const __amdgpu_buffer_rsrc_t rC = mk_rsrc(w2 + WS_CAND + (size_t)(wrow >> 6) * 4096, 4096);
        const __amdgpu_buffer_rsrc_t rPt = mk_rsrc(w2 + WS_PART, 112 * 4096);
        const int ocol = (rj >> 2) * 256 + ((lane >> 1) & 1) * 128 + (rj & 3) * 32 + (lane & 1) * 16 + (lane >> 2);
        unsigned hv, hnv;
        for (int spin = 0;; ++spin) {
            asm volatile("" ::: "memory");
            hv = __builtin_amdgcn_raw_buffer_load_b32(rC, (ocol & ~1) * 2, 0, 16);
            hnv = __builtin_amdgcn_raw_buffer_load_b32(rC, 2048 + (ocol & ~1) * 2, 0, 16);
            if (__all(max(hv, hnv) != SENT) || spin > SPIN_CAP) break;
        }
        float sgm = rbf(1.0f / (1.0f + __expf(-key2r(kw))));
        float hf = (ocol & 1) ? bfhi(hv) : bflo(hv), hnf = (ocol & 1) ? bfhi(hnv) : bflo(hnv);
        GAS unsigned short* optr = (GAS unsigned short*)(a.out + (size_t)wrow * D_LEN + ocol);
        unsigned rw = (unsigned)(size_t)red + (wid >> 2) * 1024 + (n * 4 + g) * 16 + (wid & 3) * 4, rr = (unsigned)(size_t)red + lane * 16;
        int po = wid * 16384 + (rj >> 3) * 8192 + g * 2048 + ((rj & 7) * 64 + n * 4) * 4;
        int po3 = po + 3 * 131072;
        asm volatile("" : "+v"(sgm), "+v"(optr), "+v"(hf), "+v"(hnf), "+v"(rw), "+v"(rr), "+v"(po), "+v"(po3));
        float sm;
#pragma unroll 1
        for (int spin = 0;; ++spin) {
            asm volatile("" ::: "memory");
            u32x4 pv[4];
#pragma unroll
            for (int i = 0; i < 4; ++i) pv[i] = __builtin_amdgcn_raw_buffer_load_b128(rPt, i < 3 ? po : po3, i < 3 ? i * 131072 : 0, 16);
            SB();
            unsigned t[4];
#pragma unroll
            for (int j = 0; j < 4; ++j) {
                const unsigned u0 = pv[0][j], u1 = pv[1][j], u2 = pv[2][j], u3 = pv[3][j];
                t[j] = __float_as_uint(((__uint_as_float(u0) + __uint_as_float(u1)) + __uint_as_float(u2)) + __uint_as_float(u3));
            }
            const auto r0 = __builtin_amdgcn_permlane32_swap(t[0], t[2], false, false), r1 = __builtin_amdgcn_permlane32_swap(t[1], t[3], false, false);
            const unsigned a0 = r0[0], a1 = r0[1], b0 = r1[0], b1 = r1[1];
            const auto r2 = __builtin_amdgcn_permlane16_swap(__float_as_uint(__uint_as_float(a0) + __uint_as_float(a1)),
                                                             __float_as_uint(__uint_as_float(b0) + __uint_as_float(b1)), false, false);
            const unsigned c0 = r2[0], c1 = r2[1];
            sm = __uint_as_float(c0) + __uint_as_float(c1);
            if (__builtin_expect(__builtin_amdgcn_ballot_w64(sm != sm) == 0, 1)) break;
            if (__all(max(max(umax4(pv[0]), umax4(pv[1])), max(umax4(pv[2]), umax4(pv[3]))) != SENT) || spin > SPIN_CAP) break;
            if (FIN_SLEEP) __builtin_amdgcn_s_sleep(FIN_SLEEP);
        }
        if (wid == 0) STAMP(17);
        *(LAS float*)(size_t)rw = sm;
        WAIT_LGKM0();
        BARRIER();
        if (wid == 0) {
            const f32x4 u = *(const LAS f32x4*)(size_t)rr, v = *(const LAS f32x4*)((const LAS unsigned char*)(size_t)rr + 1024);
            const f32x4 w = u + v;
            const float t = (w[0] + w[2]) + (w[1] + w[3]);
            *optr = (unsigned short)pk_bf16(hf + rbf(hnf + rbf(rbf(t) * sgm)), 0.f);
            STAMP(18);
            FLUSH();
            asm volatile("s_endpgm" ::: "memory");
        }
        __builtin_amdgcn_s_waitcnt(0x0F70);
    }
    FLUSH();
}

extern "C" void kernel_launch(void* const* d_in, const int* in_sizes, int n_in, void* d_out, int out_size, void* d_ws,
                              size_t ws_size, hipStream_t stream) {
    Args a;
    a.x = (const bf16_t*)d_in[0];
    a.cr = (const bf16_t*)d_in[1];
    a.ci = (const bf16_t*)d_in[2];
    a.wre = (const bf16_t*)d_in[3];
    a.wim = (const bf16_t*)d_in[4];
    a.w_r = (const bf16_t*)d_in[6];
    a.wg = (const bf16_t*)d_in[7];
    a.wu = (const bf16_t*)d_in[8];
    a.wd = (const bf16_t*)d_in[9];
    a.out = (bf16_t*)d_out;
    a.ws = (char*)d_ws + ((ws_size - ((size_t)24 << 20)) & ~(size_t)255);
    (void)hipFuncSetAttribute((const void*)fused_kernel, hipFuncAttributeMaxDynamicSharedMemorySize, LDS_BYTES);
    fused_kernel<<<256, 768, LDS_BYTES, stream>>>(a);
}
```

```cpp
#include <hip/hip_runtime.h>
#include <hip/hip_bf16.h>
#include <stdint.h>

typedef unsigned short bf16_t;
typedef short bf16x8 __attribute__((ext_vector_type(8)));
typedef short bf16x4 __attribute__((ext_vector_type(4)));
typedef float f32x4 __attribute__((ext_vector_type(4)));
typedef unsigned u32x4 __attribute__((ext_vector_type(4)));
typedef unsigned u32x2 __attribute__((ext_vector_type(2)));
typedef unsigned u32x3 __attribute__((ext_vector_type(3)));
typedef unsigned long long u64;
#define LAS __attribute__((address_space(3)))
#define GAS __attribute__((address_space(1)))

constexpr int S_LEN = 2048, D_LEN = 1024, F_LEN = 3584;
constexpr unsigned SENT = 0xFFFFFFFFu;
constexpr int SPIN_CAP = 1 << 17;

constexpr size_t WS_KEYS = 0;
constexpr size_t WS_FLAGS = 16384;
constexpr size_t WS_XRD = 65536;
constexpr size_t WS_FLAG1 = 196608;
constexpr size_t WS_TAG = 131072;
constexpr size_t WS_VOTE = WS_TAG + 4096;
constexpr size_t WS_PROBE = (size_t)20 << 20;
constexpr size_t WS_AT = (size_t)4 << 20;
constexpr size_t WS_Y = (size_t)12 << 20;
constexpr size_t WS_STATS = (size_t)16 << 20;
constexpr size_t WS_PART = WS_STATS + (256u << 10);
constexpr size_t WS_CAND = WS_STATS + (704u << 10);
constexpr size_t WS_PRE = WS_STATS + (832u << 10);
constexpr size_t FILL_BYTES = (size_t)1072 << 10;

__device__ __forceinline__ float bflo(unsigned v) { return __uint_as_float(v << 16); }
__device__ __forceinline__ float bfhi(unsigned v) { return __uint_as_float(v & 0xffff0000u); }
__device__ __forceinline__ unsigned pk_bf16(float lo, float hi) {
    typedef __bf16 bf2 __attribute__((ext_vector_type(2)));
    typedef float f2 __attribute__((ext_vector_type(2)));
    f2 v = {lo, hi};
    bf2 r = __builtin_convertvector(v, bf2);
    return __builtin_bit_cast(unsigned, r);
}
__device__ __forceinline__ float rbf(float x) { return __uint_as_float(pk_bf16(0.f, x)); }

template <int CTRL> __device__ __forceinline__ unsigned dppu(unsigned v) {
    return (unsigned)__builtin_amdgcn_update_dpp(0, (int)v, CTRL, 0xf, 0xf, true);
}
template <int CTRL> __device__ __forceinline__ float dppf(float v) { return __uint_as_float(dppu<CTRL>(__float_as_uint(v))); }
__device__ __forceinline__ void swap16(unsigned v, unsigned& a, unsigned& b) {
    auto r = __builtin_amdgcn_permlane16_swap(v, v, false, false);
    a = r[0]; b = r[1];
}
__device__ __forceinline__ void swap32(unsigned v, unsigned& a, unsigned& b) {
    auto r = __builtin_amdgcn_permlane32_swap(v, v, false, false);
    a = r[0]; b = r[1];
}
__device__ __forceinline__ float sum_rows(float v) {
    unsigned a, b;
    swap16(__float_as_uint(v), a, b); v = __uint_as_float(a) + __uint_as_float(b);
    swap32(__float_as_uint(v), a, b); v = __uint_as_float(a) + __uint_as_float(b);
    return v;
}
__device__ __forceinline__ float wave_sum(float v) {
    v += dppf<0xB1>(v);
    v += dppf<0x4E>(v);
    v += dppf<0x141>(v);
    v += dppf<0x140>(v);
    return sum_rows(v);
}
__device__ __forceinline__ unsigned wave_max_u32(unsigned v) {
    v = max(v, dppu<0xB1>(v));
    v = max(v, dppu<0x4E>(v));
    v = max(v, dppu<0x141>(v));
    v = max(v, dppu<0x140>(v));
    unsigned a, b;
    swap16(v, a, b); v = max(a, b);
    swap32(v, a, b); v = max(a, b);
    return v;
}
__device__ __forceinline__ unsigned wave_max_s(unsigned v) {
    v = max(v, dppu<0xB1>(v));
    v = max(v, dppu<0x4E>(v));
    v = max(v, dppu<0x141>(v));
    v = max(v, dppu<0x140>(v));
    v = max(v, dppu<0x142>(v));
    v = max(v, dppu<0x143>(v));
    return (unsigned)__builtin_amdgcn_readlane((int)v, 63);
}
__device__ __forceinline__ float sel4(int g, float a, float b, float c, float d) {
    const float t0 = (g & 1) ? b : a, t1 = (g & 1) ? d : c;
    return (g & 2) ? t1 : t0;
}
__device__ __forceinline__ float pick(u64 m, float a, float b) {
    float r;
    asm("v_cndmask_b32 %0, %1, %2, %3" : "=v"(r) : "v"(a), "v"(b), "s"(m));
    return r;
}
__device__ __forceinline__ unsigned umax4(u32x4 v) { return max(max(v[0], v[1]), max(v[2], v[3])); }

__device__ __forceinline__ void touch_line(const void* p, unsigned lds_dst) {
    unsigned keep;
    asm volatile("s_mov_b32 %0, m0\n\ts_mov_b32 m0, %2\n\ts_nop 0\n\tglobal_load_lds_dword %1, off\n\ts_mov_b32 m0, %0"
                 : "=&s"(keep) : "v"(p), "s"(lds_dst) : "memory");
}
__device__ __forceinline__ __amdgpu_buffer_rsrc_t mk_rsrc(const void* p, unsigned bytes) {
    return __builtin_amdgcn_make_buffer_rsrc((void*)p, 0, (int)bytes, 0x00020000);
}
#define LDS_LOAD(w, v, o, pol) "buffer_load_" w " " v ", %0, %2 offen" o pol " lds"
#define LDS_M0 "s_mov_b32 m0, %1\n\ts_nop 0\n\t"
__device__ __forceinline__ void dma_nt(__amdgpu_buffer_rsrc_t r, unsigned dst, int voff, int soff) {
    asm volatile(LDS_M0 LDS_LOAD("dwordx4", "%3", "", " nt") :: "s"(r), "s"(dst), "s"(soff), "v"(voff) : "memory", "m0");
}
template <bool SC1> __device__ __forceinline__ void dma_word(__amdgpu_buffer_rsrc_t r, unsigned dst, int voff) {
    if constexpr (SC1) asm volatile(LDS_M0 LDS_LOAD("dword", "%3", "", " sc1") :: "s"(r), "s"(dst), "n"(0), "v"(voff) : "memory", "m0");
    else asm volatile(LDS_M0 LDS_LOAD("dword", "%3", "", "") :: "s"(r), "s"(dst), "n"(0), "v"(voff) : "memory", "m0");
}
template <bool SC1> __device__ __forceinline__ void dma_2k(__amdgpu_buffer_rsrc_t r, unsigned dst, int v0, int v1, int soff) {
    if constexpr (SC1) asm volatile(LDS_M0 LDS_LOAD("dwordx4", "%3", "", " sc1") "\n\t" LDS_LOAD("dwordx4", "%4", " offset:1024", " sc1")
                 :: "s"(r), "s"(dst), "s"(soff), "v"(v0), "v"(v1) : "memory", "m0");
    else asm volatile(LDS_M0 LDS_LOAD("dwordx4", "%3", "", "") "\n\t" LDS_LOAD("dwordx4", "%4", " offset:1024", "")
                 :: "s"(r), "s"(dst), "s"(soff), "v"(v0), "v"(v1) : "memory", "m0");
}
template <bool SC1> __device__ __forceinline__ void dma_4k(__amdgpu_buffer_rsrc_t r, unsigned dst, const int (&v)[4], int soff) {
#define FOUR(pol) asm volatile(LDS_M0 LDS_LOAD("dwordx4", "%3", "", pol) "\n\t" LDS_LOAD("dwordx4", "%4", " offset:1024", pol) "\n\t" \
        LDS_LOAD("dwordx4", "%5", " offset:2048", pol) "\n\t" LDS_LOAD("dwordx4", "%6", " offset:3072", pol) \
        :: "s"(r), "s"(dst), "s"(soff), "v"(v[0]), "v"(v[1]), "v"(v[2]), "v"(v[3]) : "memory", "m0")
    if constexpr (SC1) FOUR(" sc1"); else FOUR("");
#undef FOUR
}
#define CODE_BYTES 25472
__device__ __forceinline__ __amdgpu_buffer_rsrc_t code_rsrc() {
    unsigned long long p; unsigned d;
    asm volatile("s_getpc_b64 %0\n1:\n\ts_mov_b32 %1, 1b-_Z12fused_kernel4Args" : "=s"(p), "=s"(d));
    return __builtin_amdgcn_make_buffer_rsrc((void*)(p - d), 0, CODE_BYTES, 0x00020000);
}
#define LD16_SC1(rs, off) __builtin_amdgcn_raw_buffer_load_b128(rs, off, 0, 16)
#define ST16_SC1(v, rs, off) __builtin_amdgcn_raw_buffer_store_b128(v, rs, off, 0, 16)

constexpr int BT = 128, BK = 64, NSTAGE = 4;
constexpr int TILE_BYTES = BT * BK * 2;
constexpr int STAGE_BYTES = 2 * TILE_BYTES;
constexpr int RING_BYTES = NSTAGE * STAGE_BYTES;
constexpr int L_DUMMY = RING_BYTES;
constexpr int L_CTL = RING_BYTES + 256;
constexpr int L_RED = RING_BYTES + 512;

#define WAIT_VM(n) asm volatile("s_waitcnt vmcnt(" #n ")" ::: "memory")
#define WAIT_VM0() do { asm volatile("" ::: "memory"); __builtin_amdgcn_s_waitcnt(0x0F70); asm volatile("s_waitcnt vmcnt(0)" ::: "memory"); } while (0)
#define WAIT_LGKM0() asm volatile("s_waitcnt lgkmcnt(0)" ::: "memory")
#define BARRIER() do { asm volatile("" ::: "memory"); __builtin_amdgcn_s_barrier(); asm volatile("" ::: "memory"); } while (0)

__device__ __forceinline__ int prow(int rho) {
    const int w = rho >> 6, m = (rho >> 4) & 3, q = (rho >> 2) & 3, r = rho & 3;
    return w * 64 + (m >> 1) * 32 + q * 8 + (m & 1) * 4 + r;
}
__device__ __forceinline__ unsigned r2key(float r, unsigned code) {
    const unsigned u = pk_bf16(0.f, r);
    return ((u ^ ((unsigned)((int)u >> 31) | 0x80000000u)) & 0xffff0000u) | code;
}
__device__ __forceinline__ float key2r(unsigned k) {
    const unsigned o = k >> 16;
    return bflo((o & 0x8000u) ? (o & 0x7fffu) : (~o & 0xffffu));
}
#define STAT_M 5
#define BQ 2
#define POLL_T (STAT_M + 5)
#define AUX_SLEEP 12
#define WD_EARLY 4
#define C2_START 2
#define TAIL_PRIO 3
#define STAT_DELAY 8
#define AUX_STAY 1
#define CATCH1 4
#define CPF_MASK1 0x1080u
#define CPF_MASK2 0x1080u
#define CPF_STAG 5
#define CPF_FIX 0x1000u
#define KEY_S 128
#define TH_LIM 6
#define TH_N 6
#define S2_W 4
#define FIN_SLEEP 1
#define POLL_SLEEP 4
__device__ __forceinline__ f32x4 smf(bf16x8 a, bf16x8 b0, bf16x8 b1, f32x4 d, unsigned idx) {
    typedef __bf16 bf8 __attribute__((ext_vector_type(8)));
    typedef __bf16 bf16v __attribute__((ext_vector_type(16)));
    const auto b = __builtin_shufflevector(b0, b1, 0, 1, 2, 3, 4, 5, 6, 7, 8, 9, 10, 11, 12, 13, 14, 15);
    return __builtin_amdgcn_smfmac_f32_16x16x64_bf16(__builtin_bit_cast(bf8, a), __builtin_bit_cast(bf16v, b), d, (int)idx, 0, 0);
}
__device__ __forceinline__ float dot2bf(unsigned a, unsigned b, float c) {
    typedef __bf16 bf2 __attribute__((ext_vector_type(2)));
    return __builtin_amdgcn_fdot2_f32_bf16(__builtin_bit_cast(bf2, a), __builtin_bit_cast(bf2, b), c, false);
}

#define STAMP(i) do { } while (0)
#define CSTAMP(i) do { } while (0)
#define FLUSH() do { } while (0)
#define TSV do { } while (0)

#define KSHIFT 4

constexpr int L_HT = ((32 + KSHIFT) % NSTAGE) * STAGE_BYTES;
constexpr int L_WD0 = ((33 + KSHIFT) % NSTAGE) * STAGE_BYTES;
constexpr int L_WD1 = ((34 + KSHIFT) % NSTAGE) * STAGE_BYTES;
constexpr int L_ST = ((35 + KSHIFT) % NSTAGE) * STAGE_BYTES;
constexpr int L_YB = RING_BYTES;
constexpr int L_ATAB = L_YB;
constexpr int LDS_BYTES = RING_BYTES + 32768;

#define SB() __builtin_amdgcn_sched_barrier(0)
template <int N> __device__ __forceinline__ void wait_vm() { asm volatile("s_waitcnt vmcnt(%0)" :: "n"(N) : "memory"); }
__device__ __forceinline__ int at_tile(int c, int par) { return ((c << 8) & 0x78000) | (((c << 2) & 0x2000) ^ par); }
__device__ __forceinline__ int at_panel(int ep) { return ep << 19; }
__device__ __forceinline__ int pre_off(int slot) { const int s_ = slot >= 56, l = slot - 56 * s_; return (l >> 2) * 16384 + s_ * 8192 + (l & 3) * 2048; }
static_assert(KSHIFT == 4, "flag samples must have replaced older contents of their slot; even tile counts");
struct Ring {
    __amdgpu_buffer_rsrc_t rP, rQ, rF;
    int voP[4], voQ[4], voF;
    LAS unsigned char* fl;
    unsigned lb;
    int sp, sq;
    int poff, par, xm;
    bool img;
    template <int LD, bool PIMG> __device__ __forceinline__ void setup(int lw, int lane) {
        const int r = lane >> 3, v0 = (PIMG ? r : (r & 3) + (r & 4) * 2) * (LD * 2) + (((lane & 7) ^ (lane >> 4)) << 4);
#pragma unroll
        for (int j = 0; j < 4; ++j) {
            voQ[j] = (v0 ^ ((j & 1) * 64)) + (PIMG ? j * 8 : (j & 1) * 16 + (j >> 1) * 4) * (LD * 2) - j * 1024;
            voP[j] = PIMG ? (lw >> 1) * 16384 + (lw & 1) * 4096 + lane * 16 : voQ[j];
        }
        img = PIMG;
    }
    __device__ __forceinline__ void seek(int col, int pof) {
        sq = col; poff = pof;
        sp = (img ? at_tile(sq, par) : sq) + poff;
    }
    __device__ __forceinline__ void step() {
        sq = (sq + BK * 2) & 4095;
        sp = (img ? at_tile(sq ^ xm, par) : sq) + poff;
        asm volatile("" : "+s"(sq), "+s"(sp));
    }
    __device__ __forceinline__ void sample() { dma_word<true>(rF, (unsigned)(size_t)fl, voF); }
    __device__ __forceinline__ unsigned peek(unsigned addr) {
        unsigned v;
        asm volatile("ds_read_b32 %0, %1\n\ts_waitcnt lgkmcnt(0)" : "=v"(v) : "v"(addr) : "memory");
        return v;
    }
    __device__ __forceinline__ void gate(unsigned want, unsigned sa, unsigned need, unsigned& have) {
#pragma unroll 1
        for (int spin = 0; spin <= SPIN_CAP && (need & ~have) != 0u; ++spin) {
            if (spin) { sample(); WAIT_VM(0); __builtin_amdgcn_s_sleep(1); }
            const unsigned long long m = __ballot(peek(sa) == want);
            have = (unsigned)m & (unsigned)(m >> 32);
        }
    }
    template <int AUX> __device__ __forceinline__ void issueP(LAS unsigned char* base) { dma_4k<AUX != 0>(rP, (unsigned)(size_t)base, voP, sp); }
    __device__ __forceinline__ void issueQ(LAS unsigned char* base) { dma_4k<false>(rQ, (unsigned)(size_t)base + TILE_BYTES, voQ, sq ^ xm); }
    template <bool P2> __device__ __forceinline__ void issue(LAS unsigned char* lds) {
        issueP<P2 ? 16 : 0>(lds + lb);
        issueQ(lds + lb);
        SB();
        if (P2) sample();
        lb = (lb + STAGE_BYTES) & (RING_BYTES - 1);
        asm volatile("" : "+s"(lb));
        step();
        SB();
    }
    template <int CAT> __device__ __forceinline__ void ramp(LAS unsigned char* lds) {
#pragma unroll 1
        for (int i = 1; i <= CAT + 1; ++i) {
            issue<false>(lds);
            if (i == CAT + 1) continue;
            WAIT_VM(8);
            BARRIER();
        }
    }
    template <bool P2, bool SAMP, int W0 = 16> __device__ __forceinline__ void steady(int cnt, int trig = 0, int v2 = 0) {
        int t, u;
#define LD(v, r, s, o) "buffer_load_dwordx4 " v ", " r ", " s " offen" o " lds\n\t"
        if constexpr (P2) {
#define BODY(SMP, TST, ENT) asm volatile("s_mov_b32 m0, %0\n" ENT "1:\n\ts_waitcnt vmcnt(16)\n2:\n\ts_barrier\n\t" \
            LD("%8", "%6", "%2", " sc1") LD("%8", "%6", "%2", " offset:1024 sc1") LD("%8", "%6", "%2", " offset:2048 sc1") LD("%8", "%6", "%2", " offset:3072 sc1") \
            SMP "s_xor_b32 %4, %1, %18\n\ts_add_i32 %1, %1, 0x80\n\ts_add_i32 m0, %0, 0x4000\n\ts_and_b32 %1, %1, 0xfff\n\t" \
            LD("%9", "%7", "%4", "") "s_xor_b32 %2, %1, %18\n\ts_lshl_b32 %5, %1, 2\n\ts_lshl_b32 %2, %2, 8\n\ts_and_b32 %2, %2, 0x78000\n\t" \
            LD("%10", "%7", "%4", " offset:1024") "s_and_b32 %5, %5, 0x2000\n\ts_xor_b32 %5, %5, %13\n\ts_or_b32 %2, %2, %5\n\ts_add_i32 %3, %3, -1\n\t" \
            LD("%11", "%7", "%4", " offset:2048") "s_add_i32 %0, %0, 0x8000\n\ts_and_b32 %0, %0, 0x1ffff\n\t" TST "s_cmp_lg_u32 %3, 0\n\t" \
            LD("%12", "%7", "%4", " offset:3072") "\n4:\n\ts_mov_b32 m0, %0\n\ts_cbranch_scc1 1b\n\ts_waitcnt vmcnt(16)\n\ts_barrier" \
            : "+s"(lb), "+s"(sq), "+s"(sp), "+s"(cnt), "=&s"(t), "=&s"(u) \
            : "s"(rP), "s"(rQ), "v"(voP[0]), "v"(voQ[0]), "v"(voQ[1]), "v"(voQ[2]), "v"(voQ[3]), "s"(par | poff), "s"(rF), "v"(voF), "s"((unsigned)(size_t)fl), "n"(W0), "s"(xm), \
              "s"(trig), "v"(v2) \
            : "memory", "scc", "m0")
            if constexpr (SAMP) BODY("s_mov_b32 m0, %16\n\ts_nop 0\n\tbuffer_load_dword %15, %14, 0 offen sc1 lds\n\t", "", "");
            else BODY("", "s_cmp_eq_u32 %3, %19\n\ts_cbranch_scc1 3b\n\t",
                      "\ts_waitcnt vmcnt(%17)\n\ts_branch 2f\n3:\n\t" LD("%12", "%7", "%4", " offset:3072") "s_mov_b32 m0, %16\n\ts_nop 0\n\tbuffer_load_dword %20, %7, 0 offen lds\n\t"
                      "s_add_i32 m0, %16, 0x200\n\ts_cmp_lg_u32 %3, 0\n\tbuffer_load_dword %15, %14, 0 offen sc1 lds\n\ts_branch 4f\n");
#undef BODY
        } else {
            asm volatile("s_mov_b32 m0, %0\n\ts_branch 2f\n1:\n\ts_waitcnt vmcnt(16)\n\ts_barrier\n2:\n\t"
                LD("%6", "%4", "%1", "") LD("%7", "%4", "%1", " offset:1024") LD("%8", "%4", "%1", " offset:2048") LD("%9", "%4", "%1", " offset:3072")
                "s_add_i32 m0, %0, 0x4000\n\ts_mov_b32 %3, %1\n\t"
                LD("%6", "%5", "%3", "") "s_add_i32 %1, %1, 0x80\n\ts_add_i32 %0, %0, 0x8000\n\t"
                LD("%7", "%5", "%3", " offset:1024") "s_and_b32 %0, %0, 0x1ffff\n\ts_add_i32 %2, %2, -1\n\t"
                LD("%8", "%5", "%3", " offset:2048") "s_cmp_lg_u32 %2, 0\n\t"
                LD("%9", "%5", "%3", " offset:3072") "s_mov_b32 m0, %0\n\ts_cbranch_scc1 1b\n\ts_waitcnt vmcnt(16)\n\ts_barrier"
                : "+s"(lb), "+s"(sq), "+s"(cnt), "=&s"(t)
                : "s"(rP), "s"(rQ), "v"(voQ[0]), "v"(voQ[1]), "v"(voQ[2]), "v"(voQ[3])
                : "memory", "scc", "m0");
            (void)u;
        }
#undef LD
    }
};

template <bool PLIN, bool FIRST, class Pre>
__device__ __forceinline__ void compute_loop(const LAS unsigned char* lds, f32x4 (&acc)[4][4], int NT, int wp, int wq, int lane, Pre pre,
                                             bf16x8 (&p0)[4], bf16x8 (&p1)[4], unsigned last) {
    const int fr = lane & 15, fq = lane >> 4, key = (fr >> 1) & 7;
    const unsigned l0 = (unsigned)(size_t)lds;
    const unsigned oP0 = l0 + (PLIN ? wp * 8192 + lane * 16 : (wp * 64 + fr) * 128 + ((fq ^ key) * 16));
    const unsigned oP1 = l0 + (PLIN ? wp * 8192 + 1024 + lane * 16 : (wp * 64 + fr) * 128 + (((4 + fq) ^ key) * 16));
    const unsigned oQ0 = l0 + TILE_BYTES + (wq * 64 + fr) * 128 + ((fq ^ key) * 16);
    const unsigned oQ1 = l0 + TILE_BYTES + (wq * 64 + fr) * 128 + (((4 + fq) ^ key) * 16);
    unsigned aP0 = oP0, aQ0 = oQ0, aP1 = oP1, aQ1 = oQ1;
    bf16x8 r0[4], r1[4], q0[4], q1[4];
    unsigned xp = 2 * STAGE_BYTES;
#pragma unroll
    for (int i = 0; i < 16; ++i) asm volatile("" : "+v"(acc[i >> 2][i & 3]));
    asm volatile("" : "+v"(aP0), "+v"(aQ0), "+v"(aP1), "+v"(aQ1));
#define MM(pp, qq, m, n) do { acc[m][n] = __builtin_amdgcn_mfma_f32_16x16x32_bf16(pp[m], qq[n], acc[m][n], 0, 0, 0); SB(); } while (0)
#define RD(dst, i, a, st) do { dst[i] = *(const LAS bf16x8*)((const LAS unsigned char*)(size_t)(a) + (st) * STAGE_BYTES + (i) * 2048); SB(); } while (0)
#define ADP(a) do { a ^= xp; asm volatile("" : "+v"(a)); SB(); } while (0)
#define ADR(a) do { a ^= 2 * STAGE_BYTES; asm volatile("" : "+v"(a)); SB(); } while (0)
    __builtin_amdgcn_s_waitcnt(0xC07F);
    SB();
    if (FIRST) BARRIER();
    SB();
    RD(p0, 0, aP0, 0); RD(q0, 0, aQ0, 0); RD(p1, 0, aP1, 0); RD(q1, 0, aQ1, 0); RD(p1, 1, aP1, 0); RD(p0, 1, aP0, 0);
    RD(p0, 2, aP0, 0); RD(p1, 2, aP1, 0); RD(p1, 3, aP1, 0); RD(p0, 3, aP0, 0);
    pre();
    SB();
    int t = NT;
#pragma unroll 1
    do {
        MM(p0, q0, 0, 0); MM(p1, q1, 0, 0); RD(q0, 1, aQ0, 0);
        MM(p1, q1, 1, 0); MM(p0, q0, 1, 0); RD(q1, 1, aQ1, 0);
        MM(p0, q0, 2, 0); MM(p1, q1, 2, 0); RD(q0, 2, aQ0, 0);
        MM(p1, q1, 3, 0); MM(p0, q0, 3, 0); RD(q1, 2, aQ1, 0);
        MM(p0, q0, 3, 1); MM(p1, q1, 3, 1); RD(q0, 3, aQ0, 0);
        MM(p1, q1, 2, 1); MM(p0, q0, 2, 1); RD(q1, 3, aQ1, 0);
        MM(p0, q0, 1, 1); MM(p1, q1, 1, 1);
        MM(p1, q1, 0, 1); MM(p0, q0, 0, 1);
        MM(p0, q0, 0, 2); MM(p1, q1, 0, 2); __builtin_amdgcn_s_waitcnt(0xC07F); SB(); BARRIER(); SB();
        MM(p1, q1, 1, 2); RD(r0, 0, aP0, 1); MM(p0, q0, 1, 2); RD(q0, 0, aQ0, 1);
        MM(p0, q0, 2, 2); RD(r1, 0, aP1, 1); MM(p1, q1, 2, 2); RD(q1, 0, aQ1, 1);
        MM(p1, q1, 3, 2); MM(p0, q0, 3, 2); RD(r1, 1, aP1, 1);
        MM(p0, q0, 3, 3); RD(r0, 1, aP0, 1); MM(p1, q1, 3, 3);
        MM(p1, q1, 2, 3); RD(r0, 2, aP0, 1); MM(p0, q0, 2, 3); RD(r1, 2, aP1, 1);
        MM(p0, q0, 1, 3); MM(p1, q1, 1, 3); RD(r1, 3, aP1, 1);
        MM(p1, q1, 0, 3); RD(r0, 3, aP0, 1); MM(p0, q0, 0, 3);

        MM(r0, q0, 0, 0); MM(r1, q1, 0, 0); RD(q0, 1, aQ0, 1);
        MM(r1, q1, 1, 0); MM(r0, q0, 1, 0); RD(q1, 1, aQ1, 1);
        MM(r0, q0, 2, 0); MM(r1, q1, 2, 0); RD(q0, 2, aQ0, 1);
        MM(r1, q1, 3, 0); MM(r0, q0, 3, 0); RD(q1, 2, aQ1, 1);
        MM(r0, q0, 3, 1); MM(r1, q1, 3, 1); RD(q0, 3, aQ0, 1);
        MM(r1, q1, 2, 1); MM(r0, q0, 2, 1); RD(q1, 3, aQ1, 1);
        MM(r0, q0, 1, 1); t -= 2; if (PLIN) xp = t == 0 ? last : 2 * STAGE_BYTES; asm volatile("" : "+s"(t), "+s"(xp)); SB(); MM(r1, q1, 1, 1); ADP(aP0);
        MM(r1, q1, 0, 1); ADP(aP1); MM(r0, q0, 0, 1); ADR(aQ0);
        MM(r0, q0, 0, 2); ADR(aQ1); MM(r1, q1, 0, 2); __builtin_amdgcn_s_waitcnt(0xC07F); SB(); BARRIER(); SB();
        MM(r1, q1, 1, 2); RD(p0, 0, aP0, 0); MM(r0, q0, 1, 2); RD(q0, 0, aQ0, 0);
        MM(r0, q0, 2, 2); RD(p1, 0, aP1, 0); MM(r1, q1, 2, 2); RD(q1, 0, aQ1, 0);
        MM(r1, q1, 3, 2); MM(r0, q0, 3, 2); RD(p1, 1, aP1, 0);
        MM(r0, q0, 3, 3); RD(p0, 1, aP0, 0); MM(r1, q1, 3, 3);
        MM(r1, q1, 2, 3); RD(p0, 2, aP0, 0); MM(r0, q0, 2, 3); RD(p1, 2, aP1, 0);
        MM(r0, q0, 1, 3); MM(r1, q1, 1, 3); RD(p1, 3, aP1, 0);
        MM(r1, q1, 0, 3); RD(p0, 3, aP0, 0); MM(r0, q0, 0, 3);
    } while (t != 0);
    SB();
#undef MM
#undef RD
#undef ADR
#undef ADP
}

struct Args {
    const bf16_t* x; const bf16_t* wre; const bf16_t* wim; char* ws; const bf16_t* cr; const bf16_t* ci;
    const bf16_t* w_r; const bf16_t* wg; const bf16_t* wu; const bf16_t* wd; bf16_t* out;
};

__device__ __forceinline__ unsigned poll_winner(const char* ws, int rep, int lane, unsigned& mine) {
    GAS unsigned* kp = (GAS unsigned*)(ws + WS_KEYS + (size_t)(rep >> 2) * 8192 + (rep & 3) * (KEY_S * 16) + ((lane >> 2) & 7) * KEY_S) + (lane & 3);
    unsigned kw;
    for (int spin = 0;; ++spin) {
            asm volatile("" ::: "memory");
        kw = __hip_atomic_load(kp, __ATOMIC_RELAXED, __HIP_MEMORY_SCOPE_AGENT);
        if (__all(kw != 0u) || spin > SPIN_CAP) break;
        __builtin_amdgcn_s_sleep(POLL_SLEEP);
    }
    mine = kw;
    return __builtin_amdgcn_readfirstlane(wave_max_u32(kw));
}

template <int W1, int W2>
__device__ __forceinline__ void stage2(char* w2, int slot, int lw, LAS unsigned char* img0, LAS unsigned char* img1, LAS unsigned char* abuf, int zoff, int lane) {
    const int g = lane >> 4, n = lane & 15;
    __amdgpu_buffer_rsrc_t rPi = mk_rsrc(w2 + WS_PRE + pre_off(slot), 2048);
    unsigned aa[5], tr[8], pto = lane * 16;
    int pi = lane * 4;
    {
        const int q = (lane >> 2) & 3, ky = q | ((g & 1) << 2);
#pragma unroll
        for (int j = 0; j < 8; ++j) {
            tr[j] = (unsigned)(size_t)(g < 2 ? img0 : img1) + (8 * (g & 1) + q) * 512 + (j ^ ky) * 32 + (lane & 3) * 8;
            asm volatile("" : "+v"(tr[j]));
        }
    }
#pragma unroll
    for (int m = 0; m < 4; ++m) aa[m] = (unsigned)(size_t)abuf + ((n >> 2) == m && ((n >> 1) & 1) == (g >> 1) ? (n & 1) * 32 + (g & 1) * 16 : zoff + g * 16);
    aa[4] = (unsigned)(size_t)abuf + ((lane >> 1) & 1) * 32 + ((lane >> 4) & 1) * 16 + ((lane >> 2) & 3) * 4 + (lane & 1) * 2;
    unsigned ix = (n & 1) ? 0xEEEEEEEEu : 0x44444444u;
    __amdgpu_buffer_rsrc_t rPt = mk_rsrc(w2 + WS_PART + __builtin_amdgcn_readfirstlane((slot >> 2) * 16384 + (lw >> 1) * 8192 + (slot & 3) * 2048 + (lw & 1) * 1024), 1024);
    asm volatile("" : "+v"(aa[0]), "+v"(aa[1]), "+v"(aa[2]), "+v"(aa[3]), "+v"(aa[4]), "+v"(pto), "+v"(ix), "+v"(pi));
    asm volatile("" : "+s"(rPt), "+s"(rPi));
    unsigned pp[8];
    auto request = [&]() {
        asm volatile("" ::: "memory");
#pragma unroll
        for (int k = 0; k < 8; ++k) pp[k] = __builtin_amdgcn_raw_buffer_load_b32(rPi, pi, k * 256, 16);
        SB();
    };
    f32x4 d[2] = {{0.f, 0.f, 0.f, 0.f}, {0.f, 0.f, 0.f, 0.f}};
    asm volatile("" : "+v"(d[0]), "+v"(d[1]));
    SB();
    wait_vm<W1>();
    request();
    if (W2 >= 0) wait_vm<W2 < 0 ? 0 : W2>();
    bf16x8 wf[16];
#pragma unroll
    for (int j = 0; j < 16; ++j) {
        LAS unsigned char* t = (LAS unsigned char*)(size_t)tr[j & 7] + (j & 8) * 32;
        const bf16x4 w0 = __builtin_amdgcn_ds_read_tr16_b64_v4i16((LAS bf16x4*)t);
        const bf16x4 w1 = __builtin_amdgcn_ds_read_tr16_b64_v4i16((LAS bf16x4*)(t + 2048));
        wf[j] = __builtin_shufflevector(w0, w1, 0, 1, 2, 3, 4, 5, 6, 7);
    }
    SB();
#pragma unroll
    for (int j = 0; j < 16; ++j) asm volatile("" : "+v"(wf[j]));
    SB();
    float sv;
#pragma unroll 1
    for (int spin = 0;; ++spin) {
        sv = ((__uint_as_float(pp[0]) + __uint_as_float(pp[1])) + (__uint_as_float(pp[2]) + __uint_as_float(pp[3]))) +
             ((__uint_as_float(pp[4]) + __uint_as_float(pp[5])) + (__uint_as_float(pp[6]) + __uint_as_float(pp[7])));
        if (__builtin_expect(__builtin_amdgcn_ballot_w64(sv != sv) == 0, 1)) break;
        if (__all(max(max(max(pp[0], pp[1]), max(pp[2], pp[3])), max(max(pp[4], pp[5]), max(pp[6], pp[7]))) != SENT) || spin > SPIN_CAP) break;
        request();
    }
    unsigned ug, uu;
    swap32(__float_as_uint(sv), ug, uu);
    const float gg = rbf(__uint_as_float(ug)), up = rbf(__uint_as_float(uu));
    const unsigned act = pk_bf16(rbf(gg * __builtin_amdgcn_rcpf(1.0f + __expf(-gg))) * up, 0.f);
    *(volatile LAS unsigned short*)(size_t)aa[4] = (unsigned short)act;
    bf16x8 af[4];
#pragma unroll
    for (int m = 0; m < 4; ++m) af[m] = *(volatile LAS bf16x8*)(size_t)aa[m];
    SB();
#pragma unroll
    for (int i = 0; i < 8; ++i) {
        const int j = 8 * (i & 1) + 2 * (i >> 1);
        d[i & 1] = smf(af[i >> 1], wf[j], wf[j + 1], d[i & 1], ix);
        SB();
    }
    u32x4 pv;
#pragma unroll
    for (int q = 0; q < 4; ++q) {
        pv[q] = min(__float_as_uint(d[q >> 1][(q & 1) * 2] + d[q >> 1][(q & 1) * 2 + 1]), SENT - 1u);
        asm volatile("" : "+v"(pv[q]));
    }
    ST16_SC1(pv, rPt, pto);
}
__device__ __forceinline__ int gu_off(int slot, int w, int lane) {
    const int hi = lane >> 5, c = lane & 31;
    return (((slot & 7) * 128 + 4 * w + hi) * F_LEN + (slot >> 3) * 256 + (((c >> 1) ^ hi ^ ((w & 2) << 1)) * 16) + (c & 1) * 8) * 2;
}
__device__ __forceinline__ int wd_off(int lw, int i, int lane) {
    const int r = 2 * (i & 5) + (lane >> 5), ky = (r & 3) | ((r >> 1) & 4), c = lane & 31;
    return (lane >> 5) * (D_LEN * 2) + lw * 512 + (((c >> 1) ^ ky) * 2 + (c & 1)) * 16;
}

__global__ __launch_bounds__(768, 3) void fused_kernel(const Args a) {
    extern __shared__ __attribute__((aligned(16))) unsigned char smem[];
    constexpr int K = S_LEN, NT1 = D_LEN / BK;
    const int tid = threadIdx.x, wid = __builtin_amdgcn_readfirstlane(tid >> 6), lane = tid & 63;
    const int xcd = blockIdx.x & 7, idx = blockIdx.x >> 3;
    TSV;
    if (wid == 4) STAMP(57);
    LAS unsigned char* lds = (LAS unsigned char*)smem;
    if (idx < 1 && wid < 4) dma_word<false>(code_rsrc(), (unsigned)(size_t)lds + L_DUMMY, tid * 128);
    SB();
    const int h = xcd & 1, xq = xcd >> 1, rt = xq * 4 + (idx & 3), ct = idx >> 2;
    volatile LAS unsigned* ctl1 = (volatile LAS unsigned*)(lds + L_CTL);
#define NBR(k) ((rt + (k)) & 3)
    unsigned kx;
    Ring ring;
    if (__builtin_expect((wid & 12) == 4, 1)) {
        __builtin_amdgcn_s_setprio(3);
        ring.template setup<D_LEN, false>(wid - 4, lane);
        ring.rP = mk_rsrc(a.x + (size_t)(rt * 4 + wid - 4) * 32 * D_LEN, 32 * D_LEN * 2);
        ring.rQ = mk_rsrc((h == 0 ? a.wre : a.wim) + (size_t)(ct * 4 + wid - 4) * 32 * D_LEN, 32 * D_LEN * 2);
        ring.lb = (wid - 4) * 4096;
        ring.sp = ring.sq = ring.poff = ring.xm = 0;
        if (wid == 4) STAMP(58);
        ring.template issue<false>(lds);
        if (wid == 4) STAMP(59);
        __builtin_amdgcn_s_setprio(0);
        ring.par = (ct & 4) << 11;
        ring.template ramp<CATCH1>(lds);
        ring.template steady<false, false>(NT1 - 2 - CATCH1);
        ring.template setup<K, true>(wid - 4, lane);
        ring.rQ = mk_rsrc((h == 0 ? a.cr : a.ci) + (size_t)(rt * 4 + wid - 4) * 32 * K, 32 * K * 2);
        ring.seek(rt * 256, 0);
        LAS unsigned char* l0 = lds + (wid - 4) * 4096;
        ring.issueQ(l0); ring.step();
        WAIT_VM(12); BARRIER();
        ring.issueQ(l0 + STAGE_BYTES);
        ring.seek((xq * 4 + NBR(1)) * 256, 0);
        const int xr = (wid & 1) * 8 + (lane >> 4);
        int vx0 = ((((wid >> 1) & 1) * 64 + xr) * D_LEN + (((lane & 15) ^ xr) * 8)) * 2;
        int vx1 = ((((wid >> 1) & 1) * 64 + xr + 4) * D_LEN + (((lane & 15) ^ (xr + 4)) * 8)) * 2 - 1024;
        __amdgpu_buffer_rsrc_t rX = mk_rsrc(a.x + (size_t)rt * BT * D_LEN + ct * BT, BT * D_LEN * 2 - ct * BT * 2);
        unsigned xd = (unsigned)(size_t)lds + L_YB + ((wid & 2) ? 16384 : 4096) + (wid & 1) * 2048;
        unsigned xe = (wid & 2) ? xd + 12288 : (unsigned)(size_t)lds + 3 * STAGE_BYTES + (wid & 1) * 2048;
        asm volatile("" : "+v"(vx0), "+v"(vx1));
        asm volatile("" : "+s"(rX), "+s"(xd), "+s"(xe));
        WAIT_VM(8); BARRIER();
        ring.issueQ(l0 + 2 * STAGE_BYTES);
        ring.seek((xq * 4 + NBR(2)) * 256, 0);
        if (h != 0) {
#pragma unroll
            for (int k = 0; k < 3; ++k) dma_2k<false>(rX, xd + k * 4096, vx0, vx1, k * (16 * D_LEN * 2));
        }
        BARRIER();
        if (h != 0) dma_2k<false>(rX, xe, vx0, vx1, 3 * (16 * D_LEN * 2));
        ring.issueQ(l0 + 3 * STAGE_BYTES);
        kx = (unsigned)__builtin_amdgcn_readfirstlane((int)ctl1[4]);
        SB();
    }
    char* ws = a.ws + ((0ull - (unsigned long long)a.ws) & 0x1fffffull);
    const int g = lane >> 4, n = lane & 15;
    LAS float* tab = (LAS float*)(lds + L_RED);
    if (wid >= 8) {
        const int aw = wid - 8;
#define T1(t) do { if (aw == 1 && (t) <= 16) STAMP(40 + (t)); } while (0)
        GAS u64* tagp = (GAS u64*)(ws + WS_TAG);
        const int r0 = xq * 512 + h * 256 + idx * 8 + aw * 2;
        const int w0 = xq * 256 + idx * 8 + aw * 2;
        const u32x4* src = (const u32x4*)((n < 8 ? a.x + (size_t)(r0 + (n >> 2)) * D_LEN
                                                 : (h == 0 ? a.wre : a.wim) + (size_t)(w0 + (n >> 2) - 2) * D_LEN) + g * 32 + (n & 3) * 8);
        if (AUX_SLEEP) __builtin_amdgcn_s_sleep(AUX_SLEEP);
        u64 tag0 = __hip_atomic_load(tagp, __ATOMIC_RELAXED, __HIP_MEMORY_SCOPE_AGENT);
        u32x4 v[8];
#pragma unroll
        for (int j = 0; j < 4; ++j) v[j] = src[j * 16];
        __builtin_amdgcn_s_setprio(3);
        SB();
        const char* cb = (const char*)(h ? a.ci : a.cr) + (size_t)rt * (512u << 10) + ct * 65536 + (lane >> 2) * 4096 + (lane & 3) * 128;
        const GAS u32x4* wrp = (const GAS u32x4*)a.w_r + lane;
        asm volatile("" : "+v"(cb), "+v"(wrp));
        unsigned mask = aw == 1 ? CPF_MASK1 : aw == 2 ? CPF_MASK2 : 0u;
        mask = (mask & CPF_FIX) | ((mask & ~CPF_FIX) << (idx % CPF_STAG));
        int ti = 2 * xq + aw - 1;
        auto warm = [&](int t) { if (t >= 4 && ((mask >> t) & 1)) { touch_line(cb + (ti & 7) * 512, L_DUMMY); ti += 2; } };
        f32x4 d1 = {0.f, 0.f, 0.f, 0.f}, d2 = {0.f, 0.f, 0.f, 0.f};
        u32x4 ones = {0x3F803F80u, 0x3F803F80u, 0x3F803F80u, 0x3F803F80u};
        auto sums = [&](int j0) __attribute__((always_inline)) {
#pragma unroll
            for (int j = j0; j < j0 + 4; ++j) {
                const bf16x8 f = __builtin_bit_cast(bf16x8, v[j]);
                d1 = __builtin_amdgcn_mfma_f32_16x16x32_bf16(f, __builtin_bit_cast(bf16x8, ones), d1, 0, 0, 0);
                d2 = __builtin_amdgcn_mfma_f32_16x16x32_bf16(f, f, d2, 0, 0, 0);
            }
            asm volatile("" : "+v"(d1), "+v"(d2));
        };
        unsigned wl, wh;
        int ro;
        __amdgpu_buffer_rsrc_t rS;
        u64 m1 = 0xAAAAAAAAAAAAAAAAull, m2 = 0xCCCCCCCCCCCCCCCCull, mh = 0xFFFFFFFF00000000ull;
#pragma unroll
        for (int t = 0; t <= 5; ++t) {
            SB(); BARRIER(); SB();
            T1(t);
            if (t == 2) {
#pragma unroll
                for (int j = 4; j < 8; ++j) v[j] = src[j * 16];
            }
            if (t == 3) {
                asm volatile("" : "+v"(ones));
                sums(0);
                asm volatile("" : "+v"(tag0));
                const u64 want = tag0 * 6364136223846793005ull + 1442695040888963407ull;
                wl = (unsigned)want; wh = (unsigned)(want >> 32);
                asm volatile("" : "+v"(wl), "+v"(wh));
            }
            if (t == 4) { sums(4); asm volatile("s_nop 7\n\ts_nop 3" : "+v"(d2)); }
            if (t == 1) {
                rS = mk_rsrc(ws + WS_XRD, 65536);
                ro = n == g * 4 ? (g < 2 ? r0 + g : 2048 - 2 + h * 1024 + w0 + g) * 16 : 65536;
                asm volatile("" : "+v"(ro));
                asm volatile("" : "+s"(rS), "+s"(m1), "+s"(m2), "+s"(mh), "+s"(mask), "+s"(ti));
            }
        }
        float sm = (d1[0] + d1[1]) + (d1[2] + d1[3]);
        float sq = pick(m2, pick(m1, d2[0], d2[1]), pick(m1, d2[2], d2[3]));
        asm volatile("s_nop 1" : "+v"(sq));
        sq += dppf<0xB1>(sq);
        sq += dppf<0x4E>(sq);
        float mean = sm * (1.0f / D_LEN), var = rbf(rbf(fmaxf(sq * (1.0f / D_LEN) - mean * mean, 0.f)) + 1e-5f);
        asm volatile("" : "+v"(sm), "+v"(mean), "+v"(var));
        SB(); BARRIER(); SB();
        T1(6);
        {
            float rden = __builtin_amdgcn_rcpf(rbf(__builtin_amdgcn_sqrtf(var)));
            asm volatile("s_nop 1" : "+v"(rden));
            const u32x4 rec = {__float_as_uint(pick(mh, rden, sm)), wl, __float_as_uint(rbf(mean) * rden), wh};
            ST16_SC1(rec, rS, ro);
        }
        const __amdgpu_buffer_rsrc_t rV = mk_rsrc(ws + WS_VOTE, 512);
        if (aw == 3 && idx < 8) __builtin_amdgcn_raw_buffer_store_b64((u32x2){ctl1[5], wl}, rV, blockIdx.x * 8 + lane * 512, 0, 16);
        SB(); BARRIER(); SB();
        T1(STAT_M + 2);
        warm(STAT_M + 2);
        if (aw == 1 || aw == 2) {
#pragma unroll 1
            for (int t = STAT_M + 3; t <= NT1 + AUX_STAY; ++t) {
                BARRIER();
                T1(t);
                warm(t);
            }
            FLUSH();
            return;
        }
        if (aw == 3) {
            if (h == 0) {
                const u32x4 w0v = wrp[0], w1v = wrp[64];
                BARRIER(); BARRIER(); BARRIER(); BARRIER();
                SB();
                float t = 0.f;
#pragma unroll
                for (int i = 0; i < 4; ++i) { t = dot2bf(w0v[i], 0x3F803F80u, t); t = dot2bf(w1v[i], 0x3F803F80u, t); }
                SB(); BARRIER(); SB();
                t = wave_sum(t);
                ctl1[1] = __float_as_uint(t);
                WAIT_LGKM0();
            }
            return;
        }
#pragma unroll
        for (int t = STAT_M + 3; t <= POLL_T; ++t) BARRIER();
        SB();
        const int o0 = (rt * BT + lane) * 16, o1 = (2048 + (h * 8 + ct) * BT + lane) * 16, os = 1024;
        u32x4 q0, q1, q2, q3;
        u32x2 qv;
        auto request = [&]() {
            asm volatile("" ::: "memory");
            q0 = LD16_SC1(rS, o0); q1 = LD16_SC1(rS, o0 + os); q2 = LD16_SC1(rS, o1); q3 = LD16_SC1(rS, o1 + os);
            qv = __builtin_amdgcn_raw_buffer_load_b64(rV, lane * 8, 0, 16);
            SB();
        };
        request();
        int b = POLL_T + 1;
        for (int spin = 0; spin <= SPIN_CAP; ++spin) {
            if (b < NT1) { BARRIER(); ++b; }
            if (b < NT1) { BARRIER(); ++b; }
            SB();
            unsigned d = ((q0[1] ^ wl) | (q1[1] ^ wl)) | ((q2[1] ^ wl) | (q3[1] ^ wl)) | ((q0[3] ^ wh) | (q1[3] ^ wh)) | ((q2[3] ^ wh) | (q3[3] ^ wh)) | (qv[1] ^ wl);
            asm volatile("" : "+v"(d));
            if (__all(d == 0u)) break;
            request();
        }
        if (b < NT1) BARRIER();
        SB();
        LAS unsigned* tb = (LAS unsigned*)tab;
        const unsigned sg = h == 0 ? 0u : 0x80000000u;
        tb[lane] = q0[0] ^ sg; tb[64 + lane] = q1[0] ^ sg; tb[128 + lane] = q0[2] ^ sg; tb[192 + lane] = q1[2] ^ sg;
        tb[256 + lane] = q2[0]; tb[320 + lane] = q3[0];
        const unsigned vt = qv[0];
        const unsigned kx = __popcll(__ballot(vt == 2u)) > __popcll(__ballot(vt == 1u)) ? 8192u : 0u;
        if (lane == 0) { ctl1[0] = wl | 2u; ctl1[2] = wl; ctl1[3] = wh; ctl1[4] = kx; }
        *(LAS u32x2*)(lds + L_YB + 2560 + 256 + lane * 8) = (u32x2){0u, 0u};
        WAIT_LGKM0();
        return;
    }
#define CTL(i) ((unsigned)__builtin_amdgcn_readfirstlane((int)ctl[i]))
    const int wp = wid & 1, wq = (wid >> 1) & 1;
#define Y_OFF ((int)WS_Y + ((rt & 7) * 8 + ct) * 65536 + (wid & 3) * 16384 + (((rt >> 3) ^ (rt >> 2)) & 1) * 8192)
#define P2_DEFS \
    char* w2 = ws + kx; \
    const int sidx = idx < 28 ? xq * 28 + idx : 112 + xq * 4 + idx - 28;     \
    volatile LAS unsigned* ctl = (volatile LAS unsigned*)(lds + (h == 0 ? L_ATAB : L_CTL)); \
    const int NT = h == 0 ? 32 + KSHIFT : 32 - KSHIFT; \
    LAS float* rstat = (LAS float*)(lds + L_ATAB + 256);     \
    const int hp_ = rt * 2 + wq;                             \
    (void)sidx; (void)ctl; (void)NT; (void)rstat; (void)hp_; (void)w2
    if (__builtin_expect(wid < 4, 1)) {
        {
            const u32x4 sent4 = {SENT, SENT, SENT, SENT}, zero4 = {0u, 0u, 0u, 0u};
            const __amdgpu_buffer_rsrc_t rI = mk_rsrc(ws + WS_STATS + (size_t)blockIdx.x * (FILL_BYTES / 256), FILL_BYTES / 256);
            const __amdgpu_buffer_rsrc_t rZ = mk_rsrc(ws + (size_t)blockIdx.x * 160, 160);
            ST16_SC1(sent4, rI, tid * 16);
            if (wid < 1) ST16_SC1(sent4, rI, 4096 + tid * 16);
            if (wid == 2) ST16_SC1(zero4, rZ, lane * 16);
        }
        if (wid == 3 && idx < 8) {
            const unsigned r = (((unsigned)__builtin_amdgcn_s_memrealtime() * 2654435761u + blockIdx.x * 40503u) >> 9) & 0x1fdf80u;
            const unsigned first = (idx & 1) << 13;
            const char* pb = ws + WS_PROBE;
            unsigned va, vb, left;
            asm volatile("s_waitcnt lgkmcnt(0)\n\ts_load_dword %0, %3, %4\n\ts_load_dword %1, %3, %5\n\ts_waitcnt lgkmcnt(1)\n\t"
                         "s_nop 15\n\ts_getreg_b32 %2, hwreg(HW_REG_IB_STS, 8, 4)\n\ts_waitcnt lgkmcnt(0)"
                         : "=&s"(va), "=&s"(vb), "=&s"(left) : "s"(pb), "s"(r | first), "s"(r | (first ^ 8192u)) : "memory");
            const unsigned nearbit = (left != 0u ? first : first ^ 8192u) >> 13;
            if (lane == 0) ctl1[5] = nearbit == (unsigned)(xcd >> 2) ? 1u : 2u;
        }
        f32x4 acc[4][4];
#pragma unroll
        for (int i = 0; i < 16; ++i) acc[i >> 2][i & 3] = (f32x4){0.f, 0.f, 0.f, 0.f};
        unsigned tc = (unsigned)(size_t)(tab + wp * 64 + g * 8), tw = (unsigned)(size_t)(tab + 256 + prow(wq * 64 + n));
        unsigned ta = (unsigned)(size_t)lds + wp * STAGE_BYTES + wq * 8192 + lane * 16;
        constexpr int NIL = 1 << 30;
        __amdgpu_buffer_rsrc_t rW = mk_rsrc(ws, 18u << 20);
        int sk = (int)WS_AT + at_panel(h * 8 + ct) + at_tile((rt * 2 + wp) * 128, (ct & 4) << 11) + wq * 16384 + lane * 16;
        int st = lane != 0 ? NIL : (int)WS_FLAG1 + (1 - wp) * 32768 + h * 16384 + (rt >> 3) * 8192 + ct * 1024 + (rt & 7) * 128 + wq * 4;
        int so = Y_OFF + lane * 16;
        unsigned sl = (unsigned)(size_t)lds + L_ST + (g != (n >> 2) ? 4096 + wid * 2048 + lane * 16 : wp * 2048 + wq * 1024 + n * 16);
        int sf = lane != 0 ? NIL : (int)WS_FLAGS + (rt * 8 + ct) * 128 + wid * 4;
        u32x4 sel[2], aw[2];
        unsigned one[2];
#pragma unroll
        for (int i = 0; i < 4; ++i) {
            one[i & 1] = g == (n >> 2) && (i & 1) == ((n >> 1) & 1) ? ((n & 1) ? 0x3F800000u : 0x00003F80u) : 0u;
            aw[0][i] = aw[1][i] = (n & 3) == 0 ? 0x3F803F80u : 0u;
        }
        if ((n & 3) == 1) {
            const bf16_t* wr = a.w_r + ct * BT + wp * 64 + g * 8;
            aw[0] = *(const u32x4*)wr; aw[1] = *(const u32x4*)(wr + 32);
        }
        unsigned ea[2];
#pragma unroll
        for (int mp = 0; mp < 2; ++mp)
            ea[mp] = (unsigned)(size_t)lds + L_YB + (wq ? 16384 : 4096) + n * 256 + (((wp * 8 + mp * 4 + g) ^ n) * 16);
        asm volatile("" : "+v"(tc), "+v"(tw), "+v"(ta), "+v"(sk), "+v"(st), "+v"(so), "+v"(sl), "+v"(sf), "+v"(one[0]), "+v"(one[1]), "+v"(ea[0]), "+v"(ea[1]));
        asm volatile("" : "+s"(rW));
        unsigned tg;
        bf16x8 e0[4], e1[4];
        auto extra = [&]() __attribute__((always_inline)) {
            bf16x8 ef[4][2];
#pragma unroll
            for (int i = 0; i < 8; ++i)
                ef[i >> 1][i & 1] = *(const LAS bf16x8*)((const LAS unsigned char*)(size_t)(ea[i & 1] + (i < 6 || wq ? 0 : 3 * STAGE_BYTES - L_YB - 16384)) + (i >> 1) * 4096);
            SB();
#pragma unroll
            for (int i = 0; i < 16; ++i)
                acc[i & 3][i >> 2] = __builtin_amdgcn_mfma_f32_16x16x32_bf16(__builtin_bit_cast(bf16x8, sel[i & 1]), ef[i >> 2][(i >> 1) & 1], acc[i & 3][i >> 2], 0, 0, 0);
        };
        if (wid == 0) STAMP(0);
        compute_loop<false, true>(lds, acc, NT1, wp, wq, lane, [&]() { if (wid == 0) STAMP(24); WAIT_VM0(); if (wid == 0) STAMP(31); }, e0, e1, 0u);
        if (wid == 0) STAMP(25);
        {
            f32x4 rd[4], mr[4];
            float wsm[4];
#pragma unroll
            for (int m = 0; m < 4; ++m) {
                rd[m] = *(const LAS f32x4*)((const LAS float*)(size_t)tc + (m >> 1) * 32 + (m & 1) * 4);
                mr[m] = *(const LAS f32x4*)((const LAS float*)(size_t)tc + 128 + (m >> 1) * 32 + (m & 1) * 4);
                wsm[m] = *((const LAS float*)(size_t)tw + (m >> 1) * 32 + (m & 1) * 4);
            }
            const unsigned kv = ctl1[4];
            tg = ctl1[0];
            SB();
            sk += (int)kv; st += (int)kv; so += (int)kv; sf += (int)kv;
            kx = (unsigned)__builtin_amdgcn_readfirstlane((int)kv);
#pragma unroll
            for (int nn = 0; nn < 4; ++nn)
#pragma unroll
                for (int mp = 0; mp < 2; ++mp) {
                    const f32x4 u = acc[2 * mp][nn] * rd[2 * mp] - mr[2 * mp] * wsm[nn];
                    const f32x4 v = acc[2 * mp + 1][nn] * rd[2 * mp + 1] - mr[2 * mp + 1] * wsm[nn];
                    u32x4 w;
                    w[0] = pk_bf16(u[0], u[1]); w[1] = pk_bf16(u[2], u[3]);
                    w[2] = pk_bf16(v[0], v[1]); w[3] = pk_bf16(v[2], v[3]);
                    ST16_SC1(w, rW, sk + (nn * 2 + mp) * 1024);
                    *(LAS u32x4*)((LAS unsigned char*)(size_t)ta + (nn * 2 + mp) * 1024) = w;
                }
        }
        if (wid == 0) STAMP(26);
        WAIT_LGKM0();
        BARRIER();
        SB();
        {
#pragma unroll
            for (int i = 0; i < 16; ++i) { acc[i >> 2][i & 3] = (f32x4){0.f, 0.f, 0.f, 0.f}; asm volatile("" : "+v"(acc[i >> 2][i & 3])); }
            sel[0] = (u32x4){one[0], one[1], 0u, 0u}; sel[1] = (u32x4){0u, 0u, one[0], one[1]};
            asm volatile("" : "+v"(st), "+v"(so), "+v"(sf), "+v"(tg), "+v"(sel[0]), "+v"(sel[1]));
            SB();
            if (h != 0) extra();
            SB();
            WAIT_VM(0);
            __builtin_amdgcn_raw_buffer_store_b32(tg, rW, st, 0, 16);
            SB();
            if (wid == 0) STAMP(27);
        }
        compute_loop<true, false>(lds, acc, h == 0 ? 32 + KSHIFT : 32 - KSHIFT, wp, wq, lane, [&]() {
            if (wid == 0) { STAMP(h == 0 ? 1 : 19); CSTAMP(22); }
        }, e0, e1, (unsigned)((wp * 8192 + 2 * STAGE_BYTES) ^ (L_YB + wid * 8192)));
        P2_DEFS;
        if (wid == 0) { STAMP(h == 0 ? 2 : 12); CSTAMP(23); }
        u32x4 hq[4][2];
        auto cvt = [&](int nn, int j) __attribute__((always_inline)) {
            const f32x4 c = acc[j][nn];
            hq[nn][j >> 1][(j & 1) * 2] = pk_bf16(c[0], c[1]);
            hq[nn][j >> 1][(j & 1) * 2 + 1] = pk_bf16(c[2], c[3]);
        };
        int hs = h;
        asm volatile("" : "+s"(hs));
        if (__builtin_expect(hs == 0, 1)) {
            f32x4 d1[4], d2[4];
            u32x3 sv;
            auto add = [&](int nn, int j) __attribute__((always_inline)) {
                acc[j][nn] = __builtin_amdgcn_mfma_f32_16x16x32_bf16(__builtin_bit_cast(bf16x8, sel[j & 1]), j < 2 ? e0[nn] : e1[nn], acc[j][nn], 0, 0, 0);
            };
            auto sum = [&](int nn, int j) __attribute__((always_inline)) {
                const bf16x8 hf = __builtin_bit_cast(bf16x8, hq[nn][j >> 1]);
                const f32x4 z = {0.f, 0.f, 0.f, 0.f};
                if (j & 1) d1[nn] = __builtin_amdgcn_mfma_f32_16x16x32_bf16(__builtin_bit_cast(bf16x8, aw[j >> 1]), hf, j < 2 ? z : d1[nn], 0, 0, 0);
                else d2[nn] = __builtin_amdgcn_mfma_f32_16x16x32_bf16(hf, hf, j < 2 ? z : d2[nn], 0, 0, 0);
            };
            auto out = [&](int nn, int j) __attribute__((always_inline)) {
                if (j == 1) asm volatile("" : "+v"(d2[nn]));
                if (j == 2) asm volatile("" : "+v"(d1[nn]));
                if (j == 1) sv[1] = __float_as_uint(sel4(n, d2[nn][0], d2[nn][1], d2[nn][2], d2[nn][3]));
                if (j == 2) { const float u0 = d1[nn][0], u1 = d1[nn][1]; sv[0] = __float_as_uint(u0); sv[2] = __float_as_uint(u1); }
                if (j == 3) *(LAS u32x3*)((LAS unsigned char*)(size_t)sl + nn * 256) = sv;
            };
#define FOUR(m, v) _Pragma("unroll") for (int j = 0; j < 4; ++j) { m; SB(); v; SB(); }
            FOUR(add(0, j), (void)0) FOUR(add(1, j), cvt(0, j)) FOUR(sum(0, j), cvt(1, j)) FOUR(add(2, j), out(0, j))
            FOUR(sum(1, j), cvt(2, j)) FOUR(add(3, j), out(1, j)) FOUR(sum(2, j), cvt(3, j)) FOUR(sum(3, j), out(2, j))
            FOUR((void)0, out(3, j))
#undef FOUR
            WAIT_LGKM0();
            BARRIER();
            if (wid == 0) STAMP(3);
#pragma unroll
            for (int nn = 0; nn < 4; ++nn)
#pragma unroll
                for (int mp = 0; mp < 2; ++mp)
                    *(LAS u32x4*)(lds + L_HT + (wq * 64 + nn * 16 + n) * 256 + (((wp * 8 + mp * 4 + g) ^ n) * 16)) = hq[nn][mp];
            WAIT_LGKM0();
            BARRIER();
            BARRIER();
            if (wid == 0) STAMP(6);
            const int crow = __builtin_amdgcn_readfirstlane((int)((volatile LAS unsigned*)(lds + L_ATAB))[2 + wq]) + wq * 64;
            const __amdgpu_buffer_rsrc_t rOut = mk_rsrc(a.out + (size_t)rt * BT * D_LEN, BT * D_LEN * 2);
#pragma unroll 2
            for (int j = 0; j < 8; ++j) {
                const int r = wq * 64 + wp * 32 + j * 4 + g;
                const float m_ = rstat[r * 2], d_ = rstat[r * 2 + 1];
                const u32x4 hh = *(const LAS u32x4*)(lds + L_HT + r * 256 + ((n ^ (r & 15)) * 16));
                u32x4 ov;
#pragma unroll
                for (int i = 0; i < 4; ++i) {
                    const unsigned u = hh[i];
                    const float h0 = bflo(u), h1 = bfhi(u);
                    const unsigned dd = pk_bf16(h0 - m_, h1 - m_);
                    const unsigned q = pk_bf16(bflo(dd) * d_, bfhi(dd) * d_);
                    ov[i] = pk_bf16(h0 + bflo(q), h1 + bfhi(q));
                }
                __builtin_amdgcn_raw_buffer_store_b128(ov, rOut, r == crow ? BT * D_LEN * 2 : (r * D_LEN + ct * BT + n * 8) * 2, 0, 0);
            }
            FLUSH();
            return;
        }
#pragma unroll
        for (int i = 0; i < 8; ++i) {
            cvt(i >> 1, (i & 1) * 2); cvt(i >> 1, (i & 1) * 2 + 1);
            __builtin_amdgcn_raw_buffer_store_b128(hq[i >> 1][i & 1], rW, so, i * 1024, 16);
            SB();
        }
        WAIT_VM0();
        __builtin_amdgcn_raw_buffer_store_b32(1u, rW, sf, 0, 16);
        if (wid == 0) STAMP(13);
    } else {
        LAS unsigned char* l0 = lds + (wid - 4) * 4096;
        P2_DEFS;
        __amdgpu_buffer_rsrc_t rD = mk_rsrc(h == 0 ? a.wd + (size_t)sidx * 32 * D_LEN : a.wg,
                                                  sidx >= 112 ? 0 : h != 0 ? D_LEN * F_LEN * 2 : wid < 8 - BQ ? 32 * D_LEN * 2 : 0);
        int vGe = h == 0 ? wd_off(wid & 3, 0, lane)
                : gu_off(sidx, (wid & 3) * 2, lane);
        auto load_wd = [&](int i) __attribute__((always_inline)) {
            dma_nt(rD, (unsigned)(size_t)lds + (i < 8 ? L_WD0 : L_WD1) + (wid & 3) * 8192 + (i & 7) * 1024, vGe ^ ((2 * (i & 1) | (i & 4)) * 32), i * (4 * D_LEN));
        };
        const int lw = wid - 4;
        constexpr int NIL = 1 << 30;
        ring.rP = ring.rF = mk_rsrc(w2, (unsigned)WS_STATS);
        ring.voP[0] += (int)WS_AT + at_panel(h * 8 + ct);
        ring.voP[1] = ring.voP[2] = ring.voP[3] = ring.voP[0];
#define FLINE(k) (((k) >> 3) * 8192 + ((k) & 7) * 128)
        const int ft = (g & 1) * 32768 + (g >> 1) * 4 + (int)WS_FLAG1 + (lw == 0 ? ct * 1024 : NIL);
        int vall = FLINE(n) + ft + h * 16384;
        ring.voF = (n >> 2) == xq ? vall : NIL;
        ring.fl = lds + L_YB + (h == 0 ? lw * 8192 : lw ? 768 : 0) + 2560 + 256;
        unsigned sa = (unsigned)(size_t)lds + L_YB + 2560 + 256 + lane * 4;
        const unsigned tg = (unsigned)__builtin_amdgcn_readfirstlane((int)ctl1[0]);
        float wsum = __uint_as_float(ctl1[1]);
        ring.lb = lw * 4096;
        int cq[3], cp[3];
        unsigned mk[3];
#pragma unroll
        for (int k = 0; k < 3; ++k) {
            const int nb = xq * 4 + NBR(k + 1);
            cq[k] = nb * 256; cp[k] = at_tile(cq[k], ring.par); mk[k] = 0x10000u << nb;
            asm volatile("" : "+s"(cq[k]), "+s"(cp[k]), "+s"(mk[k]));
        }
        unsigned m2 = (mk[0] | mk[1] | mk[2]) >> 16;
        asm volatile("" : "+s"(m2), "+s"(ring.rP));
        asm volatile("" : "+v"(vall), "+v"(ring.voF), "+v"(ring.voP[0]), "+v"(sa), "+v"(wsum));
        ring.rF = ring.rP;
        ring.voP[1] = ring.voP[2] = ring.voP[3] = ring.voP[0];
        WAIT_VM(4);
        BARRIER();
        SB();
        int ve = Y_OFF + lane * 16;
        unsigned yd = (unsigned)(size_t)lds + L_YB + lw * 8192, fa = 0u;
        auto fetch = [&]() __attribute__((always_inline)) {
            if (wid == 4) STAMP(21);
            const int v4[4] = {ve, ve, ve, ve};
#pragma unroll
            for (int k = 0; k < 2; ++k) dma_4k<true>(ring.rP, yd + k * 4096, v4, k * 4096);
        };
        __amdgpu_buffer_rsrc_t rQ2 = mk_rsrc(h == 0 ? a.ci + (size_t)(rt * 4 + lw) * 32 * K : a.wu,
                                             h == 0 ? 32 * K * 2 : sidx < 112 ? D_LEN * F_LEN * 2 : 0);
        int vC2 = ((ct & 1) * 16 + (lane >> 2)) * (K * 2) + ((xq * 1024 + 2048 + (ct >> 1) * 512 + (lane & 3) * 128) & 4095);
        int vT2 = h == 0 && n < (KSHIFT + 1) / 2 ? FLINE((xq * 4 - 1 - n) & 15) + ft + 16384 : NIL;
        int vY = lane == 0 ? (int)WS_FLAGS + (rt * 8 + ct) * 128 + lw * 4 : NIL;
        int n1 = (h == 0 ? 32 : NT) - 9, n0 = n1 - 1 - (C2_START + 2 * ct + (idx & 1));
        int xm = (idx & 1) * 128;
        int c8 = (xq * 1024 + 1024) & 4095, p8 = at_tile(c8 ^ xm, ring.par);
        int cK = (xq * 1024 + (32 - KSHIFT) * 128) & 4095, pK = at_tile(cK ^ xm, ring.par) + at_panel(8);
        auto early = [&](int st, int i0, int i1) __attribute__((always_inline)) {
#pragma unroll
            for (int i = i0; i < i1; ++i) {
                const int m = i & 1, w = (i >> 1) & 1, j = i >> 2;
                dma_nt(m ? rQ2 : rD, (unsigned)(size_t)lds + st * STAGE_BYTES + m * 16384 + lw * 4096 + w * 2048 + j * 1024, vGe ^ (j * 64 + m * 512),
                       (32 * st + 4 * w + 2 * j) * (F_LEN * 2));
            }
        };
        asm volatile("" : "+v"(vGe));
        asm volatile("" : "+s"(rQ2), "+s"(n0), "+s"(n1), "+s"(c8), "+s"(p8), "+s"(cK), "+s"(pK), "+s"(xm), "+s"(rD));
        asm volatile("" : "+v"(ve), "+v"(vC2), "+v"(vT2), "+v"(vY));
        unsigned have = 0u;
        SB();
#define GATE(m) do { if (__builtin_expect(((m) & ~have) != 0u, 0)) ring.gate(tg, sa, m, have); SB(); } while (0)
#define TILE(k, o, st) do { ring.sp = cp[k] + (o) * 256; ring.sq = cq[k] + (o); ring.template issueP<16>(l0 + (st) * STAGE_BYTES); \
                            ring.issueQ(l0 + (st) * STAGE_BYTES); ring.sample(); SB(); } while (0)
        ring.gate(tg, sa, mk[0], have);
        if (wid == 4) STAMP(28);
        ring.sp = cp[0]; ring.template issueP<16>(l0 + 2 * STAGE_BYTES); ring.sample();
        BARRIER();
        GATE(mk[1]);
        ring.sp = cp[1]; ring.template issueP<16>(l0 + 3 * STAGE_BYTES); ring.sample();
        GATE(mk[2]);
        TILE(2, 0, 0);
        WAIT_VM(15); BARRIER();
        if (wid == 4) STAMP(30);
        GATE(m2);
        ring.voF = vall;
        TILE(0, 128, 1);
        WAIT_VM(19); BARRIER();
        TILE(1, 128, 2);
        WAIT_VM(19); BARRIER();
        ring.sp = cp[2] + 128 * 256; ring.template issueP<16>(l0 + 3 * STAGE_BYTES);
        ring.sq = cq[2] + 128;
        SB();
        {
            const unsigned long long m = __ballot(ring.peek(sa) == tg);
            have = (unsigned)m & (unsigned)(m >> 32);
            if (__builtin_expect(have != 0xffffffffu, 0)) ring.gate(tg, sa, 0xffffffffu, have);
        }
        ring.fl -= 256;
        ring.voF = vT2;
        SB();
        ring.issueQ(l0 + 3 * STAGE_BYTES);
#undef GATE
#undef TILE
        ring.sq = c8; ring.sp = p8; ring.poff = 0; ring.xm = xm;
        if (wid == 4) { STAMP(29); CSTAMP(34); STAMP(36); }
        ring.template steady<true, false, 18>(n1, n0, vC2);
        ring.fl += 512;
        if (wid == 4) { CSTAMP(35); STAMP(37); }
        ring.template issueP<16>(lds + ring.lb);
        SB();
        {
            const unsigned long long m = __ballot(ring.peek(sa + 256) == tg);
            have = (unsigned)m & (unsigned)(m >> 32);
            asm volatile("" : "+s"(have));
        }
        int hs = h;
        asm volatile("" : "+s"(hs));
        if (__builtin_expect(hs == 0, 1)) {
            constexpr unsigned MK = ((1u << ((KSHIFT + 1) / 2)) - 1u) * 0x10001u;
            if (__builtin_expect((MK & ~have) != 0u, 0)) ring.gate(tg, sa + 256, MK, have);
            unsigned lq = ring.lb + TILE_BYTES, pp = ring.par | at_panel(8);
            int sq = ring.sq ^ ring.xm, cnt = KSHIFT;
            ring.lb = (ring.lb + STAGE_BYTES) & (RING_BYTES - 1);
            ring.fl -= 512;
            fa = (unsigned)(size_t)ring.fl;
            asm volatile("" : "+s"(lq), "+s"(sq), "+s"(cnt), "+s"(ring.lb), "+s"(pp), "+s"(yd));
            asm volatile("" : "+v"(fa));
            SB();
            dma_4k<false>(ring.rQ, lq, ring.voQ, sq);
            ring.voF = vY;
            ring.rQ = rQ2;
            ring.sq = cK; ring.sp = pK; ring.poff = 0; ring.par = pp;
            ring.template steady<true, true>(cnt);
            auto seen = [&]() __attribute__((always_inline)) { return __builtin_amdgcn_readfirstlane(ring.peek(fa)) == 1u; };
            if (__builtin_expect(seen(), 1)) {
                fetch();
                WAIT_VM(16); BARRIER();
                WAIT_VM(8); BARRIER();
            } else {
                ring.sample();
                WAIT_VM(10); BARRIER();
                const bool have = seen();
                if (have) { fetch(); WAIT_VM(9); } else { ring.sample(); WAIT_VM(2); }
                BARRIER();
                if (!have) {
#pragma unroll 1
                    for (int spin = 0; spin <= SPIN_CAP && !seen(); ++spin) { ring.sample(); WAIT_VM(0); }
                    fetch();
                }
            }
            if (wid == 4) STAMP(20);
            WAIT_VM0();
        } else {
            ring.issueQ(lds + ring.lb);
            WAIT_VM(16); BARRIER();
            early(0, 0, 8);
            WAIT_VM(16); BARRIER();
            early(1, 0, 2);
            WAIT_VM(10); BARRIER();
            early(1, 2, 4);
        }
        BARRIER();
        if (h == 0) {
            const int slot = sidx;
            LAS unsigned char* abuf = lds + L_ATAB + 2048 + lw * 128;
#pragma unroll
            for (int i = 0; i < WD_EARLY; ++i) load_wd(i);
            SB();
            __amdgpu_buffer_rsrc_t rS = mk_rsrc(w2 + WS_STATS + (hp_ & 15) * 16384 + (hp_ >> 4) * 8192, 8192);
            __amdgpu_buffer_rsrc_t rC = mk_rsrc(w2 + WS_CAND + (size_t)hp_ * 4096, 4096);
            __amdgpu_buffer_rsrc_t rKy = mk_rsrc(w2 + WS_KEYS + (hp_ >> 2) * KEY_S + (hp_ & 3) * 4, ct == 0 ? 8192 + 3 * KEY_S * 16 + 4 : 0);
            int vs = lane * 16, vk = !(lane & 1) ? 1 << 20 : (lane >> 2) * 8192 + (lane & 3) * (KEY_S * 16);
            int vc = (lane < 32 ? 2048 : 0) + ct * 256 + (lane & 31) * 8;
            unsigned code = 0xffffu - (unsigned)(hp_ * 64 + lane), hb = (lane & 31) * 8;
            unsigned ra = (unsigned)(size_t)(rstat + (wq * 64 + lane) * 2), ht = (unsigned)(size_t)lds + L_HT + wq * 16384;
            u64 lo = 0xFFFFFFFFull;
            unsigned sr = (unsigned)(size_t)lds + L_ST + wq * 1024 + lane * 16;
            int vw = wp ? NIL : ct * 1024 + lane * 16;
            asm volatile("" : "+v"(sr), "+v"(vw));
            asm volatile("" : "+s"(rS), "+s"(rC), "+s"(rKy), "+s"(ht), "+s"(lo));
            asm volatile("" : "+v"(vs), "+v"(vk), "+v"(vc), "+v"(code), "+v"(hb), "+v"(ra));
            __builtin_amdgcn_s_setprio(TAIL_PRIO);
            SB();
            BARRIER();
            if (wp == 0) {
                const f32x4 u = *(const LAS f32x4*)(size_t)sr, v = *(const LAS f32x4*)((const LAS unsigned char*)(size_t)sr + 2048);
                const u32x4 w = {min(__float_as_uint(u[0] + v[0]), SENT - 1u), min(__float_as_uint(u[1] + v[1]), SENT - 1u), min(__float_as_uint(u[2] + v[2]), SENT - 1u), 0u};
                ST16_SC1(w, rS, vw);
            }
            SB();
            BARRIER();
            if (lane < 16) *(LAS unsigned*)(abuf + 64 + lane * 4) = 0u;
            __builtin_amdgcn_s_sleep(STAT_DELAY);
            if (wp == 0) {
                float s1, s2, s3;
#pragma unroll 1
                for (int spin = 0;; ++spin) {
                    asm volatile("" ::: "memory");
                    u32x3 sv[8];
#pragma unroll
                    for (int j = 0; j < 8; ++j) { sv[j] = __builtin_amdgcn_raw_buffer_load_b96(rS, vs, j * 1024, 16); SB(); }
#pragma unroll
                    for (int j = 0; j < 8; ++j) {
                        const unsigned u0 = sv[j][0], u1 = sv[j][1], u2 = sv[j][2];
                        s1 = j ? s1 + __uint_as_float(u0) : __uint_as_float(u0);
                        s2 = j ? s2 + __uint_as_float(u1) : __uint_as_float(u1);
                        s3 = j ? s3 + __uint_as_float(u2) : __uint_as_float(u2);
                    }
                    float z = s1 + s3;
                    asm volatile("" : "+v"(z));
                    if (__builtin_expect(__builtin_amdgcn_ballot_w64(__builtin_isunordered(z, s2)) == 0, 1)) break;
                    unsigned mx = 0u;
#pragma unroll
                    for (int j = 0; j < 8; ++j) mx = max(max(mx, sv[j][0]), max(sv[j][1], sv[j][2]));
                    if (__all(mx != SENT) || spin > SPIN_CAP) break;
                }
                if (wid == 4) STAMP(4);
                const float mean = s1 * (1.0f / D_LEN);
                const float var = fmaxf(s2 * (1.0f / D_LEN) - mean * mean, 0.f);
                const float mb = rbf(mean), rden = __builtin_amdgcn_rcpf(rbf(__builtin_amdgcn_sqrtf(rbf(rbf(var) + 1e-5f))));
                const unsigned kb = wave_max_s(r2key((s3 - mb * wsum) * rden, code));
                __builtin_amdgcn_raw_buffer_store_b32(kb, rKy, vk, 0, 16);
                SB();
                {
                    const unsigned cr_ = ~kb & 63u;
                    u32x2 c = *(const LAS u32x2*)(size_t)(ht + cr_ * 256 + (hb ^ ((cr_ & 15) * 16)));
                    const float m_ = __int_as_float(__builtin_amdgcn_readlane(__float_as_int(mb), cr_));
                    const float d_ = __int_as_float(__builtin_amdgcn_readlane(__float_as_int(rden), cr_));
#pragma unroll
                    for (int i = 0; i < 2; ++i) {
                        const unsigned u = c[i];
                        const unsigned dd = pk_bf16(bflo(u) - m_, bfhi(u) - m_);
                        const unsigned q = pk_bf16(bflo(dd) * d_, bfhi(dd) * d_);
                        c[i] = min(__float_as_uint(pick(lo, __uint_as_float(u), __uint_as_float(q))), SENT - 1u);
                    }
                    __builtin_amdgcn_raw_buffer_store_b64(c, rC, vc, 0, 16);
                    SB();
                    *(LAS u32x2*)(size_t)ra = (u32x2){__float_as_uint(mb), __float_as_uint(rden)};
                    if (lane == 0) ctl[2 + wq] = cr_;
                }
                WAIT_LGKM0();
                if (wid == 4) STAMP(5);
            }
            BARRIER();
#pragma unroll
            for (int i = WD_EARLY; i < 16; ++i) load_wd(i);
            if (slot < 112 && wid < 8 - BQ) {
                stage2<S2_W, 8>(w2, slot, lw, lds + L_WD0 + lw * 8192, lds + L_WD1 + lw * 8192, abuf, 64, lane);
                if (wid == 4) STAMP(11);
            }
            FLUSH();
            return;
        }
    }

    P2_DEFS;
    LAS float* red = (LAS float*)(lds + L_RED);
    if (sidx < 112) {
        const int slot = sidx, grp = slot >> 3, kq = slot & 7;
        const __amdgpu_buffer_rsrc_t rG = mk_rsrc(a.wg, D_LEN * F_LEN * 2), rU = mk_rsrc(a.wu, D_LEN * F_LEN * 2);
        const int vgu = gu_off(slot, wid, lane);
        auto pair = [&](int st, int j) __attribute__((always_inline)) {
            dma_nt(rG, (unsigned)(size_t)lds + st * STAGE_BYTES + wid * 2048 + j * 1024, vgu ^ (j * 64), (32 * st + 2 * j) * (F_LEN * 2));
            dma_nt(rU, (unsigned)(size_t)lds + st * STAGE_BYTES + 16384 + wid * 2048 + j * 1024, vgu ^ (j * 64 + 512), (32 * st + 2 * j) * (F_LEN * 2));
        };
        if (wid >= 4) {
            auto one = [&](int st, int j, int m) __attribute__((always_inline)) {
                wait_vm<TH_LIM>();
                dma_nt(m ? rU : rG, (unsigned)(size_t)lds + st * STAGE_BYTES + m * 16384 + wid * 2048 + j * 1024, vgu ^ (j * 64 + m * 512), (32 * st + 2 * j) * (F_LEN * 2));
            };
#pragma unroll
            for (int i = 2; i < 2 + TH_N; ++i) one(1 + (i >> 2), (i >> 1) & 1, i & 1);
            BARRIER();
#pragma unroll
            for (int i = 2 + TH_N; i < 12; i += 2) pair(1 + (i >> 2), (i >> 1) & 1);
        } else {
            BARRIER();
            pair(1, 1); pair(2, 0); pair(2, 1); pair(3, 0); pair(3, 1);
        }
        if (wid >= 8 - BQ) {
            const __amdgpu_buffer_rsrc_t rD = mk_rsrc(a.wd + (size_t)slot * 32 * D_LEN, 32 * D_LEN * 2);
            const int vd = wd_off(wid - 4, 0, lane);
#pragma unroll
            for (int i = 0; i < 16; ++i)
                dma_nt(rD, (unsigned)(size_t)lds + L_YB + (wid & 1) * 16384 + i * 1024, vd ^ ((2 * (i & 1) | (i & 4)) * 32), i * (4 * D_LEN));
        }
        const __amdgpu_buffer_rsrc_t rK = mk_rsrc(w2, wid == 0 ? (unsigned)(WS_CAND + 32 * 4096) : 0u);
        const int ok_ = lane < 40 ? (int)WS_KEYS + (xcd >> 2) * 8192 + (xcd & 3) * (KEY_S * 16) + (lane - 32) * KEY_S : (int)(WS_CAND + 32 * 4096);
        const int oc = (int)WS_CAND + 2048 + kq * 256 + n * 16;
        int best = 0;
        u32x4 v = {0u, 0u, 0u, 0u};
        auto request = [&]() {
            asm volatile("" ::: "memory");
            v = LD16_SC1(rK, lane < 32 ? oc + best * 4096 : ok_);
            SB();
        };
        auto settled = [&]() {
            const unsigned mx = umax4(v), mn = min(min(v[0], v[1]), min(v[2], v[3]));
            unsigned fv = lane < 32 ? ~mx : mn | (unsigned)(lane >> 3 ^ 4);
            asm volatile("" : "+v"(fv));
            const bool fine = fv != 0u;
            unsigned km = max(mx, dppu<0xB1>(mx));
            km = max(km, dppu<0x4E>(km));
            km = max(km, dppu<0x141>(km));
            const int nb = (int)(((0xffffu - ((unsigned)__builtin_amdgcn_readlane((int)km, 32) & 0xffffu)) >> 6) & 31u);
            const bool same = nb == best;
            best = nb;
            return __all(fine) && same;
        };
        if (wid == 0) {
            request();
            WAIT_VM(0);
            settled();
            request();
        } else if (wid >= 8 - BQ) WAIT_VM(16);
        else WAIT_VM(0);
        BARRIER();
        if (wid == 0) STAMP(8);
        bf16x8 wfr[4][4];
        {
            const int q = (lane >> 2) & 3, ky = q | ((g & 1) << 2);
#pragma unroll
            for (int u = 0; u < 4; ++u) {
                const int tro = (u >> 1) * 16384 + (8 * g + q) * 512 + (((wid * 2 + (u & 1)) ^ ky) * 32) + (lane & 3) * 8;
#pragma unroll
                for (int ks = 0; ks < 4; ++ks) {
                    const bf16x4 w0 = __builtin_amdgcn_ds_read_tr16_b64_v4i16((LAS bf16x4*)(lds + tro + ks * STAGE_BYTES));
                    const bf16x4 w1 = __builtin_amdgcn_ds_read_tr16_b64_v4i16((LAS bf16x4*)(lds + tro + ks * STAGE_BYTES + 2048));
                    wfr[u][ks] = __builtin_shufflevector(w0, w1, 0, 1, 2, 3, 4, 5, 6, 7);
                }
            }
            SB();
#pragma unroll
            for (int i = 0; i < 16; ++i) asm volatile("" : "+v"(wfr[i >> 2][i & 3]));
        }
        if (wid >= 8 - BQ && lane < 16) *(LAS unsigned*)(lds + 8192 + wid * 64 + lane * 4) = 0u;
        __amdgpu_buffer_rsrc_t rPo = mk_rsrc(w2 + WS_PRE, 112 * 2048);
        int po = (n & 3) == 0 ? pre_off((grp ^ (g >> 1)) * 8 + wid) + kq * 256 + g * 64 + n * 4 : 112 * 2048;
        unsigned xw = (unsigned)(size_t)lds + (n >> 3) * 16384 + (g & 1) * 8192 + (n & 7) * 8, xr = (unsigned)(size_t)lds + (n & 1) * 8192 + g * 16;
        unsigned ix = (n & 1) ? 0xEEEEEEEEu : 0x44444444u;
        asm volatile("" : "+v"(po), "+v"(xw), "+v"(xr), "+v"(ix));
        u64 m1 = 0xFFFF0000FFFF0000ull, m2 = 0xFFFFFFFF00000000ull;
        f32x4 ac[4];
#pragma unroll
        for (int u = 0; u < 4; ++u) { ac[u] = (f32x4){0.f, 0.f, 0.f, 0.f}; asm volatile("" : "+v"(ac[u])); }
        asm volatile("" : "+s"(rPo), "+s"(m1), "+s"(m2));
        if (wid == 0) {
#pragma unroll 1
            for (int spin = 0; spin <= SPIN_CAP && !settled(); ++spin) {
                __builtin_amdgcn_s_sleep(1);
                request();
            }
            __builtin_amdgcn_s_waitcnt(0x0F70);
            if (lane < 32) *(LAS u32x2*)(size_t)xw = (u32x2){lane < 16 ? v[0] : v[1], lane < 16 ? v[2] : v[3]};
            WAIT_LGKM0();
            STAMP(9);
        }
        BARRIER();
        {
            bf16x8 xf[2];
#pragma unroll
            for (int j = 0; j < 2; ++j) xf[j] = *(const LAS bf16x8*)((const LAS unsigned char*)(size_t)xr + j * 16384);
            SB();
            float rs[3];
#pragma unroll
            for (int i = 0; i < 8; ++i) {
                const int u = i >> 1, j = i & 1;
                ac[u] = smf(xf[j], wfr[u][2 * j], wfr[u][2 * j + 1], ac[u], ix);
                SB();
                if (i == 4) { asm volatile("" : "+v"(ac[0])); rs[0] = ac[0][0] + ac[0][1]; asm volatile("" : "+v"(rs[0])); }
                if (i == 6) { asm volatile("" : "+v"(ac[1])); rs[0] = pick(m1, rs[0], ac[1][0] + ac[1][1]); asm volatile("" : "+v"(rs[0])); }
                if (i == 7) { asm volatile("" : "+v"(ac[2])); rs[2] = ac[2][0] + ac[2][1]; asm volatile("" : "+v"(rs[2])); }
                SB();
            }
            const unsigned e = min(__float_as_uint(pick(m2, rs[0], pick(m1, rs[2], ac[3][0] + ac[3][1]))), SENT - 1u);
            const u32x4 sv = {e, dppu<0x55>(e), dppu<0xAA>(e), dppu<0xFF>(e)};
            ST16_SC1(sv, rPo, po);
            SB();
            asm volatile("" :: "v"(v));
        }
        if (wid == 0) STAMP(14);
        if (wid >= 8 - BQ) {
            stage2<1, -1>(w2, slot, wid - 4, lds + L_YB + (wid & 1) * 16384, lds + L_YB + (wid & 1) * 16384 + 8192, lds + wid * 64, 8192, lane);
            if (wid == 7) STAMP(10);
        }
    } else {
        const int rj = sidx - 112;
        if (blockIdx.x == 255 && tid == 0) {
            const u64 want = ((u64)ctl1[3] << 32) | ctl1[2];
            __hip_atomic_store((GAS u64*)(ws + WS_TAG), want, __ATOMIC_RELAXED, __HIP_MEMORY_SCOPE_AGENT);
        }
        if (wid == 4) {
            unsigned kk;
            const unsigned k = poll_winner(w2, xcd, lane, kk);
            if (lane == 0) ctl[4] = k;
            if (lane < 32) ctl[8 + lane] = kk;
            WAIT_LGKM0();
        }
        BARRIER();
        const unsigned kw = CTL(4);
        if (wid == 0) STAMP(16);
        {
            const unsigned kc = ctl[8 + wid * 4 + g];
            const __amdgpu_buffer_rsrc_t rL = mk_rsrc(w2 + WS_CAND + (size_t)wid * 16384, 16384);
            u32x2 hv2, nv2;
            for (int spin = 0;; ++spin) {
                asm volatile("" ::: "memory");
                hv2 = __builtin_amdgcn_raw_buffer_load_b64(rL, g * 4096 + (rj * 64 + n * 4) * 2, 0, 16);
                nv2 = __builtin_amdgcn_raw_buffer_load_b64(rL, g * 4096 + 2048 + (rj * 64 + n * 4) * 2, 0, 16);
                if (__all(max(max(hv2[0], hv2[1]), max(nv2[0], nv2[1])) != SENT) || spin > SPIN_CAP) break;
                __builtin_amdgcn_s_sleep(1);
            }
            u32x2 ov;
#pragma unroll
            for (int i = 0; i < 2; ++i) {
                const unsigned u = hv2[i], q = nv2[i];
                ov[i] = pk_bf16(bflo(u) + bflo(q), bfhi(u) + bfhi(q));
            }
            const int crow = (int)(0xffffu - (kc & 0xffffu)) & (S_LEN - 1);
            if (kc != kw) *(u32x2*)(a.out + (size_t)crow * D_LEN + rj * 64 + n * 4) = ov;
        }
        const int wrow = (int)(0xffffu - (kw & 0xffffu)) & (S_LEN - 1);
        const __amdgpu_buffer_rsrc_t rC = mk_rsrc(w2 + WS_CAND + (size_t)(wrow >> 6) * 4096, 4096);
        const __amdgpu_buffer_rsrc_t rPt = mk_rsrc(w2 + WS_PART, 112 * 4096);
        const int ocol = (rj >> 2) * 256 + ((lane >> 1) & 1) * 128 + (rj & 3) * 32 + (lane & 1) * 16 + (lane >> 2);
        unsigned hv, hnv;
        for (int spin = 0;; ++spin) {
            asm volatile("" ::: "memory");
            hv = __builtin_amdgcn_raw_buffer_load_b32(rC, (ocol & ~1) * 2, 0, 16);
            hnv = __builtin_amdgcn_raw_buffer_load_b32(rC, 2048 + (ocol & ~1) * 2, 0, 16);
            if (__all(max(hv, hnv) != SENT) || spin > SPIN_CAP) break;
        }
        float sgm = rbf(1.0f / (1.0f + __expf(-key2r(kw))));
        float hf = (ocol & 1) ? bfhi(hv) : bflo(hv), hnf = (ocol & 1) ? bfhi(hnv) : bflo(hnv);
        GAS unsigned short* optr = (GAS unsigned short*)(a.out + (size_t)wrow * D_LEN + ocol);
        unsigned rw = (unsigned)(size_t)red + (wid >> 2) * 1024 + (n * 4 + g) * 16 + (wid & 3) * 4, rr = (unsigned)(size_t)red + lane * 16;
        int po = wid * 16384 + (rj >> 3) * 8192 + g * 2048 + ((rj & 7) * 64 + n * 4) * 4;
        int po3 = po + 3 * 131072;
        asm volatile("" : "+v"(sgm), "+v"(optr), "+v"(hf), "+v"(hnf), "+v"(rw), "+v"(rr), "+v"(po), "+v"(po3));
        float sm;
#pragma unroll 1
        for (int spin = 0;; ++spin) {
            asm volatile("" ::: "memory");
            u32x4 pv[4];
#pragma unroll
            for (int i = 0; i < 4; ++i) pv[i] = __builtin_amdgcn_raw_buffer_load_b128(rPt, i < 3 ? po : po3, i < 3 ? i * 131072 : 0, 16);
            SB();
            unsigned t[4];
#pragma unroll
            for (int j = 0; j < 4; ++j) {
                const unsigned u0 = pv[0][j], u1 = pv[1][j], u2 = pv[2][j], u3 = pv[3][j];
                t[j] = __float_as_uint(((__uint_as_float(u0) + __uint_as_float(u1)) + __uint_as_float(u2)) + __uint_as_float(u3));
            }
            const auto r0 = __builtin_amdgcn_permlane32_swap(t[0], t[2], false, false), r1 = __builtin_amdgcn_permlane32_swap(t[1], t[3], false, false);
            const unsigned a0 = r0[0], a1 = r0[1], b0 = r1[0], b1 = r1[1];
            const auto r2 = __builtin_amdgcn_permlane16_swap(__float_as_uint(__uint_as_float(a0) + __uint_as_float(a1)),
                                                             __float_as_uint(__uint_as_float(b0) + __uint_as_float(b1)), false, false);
            const unsigned c0 = r2[0], c1 = r2[1];
            sm = __uint_as_float(c0) + __uint_as_float(c1);
            if (__builtin_expect(__builtin_amdgcn_ballot_w64(sm != sm) == 0, 1)) break;
            if (__all(max(max(umax4(pv[0]), umax4(pv[1])), max(umax4(pv[2]), umax4(pv[3]))) != SENT) || spin > SPIN_CAP) break;
            if (FIN_SLEEP) __builtin_amdgcn_s_sleep(FIN_SLEEP);
        }
        if (wid == 0) STAMP(17);
        *(LAS float*)(size_t)rw = sm;
        WAIT_LGKM0();
        BARRIER();
        if (wid == 0) {
            const f32x4 u = *(const LAS f32x4*)(size_t)rr, v = *(const LAS f32x4*)((const LAS unsigned char*)(size_t)rr + 1024);
            const f32x4 w = u + v;
            const float t = (w[0] + w[2]) + (w[1] + w[3]);
            *optr = (unsigned short)pk_bf16(hf + rbf(hnf + rbf(rbf(t) * sgm)), 0.f);
            STAMP(18);
            FLUSH();
            asm volatile("s_endpgm" ::: "memory");
        }
        __builtin_amdgcn_s_waitcnt(0x0F70);
    }
    FLUSH();
}

extern "C" void kernel_launch(void* const* d_in, const int* in_sizes, int n_in, void* d_out, int out_size, void* d_ws,
                              size_t ws_size, hipStream_t stream) {
    Args a;
    a.x = (const bf16_t*)d_in[0];
    a.cr = (const bf16_t*)d_in[1];
    a.ci = (const bf16_t*)d_in[2];
    a.wre = (const bf16_t*)d_in[3];
    a.wim = (const bf16_t*)d_in[4];
    a.w_r = (const bf16_t*)d_in[6];
    a.wg = (const bf16_t*)d_in[7];
    a.wu = (const bf16_t*)d_in[8];
    a.wd = (const bf16_t*)d_in[9];
    a.out = (bf16_t*)d_out;
    a.ws = (char*)d_ws + ((ws_size - ((size_t)24 << 20)) & ~(size_t)255);
    (void)hipFuncSetAttribute((const void*)fused_kernel, hipFuncAttributeMaxDynamicSharedMemorySize, LDS_BYTES);
    fused_kernel<<<256, 768, LDS_BYTES, stream>>>(a);
}
```
